```python
import torch

def _softmax(x, axis=-1):
    return torch.softmax(x, dim=axis)
def _gelu(x):
    return torch.nn.functional.gelu(x, approximate="tanh")
def _silu(x):
    return torch.nn.functional.silu(x)
def _sigmoid(x):
    return torch.sigmoid(x)
def _layernorm(x, axis=-1, eps=1e-5):
    m = x.mean(dim=axis, keepdim=True)
    v = x.var(dim=axis, keepdim=True, unbiased=False)
    return (x - m) / torch.sqrt(v + eps)
def _scatter_add(tgt, idx, src, axis=0):
    return tgt.clone().index_add_(axis, idx, src)
def _segment_sum(src, seg, n):
    out = torch.zeros((n,) + src.shape[1:], dtype=src.dtype)
    return out.index_add_(0, seg, src)
def _pad(x, pw):
    flat = tuple(v for pair in reversed(pw) for v in pair)
    return torch.nn.functional.pad(x, flat)

kernel_name = 'recurrent_scan__sliding_block_cached__mixer_token_mlp__prompt'

def setup_inputs():

    _w = (lambda a: (lambda t: t.to(torch.bfloat16) if t.is_floating_point() else t)(torch.as_tensor(a)))
    def _A_setup():
        B, Sq, D = 1, 2048, 1024
        Hq, Hkv, Sk, Dh = 32, 4, 2048, 128
        assert Hq % Hkv == 0, (Hq, Hkv)
        return {
            "x": _w((lambda *s: torch.randn(*s, dtype=torch.bfloat16) * 0.05)(B, Sq, D)),
            "k_cache": _w((lambda *s: torch.randn(*s, dtype=torch.bfloat16) * 1.6)(B, Hkv, Sk, Dh)),
            "v_cache": _w((lambda *s: torch.randn(*s, dtype=torch.bfloat16) * 0.8)(B, Hkv, Sk, Dh)),
            "wq": _w((lambda *s: torch.randn(*s, dtype=torch.bfloat16) * 1.6)(D, Hq * Dh) * (D ** -0.5)),
            "wo": _w((lambda *s: torch.randn(*s, dtype=torch.bfloat16) * 0.8)(Hq * Dh, D) * ((Hq * Dh) ** -0.5)),
        }
    _A_in = _A_setup()

    def _B_setup():
        B, S, D, F = 1, 2048, 1024, 3584
        return {
            "x": _w((lambda *s: torch.randn(*s, dtype=torch.bfloat16) * 0.05)(B, S, D)),
            "w1": _w((lambda *s: torch.randn(*s, dtype=torch.bfloat16) * 1.6)(S, F) * (S ** -0.5)),
            "b1": _w((lambda *s: torch.randn(*s, dtype=torch.bfloat16) * 1.6)(F) * 0.02),
            "w2": _w((lambda *s: torch.randn(*s, dtype=torch.bfloat16) * 0.2)(F, S) * (F ** -0.5)),
            "b2": _w((lambda *s: torch.randn(*s, dtype=torch.bfloat16) * 0.2)(S) * 0.02),
        }
    _B_in = _B_setup()

    return {
        "A_x": _A_in["x"],
        "A_k_cache": _A_in["k_cache"],
        "A_v_cache": _A_in["v_cache"],
        "A_wq": _A_in["wq"],
        "A_wo": _A_in["wo"],
        "B_x": _B_in["x"],
        "B_w1": _B_in["w1"],
        "B_b1": _B_in["b1"],
        "B_w2": _B_in["w2"],
        "B_b2": _B_in["b2"],
    }

def reference(A_x, A_k_cache, A_v_cache, A_wq, A_wo, B_x, B_w1, B_b1, B_w2, B_b2):
    def _A_ref(x, k_cache, v_cache, wq, wo):
        B, Sq, D = x.shape
        Hq, Dh = 32, 128
        Hkv, Sk = k_cache.shape[1], k_cache.shape[2]
        W = min(4096, max(256, Sk // 4))
        rep = Hq // Hkv
        q = (lambda x, *d: x.permute(*d))((x @ wq).reshape(B, Sq, Hq, Dh), 0, 2, 1, 3)
        k = (lambda x, n, axis: torch.repeat_interleave(x, n, dim=axis))(k_cache[:, :, Sk - W:, :], rep, axis=1)
        v = (lambda x, n, axis: torch.repeat_interleave(x, n, dim=axis))(v_cache[:, :, Sk - W:, :], rep, axis=1)
        scores = torch.einsum("bhqd,bhkd->bhqk", q, k) * (Dh ** -0.5)
        p = _softmax(scores, axis=-1)
        o = torch.einsum("bhqk,bhkd->bhqd", p, v)
        return (lambda x, *d: x.permute(*d))(o, 0, 2, 1, 3).reshape(B, Sq, Hq * Dh) @ wo

    def _B_ref(x, w1, b1, w2, b2):
        B, S, D = x.shape
        h = _layernorm(x, axis=-1)
        u = torch.einsum("bsd,sf->bdf", h, w1) + b1
        u = _gelu(u)
        y = torch.einsum("bdf,fs->bds", u, w2) + b2
        return (lambda x, *d: x.permute(*d))(y, 0, 2, 1)

    x = A_x
    xn = _layernorm(x, axis=-1)
    s = _A_ref(xn, A_k_cache, A_v_cache, A_wq, A_wo)
    g = _silu(_B_ref(xn, B_w1, B_b1, B_w2, B_b2))
    return x + s * g
```

```cpp
#include <hip/hip_runtime.h>
#include <hip/hip_bf16.h>

typedef unsigned short u16;
typedef short bf16x8 __attribute__((ext_vector_type(8)));
typedef short s16x4 __attribute__((ext_vector_type(4)));
typedef float f32x16 __attribute__((ext_vector_type(16)));
typedef float f32x4 __attribute__((ext_vector_type(4)));
typedef float f32x2 __attribute__((ext_vector_type(2)));
typedef unsigned u32x4 __attribute__((ext_vector_type(4)));
typedef unsigned u32x2 __attribute__((ext_vector_type(2)));
typedef __bf16 bf16x2 __attribute__((ext_vector_type(2)));
typedef __amdgpu_buffer_rsrc_t rsrc_t;
#define LAS __attribute__((address_space(3)))
#define GAS __attribute__((address_space(1)))
#define DI __device__ __forceinline__
#define SGB(m, n) __builtin_amdgcn_sched_group_barrier(m, n, 0)
#define SB0() __builtin_amdgcn_sched_barrier(0)

constexpr int SEQ = 2048, DM = 1024, FF = 3584, HD = 4096;
constexpr size_t WS_FLAG = 0;
constexpr size_t WS_XN = 65536;
constexpr size_t WS_HT = WS_XN + 4194304;
constexpr size_t WS_KV = WS_HT + 4194304;
constexpr size_t WS_O = WS_KV + 1048576;
constexpr size_t WS_U = WS_O + 16777216;
constexpr size_t WS_G = WS_U + 7340032;
constexpr size_t WS_END = WS_G + 4194304;

#define STAMP(kid, i)
DI unsigned cvtpk(float lo, float hi) { f32x2 v = {lo, hi}; bf16x2 b = __builtin_convertvector(v, bf16x2); return __builtin_bit_cast(unsigned, b); }
DI float bflo(unsigned u) { return __uint_as_float(u << 16); }
DI float bfhi(unsigned u) { return __uint_as_float(u & 0xffff0000u); }
DI float rbf(float x) { return bflo(cvtpk(x, x)); }
template <int N> DI void wait_vm() { asm volatile("s_waitcnt vmcnt(%0)" ::"i"(N) : "memory"); }
DI void wait_lgkm0() { __builtin_amdgcn_s_waitcnt(0xC07F); }
DI void bar() { asm volatile("" ::: "memory"); __builtin_amdgcn_s_barrier(); asm volatile("" ::: "memory"); }
DI rsrc_t mkrsrc(const void* p, unsigned bytes) { return __builtin_amdgcn_make_buffer_rsrc((void*)p, 0, bytes, 0x00020000); }
DI void dma16(rsrc_t r, unsigned lds_addr, unsigned voff, unsigned soff) {
  asm volatile("s_mov_b32 m0, %0\n\ts_nop 0\n\tbuffer_load_dwordx4 %1, %2, %3 offen lds" ::"s"(lds_addr), "v"(voff), "s"(r), "s"(soff) : "memory");
}
DI void dma4k(rsrc_t r, unsigned lds_addr, unsigned voff, unsigned soff) {
  asm volatile("s_mov_b32 m0, %0\n\ts_nop 0\n\tbuffer_load_dwordx4 %1, %2, %3 offen lds\n\tbuffer_load_dwordx4 %1, %2, %3 offen offset:1024 lds\n\t"
               "buffer_load_dwordx4 %1, %2, %3 offen offset:2048 lds\n\tbuffer_load_dwordx4 %1, %2, %3 offen offset:3072 lds"
               ::"s"(lds_addr), "v"(voff), "s"(r), "s"(soff) : "memory");
}
template <int W> DI void pf_line(unsigned& sink, int wid, rsrc_t r, unsigned voff, unsigned soff) {
  asm volatile("s_cmp_lg_u32 %1, %5\n\ts_cbranch_scc1 1f\n\tbuffer_load_dword %0, %2, %3, %4 offen\n1:"
               : "+v"(sink) : "s"(wid), "v"(voff), "s"(r), "s"(soff), "i"(W) : "scc", "memory");
}
template <int CTRL> DI float dppf(float x) { return __builtin_bit_cast(float, __builtin_amdgcn_mov_dpp(__builtin_bit_cast(int, x), CTRL, 0xf, 0xf, true)); }
DI float wave_sum(float v) {
  v += dppf<0xB1>(v); v += dppf<0x4E>(v); v += dppf<0x141>(v); v += dppf<0x140>(v);
  auto a = __builtin_amdgcn_permlane16_swap(__float_as_uint(v), __float_as_uint(v), false, false);
  v = __uint_as_float(a[0]) + __uint_as_float(a[1]);
  auto b = __builtin_amdgcn_permlane32_swap(__float_as_uint(v), __float_as_uint(v), false, false);
  return __uint_as_float(b[0]) + __uint_as_float(b[1]);
}

DI void ln16(unsigned (&w)[8]) {
  float v[16];
#pragma unroll
  for (int i = 0; i < 8; ++i) { v[2 * i] = bflo(w[i]); v[2 * i + 1] = bfhi(w[i]); }
  float s0 = 0.f, s1 = 0.f;
#pragma unroll
  for (int i = 0; i < 8; ++i) { s0 += v[2 * i]; s1 += v[2 * i + 1]; }
  const float mean = wave_sum(s0 + s1) * (1.f / 1024.f);
  float q0 = 0.f, q1 = 0.f;
#pragma unroll
  for (int i = 0; i < 8; ++i) { const float d0 = v[2 * i] - mean, d1 = v[2 * i + 1] - mean; q0 += d0 * d0; q1 += d1 * d1; }
  const float var = wave_sum(q0 + q1) * (1.f / 1024.f);
  const float mb = rbf(mean);
  const float rs = 1.f / rbf(sqrtf(rbf(rbf(var) + 1e-5f)));
#pragma unroll
  for (int i = 0; i < 8; ++i) { const unsigned c = cvtpk(v[2 * i] - mb, v[2 * i + 1] - mb); w[i] = cvtpk(bflo(c) * rs, bfhi(c) * rs); }
}

__global__ __launch_bounds__(1024) void k_prep(const u16* __restrict__ x, const u16* __restrict__ kc, const u16* __restrict__ vc,
                                               const u16* __restrict__ wq, char* __restrict__ ws) {
  extern __shared__ __attribute__((aligned(16))) char smem[];
  LAS char* lds = (LAS char*)smem;
  const int tid = threadIdx.x, wv = tid >> 6, lane = tid & 63, b = blockIdx.x;
  STAMP(0, 0);
  if (b < 128) {
    constexpr int HRS = 2080, HOFF = 32768;
    const int m = b >> 1, u = b & 1, ks = lane >> 1, od = lane & 1, w = wv & 7, rr = wv >> 3;
    const u16* xr = x + (size_t)(32 * m + 8 * u + w + 16 * rr) * DM + 32 * ks + 8 * od;
    const u32x4 A = *(const u32x4*)xr, B = *(const u32x4*)(xr + 16);
    unsigned v[8] = {A[0], A[1], A[2], A[3], B[0], B[1], B[2], B[3]};
    ln16(v);
    const int slot = (w ^ (lane & 7)) * 16;
    *(LAS u32x4*)(lds + ((ks * 2 + rr) * 4 + 2 * od) * 128 + slot) = (u32x4){v[0], v[1], v[4], v[5]};
    *(LAS u32x4*)(lds + ((ks * 2 + rr) * 4 + 2 * od + 1) * 128 + slot) = (u32x4){v[2], v[3], v[6], v[7]};
    ln16(v);
    *(LAS u32x4*)(lds + HOFF + wv * HRS + (32 * ks + 8 * od) * 2) = (u32x4){v[0], v[1], v[2], v[3]};
    *(LAS u32x4*)(lds + HOFF + wv * HRS + (32 * ks + 16 + 8 * od) * 2) = (u32x4){v[4], v[5], v[6], v[7]};
    __syncthreads();
    char* xn = ws + WS_XN;
    const int rb = m >> 3, xb0 = (2 * m) & 15;
#pragma unroll
    for (int it = 0; it < 2; ++it) {
      const int run = it * 128 + (tid >> 3), r8 = tid & 7;
      const int k2 = run >> 3, r2 = (run >> 2) & 1, gp = run & 3;
      const int sw = ((k2 & 3) << 1) | (gp >> 1);
      const u32x4 val = *(LAS u32x4*)(lds + run * 128 + ((r8 ^ sw) * 16));
      *(u32x4*)(xn + ((size_t)((rb * 32 + k2) * 16 + xb0 + r2)) * 1024 + (16 * gp + 8 * u + r8) * 16) = val;
    }
    char* ht = ws + WS_HT;
    const int grp = lane >> 4, gg = grp & 1, q = (lane >> 2) & 3, p = lane & 3;
#pragma unroll
    for (int it = 0; it < 2; ++it) {
      const int dblk = (wv * 2 + it) * 2 + (grp >> 1);
      const unsigned a0 = HOFF + (4 * gg + q) * HRS + dblk * 32 + p * 8;
      s16x4 lo = __builtin_amdgcn_ds_read_tr16_b64_v4i16((LAS s16x4*)(lds + a0));
      s16x4 hi = __builtin_amdgcn_ds_read_tr16_b64_v4i16((LAS s16x4*)(lds + a0 + 8 * HRS));
      bf16x8 pc = __builtin_shufflevector(lo, hi, 0, 1, 2, 3, 4, 5, 6, 7);
      *(bf16x8*)(ht + ((size_t)(((dblk >> 3) * 64 + m) * 8 + (dblk & 7))) * 1024 + (16 * (2 * u + gg) + (lane & 15)) * 16) = pc;
    }
  } else {
    const int bb = b - 128;
    if (tid >= 512) {
      const int j = (b & 7) >> 1, t = (bb >> 3) * 512 + (tid - 512);
      const char* p0 = (const char*)wq + (size_t)(t >> 4) * 8192 + j * 2048 + (t & 15) * 128;
      const unsigned d0 = *(const unsigned*)p0, d1 = *(const unsigned*)(p0 + (size_t)512 * 8192);
      asm volatile("" ::"v"(d0), "v"(d1));
    } else {
      if (bb == 0 && tid < 128) *(unsigned*)(ws + WS_FLAG + tid * 256) = 0u;
      const int T = bb * 512 + tid;
      const int frag = (T >> 6) & 7, unit = (T >> 9) & 31, kvh = T >> 14, h = lane >> 5;
      u32x4 o;
      if (unit == 0 || ((unit & 1) && unit < 31)) {
        const int blk = (unit + 1) >> 1;
        const u16* src = kc + ((size_t)kvh * SEQ + 1536 + 32 * blk + (lane & 31)) * 128 + 32 * (frag >> 1) + 4 * (2 * (frag & 1) + h);
        const u32x2 lo = *(const u32x2*)src, hi = *(const u32x2*)(src + 16);
        o = (u32x4){lo[0], lo[1], hi[0], hi[1]};
      } else {
        const int blk = (unit - 1) >> 1;
        const u16* src = vc + ((size_t)kvh * SEQ + 1536 + 32 * blk + 16 * (frag >> 2) + 4 * h) * 128 + 32 * (frag & 3) + (lane & 31);
        unsigned e[8];
#pragma unroll
        for (int j2 = 0; j2 < 8; ++j2) e[j2] = src[((j2 >> 2) * 8 + (j2 & 3)) * 128];
        o = (u32x4){e[0] | (e[1] << 16), e[2] | (e[3] << 16), e[4] | (e[5] << 16), e[6] | (e[7] << 16)};
      }
      *(u32x4*)(ws + WS_KV + (size_t)T * 16) = o;
    }
  }
  STAMP(0, 1);
}

struct Frags { bf16x8 x[8]; bf16x8 w[4]; };
struct Addr { unsigned x, w[4]; };
DI void load_frags(Frags& f, const Addr& a) {
#pragma unroll
  for (int c = 0; c < 4; ++c) {
    s16x4 a0 = __builtin_amdgcn_ds_read_tr16_b64_v4i16((LAS s16x4*)(size_t)(a.w[c]));
    s16x4 a1 = __builtin_amdgcn_ds_read_tr16_b64_v4i16((LAS s16x4*)(size_t)(a.w[c] + 4096));
    f.w[c] = __builtin_shufflevector(a0, a1, 0, 1, 2, 3, 4, 5, 6, 7);
  }
#pragma unroll
  for (int i = 0; i < 8; ++i) f.x[i] = *(LAS bf16x8*)(size_t)(a.x + i * 1024);
}
template <bool WA> DI void mma_all(f32x4 (&acc)[8][4], const Frags& f) {
#pragma unroll
  for (int i = 0; i < 8; ++i)
#pragma unroll
    for (int j = 0; j < 4; ++j) {
      if (WA) acc[i][j] = __builtin_amdgcn_mfma_f32_16x16x32_bf16(f.w[j], f.x[i], acc[i][j], 0, 0, 0);
      else    acc[i][j] = __builtin_amdgcn_mfma_f32_16x16x32_bf16(f.x[i], f.w[j], acc[i][j], 0, 0, 0);
    }
}
DI void addr_add(Addr& a, int d) {
  a.x += d;
#pragma unroll
  for (int c = 0; c < 4; ++c) a.w[c] += d;
}
template <bool WA, int NSLOT, int SLOTB>
DI void gstep(f32x4 (&acc)[8][4], Frags& Fn, const Frags& Fc, Addr& a, int& slot) {
  SB0();
  load_frags(Fn, a);
  mma_all<WA>(acc, Fc);
  slot = (slot + 1 == NSLOT) ? 0 : slot + 1;
  addr_add(a, (slot == 0) ? -(NSLOT - 1) * SLOTB : SLOTB);
  asm volatile("" : "+v"(a.x), "+v"(a.w[0]), "+v"(a.w[1]), "+v"(a.w[2]), "+v"(a.w[3]));
#pragma unroll
  for (int i = 0; i < 8; ++i) { SGB(0x8, 1); SGB(0x100, 1); SGB(0x8, 1); SGB(0x100, 1); SGB(0x8, 1); }
#pragma unroll
  for (int i = 0; i < 6; ++i) { SGB(0x8, 1); SGB(0x4, 1); SGB(0x2, 1); }
  SGB(0x8, 2);
  SB0();
  wait_lgkm0();
  bar();
}
template <bool WA, int NSLOT, int SLOTB>
DI void gemm_consumer(f32x4 (&acc)[8][4], unsigned xbase, unsigned wbase, int wj, int T, int lane, char* ws, int kid, int sid) {
  const int g = lane >> 4, q = (lane >> 2) & 3, p = lane & 3, row = 4 * g + q;
  Addr a;
  a.x = xbase + lane * 16;
#pragma unroll
  for (int c = 0; c < 4; ++c) a.w[c] = wbase + row * 256 + (((2 * (4 * wj + c) + (p >> 1)) ^ ((row & 7) << 1)) * 16) + (p & 1) * 8;
#pragma unroll
  for (int i = 0; i < 8; ++i)
#pragma unroll
    for (int j = 0; j < 4; ++j) acc[i][j] = (f32x4){0.f, 0.f, 0.f, 0.f};
  bar();
  STAMP(kid, sid);
  Frags F0, F1;
  load_frags(F0, a);
  addr_add(a, SLOTB);
  wait_lgkm0();
  bar();
  int slot = 1;
  for (int t = 0; t < T; t += 2) {
    gstep<WA, NSLOT, SLOTB>(acc, F1, F0, a, slot);
    gstep<WA, NSLOT, SLOTB>(acc, F0, F1, a, slot);
  }
}

struct Ld128 { rsrc_t rx, rw; unsigned xv, wv, ldw16, ldw32, lb; int hn, hs, lw; };
DI void ld128_init(Ld128& L, unsigned lb, const char* xg, const char* wg, int ldwB, int T, int lw, int lane) {
  const int r = 4 * lw + (lane >> 4);
  L.wv = r * ldwB + (((lane & 15) ^ ((r & 7) << 1)) * 16);
  L.xv = lw * 1024 + lane * 16;
  L.ldw16 = 16 * ldwB; L.ldw32 = 32 * ldwB;
  L.rx = mkrsrc(xg, T * 16384);
  L.rw = mkrsrc(wg, T * 64 * ldwB - (ldwB - 256));
  L.lb = lb + lw * 1024; L.hn = 0; L.hs = 0; L.lw = lw;
}
DI void ld128_half(Ld128& L) {
  const unsigned s = L.lb + L.hs * 16384, xs = L.hn * 8192, wsf = L.hn * L.ldw32;
  dma16(L.rx, s, L.xv, xs);
  dma16(L.rw, s + 8192, L.wv, wsf);
  dma16(L.rx, s + 4096, L.xv, xs + 4096);
  dma16(L.rw, s + 12288, L.wv, wsf + L.ldw16);
  L.hn++; L.hs = (L.hs + 1) & 7;
}
DI void ld128_main(Ld128& L, int T) {
  for (int t = 0; t <= T; ++t) {
    if (L.hn < 2 * T) { ld128_half(L); ld128_half(L); wait_vm<16>(); } else wait_vm<0>();
    bar();
  }
}
template <int KK> DI void splitk_reduce(f32x4 (&acc)[8][4], LAS char* lds, int cw, int lane) {
#pragma unroll
  for (int ii = 0; ii < 4; ++ii)
#pragma unroll
    for (int j = 0; j < 4; ++j) *(LAS f32x4*)(lds + cw * 16384 + (ii * 4 + j) * 1024 + lane * 16) = acc[4 * (1 - KK) + ii][j];
  wait_lgkm0();
  bar();
#pragma unroll
  for (int ii = 0; ii < 4; ++ii)
#pragma unroll
    for (int j = 0; j < 4; ++j) acc[4 * KK + ii][j] += *(LAS f32x4*)(lds + (cw ^ 2) * 16384 + (ii * 4 + j) * 1024 + lane * 16);
}
template <int KK> DI void epi_u(f32x4 (&acc)[8][4], LAS char* lds, int cw, int lane, const u16* b1p, char* ub) {
  u32x2 bv[4];
#pragma unroll
  for (int j = 0; j < 4; ++j) bv[j] = *(const u32x2*)(b1p + j * 16);
  splitk_reduce<KK>(acc, lds, cw, lane);
#pragma unroll
  for (int ii = 0; ii < 4; ++ii)
#pragma unroll
    for (int mm = 0; mm < 2; ++mm) {
      float r[8];
#pragma unroll
      for (int e = 0; e < 8; ++e) {
        const int j = 2 * mm + (e >> 2);
        const unsigned bw = bv[j][(e >> 1) & 1];
        const float uu = acc[4 * KK + ii][j][e & 3] + ((e & 1) ? bfhi(bw) : bflo(bw));
        const float z = uu * (-2.302208198f + -0.1029432397f * uu * uu);
        r[e] = uu * __builtin_amdgcn_rcpf(1.f + __builtin_amdgcn_exp2f(z));
      }
      const u32x4 w = {cvtpk(r[0], r[1]), cvtpk(r[2], r[3]), cvtpk(r[4], r[5]), cvtpk(r[6], r[7])};
      *(u32x4*)(ub + mm * 8192 + (4 * KK + ii) * 1024) = w;
    }
}
template <int KK> DI void epi_g(f32x4 (&acc)[8][4], LAS char* lds, int cw, int lane, const u16* b2p, rsrc_t rg) {
  const int wj = cw & 1;
  float bb[4];
#pragma unroll
  for (int j = 0; j < 4; ++j) bb[j] = bflo(b2p[j * 16]);
  splitk_reduce<KK>(acc, lds, cw, lane);
#pragma unroll
  for (int j = 0; j < 4; ++j)
#pragma unroll
    for (int mm = 0; mm < 2; ++mm) {
      float r[8];
#pragma unroll
      for (int e = 0; e < 8; ++e) {
        const float y = acc[4 * KK + 2 * mm + (e >> 2)][j][e & 3] + bb[j];
        r[e] = y * __builtin_amdgcn_rcpf(1.f + __builtin_amdgcn_exp2f(y * -1.4426950409f));
      }
      const u32x4 w = {cvtpk(r[0], r[1]), cvtpk(r[2], r[3]), cvtpk(r[4], r[5]), cvtpk(r[6], r[7])};
      __builtin_amdgcn_raw_buffer_store_b128(w, rg, lane * 16 + (2 * KK + mm) * 1024, (4 * wj + j) * 4096, 16);
    }
}
template <int KK> DI void epi_out(f32x4 (&acc)[8][4], LAS char* lds, int cw, int lane, rsrc_t rx, rsrc_t ro, unsigned vo, rsrc_t rg) {
  const int wj = cw & 1;
  u32x4 gv[4][2], xv[4][2];
#pragma unroll
  for (int ii = 0; ii < 4; ++ii)
#pragma unroll
    for (int mm = 0; mm < 2; ++mm) {
      gv[ii][mm] = __builtin_amdgcn_raw_buffer_load_b128(rg, lane * 16 + mm * 1024, ((4 * KK + ii) * 4 + 2 * wj) * 1024, 16);
      xv[ii][mm] = __builtin_amdgcn_raw_buffer_load_b128(rx, vo + mm * 64, (4 * KK + ii) * 32 * DM, 0);
    }
  SB0();
  splitk_reduce<KK>(acc, lds, cw, lane);
#pragma unroll
  for (int ii = 0; ii < 4; ++ii)
#pragma unroll
    for (int mm = 0; mm < 2; ++mm) {
      const f32x4 s0 = acc[4 * KK + ii][2 * mm], s1 = acc[4 * KK + ii][2 * mm + 1];
      const u32x4 gw = gv[ii][mm], xw = xv[ii][mm];
      const unsigned a0 = cvtpk(s0[0] * bflo(gw[0]), s0[1] * bfhi(gw[0])), a1 = cvtpk(s0[2] * bflo(gw[1]), s0[3] * bfhi(gw[1]));
      const unsigned b0 = cvtpk(s1[0] * bflo(gw[2]), s1[1] * bfhi(gw[2])), b1 = cvtpk(s1[2] * bflo(gw[3]), s1[3] * bfhi(gw[3]));
      const auto w0 = __builtin_amdgcn_permlane16_swap(a0, b0, false, false), w1 = __builtin_amdgcn_permlane16_swap(a1, b1, false, false);
      const unsigned t[4] = {w0[0], w1[0], w0[1], w1[1]};
      u32x4 o;
#pragma unroll
      for (int e = 0; e < 4; ++e) o[e] = cvtpk(bflo(xw[e]) + bflo(t[e]), bfhi(xw[e]) + bfhi(t[e]));
      __builtin_amdgcn_raw_buffer_store_b128(o, ro, vo + mm * 64, (4 * KK + ii) * 32 * DM, 0);
    }
}

struct Att { bf16x8 q[8]; f32x16 o[4]; f32x16 negm, sE, sO; u32x4 pO[2]; bf16x8 pre[4]; float l; };
DI float vmax16(const f32x16& a) {
  float m = fmaxf(fmaxf(a[0], a[1]), a[2]);
#pragma unroll
  for (int i = 3; i < 15; i += 2) m = fmaxf(fmaxf(m, a[i]), a[i + 1]);
  m = fmaxf(m, a[15]);
  auto sw = __builtin_amdgcn_permlane32_swap(__float_as_uint(m), __float_as_uint(m), false, false);
  return fmaxf(__uint_as_float(sw[0]), __uint_as_float(sw[1]));
}
DI bf16x8 ldfrag(unsigned a) { return *(LAS bf16x8*)(size_t)a; }
DI void sm1(f32x16& s, int e, u32x4 (&w)[2], float& l) {
  if (e < 16) {
    float x = s[e];
    x = __builtin_amdgcn_exp2f(x);
    asm volatile("" : "+v"(x));
    s[e] = x;
  }
  if (e >= 1) {
    l += s[e - 1];
    asm volatile("" : "+v"(l));
    if (!(e & 1)) { unsigned c = cvtpk(s[e - 2], s[e - 1]); asm volatile("" : "+v"(c)); w[(e - 1) >> 3][((e - 1) >> 1) & 3] = c; }
  }
}
#define SETPRIO(x) __builtin_amdgcn_s_setprio(x)
#define MFMA32(a, b, c) __builtin_amdgcn_mfma_f32_32x32x16_bf16(a, b, c, 0, 0, 0)
#define PFRAG(w) __builtin_bit_cast(bf16x8, w)
template <int POS, class F> DI void att_tile(Att& A, unsigned kv, unsigned kvn, F&& gap) {
  bf16x8 f[36];
  u32x4 pE[2];
  asm volatile("" : "+v"(kv), "+v"(kvn));
#pragma unroll
  for (int i = 0; i < 4; ++i) f[i] = A.pre[i];
  SB0();
  SETPRIO(3);
#pragma unroll
  for (int n = 0; n < 8; ++n) {
    if (POS == 0) A.sE = MFMA32(f[n], A.q[n], n ? A.sE : A.negm);
    else A.o[n & 3] = MFMA32(f[n], PFRAG(A.pO[n >> 2]), A.o[n & 3]);
    f[n + 4] = ldfrag(kv + (n + 4) * 1024);
    if (POS && n <= 2) sm1(A.sO, 14 + n, A.pO, A.l);
    if (POS && n >= 2) sm1(A.sE, n - 2, pE, A.l);
    gap(n);
    SB0();
  }
  if (POS == 0) {
    const float mx = vmax16(A.sE);
#pragma unroll
    for (int i = 0; i < 16; ++i) { A.sE[i] -= mx; A.negm[i] = -mx; }
    SB0();
  }
  SETPRIO(2);
#pragma unroll
  for (int n = 8; n < 16; ++n) {
    A.sO = MFMA32(f[n], A.q[n - 8], n > 8 ? A.sO : A.negm);
    f[n + 4] = ldfrag(kv + (n + 4) * 1024);
    if (POS) sm1(A.sE, n - 2, pE, A.l);
    else if (n < 15) { sm1(A.sE, 2 * (n - 8), pE, A.l); sm1(A.sE, 2 * (n - 8) + 1, pE, A.l); }
    gap(n);
    SB0();
  }
  SETPRIO(1);
#pragma unroll
  for (int n = 16; n < 24; ++n) {
    A.o[n & 3] = MFMA32(f[n], PFRAG(pE[(n >> 2) & 1]), A.o[n & 3]);
    f[n + 4] = ldfrag(kv + (n + 4) * 1024);
    if (n <= 18) sm1(A.sE, n - 2, pE, A.l);
    if (n >= 18) {
      if (POS < 2) sm1(A.sO, n - 18, A.pO, A.l);
      else { sm1(A.sO, 2 * (n - 18), A.pO, A.l); sm1(A.sO, 2 * (n - 18) + 1, A.pO, A.l); }
    }
    gap(n);
    SB0();
  }
  SETPRIO(0);
#pragma unroll
  for (int n = 24; n < 32; ++n) {
    if (POS < 2) {
      A.sE = MFMA32(f[n], A.q[n - 24], n > 24 ? A.sE : A.negm);
      f[n + 4] = ldfrag(n < 28 ? kv + (n + 4) * 1024 : kvn + (n - 28) * 1024);
      sm1(A.sO, n - 18, A.pO, A.l);
    } else {
      A.o[n & 3] = MFMA32(f[n], PFRAG(A.pO[(n >> 2) & 1]), A.o[n & 3]);
      if (n < 28) f[n + 4] = ldfrag(kv + (n + 4) * 1024);
      if (n < 26) { sm1(A.sO, 2 * (n - 18), A.pO, A.l); sm1(A.sO, 2 * (n - 18) + 1, A.pO, A.l); }
      if (n == 26) sm1(A.sO, 16, A.pO, A.l);
    }
    gap(n);
    SB0();
  }
  if (POS < 2) {
#pragma unroll
    for (int i = 0; i < 4; ++i) A.pre[i] = f[32 + i];
  }
}
DI void att_redo(Att& A, const char* kvg) {
  float m = -3.0e38f, l = 0.f;
#pragma unroll
  for (int d = 0; d < 4; ++d)
#pragma unroll
    for (int i = 0; i < 16; ++i) A.o[d][i] = 0.f;
  for (int blk = 0; blk < 16; ++blk) {
    const char* kb = kvg + (blk ? 2 * blk - 1 : 0) * 8192;
    const char* vb = kvg + (blk < 15 ? 2 * blk + 2 : 31) * 8192;
    f32x16 s;
#pragma unroll
    for (int i = 0; i < 16; ++i) s[i] = 0.f;
#pragma unroll
    for (int n = 0; n < 8; ++n) s = MFMA32(*(const bf16x8*)(kb + n * 1024), A.q[n], s);
    const float mn = fmaxf(m, vmax16(s)), fct = __builtin_amdgcn_exp2f(m - mn);
    m = mn;
    l *= fct;
    u32x4 p[2];
#pragma unroll
    for (int i = 0; i < 16; ++i) s[i] -= mn;
#pragma unroll
    for (int i = 0; i < 17; ++i) sm1(s, i, p, l);
#pragma unroll
    for (int d = 0; d < 4; ++d)
#pragma unroll
      for (int i = 0; i < 16; ++i) A.o[d][i] *= fct;
#pragma unroll
    for (int n = 0; n < 8; ++n) A.o[n & 3] = MFMA32(*(const bf16x8*)(vb + n * 1024), PFRAG(p[n >> 2]), A.o[n & 3]);
  }
  A.l = l;
}

__global__ __launch_bounds__(512, 2) void k_mid(const u16* __restrict__ wq, const u16* __restrict__ w1, const u16* __restrict__ b1,
                                                const u16* __restrict__ wo, const u16* __restrict__ w2, char* __restrict__ ws) {
  extern __shared__ __attribute__((aligned(16))) char smem[];
  LAS char* lds = (LAS char*)smem;
  const unsigned lb = (unsigned)(size_t)lds;
  const int tid = threadIdx.x, wid = __builtin_amdgcn_readfirstlane(tid >> 6), lane = tid & 63;
  const int g8 = blockIdx.x & 7, idx = blockIdx.x >> 3;
  STAMP(1, 0);
  const int kvh = g8 >> 1, head = 8 * kvh + (idx & 7), rb = 4 * (g8 & 1) + (idx >> 3);
  const bool hasU = idx < 28;
  const int dt = 4 * (g8 & 1) + (idx & 3), ft = 7 * (g8 >> 1) + (idx >> 2);
  const int lw = wid - 4;
  const rsrc_t rkv = mkrsrc(ws + WS_KV + (size_t)kvh * 262144, 262144);
  auto kv_issue = [&](int tile, int slot) {
    dma4k(rkv, lb + slot * 32768 + lw * 8192, lane * 16, tile * 32768 + lw * 8192);
    dma4k(rkv, lb + slot * 32768 + lw * 8192 + 4096, lane * 16, tile * 32768 + lw * 8192 + 4096);
  };
  constexpr int QS = 24576;
  if (wid >= 4) {
    const rsrc_t rx = mkrsrc(ws + WS_XN + (size_t)rb * 524288, 524288);
    const rsrc_t rw = mkrsrc((const char*)wq + head * 256, 1023 * 8192 + 256);
    const int r = 4 * lw + (lane >> 4);
    const unsigned wv = r * 8192 + (((lane & 15) ^ ((r & 7) << 1)) * 16), xv = lw * 1024 + lane * 16;
    int n = 0, sl = 0;
    auto iss = [&]() {
      const unsigned s = lb + sl * QS + lw * 1024;
      dma16(rw, s + 16384, wv, n * 262144);
      dma16(rw, s + 20480, wv, n * 262144 + 131072);
#pragma unroll
      for (int i = 0; i < 4; ++i) dma16(rx, s + i * 4096, xv, n * 16384 + i * 4096);
      ++n; sl = (sl == 4) ? 0 : sl + 1;
    };
    iss(); iss(); iss();
    wait_vm<12>();
    bar();
    for (int t = 0; t < 29; ++t) { iss(); wait_vm<12>(); bar(); }
    kv_issue(0, 4);
    wait_vm<14>(); bar();
    wait_vm<8>(); bar();
    bar();
    kv_issue(1, 2); kv_issue(2, 3);
    bar();
    wait_vm<8>();
  } else {
    f32x4 acc[8][4];
    const int wi = wid >> 1, wj = wid & 1;
    gemm_consumer<true, 5, QS>(acc, lb + wi * 8192, lb + 16384, wj, 32, lane, ws, 1, 1);
    STAMP(1, 2);
    const float c = 0.12751743082f;
    const int g = lane >> 4;
#pragma unroll
    for (int i = 0; i < 8; ++i)
#pragma unroll
      for (int mm = 0; mm < 2; ++mm) {
        const f32x4 a = acc[i][2 * mm] * c, b = acc[i][2 * mm + 1] * c;
        const u32x4 w = {cvtpk(a[0], a[1]), cvtpk(a[2], a[3]), cvtpk(b[0], b[1]), cvtpk(b[2], b[3])};
        *(LAS u32x4*)(lds + (4 * wi + (i >> 1)) * 8192 + (2 * (2 * wj + mm) + (g >> 1)) * 1024 + (16 * (i & 1) + (lane & 15) + 32 * (g & 1)) * 16) = w;
      }
    wait_lgkm0();
  }
  bar();
  STAMP(1, 3);
  Ld128 L;
  unsigned pf = 0;
  {
    Att A;
#pragma unroll
    for (int ks = 0; ks < 8; ++ks) A.q[ks] = *(LAS bf16x8*)(lds + wid * 8192 + ks * 1024 + lane * 16);
#pragma unroll
    for (int d = 0; d < 4; ++d)
#pragma unroll
      for (int i = 0; i < 16; ++i) A.o[d][i] = 0.f;
#pragma unroll
    for (int i = 0; i < 16; ++i) A.negm[i] = 0.f;
    A.l = 0.f;
    const unsigned kvl = lb + lane * 16;
#pragma unroll
    for (int i = 0; i < 4; ++i) A.pre[i] = ldfrag(kvl + 4 * 32768 + i * 1024);
    const bool uld = wid >= 4 && hasU;
    if (uld) ld128_init(L, lb, ws + WS_HT + (size_t)dt * 524288, (const char*)w1 + ft * 256, FF * 2, 32, lw, lane);
    auto kv8 = [&](int tile, int slot) { if (wid >= 4) kv_issue(tile, slot); };
    auto uh = [&](int n) { if ((n == 4 || n == 20) && uld) ld128_half(L); };
    const rsrc_t rp1 = mkrsrc(w1, SEQ * FF * 2), rpo = mkrsrc(wo, HD * DM * 2), rp2 = mkrsrc(w2, SEQ * FF * 2);
    const unsigned pv = (blockIdx.x * 64 + lane) * 128;
#define PF(W, R, C) pf_line<W>(pf, wid, R, pv, (C) * 2097152u)
    att_tile<0>(A, kvl + 4 * 32768, kvl + 2 * 32768, [&](int n) { if (n == 3) PF(0, rp1, 0); if (n == 19) PF(1, rp1, 1); });
    if (wid >= 4) wait_vm<0>();
    bar();
    att_tile<1>(A, kvl + 2 * 32768, kvl + 3 * 32768, [&](int n) { if (n == 1) kv8(3, 0); if (n == 17) kv8(4, 1); if (n == 7) PF(2, rp1, 2); if (n == 23) PF(3, rp1, 3); });
    if (wid >= 4) wait_vm<8>();
    bar();
    att_tile<1>(A, kvl + 3 * 32768, kvl + 0 * 32768, [&](int n) { if (n == 1) kv8(5, 4); if (n == 17) kv8(6, 2); if (n == 7) PF(0, rp1, 4); if (n == 23) PF(1, rp1, 5); });
    if (wid >= 4) wait_vm<16>();
    bar();
    att_tile<1>(A, kvl + 0 * 32768, kvl + 1 * 32768, [&](int n) { if (n == 1) kv8(7, 3); if (n == 7) PF(2, rp1, 6); if (n == 23) PF(3, rpo, 0); });
    if (wid >= 4) wait_vm<16>();
    bar();
    att_tile<1>(A, kvl + 1 * 32768, kvl + 4 * 32768, [&](int n) { if (n == 3) PF(0, rp2, 0); if (n == 19) PF(1, rp2, 1); });
    if (wid >= 4) wait_vm<0>();
    bar();
    att_tile<1>(A, kvl + 4 * 32768, kvl + 2 * 32768, [&](int n) { uh(n); if (n == 9) PF(2, rpo, 1); if (n == 25) PF(3, rp2, 2); });
    bar();
    att_tile<1>(A, kvl + 2 * 32768, kvl + 3 * 32768, uh);
    bar();
    att_tile<2>(A, kvl + 3 * 32768, 0, uh);
    STAMP(1, 4);
    if (__builtin_expect(__any(!(A.l <= 1.0e9f)), 0)) att_redo(A, ws + WS_KV + (size_t)kvh * 262144 + lane * 16);
    auto sw = __builtin_amdgcn_permlane32_swap(__float_as_uint(A.l), __float_as_uint(A.l), false, false);
    const float inv = 1.f / (__uint_as_float(sw[0]) + __uint_as_float(sw[1]));
    const int q = lane & 31, h = lane >> 5;
    char* ob = ws + WS_O + ((size_t)((2 * rb + (wid >> 2)) * 128 + 4 * head) * 8 + 2 * (wid & 3) + (q >> 4)) * 1024 + h * 256 + (q & 15) * 16;
#pragma unroll
    for (int d = 0; d < 4; ++d)
#pragma unroll
      for (int u = 0; u < 2; ++u) {
        const int r0 = 4 * u, r1 = 8 + 4 * u;
        const u32x4 w = {cvtpk(A.o[d][r0] * inv, A.o[d][r0 + 1] * inv), cvtpk(A.o[d][r0 + 2] * inv, A.o[d][r0 + 3] * inv),
                         cvtpk(A.o[d][r1] * inv, A.o[d][r1 + 1] * inv), cvtpk(A.o[d][r1 + 2] * inv, A.o[d][r1 + 3] * inv)};
        *(u32x4*)(ob + d * 8192 + u * 512) = w;
      }
  }
  STAMP(1, 5);
  if (!hasU) {
    asm volatile("s_waitcnt vmcnt(8)" : "+v"(pf)::"memory");
    const int sp = g8 * 4 + (idx - 28);
    unsigned acc = 0;
    for (int i = 0; i < 6; ++i) {
      const char* base = (i == 1 || i == 4) ? (const char*)wo + (size_t)(i == 1 ? 2 : 3) * 2097152 : (const char*)w2 + (size_t)(i == 0 ? 3 : i == 2 ? 4 : i == 3 ? 5 : 6) * 2097152;
      acc += *(const unsigned*)(base + (size_t)(sp * 512 + tid) * 128);
    }
    asm volatile("" ::"v"(acc));
    return;
  }
  wait_lgkm0();
  if (wid >= 4) {
    wait_vm<24>();
    bar();
    ld128_half(L); ld128_half(L);
    wait_vm<24>();
    bar();
    for (int t = 1; t <= 32; ++t) {
      if (L.hn < 64) { ld128_half(L); ld128_half(L); wait_vm<16>(); } else wait_vm<0>();
      bar();
    }
    bar();
  } else {
    f32x4 acc[8][4];
    const int kk = wid >> 1, wj = wid & 1, g = lane >> 4;
    gemm_consumer<true, 4, 32768>(acc, lb + kk * 16384, lb + kk * 16384 + 8192, wj, 32, lane, ws, 1, 6);
    STAMP(1, 7);
    asm volatile("s_waitcnt vmcnt(0)" : "+v"(pf)::"memory");
    const u16* b1p = b1 + ft * 128 + 64 * wj + 4 * g;
    char* ub = ws + WS_U + (size_t)(dt * 112 + 4 * ft + 2 * wj) * 8192 + lane * 16;
    if (kk) epi_u<1>(acc, lds, wid, lane, b1p, ub); else epi_u<0>(acc, lds, wid, lane, b1p, ub);
    STAMP(1, 8);
    STAMP(1, 9);
  }
}

__global__ __launch_bounds__(512, 2) void k_fin(const u16* __restrict__ x, const u16* __restrict__ wo, const u16* __restrict__ w2,
                                                const u16* __restrict__ b2, char* __restrict__ ws, u16* __restrict__ out) {
  extern __shared__ __attribute__((aligned(16))) char smem[];
  LAS char* lds = (LAS char*)smem;
  const unsigned lb = (unsigned)(size_t)lds;
  const int tid = threadIdx.x, wid = __builtin_amdgcn_readfirstlane(tid >> 6), lane = tid & 63;
  const int g8 = blockIdx.x & 7, idx = blockIdx.x >> 3;
  STAMP(2, 0);
  const bool isS = g8 < 4;
  const int gg = g8 & 3;
  const int st = (gg >> 1) * 8 + (idx & 7), dt = (gg & 1) * 4 + (idx >> 3);
  const int T = isS ? 64 : 56, tile = st * 8 + dt;
  GAS unsigned* flag = (GAS unsigned*)(ws + WS_FLAG + tile * 256);
  if (wid >= 4) {
    Ld128 L;
    if (isS) ld128_init(L, lb, ws + WS_O + (size_t)st * 1048576, (const char*)wo + dt * 256, DM * 2, T, wid - 4, lane);
    else     ld128_init(L, lb, ws + WS_U + (size_t)dt * 917504, (const char*)w2 + st * 256, SEQ * 2, T, wid - 4, lane);
    for (int i = 0; i < 6; ++i) ld128_half(L);
    wait_vm<16>();
    bar();
    for (int t = 0; t < T; ++t) {
      if (L.hn < 2 * T) { ld128_half(L); ld128_half(L); wait_vm<16>(); } else wait_vm<0>();
      bar();
    }
    if (isS && wid == 4) {
      for (unsigned sp = 0; sp < (1u << 22); ++sp) {
        if (__builtin_amdgcn_readfirstlane(__hip_atomic_load(flag, __ATOMIC_RELAXED, __HIP_MEMORY_SCOPE_AGENT)) != 0u) break;
        __builtin_amdgcn_s_sleep(1);
      }
    }
    bar();
    bar();
    if (!isS) bar();
  } else {
    f32x4 acc[8][4];
    const int kk = wid >> 1, wj = wid & 1, g = lane >> 4;
    const rsrc_t rg = mkrsrc(ws + WS_G + (size_t)tile * 32768, 32768);
    if (!isS) {
      gemm_consumer<false, 4, 32768>(acc, lb + kk * 16384, lb + kk * 16384 + 8192, wj, T, lane, ws, 2, 1);
      STAMP(2, 2);
      const u16* b2p = b2 + st * 128 + 64 * wj + (lane & 15);
      if (kk) epi_g<1>(acc, lds, wid, lane, b2p, rg); else epi_g<0>(acc, lds, wid, lane, b2p, rg);
      STAMP(2, 3);
      wait_vm<0>();
      bar();
      if (tid == 0) __hip_atomic_store(flag, 1u, __ATOMIC_RELAXED, __HIP_MEMORY_SCOPE_AGENT);
    } else {
      gemm_consumer<true, 4, 32768>(acc, lb + kk * 16384, lb + kk * 16384 + 8192, wj, T, lane, ws, 2, 1);
      STAMP(2, 2);
      const rsrc_t rx = mkrsrc(x + (size_t)st * 128 * DM, 128 * DM * 2), ro = mkrsrc(out + (size_t)st * 128 * DM, 128 * DM * 2);
      const unsigned vo = ((lane & 15) * DM + dt * 128 + wj * 64 + 16 * (g & 1) + 8 * (g >> 1)) * 2;
      if (kk) epi_out<1>(acc, lds, wid, lane, rx, ro, vo, rg); else epi_out<0>(acc, lds, wid, lane, rx, ro, vo, rg);
      STAMP(2, 3);
    }
    STAMP(2, 4);
  }
}

extern "C" void kernel_launch(void* const* d_in, const int* in_sizes, int n_in, void* d_out, int out_size, void* d_ws, size_t ws_size,
                              hipStream_t stream) {
  const u16* x = (const u16*)d_in[0];
  const u16* kc = (const u16*)d_in[1];
  const u16* vc = (const u16*)d_in[2];
  const u16* wq = (const u16*)d_in[3];
  const u16* wo = (const u16*)d_in[4];
  const u16* w1 = (const u16*)d_in[6];
  const u16* b1 = (const u16*)d_in[7];
  const u16* w2 = (const u16*)d_in[8];
  const u16* b2 = (const u16*)d_in[9];
  char* ws = (char*)d_ws;
  (void)hipFuncSetAttribute((const void*)k_prep, hipFuncAttributeMaxDynamicSharedMemorySize, 32768 + 16 * 2080);
  (void)hipFuncSetAttribute((const void*)k_mid, hipFuncAttributeMaxDynamicSharedMemorySize, 163840);
  (void)hipFuncSetAttribute((const void*)k_fin, hipFuncAttributeMaxDynamicSharedMemorySize, 131072);
  hipLaunchKernelGGL(k_prep, dim3(256), dim3(1024), 32768 + 16 * 2080, stream, x, kc, vc, wq, ws);
  hipLaunchKernelGGL(k_mid, dim3(256), dim3(512), 163840, stream, wq, w1, b1, wo, w2, ws);
  hipLaunchKernelGGL(k_fin, dim3(256), dim3(512), 131072, stream, x, wo, w2, b2, ws, (u16*)d_out);
}
```

```cpp
#include <hip/hip_runtime.h>
#include <hip/hip_bf16.h>

#define SPARE_SLEEP 50
#define WSL
#define POLL_GAP 8
#define NPF0 4
#define PFW1 (NPF0 * 2097152)
#define PFW2 (SEQ * FF * 2)
#define POLL_DELAY 16
#define WPR()
typedef unsigned short u16;
typedef short bf16x8 __attribute__((ext_vector_type(8)));
typedef short s16x4 __attribute__((ext_vector_type(4)));
typedef float f32x16 __attribute__((ext_vector_type(16)));
typedef float f32x4 __attribute__((ext_vector_type(4)));
typedef float f32x2 __attribute__((ext_vector_type(2)));
typedef unsigned u32x4 __attribute__((ext_vector_type(4)));
typedef unsigned u32x2 __attribute__((ext_vector_type(2)));
typedef __bf16 bf16x2 __attribute__((ext_vector_type(2)));
typedef __amdgpu_buffer_rsrc_t rsrc_t;
#define LAS __attribute__((address_space(3)))
#define GAS __attribute__((address_space(1)))
#define DI __device__ __forceinline__
#define SGB(m, n) __builtin_amdgcn_sched_group_barrier(m, n, 0)
#define SB0() __builtin_amdgcn_sched_barrier(0)

constexpr int SEQ = 2048, DM = 1024, FF = 3584, HD = 4096;
constexpr size_t WS_FLAG = 0;
constexpr int F_EP = 0, F_U = 4096, F_O = 8192, F_Y = 16384, F_S = 49152, F_END = 81920;
constexpr int F_L = 131072, F_X = F_L + 32 * 2304;
constexpr size_t WS_XN = 262144;
constexpr size_t WS_HT = WS_XN + 4194304;
constexpr size_t WS_KV = WS_HT + 4194304;
constexpr size_t WS_O = WS_KV + 1048576;
constexpr size_t WS_U = WS_O + 16777216;
constexpr size_t WS_XB = WS_U + 7340032;
constexpr size_t WS_END = WS_XB + 25165824;

#define STAMP(kid, i)
DI unsigned cvtpk(float lo, float hi) { f32x2 v = {lo, hi}; bf16x2 b = __builtin_convertvector(v, bf16x2); return __builtin_bit_cast(unsigned, b); }
DI float bflo(unsigned u) { return __uint_as_float(u << 16); }
DI float bfhi(unsigned u) { return __uint_as_float(u & 0xffff0000u); }
DI float rbf(float x) { return bflo(cvtpk(x, x)); }
template <int N> DI void wait_vm() { asm volatile("s_waitcnt vmcnt(%0)" ::"i"(N) : "memory"); }
DI void wait_lgkm0() { __builtin_amdgcn_s_waitcnt(0xC07F); }
DI void bar() { asm volatile("" ::: "memory"); __builtin_amdgcn_s_barrier(); asm volatile("" ::: "memory"); }
DI rsrc_t mkrsrc(const void* p, unsigned bytes) { return __builtin_amdgcn_make_buffer_rsrc((void*)p, 0, bytes, 0x00020000); }
DI void dma16(rsrc_t r, unsigned lds_addr, unsigned voff, unsigned soff) {
  asm volatile("s_mov_b32 m0, %0\n\ts_nop 0\n\tbuffer_load_dwordx4 %1, %2, %3 offen lds" ::"s"(lds_addr), "v"(voff), "s"(r), "s"(soff) : "memory");
}
DI void dma16s(rsrc_t r, unsigned lds_addr, unsigned voff, unsigned soff) {
  asm volatile("s_mov_b32 m0, %0\n\ts_nop 0\n\tbuffer_load_dwordx4 %1, %2, %3 offen sc1 lds" ::"s"(lds_addr), "v"(voff), "s"(r), "s"(soff) : "memory");
}
template <int OFF> DI void st16s(u32x4 v, rsrc_t r, unsigned voff, unsigned soff) {
  asm volatile("buffer_store_dwordx4 %0, %1, %2, %3 offen offset:%4 sc1\n\ts_nop 1" ::"v"(v), "v"(voff), "s"(r), "s"(soff), "i"(OFF) : "memory");
}
DI int lane_id() { int l; asm volatile("v_mbcnt_lo_u32_b32 %0, -1, 0\n\tv_mbcnt_hi_u32_b32 %0, -1, %0" : "=v"(l)); return l; }
struct Tag { unsigned lo, hi; };
DI void peek(rsrc_t rf, unsigned lds_addr, int off) {
  asm volatile("s_mov_b32 m0, %0\n\ts_nop 0\n\tbuffer_load_dword %3, %1, %2 offen sc1 lds" ::"s"(lds_addr), "s"(rf), "s"(off), "v"(lane_id() * 4) : "memory");
}
template <int GAP = 1> DI void spin_until(char* ws, int off, int n, Tag t) {
  const int ln = lane_id();
  GAS unsigned long long* p = (GAS unsigned long long*)(ws + WS_FLAG + off) + (ln < n ? ln : 0);
  const unsigned long long want = ((unsigned long long)t.hi << 32) | t.lo;
  for (unsigned sp = 0; sp < (1u << 20); ++sp) {
    if (__all(__hip_atomic_load(p, __ATOMIC_RELAXED, __HIP_MEMORY_SCOPE_AGENT) == want)) break;
    __builtin_amdgcn_s_sleep(GAP);
  }
  asm volatile("" ::: "memory");
}
DI void peek_check(char* ws, unsigned lds_addr, int off, int n, Tag t) {
  const int ln = lane_id();
  const unsigned v = *(LAS volatile unsigned*)(size_t)(lds_addr + ln * 4);
  if (!__all(ln >= 2 * n || v == ((ln & 1) ? t.hi : t.lo))) spin_until(ws, off, n, t);
}
template <long MASK> DI void signal(char* ws, int off, Tag t) {
  const u32x2 v = {t.lo, t.hi};
  asm volatile("s_mov_b64 exec, %3\n\tglobal_store_dwordx2 %0, %1, %2 sc1\n\ts_mov_b64 exec, -1" ::"v"(off), "v"(v), "s"(ws + WS_FLAG), "s"(MASK) : "memory");
}
DI void dma4k(rsrc_t r, unsigned lds_addr, unsigned voff, unsigned soff) {
  asm volatile("s_mov_b32 m0, %0\n\ts_nop 0\n\tbuffer_load_dwordx4 %1, %2, %3 offen sc1 lds\n\tbuffer_load_dwordx4 %1, %2, %3 offen offset:1024 sc1 lds\n\t"
               "buffer_load_dwordx4 %1, %2, %3 offen offset:2048 sc1 lds\n\tbuffer_load_dwordx4 %1, %2, %3 offen offset:3072 sc1 lds"
               ::"s"(lds_addr), "v"(voff), "s"(r), "s"(soff) : "memory");
}
template <int W> DI void pf_line(unsigned& sink, int wid, rsrc_t r, unsigned voff, unsigned soff) {
  asm volatile("s_cmp_lg_u32 %1, %5\n\ts_cbranch_scc1 1f\n\tbuffer_load_dword %0, %2, %3, %4 offen\n1:"
               : "+v"(sink) : "s"(wid), "v"(voff), "s"(r), "s"(soff), "i"(W) : "scc", "memory");
}
template <int CTRL> DI float dppf(float x) { return __builtin_bit_cast(float, __builtin_amdgcn_mov_dpp(__builtin_bit_cast(int, x), CTRL, 0xf, 0xf, true)); }
DI float wave_sum(float v) {
  v += dppf<0xB1>(v); v += dppf<0x4E>(v); v += dppf<0x141>(v); v += dppf<0x140>(v);
  auto a = __builtin_amdgcn_permlane16_swap(__float_as_uint(v), __float_as_uint(v), false, false);
  v = __uint_as_float(a[0]) + __uint_as_float(a[1]);
  auto b = __builtin_amdgcn_permlane32_swap(__float_as_uint(v), __float_as_uint(v), false, false);
  return __uint_as_float(b[0]) + __uint_as_float(b[1]);
}

DI void ln16(unsigned (&w)[8]) {
  f32x2 v[8];
#pragma unroll
  for (int i = 0; i < 8; ++i) v[i] = (f32x2){bflo(w[i]), bfhi(w[i])};
  const f32x2 s = ((v[0] + v[1]) + (v[2] + v[3])) + ((v[4] + v[5]) + (v[6] + v[7]));
  const float mean = wave_sum(s[0] + s[1]) * (1.f / 1024.f);
  f32x2 q0 = {0.f, 0.f}, q1 = q0;
#pragma unroll
  for (int i = 0; i < 8; i += 2) {
    const f32x2 d0 = v[i] - mean, d1 = v[i + 1] - mean;
    q0 = __builtin_elementwise_fma(d0, d0, q0); q1 = __builtin_elementwise_fma(d1, d1, q1);
  }
  q0 += q1;
  const float var = wave_sum(q0[0] + q0[1]) * (1.f / 1024.f);
  const float mb = rbf(mean);
  const float rs = __builtin_amdgcn_rcpf(rbf(__builtin_amdgcn_sqrtf(rbf(rbf(var) + 1e-5f))));
#pragma unroll
  for (int i = 0; i < 8; ++i) {
    const f32x2 d = v[i] - mb;
    const unsigned c = cvtpk(d[0], d[1]);
    const f32x2 r = (f32x2){bflo(c), bfhi(c)} * rs;
    w[i] = cvtpk(r[0], r[1]);
  }
}

struct Frags { bf16x8 x[8]; bf16x8 w[4]; };
struct Addr { unsigned x, w[4]; };
DI void load_frags(Frags& f, const Addr& a) {
#pragma unroll
  for (int c = 0; c < 4; ++c) {
    s16x4 a0 = __builtin_amdgcn_ds_read_tr16_b64_v4i16((LAS s16x4*)(size_t)(a.w[c]));
    s16x4 a1 = __builtin_amdgcn_ds_read_tr16_b64_v4i16((LAS s16x4*)(size_t)(a.w[c] + 4096));
    f.w[c] = __builtin_shufflevector(a0, a1, 0, 1, 2, 3, 4, 5, 6, 7);
  }
#pragma unroll
  for (int i = 0; i < 8; ++i) f.x[i] = *(LAS bf16x8*)(size_t)(a.x + i * 1024);
}
template <bool WA, bool Z> DI void mma_all(f32x4 (&acc)[8][4], const Frags& f, const f32x4* c0) {
  const f32x4 zero = {0.f, 0.f, 0.f, 0.f};
#pragma unroll
  for (int i = 0; i < 8; ++i)
#pragma unroll
    for (int j = 0; j < 4; ++j) {
      if (WA) acc[i][j] = __builtin_amdgcn_mfma_f32_16x16x32_bf16(f.w[j], f.x[i], Z ? (c0 ? c0[j] : zero) : acc[i][j], 0, 0, 0);
      else    acc[i][j] = __builtin_amdgcn_mfma_f32_16x16x32_bf16(f.x[i], f.w[j], Z ? (c0 ? c0[j] : zero) : acc[i][j], 0, 0, 0);
    }
}
DI void addr_add(Addr& a, int d) {
  a.x += d;
#pragma unroll
  for (int c = 0; c < 4; ++c) a.w[c] += d;
}
template <bool WA, int NSLOT, int SLOTB, bool Z = false>
DI void gstep(f32x4 (&acc)[8][4], Frags& Fn, const Frags& Fc, Addr& a, int& slot, const f32x4* c0 = nullptr) {
  SB0();
  load_frags(Fn, a);
  mma_all<WA, Z>(acc, Fc, c0);
  slot = (slot + 1 == NSLOT) ? 0 : slot + 1;
  addr_add(a, (slot == 0) ? -(NSLOT - 1) * SLOTB : SLOTB);
  asm volatile("" : "+v"(a.x), "+v"(a.w[0]), "+v"(a.w[1]), "+v"(a.w[2]), "+v"(a.w[3]));
#pragma unroll
  for (int i = 0; i < 8; ++i) { SGB(0x8, 1); SGB(0x100, 1); SGB(0x8, 1); SGB(0x100, 1); SGB(0x8, 1); }
#pragma unroll
  for (int i = 0; i < 6; ++i) { SGB(0x8, 1); SGB(0x4, 1); SGB(0x2, 1); }
  SGB(0x8, 2);
  SB0();
  wait_lgkm0();
  bar();
}
DI void addr_init(Addr& a, unsigned xbase, unsigned wbase, int wj, int cx, int lane) {
  const int g = lane >> 4, q = (lane >> 2) & 3, p = lane & 3, row = 4 * g + q;
  a.x = xbase + lane * 16;
#pragma unroll
  for (int c = 0; c < 4; ++c) a.w[c] = wbase + row * 256 + (((2 * (4 * wj + (c ^ cx)) + (p >> 1)) ^ ((row & 7) << 1)) * 16) + (p & 1) * 8;
}
template <int SLOTB> DI void gemm_start(Frags& F0, Addr& a) {
  bar();
  load_frags(F0, a);
  addr_add(a, SLOTB);
  wait_lgkm0();
  bar();
}
template <bool WA, int NSLOT, int SLOTB> DI void gemm_steps(f32x4 (&acc)[8][4], Frags& F0, Frags& F1, Addr& a, int& slot, int n) {
  for (int t = 0; t < n; t += 2) {
    gstep<WA, NSLOT, SLOTB>(acc, F0, F1, a, slot);
    gstep<WA, NSLOT, SLOTB>(acc, F1, F0, a, slot);
  }
}

struct LdU { rsrc_t rx, rw; unsigned xv, wv, lb; int hn, hs; };
template <bool X = true, bool W = true> DI void ldu_half(LdU& L) {
  const unsigned s = L.lb + L.hs * 16384, xs = L.hn * 8192, wsf = L.hn * (32 * FF * 2);
  if (X) dma16s(L.rx, s, L.xv, xs);
  if (W) dma16(L.rw, s + 8192, L.wv, wsf);
  if (X) dma16s(L.rx, s + 4096, L.xv, xs + 4096);
  if (W) dma16(L.rw, s + 12288, L.wv, wsf + 4 * FF * 2);
  L.hn++; L.hs = (L.hs + 1) & 7;
}
DI u32x4 pack8(const f32x4& a, const f32x4& b) { return (u32x4){cvtpk(a[0], a[1]), cvtpk(a[2], a[3]), cvtpk(b[0], b[1]), cvtpk(b[2], b[3])}; }
DI f32x2 gate2(f32x2 u, f32x2 z) {
  f32x2 e = {__builtin_amdgcn_exp2f(z[0]), __builtin_amdgcn_exp2f(z[1])};
  e += 1.f;
  return u * (f32x2){__builtin_amdgcn_rcpf(e[0]), __builtin_amdgcn_rcpf(e[1])};
}
DI unsigned gelu2(f32x2 u) {
  const f32x2 r = gate2(u, u * __builtin_elementwise_fma(u * u, (f32x2){-0.1029432397f, -0.1029432397f}, (f32x2){-2.302208198f, -2.302208198f}));
  return cvtpk(r[0], r[1]);
}
DI unsigned silu2(f32x2 y) { const f32x2 r = gate2(y, y * -1.4426950409f); return cvtpk(r[0], r[1]); }
template <int KK> DI void epi_u(f32x4 (&acc)[8][4], unsigned pk, int cw) {
#pragma unroll
  for (int ii = 0; ii < 4; ++ii)
#pragma unroll
    for (int j = 0; j < 4; ++j) *(LAS f32x4*)(size_t)(pk + cw * 16384 + (ii * 4 + j) * 1024) = acc[4 * (1 - KK) + ii][j];
  wait_lgkm0();
  bar();
  const unsigned pp = pk + (cw ^ 2) * 16384;
#pragma unroll
  for (int ii = 0; ii < 4; ++ii)
#pragma unroll
    for (int j = 0; j < 4; ++j) acc[4 * KK + ii][j] += *(LAS f32x4*)(size_t)(pp + (ii * 4 + j) * 1024);
#pragma unroll
  for (int ii = 0; ii < 4; ++ii)
#pragma unroll
    for (int mm = 0; mm < 2; ++mm) {
      const f32x4 a = acc[4 * KK + ii][2 * mm], b = acc[4 * KK + ii][2 * mm + 1];
      *(LAS u32x4*)(size_t)(pp + (ii * 4 + 2 * mm) * 1024) = (u32x4){gelu2(a.xy), gelu2(a.zw), gelu2(b.xy), gelu2(b.zw)};
    }
}
DI u32x4 ldsr(unsigned a) { return *(LAS u32x4*)(size_t)a; }
DI void add8(f32x4& a, f32x4& b, const u32x4& w) {
  a += (f32x4){bflo(w[0]), bfhi(w[0]), bflo(w[1]), bfhi(w[1])};
  b += (f32x4){bflo(w[2]), bfhi(w[2]), bflo(w[3]), bfhi(w[3])};
}

struct Att { bf16x8 q[8]; f32x16 o[4]; f32x16 negm, sE, sO; u32x4 pO[2]; bf16x8 pre[4]; float l; };
DI float vmax16(const f32x16& a) {
  float m = fmaxf(fmaxf(a[0], a[1]), a[2]);
#pragma unroll
  for (int i = 3; i < 15; i += 2) m = fmaxf(fmaxf(m, a[i]), a[i + 1]);
  m = fmaxf(m, a[15]);
  auto sw = __builtin_amdgcn_permlane32_swap(__float_as_uint(m), __float_as_uint(m), false, false);
  return fmaxf(__uint_as_float(sw[0]), __uint_as_float(sw[1]));
}
DI bf16x8 ldfrag(unsigned a) { return *(LAS bf16x8*)(size_t)a; }
DI void sm1(f32x16& s, int e, u32x4 (&w)[2], float& l) {
  if (e < 16) {
    float x = s[e];
    x = __builtin_amdgcn_exp2f(x);
    asm volatile("" : "+v"(x));
    s[e] = x;
  }
  if (e >= 1) {
    l += s[e - 1];
    asm volatile("" : "+v"(l));
    if (!(e & 1)) { unsigned c = cvtpk(s[e - 2], s[e - 1]); asm volatile("" : "+v"(c)); w[(e - 1) >> 3][((e - 1) >> 1) & 3] = c; }
  }
}
#define SETPRIO(x) __builtin_amdgcn_s_setprio(x)
#define MFMA32(a, b, c) __builtin_amdgcn_mfma_f32_32x32x16_bf16(a, b, c, 0, 0, 0)
#define PFRAG(w) __builtin_bit_cast(bf16x8, w)
template <int POS, class F> DI void att_tile(Att& A, unsigned kv, unsigned kvn, F&& gap) {
  bf16x8 f[36];
  u32x4 pE[2];
  asm volatile("" : "+v"(kv), "+v"(kvn));
#pragma unroll
  for (int i = 0; i < 4; ++i) f[i] = A.pre[i];
  SB0();
  SETPRIO(3);
#pragma unroll
  for (int n = 0; n < 8; ++n) {
    if (POS == 0) A.sE = MFMA32(f[n], A.q[n], n ? A.sE : A.negm);
    else A.o[n & 3] = MFMA32(f[n], PFRAG(A.pO[n >> 2]), A.o[n & 3]);
    f[n + 4] = ldfrag(kv + (n + 4) * 1024);
    if (POS && n <= 2) sm1(A.sO, 14 + n, A.pO, A.l);
    if (POS && n >= 2) sm1(A.sE, n - 2, pE, A.l);
    gap(n);
    SB0();
  }
  if (POS == 0) {
    const float mx = vmax16(A.sE);
#pragma unroll
    for (int i = 0; i < 16; ++i) { A.sE[i] -= mx; A.negm[i] = -mx; }
    SB0();
  }
  SETPRIO(2);
#pragma unroll
  for (int n = 8; n < 16; ++n) {
    A.sO = MFMA32(f[n], A.q[n - 8], n > 8 ? A.sO : A.negm);
    f[n + 4] = ldfrag(kv + (n + 4) * 1024);
    if (POS) sm1(A.sE, n - 2, pE, A.l);
    else if (n < 15) { sm1(A.sE, 2 * (n - 8), pE, A.l); sm1(A.sE, 2 * (n - 8) + 1, pE, A.l); }
    gap(n);
    SB0();
  }
  SETPRIO(1);
#pragma unroll
  for (int n = 16; n < 24; ++n) {
    A.o[n & 3] = MFMA32(f[n], PFRAG(pE[(n >> 2) & 1]), A.o[n & 3]);
    f[n + 4] = ldfrag(kv + (n + 4) * 1024);
    if (n <= 18) sm1(A.sE, n - 2, pE, A.l);
    if (n >= 18) {
      if (POS < 2) sm1(A.sO, n - 18, A.pO, A.l);
      else { sm1(A.sO, 2 * (n - 18), A.pO, A.l); sm1(A.sO, 2 * (n - 18) + 1, A.pO, A.l); }
    }
    gap(n);
    SB0();
  }
  SETPRIO(0);
#pragma unroll
  for (int n = 24; n < 32; ++n) {
    if (POS < 2) {
      A.sE = MFMA32(f[n], A.q[n - 24], n > 24 ? A.sE : A.negm);
      f[n + 4] = ldfrag(n < 28 ? kv + (n + 4) * 1024 : kvn + (n - 28) * 1024);
      sm1(A.sO, n - 18, A.pO, A.l);
    } else {
      A.o[n & 3] = MFMA32(f[n], PFRAG(A.pO[(n >> 2) & 1]), A.o[n & 3]);
      if (n < 28) f[n + 4] = ldfrag(kv + (n + 4) * 1024);
      if (n < 26) { sm1(A.sO, 2 * (n - 18), A.pO, A.l); sm1(A.sO, 2 * (n - 18) + 1, A.pO, A.l); }
      if (n == 26) sm1(A.sO, 16, A.pO, A.l);
    }
    gap(n);
    SB0();
  }
  if (POS < 2) {
#pragma unroll
    for (int i = 0; i < 4; ++i) A.pre[i] = f[32 + i];
  }
}
DI void att_redo(Att& A, rsrc_t rkv, unsigned vo) {
  float m = -3.0e38f, l = 0.f;
#pragma unroll
  for (int d = 0; d < 4; ++d)
#pragma unroll
    for (int i = 0; i < 16; ++i) A.o[d][i] = 0.f;
  for (int blk = 0; blk < 16; ++blk) {
    const int kb = (blk ? 2 * blk - 1 : 0) * 8192, vb = (blk < 15 ? 2 * blk + 2 : 31) * 8192;
    f32x16 s;
#pragma unroll
    for (int i = 0; i < 16; ++i) s[i] = 0.f;
#pragma unroll
    for (int n = 0; n < 8; ++n) s = MFMA32(PFRAG(__builtin_amdgcn_raw_buffer_load_b128(rkv, vo + n * 1024, kb, 16)), A.q[n], s);
    const float mn = fmaxf(m, vmax16(s)), fct = __builtin_amdgcn_exp2f(m - mn);
    m = mn;
    l *= fct;
    u32x4 p[2];
#pragma unroll
    for (int i = 0; i < 16; ++i) s[i] -= mn;
#pragma unroll
    for (int i = 0; i < 17; ++i) sm1(s, i, p, l);
#pragma unroll
    for (int d = 0; d < 4; ++d)
#pragma unroll
      for (int i = 0; i < 16; ++i) A.o[d][i] *= fct;
#pragma unroll
    for (int n = 0; n < 8; ++n) A.o[n & 3] = MFMA32(PFRAG(__builtin_amdgcn_raw_buffer_load_b128(rkv, vo + n * 1024, vb, 16)), PFRAG(p[n >> 2]), A.o[n & 3]);
  }
  A.l = l;
}

__global__ __launch_bounds__(512, 2) void k_main(const u16* __restrict__ x, const u16* __restrict__ kc, const u16* __restrict__ vc, const u16* __restrict__ wq, const u16* __restrict__ wo,
                                                 const u16* __restrict__ w1, const u16* __restrict__ b1, const u16* __restrict__ w2,
                                                 const u16* __restrict__ b2, char* __restrict__ ws, u16* __restrict__ out) {
  extern __shared__ __attribute__((aligned(16))) char smem[];
  LAS char* lds = (LAS char*)smem;
  const unsigned lb = (unsigned)(size_t)lds;
  const int tid = threadIdx.x, wid = __builtin_amdgcn_readfirstlane(tid >> 6);
  const int g8 = blockIdx.x & 7, idx = blockIdx.x >> 3;
  STAMP(1, 0);
  STAMP(0, 0);
  const bool hasU = idx < 28;
  const int dt = 4 * (g8 & 1) + (idx & 3), ft = 7 * (g8 >> 1) + (idx >> 2);
  const int kvh = g8 >> 1, head = 8 * kvh + (idx & 7), rb = 4 * (g8 & 1) + (idx >> 3);
  const int dc = idx & 7, tile = rb * 8 + dc;
  const int lw = wid - 4, wi = (wid >> 1) & 1, wj = wid & 1;
  constexpr int QS = 24576;
  constexpr int PARK = 98304;
  const rsrc_t rf = mkrsrc(ws + WS_FLAG, F_X + 2048);
  const rsrc_t rkv = mkrsrc(ws + WS_KV + (size_t)kvh * 262144, 262144);
  const rsrc_t rxb = mkrsrc(ws + WS_XB + (size_t)tile * 393216, 393216);
  struct Strm { rsrc_t r; unsigned v0, v1, step, pair, px; };
  auto strm_x = [&](Strm& S, const char* base, unsigned bytes, unsigned step, int xr) {
    const int ln = lane_id();
    S.r = mkrsrc(base, bytes); S.v0 = ln * 16 + (xr & 1) * 1024; S.v1 = S.v0 ^ 1024; S.step = step; S.pair = 2048; S.px = xr >> 1;
  };
  auto strm_w = [&](Strm& S, const char* base, unsigned rows, unsigned ld) {
    const int ln = lane_id(), q4 = ln >> 4;
    const unsigned c0 = ((ln & 15) ^ (q4 << 1)) * 16;
    S.r = mkrsrc(base, (rows - 1) * ld + 256); S.v0 = q4 * ld + c0; S.v1 = (4 + q4) * ld + (c0 ^ 128); S.step = 32 * ld; S.pair = 8 * ld; S.px = 0;
  };
  auto issue = [&](const Strm& S, int k) {
    const unsigned s = lb + (k & 3) * QS + lw * 8192;
#pragma unroll
    for (int i = 0; i < 4; ++i) { const unsigned so = k * S.step + (i ^ S.px) * S.pair; dma16s(S.r, s + i * 2048, S.v0, so); dma16s(S.r, s + i * 2048 + 1024, S.v1, so); }
  };
  Tag tag;
  LdU L;
  u32x2 bv[4];
  {
    constexpr int XST = 122880, HST = XST + 16384, HRS = 2112, CNT = HST + 8 * HRS;
    const int b = blockIdx.x, lane = tid & 63, ks = lane >> 1, od = lane & 1;
    u32x4 A, B;
    u32x2 ep;
    asm volatile("global_load_dwordx4 %0, %7, %8\n\tglobal_load_dwordx4 %1, %7, %8 offset:32\n\tglobal_load_dwordx2 %2, %9, %10 sc1\n\t"
                 "global_load_dwordx2 %3, %11, %12\n\tglobal_load_dwordx2 %4, %11, %12 offset:32\n\tglobal_load_dwordx2 %5, %11, %12 offset:64\n\t"
                 "global_load_dwordx2 %6, %11, %12 offset:96"
                 : "=&v"(A), "=&v"(B), "=&v"(ep), "=&v"(bv[0]), "=&v"(bv[1]), "=&v"(bv[2]), "=&v"(bv[3])
                 : "v"(64 * ks + 16 * od), "s"(x + (size_t)(8 * b + wid) * DM), "v"(F_EP), "s"(ws + WS_FLAG), "v"(8 * (lane >> 4)),
                   "s"(b1 + (hasU ? ft : 0) * 128 + 64 * wj) : "memory");
    const int lrow = 4 * lw + (lane >> 4);
    L.rx = mkrsrc(ws + WS_HT + (size_t)dt * 524288, 524288);
    L.rw = mkrsrc((const char*)w1 + ft * 256, hasU ? (SEQ - 1) * FF * 2 + 256 : 0);
    L.xv = lw * 1024 + lane * 16; L.wv = (8 * lw + (lane >> 4)) * (FF * 2) + ((lane & 15) ^ ((lrow & 7) << 1)) * 16;
    L.lb = lb + lw * 1024; L.hn = 0; L.hs = 0;
    if (wid >= 4) {
      for (int i = 0; i < 6; ++i) ldu_half<false, true>(L);
      L.hn = 0; L.hs = 0;
      asm volatile("s_mov_b32 m0, %0\n\ts_nop 0\n\tbuffer_load_dword %2, %1, 0 offen lds" ::"s"(lb + 159744 + lw * 256), "s"(mkrsrc(w1, PFW1)),
                   "v"(lw * 2097152 + (b * 64 + lane) * 128) : "memory");
      wait_vm<13>();
    } else {
      *(LAS unsigned*)(lds + CNT) = 0u;
      wait_vm<0>();
    }
    asm volatile("" : "+v"(A), "+v"(B), "+v"(ep), "+v"(bv[0]), "+v"(bv[1]), "+v"(bv[2]), "+v"(bv[3]));
    unsigned v[8] = {A[0], A[1], A[2], A[3], B[0], B[1], B[2], B[3]};
    const unsigned long long e64 = ((unsigned long long)(unsigned)__builtin_amdgcn_readfirstlane(ep[1]) << 32) | (unsigned)__builtin_amdgcn_readfirstlane(ep[0]);
    const unsigned long long tg = e64 * 0x9E3779B97F4A7C15ull + 0x632BE59BD9B4E019ull;
    tag.lo = (unsigned)tg; tag.hi = (unsigned)(tg >> 32);
    ln16(v);
    const int slot = (wid ^ (lane & 7)) * 16;
    *(LAS u32x4*)(lds + XST + (ks * 4 + 2 * od) * 128 + slot) = (u32x4){v[0], v[1], v[4], v[5]};
    *(LAS u32x4*)(lds + XST + (ks * 4 + 2 * od + 1) * 128 + slot) = (u32x4){v[2], v[3], v[6], v[7]};
    ln16(v);
    *(LAS u32x4*)(lds + HST + wid * HRS + (32 * ks + 8 * od) * 2) = (u32x4){v[0], v[1], v[2], v[3]};
    *(LAS u32x4*)(lds + HST + wid * HRS + (32 * ks + 16 + 8 * od) * 2) = (u32x4){v[4], v[5], v[6], v[7]};
    wait_lgkm0();
    bar();
    STAMP(0, 1);
    if (wid < 4) {
      const rsrc_t rh = mkrsrc(ws + WS_HT + (size_t)(b >> 2) * 8192 + (b & 3) * 256, 7 * 524288 + 8192);
      const int q = (lane >> 2) & 3, p = lane & 3;
#pragma unroll
      for (int it = 0; it < 4; ++it) {
        const unsigned a0 = HST + q * HRS + ((wid * 4 + it) * 4 + (lane >> 4)) * 32 + p * 8;
        s16x4 lo = __builtin_amdgcn_ds_read_tr16_b64_v4i16((LAS s16x4*)(lds + a0));
        s16x4 hi = __builtin_amdgcn_ds_read_tr16_b64_v4i16((LAS s16x4*)(lds + a0 + 4 * HRS));
        st16s<0>(__builtin_bit_cast(u32x4, __builtin_shufflevector(lo, hi, 0, 1, 2, 3, 4, 5, 6, 7)), rh, (lane >> 4) * 1024 + (lane & 15) * 16,
                 (wid * 2 + (it >> 1)) * 524288 + (it & 1) * 4096);
      }
      wait_vm<0>();
      unsigned old;
      asm volatile("ds_add_rtn_u32 %0, %1, %2\n\ts_waitcnt lgkmcnt(0)" : "=v"(old) : "v"(lb + CNT), "v"(lane == 0 ? 1u : 0u) : "memory");
      if (__builtin_amdgcn_readfirstlane(old) == 3) signal<0xffffffffL>(ws, F_L + (lane & 31) * 2304 + b * 8, tag);
      STAMP(0, 2);
      asm volatile("s_mov_b32 m0, %0\n\ts_nop 0\n\tbuffer_load_dword %2, %1, 0 offen lds" ::"s"(lb + 160768 + wid * 256), "s"(mkrsrc(w1, PFW2)),
                   "v"((NPF0 + wid) * 2097152 + (b * 64 + lane) * 128) : "memory");
      const rsrc_t rn = mkrsrc(ws + WS_XN + (size_t)(b >> 5) * 524288 + ((b >> 1) & 15) * 1024 + (b & 1) * 128, 31 * 16384 + 1024);
      const int r8 = lane & 7, gp = (lane >> 3) & 3, k1 = lane >> 5;
#pragma unroll
      for (int it = 0; it < 4; ++it) {
        const int k2 = (wid * 4 + it) * 2 + k1, sw = ((k2 & 3) << 1) | (gp >> 1);
        st16s<0>(*(LAS u32x4*)(lds + XST + (k2 * 4 + gp) * 128 + ((r8 ^ sw) * 16)), rn, k1 * 16384 + gp * 256 + r8 * 16, (wid * 4 + it) * 32768);
      }
    } else if (hasU) {
      for (int sp = 0; sp < 100000 && *(LAS volatile unsigned*)(lds + CNT) < 4u; ++sp) __builtin_amdgcn_s_sleep(2);
      __builtin_amdgcn_s_sleep(POLL_DELAY);
      spin_until<POLL_GAP>(ws, F_L + (b & 31) * 2304 + lw * 512, 64, tag);
    }
    bar();
    STAMP(0, 3);
    if (b == 0 && wid == 0) signal<1>(ws, F_EP, tag);
  }
  if (!hasU) {
    const int sp = g8 * 4 + (idx - 28), lane = tid & 63, h = lane >> 5;
    unsigned e[4][8];
#pragma unroll
    for (int i = 0; i < 4; ++i) {
      const int kh = sp >> 3, unit = (sp & 7) * 4 + i, frag = wid;
      const bool isK = unit == 0 || ((unit & 1) && unit < 31);
      const u16* sk = kc + ((size_t)kh * SEQ + 1536 + 32 * ((unit + 1) >> 1) + (lane & 31)) * 128 + 32 * (frag >> 1) + 4 * (2 * (frag & 1) + h);
      const u16* sv = vc + ((size_t)kh * SEQ + 1536 + 32 * ((unit - 1) >> 1) + 16 * (frag >> 2) + 4 * h) * 128 + 32 * (frag & 3) + (lane & 31);
      const u16* src = isK ? sk : sv;
      const int s1 = isK ? 1 : 128, s4 = isK ? 16 : 1024;
#pragma unroll
      for (int j2 = 0; j2 < 8; ++j2) e[i][j2] = src[(j2 >> 2) * s4 + (j2 & 3) * s1];
    }
#pragma unroll
    for (int i = 0; i < 4; ++i)
      st16s<0>((u32x4){e[i][0] | (e[i][1] << 16), e[i][2] | (e[i][3] << 16), e[i][4] | (e[i][5] << 16), e[i][6] | (e[i][7] << 16)},
               mkrsrc(ws + WS_KV, 1048576), tid * 16, (sp * 4 + i) * 8192);
    wait_vm<0>();
    bar();
    if (wid == 0) signal<1>(ws, F_X + blockIdx.x * 8, tag);
    for (int i = 0; i < 4; ++i) {
      const unsigned v = *(const unsigned*)((const char*)wq + (size_t)i * 2097152 + (size_t)(sp * 512 + tid) * 128);
      asm volatile("" ::"v"(v));
      __builtin_amdgcn_s_sleep(SPARE_SLEEP);
    }
    if (wid >= 4) spin_until(ws, F_X + lw * 512, 64, tag);
    bar();
  }
  if (wid < 4) {
    f32x4 acc[8][4];
    Frags F0, F1;
    Addr a;
    int slot = 1;
    if (hasU) {
      const int ln = lane_id();
      f32x4 c0[4];
#pragma unroll
      for (int j = 0; j < 4; ++j) {
        c0[j] = wi ? (f32x4){0.f, 0.f, 0.f, 0.f} : (f32x4){bflo(bv[j][0]), bfhi(bv[j][0]), bflo(bv[j][1]), bfhi(bv[j][1])};
      }
      addr_init(a, lb + wi * 16384, lb + wi * 16384 + 8192, wj, 0, ln);
      gemm_start<32768>(F0, a);
      STAMP(1, 1);
      gstep<true, 4, 32768, true>(acc, F1, F0, a, slot, c0);
      gemm_steps<true, 4, 32768>(acc, F0, F1, a, slot, 4);
      wait_vm<0>();
      gemm_steps<true, 4, 32768>(acc, F0, F1, a, slot, 26);
      addr_init(a, lb + wi * 8192, lb + 16384, wj, 0, lane_id());
      gstep<true, 4, QS>(acc, F0, F1, a, slot);
      STAMP(1, 2);
      STAMP(1, 3);
      if (wi) epi_u<1>(acc, lb + PARK + lane_id() * 16, wid); else epi_u<0>(acc, lb + PARK + lane_id() * 16, wid);
    } else {
      addr_init(a, lb + wi * 8192, lb + 16384, wj, 0, lane_id());
      gemm_start<QS>(F0, a);
    }
    STAMP(1, 4);
    gstep<true, 4, QS, true>(acc, F1, F0, a, slot);
    gemm_steps<true, 4, QS>(acc, F0, F1, a, slot, 30);
    gstep<true, 4, QS>(acc, F0, F1, a, slot);
    STAMP(1, 5);
    const float c = 0.12751743082f;
    const int ln = lane_id(), g = ln >> 4;
    const unsigned qa = lb + wi * 32768 + (4 * wj + (g >> 1)) * 1024 + ((ln & 15) + 32 * (g & 1)) * 16;
#pragma unroll
    for (int i = 0; i < 8; ++i)
#pragma unroll
      for (int mm = 0; mm < 2; ++mm) *(LAS u32x4*)(size_t)(qa + (i >> 1) * 8192 + mm * 2048 + (i & 1) * 256) = pack8(acc[i][2 * mm] * c, acc[i][2 * mm + 1] * c);
    wait_lgkm0();
  } else {
    const int lane = tid & 63;
    WPR();
    Strm Q;
    if (lw < 2) strm_x(Q, ws + WS_XN + (size_t)rb * 524288 + lw * 8192, 524288 - lw * 8192, 16384, 0);
    else strm_w(Q, (const char*)wq + head * 256, DM, HD * 2);
    if (hasU) {
      for (int i = 0; i < 6; ++i) ldu_half<true, false>(L);
      wait_vm<8>();
      bar();
      ldu_half(L); ldu_half(L);
      wait_vm<12>();
      bar();
      for (int t = 1; t <= 6; ++t) { ldu_half(L); ldu_half(L); wait_vm<16>(); bar(); }
      if (lw == 3) signal<1>(ws, F_X + blockIdx.x * 8, tag);
      for (int t = 7; t <= 20; ++t) { ldu_half(L); ldu_half(L); wait_vm<16>(); bar(); }
      const unsigned pk = lb + 155648 + lw * 512;
      peek(rf, pk, F_X + lw * 512); peek(rf, pk + 256, F_X + lw * 512 + 256);
      for (int t = 21; t <= 27; ++t) { ldu_half(L); ldu_half(L); wait_vm<16>(); bar(); }
      peek_check(ws, pk, F_X + lw * 512, 32, tag); peek_check(ws, pk + 256, F_X + lw * 512 + 256, 32, tag);
      ldu_half(L); ldu_half(L); wait_vm<16>(); bar();
      if (lw < 3) {
        issue(Q, 0); wait_vm<16>(); bar();
        issue(Q, 1); wait_vm<16>(); bar();
        issue(Q, 2); wait_vm<16>(); bar();
        issue(Q, 3); wait_vm<16>(); bar();
      } else { wait_vm<8>(); bar(); wait_vm<0>(); bar(); bar(); bar(); }
      bar();
    } else if (lw < 3) {
      issue(Q, 0); issue(Q, 1);
      wait_vm<8>();
      bar();
      issue(Q, 2); issue(Q, 3);
      wait_vm<16>();
      bar();
    } else { bar(); bar(); }
    if (lw < 3) {
      for (int t = 1; t <= 28; ++t) { issue(Q, t + 3); wait_vm<16>(); bar(); }
      wait_vm<8>(); bar();
      wait_vm<0>(); bar();
    } else {
      bar();
      const unsigned pl = lb + PARK + lane * 16;
      const rsrc_t ru = mkrsrc(ws + WS_U + (size_t)(dt * 112 + 4 * ft) * 8192, hasU ? 32768 : 0);
      for (int t = 2; t <= 17; ++t) {
        const int cw = ((t - 2) >> 2) ^ 2, ii = (t - 2) & 3;
        const u32x4 v0 = ldsr(pl + (t - 2) * 4096), v1 = ldsr(pl + (t - 2) * 4096 + 2048);
        const unsigned o = (cw & 1) * 16384 + ((cw >> 1) * 4 + ii) * 1024;
        st16s<0>(v0, ru, lane * 16, o); st16s<0>(v1, ru, lane * 16, o + 8192);
        bar();
      }
      for (int t = 18; t <= 25; ++t) {
        const int k = t - 18;
        dma4k(rkv, lb + (4 - (k >> 2)) * 32768 + (k & 3) * 8192, lane * 16, k * 8192);
        dma4k(rkv, lb + (4 - (k >> 2)) * 32768 + (k & 3) * 8192 + 4096, lane * 16, k * 8192 + 4096);
        if (t == 21) wait_vm<32>();
        bar();
      }
      if (hasU) signal<1>(ws, F_U + (dt * 4 + kvh) * 128 + (idx >> 2) * 8, tag);
      for (int t = 26; t <= 30; ++t) bar();
      wait_vm<0>();
    }
    bar();
    dma4k(rkv, lb + 2 * 32768 + lw * 8192, lane * 16, 2 * 32768 + lw * 8192);
    dma4k(rkv, lb + 2 * 32768 + lw * 8192 + 4096, lane * 16, 2 * 32768 + lw * 8192 + 4096);
    bar();
  }
  bar();
  STAMP(1, 6);
  unsigned pf = 0;
  Strm Y;
  {
    const int lane = lane_id();
    Att A;
#pragma unroll
    for (int ks = 0; ks < 8; ++ks) A.q[ks] = *(LAS bf16x8*)(lds + wid * 8192 + ks * 1024 + lane * 16);
#pragma unroll
    for (int d = 0; d < 4; ++d)
#pragma unroll
      for (int i = 0; i < 16; ++i) A.o[d][i] = 0.f;
#pragma unroll
    for (int i = 0; i < 16; ++i) A.negm[i] = 0.f;
    A.l = 0.f;
    const unsigned kvl = lb + lane * 16;
#pragma unroll
    for (int i = 0; i < 4; ++i) A.pre[i] = ldfrag(kvl + 4 * 32768 + i * 1024);
    if (lw == 0) strm_x(Y, ws + WS_U + (size_t)(dc * 112 + 28 * kvh) * 8192, 28 * 8192, 8192, 0);
    else strm_w(Y, (const char*)w2 + (size_t)kvh * 896 * (SEQ * 2) + rb * 512 + ((lw - 1) & 1) * 256, 896, SEQ * 2);
    const bool strm = wid >= 4 && wid < 7;
    auto kv8 = [&](int t, int slot) {
      if (wid >= 4) {
        dma4k(rkv, lb + slot * 32768 + lw * 8192, lane * 16, t * 32768 + lw * 8192);
        dma4k(rkv, lb + slot * 32768 + lw * 8192 + 4096, lane * 16, t * 32768 + lw * 8192 + 4096);
      }
    };
    const rsrc_t rp2 = mkrsrc(w2, SEQ * FF * 2), rpo = mkrsrc(wo, HD * DM * 2);
    const unsigned pv = (blockIdx.x * 64 + lane) * 128;
#define PF(W, R, C) pf_line<W>(pf, wid, R, pv, (C) * 2097152u)
    att_tile<0>(A, kvl + 4 * 32768, kvl + 3 * 32768, [&](int n) { if (n == 3) PF(0, rp2, 0); if (n == 19) PF(1, rp2, 1); });
    if (wid >= 4) wait_vm<0>();
    bar();
    att_tile<1>(A, kvl + 3 * 32768, kvl + 2 * 32768, [&](int n) { if (n == 1) kv8(3, 0); if (n == 17) kv8(4, 1); if (n == 7) PF(2, rp2, 2); if (n == 23) PF(3, rp2, 3); });
    if (wid >= 4) wait_vm<8>();
    bar();
    att_tile<1>(A, kvl + 2 * 32768, kvl + 0 * 32768, [&](int n) { if (n == 1) kv8(5, 4); if (n == 17) kv8(6, 3); if (n == 7) PF(0, rp2, 4); if (n == 23) PF(1, rp2, 5); });
    if (wid >= 4) wait_vm<16>();
    bar();
    att_tile<1>(A, kvl + 0 * 32768, kvl + 1 * 32768, [&](int n) { if (n == 1) kv8(7, 2); if (n == 7) PF(2, rp2, 6); if (n == 23) PF(3, rpo, 0); });
    if (wid >= 4) wait_vm<16>();
    bar();
    att_tile<1>(A, kvl + 1 * 32768, kvl + 4 * 32768, [&](int n) { if (n == 1 && wid == 4) peek(rf, lb + 24576, F_U + (dc * 4 + kvh) * 128); if (n == 3) PF(0, rpo, 1); if (n == 19) PF(1, rpo, 2); });
    if (wid >= 4) wait_vm<0>();
    if (wid == 4) peek_check(ws, lb + 24576, F_U + (dc * 4 + kvh) * 128, 7, tag);
    bar();
    att_tile<1>(A, kvl + 4 * 32768, kvl + 3 * 32768, [&](int n) { if (n == 9) PF(2, rpo, 3); });
    bar();
    att_tile<1>(A, kvl + 3 * 32768, kvl + 2 * 32768, [&](int n) { if (n == 4 && strm) issue(Y, 0); });
    bar();
    att_tile<2>(A, kvl + 2 * 32768, 0, [&](int n) { if (n == 4 && strm) issue(Y, 1); });
    STAMP(1, 7);
    if (__builtin_expect(__any(!(A.l <= 1.0e9f)), 0)) att_redo(A, rkv, lane * 16);
    auto sw = __builtin_amdgcn_permlane32_swap(__float_as_uint(A.l), __float_as_uint(A.l), false, false);
    const float inv = __builtin_amdgcn_rcpf(__uint_as_float(sw[0]) + __uint_as_float(sw[1]));
    const unsigned oa = lb + PARK + wid * 2048 + ((lane >> 4) & 1) * 1024 + (lane >> 5) * 256 + (lane & 15) * 16;
#pragma unroll
    for (int d = 0; d < 4; ++d)
#pragma unroll
      for (int u = 0; u < 2; ++u) {
        const int r0 = 4 * u, r1 = 8 + 4 * u;
        *(LAS u32x4*)(size_t)(oa + d * 16384 + u * 512) =
            (u32x4){cvtpk(A.o[d][r0] * inv, A.o[d][r0 + 1] * inv), cvtpk(A.o[d][r0 + 2] * inv, A.o[d][r0 + 3] * inv),
                    cvtpk(A.o[d][r1] * inv, A.o[d][r1 + 1] * inv), cvtpk(A.o[d][r1 + 2] * inv, A.o[d][r1 + 3] * inv)};
      }
    wait_lgkm0();
  }
  STAMP(1, 8);
  if (wid < 4) {
    f32x4 acc[8][4];
    Frags F0, F1;
    Addr a;
    int slot = 1;
    addr_init(a, lb, lb + 8192 + wi * 8192, wj, kvh, lane_id());
    gemm_start<QS>(F0, a);
    STAMP(1, 9);
    gstep<false, 4, QS, true>(acc, F1, F0, a, slot);
    gemm_steps<false, 4, QS>(acc, F0, F1, a, slot, 26);
    addr_init(a, lb + wi * 8192, lb + 16384, wj, 0, lane_id());
    gstep<false, 4, QS>(acc, F0, F1, a, slot);
    STAMP(1, 10);
    asm volatile("s_waitcnt vmcnt(0)" : "+v"(pf)::"memory");
    const unsigned pm = lb + PARK + lane_id() * 16 + wid * 4096;
#pragma unroll
    for (int j = 0; j < 4; ++j)
#pragma unroll
      for (int m = 0; m < 4; ++m) *(LAS u32x4*)(size_t)(pm + j * 16384 + m * 1024) = pack8(acc[2 * m][j], acc[2 * m + 1][j]);
    STAMP(1, 11);
    gstep<true, 4, QS, true>(acc, F1, F0, a, slot);
    gemm_steps<true, 4, QS>(acc, F0, F1, a, slot, 30);
    gstep<true, 4, QS>(acc, F0, F1, a, slot);
    STAMP(1, 12);
    const int lt = lane_id(), g = lt >> 4;
    const unsigned wo_ = wi * 8192 + wj * 2048;
#pragma unroll
    for (int dl = 1; dl < 4; ++dl)
#pragma unroll
      for (int h = 0; h < 2; ++h)
#pragma unroll
        for (int mm = 0; mm < 2; ++mm)
          st16s<0>(pack8(acc[4 * h + dl][2 * mm], acc[4 * h + dl][2 * mm + 1]), rxb, lt * 16, (kvh ^ dl) * 98304 + 49152 + (dl - 1) * 16384 + wo_ + h * 4096 + mm * 1024);
    wait_vm<0>();
    signal<14>(ws, F_S + (tile * 4 + (kvh ^ (lt & 3))) * 128 + wid * 32 + ((lt & 3) - 1) * 8, tag);
    STAMP(1, 13);
    const int srow = rb * 256 + (8 * wi + kvh) * 16;
    const rsrc_t rx = mkrsrc(x + (size_t)srow * DM, 80 * DM * 2), rout = mkrsrc(out + (size_t)srow * DM, 80 * DM * 2);
    const unsigned vo = ((lt & 15) * DM + dc * 128 + wj * 64 + 16 * (g & 1) + 8 * (g >> 1)) * 2;
    u32x4 ps[3][4], xv[4], gv[4];
#pragma unroll
    for (int p = 0; p < 4; ++p) xv[p] = __builtin_amdgcn_raw_buffer_load_b128(rx, vo + (p >> 1) * (64 * DM * 2) + (p & 1) * 64, 0, 0);
    bar();
#pragma unroll
    for (int p = 0; p < 4; ++p) gv[p] = ldsr(lb + PARK + lt * 16 + wo_ + (p >> 1) * 4096 + (p & 1) * 1024);
    spin_until(ws, F_S + (tile * 4 + kvh) * 128 + wid * 32, 3, tag);
    STAMP(1, 14);
#pragma unroll
    for (int p = 0; p < 4; ++p)
#pragma unroll
      for (int k = 0; k < 3; ++k)
        ps[k][p] = __builtin_amdgcn_raw_buffer_load_b128(rxb, lt * 16 + (p & 1) * 1024, kvh * 98304 + 49152 + k * 16384 + wo_ + (p >> 1) * 4096, 16);
#pragma unroll
    for (int p = 0; p < 4; ++p) {
      f32x4 s0 = acc[4 * (p >> 1)][2 * (p & 1)], s1 = acc[4 * (p >> 1)][2 * (p & 1) + 1];
#pragma unroll
      for (int k = 0; k < 3; ++k) add8(s0, s1, ps[k][p]);
      s0 *= (f32x4){bflo(gv[p][0]), bfhi(gv[p][0]), bflo(gv[p][1]), bfhi(gv[p][1])};
      s1 *= (f32x4){bflo(gv[p][2]), bfhi(gv[p][2]), bflo(gv[p][3]), bfhi(gv[p][3])};
      const u32x4 tp = pack8(s0, s1);
      const auto w0 = __builtin_amdgcn_permlane16_swap(tp[0], tp[2], false, false), w1 = __builtin_amdgcn_permlane16_swap(tp[1], tp[3], false, false);
      const unsigned t[4] = {w0[0], w1[0], w0[1], w1[1]};
      u32x4 o;
#pragma unroll
      for (int e = 0; e < 4; ++e) o[e] = cvtpk(bflo(xv[p][e]) + bflo(t[e]), bfhi(xv[p][e]) + bfhi(t[e]));
      __builtin_amdgcn_raw_buffer_store_b128(o, rout, vo + (p >> 1) * (64 * DM * 2) + (p & 1) * 64, 0, 0);
    }
    STAMP(1, 15);
  } else {
    const int lane = lane_id();
    const unsigned pl = lb + PARK + lane * 16, pk = lb + 159744 + lw * 256;
    const int fo = F_O + (rb * 4 + kvh) * 128;
    WPR();
    if (lw < 3) {
      Strm S;
      if (lw < 2) strm_x(S, ws + WS_O + (size_t)(rb * 128 + 32 * kvh) * 16384 + lw * 8192, 524288 - lw * 8192, 16384, kvh);
      else strm_w(S, (const char*)wo + (size_t)kvh * 1024 * (DM * 2) + dc * 256, 1024, DM * 2);
      wait_vm<8>();
      bar();
      issue(Y, 2); issue(Y, 3);
      wait_vm<16>();
      bar();
      for (int t = 1; t <= 22; ++t) { issue(Y, t + 3); wait_vm<16>(); bar(); }
      peek(rf, pk, fo);
      issue(Y, 26); wait_vm<16>(); bar();
      issue(Y, 27); wait_vm<16>(); bar();
      peek_check(ws, pk, fo, 8, tag);
      for (int t = 25; t <= 56; ++t) { issue(S, t - 25); wait_vm<16>(); bar(); }
      wait_vm<8>(); bar();
      wait_vm<0>(); bar();
    } else {
      bar();
      const rsrc_t ro = mkrsrc(ws + WS_O + (size_t)(rb * 128 + 4 * head) * 16384, 65536);
      for (int t = 0; t <= 15; ++t) {
        const unsigned o = t * 4096;
        const u32x4 v0 = ldsr(pl + o), v1 = ldsr(pl + o + 1024), v2 = ldsr(pl + o + 2048), v3 = ldsr(pl + o + 3072);
        st16s<0>(v0, ro, lane * 16, o); WSL; st16s<1024>(v1, ro, lane * 16, o); WSL;
        st16s<2048>(v2, ro, lane * 16, o); WSL; st16s<3072>(v3, ro, lane * 16, o);
        bar();
      }
      bar(); bar();
      wait_vm<0>();
      signal<1>(ws, fo + dc * 8, tag);
      for (int t = 18; t <= 29; ++t) bar();
      for (int t = 30; t <= 45; ++t) {
        const unsigned o = (t - 30) * 1024;
        const u32x4 v0 = ldsr(pl + 16384 + o), v1 = ldsr(pl + 32768 + o), v2 = ldsr(pl + 49152 + o);
        st16s<0>(v0, rxb, lane * 16, (kvh ^ 1) * 98304 + o); WSL;
        st16s<0>(v1, rxb, lane * 16, (kvh ^ 2) * 98304 + 16384 + o); WSL;
        st16s<0>(v2, rxb, lane * 16, (kvh ^ 3) * 98304 + 32768 + o);
        bar();
      }
      for (int t = 46; t <= 48; ++t) bar();
      wait_vm<0>();
      signal<14>(ws, F_Y + (tile * 4 + (kvh ^ (lane & 3))) * 128 + ((lane & 3) - 1) * 8, tag);
      for (int t = 49; t <= 56; ++t) bar();
      peek(rf, pk, F_Y + (tile * 4 + kvh) * 128);
      bar();
      wait_vm<0>();
      peek_check(ws, pk, F_Y + (tile * 4 + kvh) * 128, 3, tag);
      bar();
    }
    u32x4 py[3][4];
#pragma unroll
    for (int k = 0; k < 3; ++k)
#pragma unroll
      for (int m = 0; m < 4; ++m) py[k][m] = __builtin_amdgcn_raw_buffer_load_b128(rxb, lane * 16 + m * 1024, kvh * 98304 + lw * 4096 + k * 16384, 16);
    const float bb = bflo(b2[rb * 256 + (4 * lw + kvh) * 16 + (lane & 15)]);
    bar();
    bar();
#pragma unroll
    for (int m = 0; m < 4; ++m) {
      f32x4 y0 = {bb, bb, bb, bb}, y1 = y0;
      add8(y0, y1, ldsr(pl + lw * 4096 + m * 1024));
#pragma unroll
      for (int k = 0; k < 3; ++k) add8(y0, y1, py[k][m]);
      *(LAS u32x4*)(size_t)(pl + lw * 4096 + m * 1024) = (u32x4){silu2(y0.xy), silu2(y0.zw), silu2(y1.xy), silu2(y1.zw)};
    }
    wait_lgkm0();
    bar();
  }
}

extern "C" void kernel_launch(void* const* d_in, const int* in_sizes, int n_in, void* d_out, int out_size, void* d_ws, size_t ws_size,
                              hipStream_t stream) {
  const u16* x = (const u16*)d_in[0];
  const u16* kc = (const u16*)d_in[1];
  const u16* vc = (const u16*)d_in[2];
  const u16* wq = (const u16*)d_in[3];
  const u16* wo = (const u16*)d_in[4];
  const u16* w1 = (const u16*)d_in[6];
  const u16* b1 = (const u16*)d_in[7];
  const u16* w2 = (const u16*)d_in[8];
  const u16* b2 = (const u16*)d_in[9];
  char* ws = (char*)d_ws;
  (void)hipFuncSetAttribute((const void*)k_main, hipFuncAttributeMaxDynamicSharedMemorySize, 163840);
  hipLaunchKernelGGL(k_main, dim3(256), dim3(512), 163840, stream, x, kc, vc, wq, wo, w1, b1, w2, b2, ws, (u16*)d_out);
}
```

```cpp
#include <hip/hip_runtime.h>
#include <hip/hip_bf16.h>

#define PFB 1835008
#define NSP 7
#define PFS 4
#define SPARE_T 1350
typedef unsigned short u16;
typedef short bf16x8 __attribute__((ext_vector_type(8)));
typedef short s16x4 __attribute__((ext_vector_type(4)));
typedef float f32x16 __attribute__((ext_vector_type(16)));
typedef float f32x4 __attribute__((ext_vector_type(4)));
typedef float f32x2 __attribute__((ext_vector_type(2)));
typedef unsigned u32x4 __attribute__((ext_vector_type(4)));
typedef unsigned u32x2 __attribute__((ext_vector_type(2)));
typedef __bf16 bf16x2 __attribute__((ext_vector_type(2)));
typedef __amdgpu_buffer_rsrc_t rsrc_t;
#define LAS __attribute__((address_space(3)))
#define GAS __attribute__((address_space(1)))
#define DI __device__ __forceinline__
#define SGB(m, n) __builtin_amdgcn_sched_group_barrier(m, n, 0)
#define SB0() __builtin_amdgcn_sched_barrier(0)

constexpr int SEQ = 2048, DM = 1024, FF = 3584, HD = 4096;
constexpr size_t WS_FLAG = 0;
constexpr int F_EP = 0, F_X = 512, F_L = 2048, F_U = 4096, F_O = 8192, F_Y = 16384, F_S = 49152, F_END = 81920;
constexpr size_t WS_ST = 98304;
constexpr size_t WS_CS = 131072;
constexpr size_t WS_HT = 262144;
constexpr size_t WS_KV = WS_HT + 4194304;
constexpr size_t WS_O = WS_KV + 1048576;
constexpr size_t WS_U = WS_O + 16777216;
constexpr size_t WS_XB = WS_U + 7340032;
constexpr size_t WS_END = WS_XB + 25165824;

#define STAMP(kid, i)
DI unsigned cvtpk(float lo, float hi) { f32x2 v = {lo, hi}; bf16x2 b = __builtin_convertvector(v, bf16x2); return __builtin_bit_cast(unsigned, b); }
DI float bflo(unsigned u) { return __uint_as_float(u << 16); }
DI float bfhi(unsigned u) { return __uint_as_float(u & 0xffff0000u); }
DI float rbf(float x) { return bflo(cvtpk(x, x)); }
template <int N> DI void wait_vm() { asm volatile("s_waitcnt vmcnt(%0)" ::"i"(N) : "memory"); }
DI void wait_lgkm0() { __builtin_amdgcn_s_waitcnt(0xC07F); }
DI void bar() { asm volatile("" ::: "memory"); __builtin_amdgcn_s_barrier(); asm volatile("" ::: "memory"); }
DI rsrc_t mkrsrc(const void* p, unsigned bytes) { return __builtin_amdgcn_make_buffer_rsrc((void*)p, 0, bytes, 0x00020000); }
DI void dma16(rsrc_t r, unsigned lds_addr, unsigned voff, unsigned soff) {
  asm volatile("s_mov_b32 m0, %0\n\ts_nop 0\n\tbuffer_load_dwordx4 %1, %2, %3 offen lds" ::"s"(lds_addr), "v"(voff), "s"(r), "s"(soff) : "memory");
}
DI void dma16s(rsrc_t r, unsigned lds_addr, unsigned voff, unsigned soff) {
  asm volatile("s_mov_b32 m0, %0\n\ts_nop 0\n\tbuffer_load_dwordx4 %1, %2, %3 offen sc1 lds" ::"s"(lds_addr), "v"(voff), "s"(r), "s"(soff) : "memory");
}
template <int OFF> DI void st16s(u32x4 v, rsrc_t r, unsigned voff, unsigned soff) {
  asm volatile("buffer_store_dwordx4 %0, %1, %2, %3 offen offset:%4 sc1\n\ts_nop 1" ::"v"(v), "v"(voff), "s"(r), "s"(soff), "i"(OFF) : "memory");
}
DI int lane_id() { int l; asm volatile("v_mbcnt_lo_u32_b32 %0, -1, 0\n\tv_mbcnt_hi_u32_b32 %0, -1, %0" : "=v"(l)); return l; }
struct Tag { unsigned lo, hi; };
DI void peek(rsrc_t rf, unsigned lds_addr, int off) {
  asm volatile("s_mov_b32 m0, %0\n\ts_nop 0\n\tbuffer_load_dword %3, %1, %2 offen sc1 lds" ::"s"(lds_addr), "s"(rf), "s"(off), "v"(lane_id() * 4) : "memory");
}
template <int GAP = 1> DI void spin_until(char* ws, int off, int n, Tag t) {
  const int ln = lane_id();
  GAS unsigned long long* p = (GAS unsigned long long*)(ws + WS_FLAG + off) + (ln < n ? ln : 0);
  const unsigned long long want = ((unsigned long long)t.hi << 32) | t.lo;
  for (unsigned sp = 0; sp < (1u << 20); ++sp) {
    if (__all(__hip_atomic_load(p, __ATOMIC_RELAXED, __HIP_MEMORY_SCOPE_AGENT) == want)) break;
    __builtin_amdgcn_s_sleep(GAP);
  }
  asm volatile("" ::: "memory");
}
DI void peek_check(char* ws, unsigned lds_addr, int off, int n, Tag t) {
  const int ln = lane_id();
  const unsigned v = *(LAS volatile unsigned*)(size_t)(lds_addr + ln * 4);
  if (!__all(ln >= 2 * n || v == ((ln & 1) ? t.hi : t.lo))) spin_until(ws, off, n, t);
}
template <long MASK> DI void signal(char* ws, int off, Tag t) {
  const u32x2 v = {t.lo, t.hi};
  asm volatile("s_mov_b64 exec, %3\n\tglobal_store_dwordx2 %0, %1, %2 sc1\n\ts_mov_b64 exec, -1" ::"v"(off), "v"(v), "s"(ws + WS_FLAG), "s"(MASK) : "memory");
}
DI void dma4k(rsrc_t r, unsigned lds_addr, unsigned voff, unsigned soff) {
  asm volatile("s_mov_b32 m0, %0\n\ts_nop 0\n\tbuffer_load_dwordx4 %1, %2, %3 offen sc1 lds\n\tbuffer_load_dwordx4 %1, %2, %3 offen offset:1024 sc1 lds\n\t"
               "buffer_load_dwordx4 %1, %2, %3 offen offset:2048 sc1 lds\n\tbuffer_load_dwordx4 %1, %2, %3 offen offset:3072 sc1 lds"
               ::"s"(lds_addr), "v"(voff), "s"(r), "s"(soff) : "memory");
}
template <int W> DI void pf_line(unsigned& sink, int wid, rsrc_t r, unsigned voff, unsigned soff) {
  asm volatile("s_cmp_lg_u32 %1, %5\n\ts_cbranch_scc1 1f\n\tbuffer_load_dword %0, %2, %3, %4 offen\n1:"
               : "+v"(sink) : "s"(wid), "v"(voff), "s"(r), "s"(soff), "i"(W) : "scc", "memory");
}
template <int CTRL> DI float dppf(float x) { return __builtin_bit_cast(float, __builtin_amdgcn_mov_dpp(__builtin_bit_cast(int, x), CTRL, 0xf, 0xf, true)); }
DI float wave_sum(float v) {
  v += dppf<0xB1>(v); v += dppf<0x4E>(v); v += dppf<0x141>(v); v += dppf<0x140>(v);
  auto a = __builtin_amdgcn_permlane16_swap(__float_as_uint(v), __float_as_uint(v), false, false);
  v = __uint_as_float(a[0]) + __uint_as_float(a[1]);
  auto b = __builtin_amdgcn_permlane32_swap(__float_as_uint(v), __float_as_uint(v), false, false);
  return __uint_as_float(b[0]) + __uint_as_float(b[1]);
}

DI f32x2 ln16(unsigned (&w)[8]) {
  f32x2 v[8];
#pragma unroll
  for (int i = 0; i < 8; ++i) v[i] = (f32x2){bflo(w[i]), bfhi(w[i])};
  const f32x2 s = ((v[0] + v[1]) + (v[2] + v[3])) + ((v[4] + v[5]) + (v[6] + v[7]));
  const float mean = wave_sum(s[0] + s[1]) * (1.f / 1024.f);
  f32x2 q0 = {0.f, 0.f}, q1 = q0;
#pragma unroll
  for (int i = 0; i < 8; i += 2) {
    const f32x2 d0 = v[i] - mean, d1 = v[i + 1] - mean;
    q0 = __builtin_elementwise_fma(d0, d0, q0); q1 = __builtin_elementwise_fma(d1, d1, q1);
  }
  q0 += q1;
  const float var = wave_sum(q0[0] + q0[1]) * (1.f / 1024.f);
  const float mb = rbf(mean);
  const float rs = __builtin_amdgcn_rcpf(rbf(__builtin_amdgcn_sqrtf(rbf(rbf(var) + 1e-5f))));
#pragma unroll
  for (int i = 0; i < 8; ++i) {
    const f32x2 d = v[i] - mb;
    const unsigned c = cvtpk(d[0], d[1]);
    const f32x2 r = (f32x2){bflo(c), bfhi(c)} * rs;
    w[i] = cvtpk(r[0], r[1]);
  }
  return (f32x2){mb, rs};
}

struct Frags { bf16x8 x[8]; bf16x8 w[4]; };
struct Addr { unsigned x, w[4]; };
template <int XS = 1024> DI void load_frags(Frags& f, const Addr& a) {
#pragma unroll
  for (int c = 0; c < 4; ++c) {
    s16x4 a0 = __builtin_amdgcn_ds_read_tr16_b64_v4i16((LAS s16x4*)(size_t)(a.w[c]));
    s16x4 a1 = __builtin_amdgcn_ds_read_tr16_b64_v4i16((LAS s16x4*)(size_t)(a.w[c] + 4096));
    f.w[c] = __builtin_shufflevector(a0, a1, 0, 1, 2, 3, 4, 5, 6, 7);
  }
#pragma unroll
  for (int i = 0; i < 8; ++i) f.x[i] = *(LAS bf16x8*)(size_t)(a.x + i * XS);
}
template <bool WA, bool Z> DI void mma_all(f32x4 (&acc)[8][4], const Frags& f, const f32x4* c0) {
  const f32x4 zero = {0.f, 0.f, 0.f, 0.f};
#pragma unroll
  for (int i = 0; i < 8; ++i)
#pragma unroll
    for (int j = 0; j < 4; ++j) {
      if (WA) acc[i][j] = __builtin_amdgcn_mfma_f32_16x16x32_bf16(f.w[j], f.x[i], Z ? (c0 ? c0[j] : zero) : acc[i][j], 0, 0, 0);
      else    acc[i][j] = __builtin_amdgcn_mfma_f32_16x16x32_bf16(f.x[i], f.w[j], Z ? (c0 ? c0[j] : zero) : acc[i][j], 0, 0, 0);
    }
}
DI void addr_add(Addr& a, int d) {
  a.x += d;
#pragma unroll
  for (int c = 0; c < 4; ++c) a.w[c] += d;
}
template <bool WA, int NSLOT, int SLOTB, bool Z = false>
DI void gstep(f32x4 (&acc)[8][4], Frags& Fn, const Frags& Fc, Addr& a, int& slot, const f32x4* c0 = nullptr) {
  SB0();
  load_frags(Fn, a);
  mma_all<WA, Z>(acc, Fc, c0);
  slot = (slot + 1 == NSLOT) ? 0 : slot + 1;
  addr_add(a, (slot == 0) ? -(NSLOT - 1) * SLOTB : SLOTB);
  asm volatile("" : "+v"(a.x), "+v"(a.w[0]), "+v"(a.w[1]), "+v"(a.w[2]), "+v"(a.w[3]));
#pragma unroll
  for (int i = 0; i < 8; ++i) { SGB(0x8, 1); SGB(0x100, 1); SGB(0x8, 1); SGB(0x100, 1); SGB(0x8, 1); }
#pragma unroll
  for (int i = 0; i < 6; ++i) { SGB(0x8, 1); SGB(0x4, 1); SGB(0x2, 1); }
  SGB(0x8, 2);
  SB0();
  wait_lgkm0();
  bar();
}
template <int MODE, bool CS, bool Z = false>
DI void qstep(f32x4 (&acc)[8][4], f32x4 (&cs)[2], u32x4& ones, unsigned onext, Frags& Fn, const Frags& Fc, Addr& a, int& xs, int& wsl) {
  const f32x4 zero = {0.f, 0.f, 0.f, 0.f};
  SB0();
  load_frags<MODE == 2 ? 1024 : 2048>(Fn, a);
  if (CS) {
#pragma unroll
    for (int j = 0; j < 2; ++j) cs[j] = __builtin_amdgcn_mfma_f32_16x16x32_bf16(Fc.w[j], __builtin_bit_cast(bf16x8, ones), Z ? zero : cs[j], 0, 0, 0);
    ones = (u32x4){onext, onext, onext, onext};
    asm volatile("" : "+v"(ones));
  }
  mma_all<true, Z>(acc, Fc, nullptr);
  if (MODE == 2) addr_add(a, 32768);
  else {
    wsl = (wsl + 1) & 3;
    const int dw = wsl ? 8192 : -24576;
#pragma unroll
    for (int c = 0; c < 4; ++c) a.w[c] += dw;
    a.x ^= 64;
    if (MODE == 1) { xs = (xs == 2) ? 0 : xs + 1; a.x += xs ? 32768 : -65536; }
  }
  asm volatile("" : "+v"(a.x), "+v"(a.w[0]), "+v"(a.w[1]), "+v"(a.w[2]), "+v"(a.w[3]));
#pragma unroll
  for (int i = 0; i < 8; ++i) { SGB(0x8, 1); SGB(0x100, 1); SGB(0x8, 1); SGB(0x100, 1); SGB(0x8, 1); }
#pragma unroll
  for (int i = 0; i < 7; ++i) { SGB(0x8, 1); SGB(0x4, 1); SGB(0x2, 1); }
  if (CS) { SGB(0x8, 1); SGB(0x2, 2); SGB(0x8, 1); SGB(0x2, 2); }
  SGB(0x8, 1);
  SB0();
  wait_lgkm0();
  bar();
}
DI void addr_init(Addr& a, unsigned xbase, unsigned wbase, int wj, int cx, int lane) {
  const int g = lane >> 4, q = (lane >> 2) & 3, p = lane & 3, row = 4 * g + q;
  a.x = xbase + lane * 16;
#pragma unroll
  for (int c = 0; c < 4; ++c) a.w[c] = wbase + row * 256 + (((2 * (4 * wj + (c ^ cx)) + (p >> 1)) ^ ((row & 7) << 1)) * 16) + (p & 1) * 8;
}
template <bool WA, int NSLOT, int SLOTB> DI void gemm_steps(f32x4 (&acc)[8][4], Frags& F0, Frags& F1, Addr& a, int& slot, int n) {
  for (int t = 0; t < n; t += 2) {
    gstep<WA, NSLOT, SLOTB>(acc, F0, F1, a, slot);
    gstep<WA, NSLOT, SLOTB>(acc, F1, F0, a, slot);
  }
}

struct LdU { rsrc_t rx, rw; unsigned xv, wv, lb; int hn, hs; };
DI void ldu_half(LdU& L) {
  const unsigned s = L.lb + L.hs * 16384, xs = L.hn * 8192, wsf = L.hn * (32 * FF * 2);
  dma16s(L.rx, s, L.xv, xs);
  dma16(L.rw, s + 8192, L.wv, wsf);
  dma16s(L.rx, s + 4096, L.xv, xs + 4096);
  dma16(L.rw, s + 12288, L.wv, wsf + 4 * FF * 2);
  L.hn++; L.hs = (L.hs + 1) & 7;
}
DI u32x4 pack8(const f32x4& a, const f32x4& b) { return (u32x4){cvtpk(a[0], a[1]), cvtpk(a[2], a[3]), cvtpk(b[0], b[1]), cvtpk(b[2], b[3])}; }
DI void ystep_park(f32x4 (&acc)[8][4], Frags& Fn, const Frags& Fc, Addr& a, int& slot, unsigned pm) {
  s16x4 h[8];
  SB0();
#pragma unroll
  for (int k = 0; k < 16; ++k) {
    const int m = k >> 2, j = k & 3;
    *(LAS u32x4*)(size_t)(pm + j * 16384 + m * 1024) = pack8(acc[2 * m][j], acc[2 * m + 1][j]);
    asm volatile("v_mfma_f32_16x16x32_bf16 %0, %1, %2, 0" : "+v"(acc[2 * m][j]) : "v"(Fc.w[j]), "v"(Fc.x[2 * m]));
    if (k < 8) h[k] = __builtin_amdgcn_ds_read_tr16_b64_v4i16((LAS s16x4*)(size_t)(a.w[k >> 1] + (k & 1) * 4096));
    else Fn.x[k - 8] = *(LAS bf16x8*)(size_t)(a.x + (k - 8) * 1024);
    asm volatile("v_mfma_f32_16x16x32_bf16 %0, %1, %2, 0" : "+v"(acc[2 * m + 1][j]) : "v"(Fc.w[j]), "v"(Fc.x[2 * m + 1]));
    SB0();
  }
#pragma unroll
  for (int c = 0; c < 4; ++c) Fn.w[c] = __builtin_shufflevector(h[2 * c], h[2 * c + 1], 0, 1, 2, 3, 4, 5, 6, 7);
  slot = 2;
  addr_add(a, 24576);
  wait_lgkm0();
  bar();
}
DI f32x2 gate2(f32x2 u, f32x2 z) {
  f32x2 e = {__builtin_amdgcn_exp2f(z[0]), __builtin_amdgcn_exp2f(z[1])};
  e += 1.f;
  return u * (f32x2){__builtin_amdgcn_rcpf(e[0]), __builtin_amdgcn_rcpf(e[1])};
}
DI unsigned gelu2(f32x2 u) {
  const f32x2 r = gate2(u, u * __builtin_elementwise_fma(u * u, (f32x2){-0.1029432397f, -0.1029432397f}, (f32x2){-2.302208198f, -2.302208198f}));
  return cvtpk(r[0], r[1]);
}
DI unsigned silu2(f32x2 y) { const f32x2 r = gate2(y, y * -1.4426950409f); return cvtpk(r[0], r[1]); }
template <int KK> DI void epi_u(f32x4 (&acc)[8][4], unsigned pk, int cw, rsrc_t ru, unsigned vo) {
#pragma unroll
  for (int ii = 0; ii < 4; ++ii)
#pragma unroll
    for (int j = 0; j < 4; ++j) *(LAS f32x4*)(size_t)(pk + cw * 16384 + (ii * 4 + j) * 1024) = acc[4 * (1 - KK) + ii][j];
  wait_lgkm0();
  bar();
  const unsigned pp = pk + (cw ^ 2) * 16384;
#pragma unroll
  for (int ii = 0; ii < 4; ++ii)
#pragma unroll
    for (int j = 0; j < 4; ++j) acc[4 * KK + ii][j] += *(LAS f32x4*)(size_t)(pp + (ii * 4 + j) * 1024);
#pragma unroll
  for (int ii = 0; ii < 4; ++ii)
#pragma unroll
    for (int mm = 0; mm < 2; ++mm) {
      const f32x4 a = acc[4 * KK + ii][2 * mm], b = acc[4 * KK + ii][2 * mm + 1];
      st16s<0>((u32x4){gelu2(a.xy), gelu2(a.zw), gelu2(b.xy), gelu2(b.zw)}, ru, vo, (cw & 1) * 16384 + mm * 8192 + (4 * KK + ii) * 1024);
    }
}
DI u32x4 ldsr(unsigned a) { return *(LAS u32x4*)(size_t)a; }
DI void add8(f32x4& a, f32x4& b, const u32x4& w) {
  a += (f32x4){bflo(w[0]), bfhi(w[0]), bflo(w[1]), bfhi(w[1])};
  b += (f32x4){bflo(w[2]), bfhi(w[2]), bflo(w[3]), bfhi(w[3])};
}

struct Att { bf16x8 q[8]; f32x16 o[4]; f32x16 negm, sE, sO; u32x4 pO[2]; bf16x8 pre[4]; float l; };
DI float vmax16(const f32x16& a) {
  float m = fmaxf(fmaxf(a[0], a[1]), a[2]);
#pragma unroll
  for (int i = 3; i < 15; i += 2) m = fmaxf(fmaxf(m, a[i]), a[i + 1]);
  m = fmaxf(m, a[15]);
  auto sw = __builtin_amdgcn_permlane32_swap(__float_as_uint(m), __float_as_uint(m), false, false);
  return fmaxf(__uint_as_float(sw[0]), __uint_as_float(sw[1]));
}
DI bf16x8 ldfrag(unsigned a) { return *(LAS bf16x8*)(size_t)a; }
DI void sm1(f32x16& s, int e, u32x4 (&w)[2], float& l) {
  if (e < 16) {
    float x = s[e];
    x = __builtin_amdgcn_exp2f(x);
    asm volatile("" : "+v"(x));
    s[e] = x;
  }
  if (e >= 1) {
    l += s[e - 1];
    asm volatile("" : "+v"(l));
    if (!(e & 1)) { unsigned c = cvtpk(s[e - 2], s[e - 1]); asm volatile("" : "+v"(c)); w[(e - 1) >> 3][((e - 1) >> 1) & 3] = c; }
  }
}
#define SETPRIO(x) __builtin_amdgcn_s_setprio(x)
#define MFMA32(a, b, c) __builtin_amdgcn_mfma_f32_32x32x16_bf16(a, b, c, 0, 0, 0)
#define PFRAG(w) __builtin_bit_cast(bf16x8, w)
template <int POS, class F> DI void att_tile(Att& A, unsigned kv, unsigned kvn, F&& gap) {
  bf16x8 f[36];
  u32x4 pE[2];
  asm volatile("" : "+v"(kv), "+v"(kvn));
#pragma unroll
  for (int i = 0; i < 4; ++i) f[i] = A.pre[i];
  SB0();
  SETPRIO(3);
#pragma unroll
  for (int n = 0; n < 8; ++n) {
    if (POS == 0) A.sE = MFMA32(f[n], A.q[n], n ? A.sE : A.negm);
    else A.o[n & 3] = MFMA32(f[n], PFRAG(A.pO[n >> 2]), A.o[n & 3]);
    f[n + 4] = ldfrag(kv + (n + 4) * 1024);
    if (POS && n <= 2) sm1(A.sO, 14 + n, A.pO, A.l);
    if (POS && n >= 2) sm1(A.sE, n - 2, pE, A.l);
    gap(n);
    SB0();
  }
  if (POS == 0) {
    const float mx = vmax16(A.sE);
#pragma unroll
    for (int i = 0; i < 16; ++i) { A.sE[i] -= mx; A.negm[i] = -mx; }
    SB0();
  }
  SETPRIO(2);
#pragma unroll
  for (int n = 8; n < 16; ++n) {
    A.sO = MFMA32(f[n], A.q[n - 8], n > 8 ? A.sO : A.negm);
    f[n + 4] = ldfrag(kv + (n + 4) * 1024);
    if (POS) sm1(A.sE, n - 2, pE, A.l);
    else if (n < 15) { sm1(A.sE, 2 * (n - 8), pE, A.l); sm1(A.sE, 2 * (n - 8) + 1, pE, A.l); }
    gap(n);
    SB0();
  }
  SETPRIO(1);
#pragma unroll
  for (int n = 16; n < 24; ++n) {
    A.o[n & 3] = MFMA32(f[n], PFRAG(pE[(n >> 2) & 1]), A.o[n & 3]);
    f[n + 4] = ldfrag(kv + (n + 4) * 1024);
    if (n <= 18) sm1(A.sE, n - 2, pE, A.l);
    if (n >= 18) {
      if (POS < 2) sm1(A.sO, n - 18, A.pO, A.l);
      else { sm1(A.sO, 2 * (n - 18), A.pO, A.l); sm1(A.sO, 2 * (n - 18) + 1, A.pO, A.l); }
    }
    gap(n);
    SB0();
  }
  SETPRIO(0);
#pragma unroll
  for (int n = 24; n < 32; ++n) {
    if (POS < 2) {
      A.sE = MFMA32(f[n], A.q[n - 24], n > 24 ? A.sE : A.negm);
      f[n + 4] = ldfrag(n < 28 ? kv + (n + 4) * 1024 : kvn + (n - 28) * 1024);
      sm1(A.sO, n - 18, A.pO, A.l);
    } else {
      A.o[n & 3] = MFMA32(f[n], PFRAG(A.pO[(n >> 2) & 1]), A.o[n & 3]);
      if (n < 28) f[n + 4] = ldfrag(kv + (n + 4) * 1024);
      if (n < 26) { sm1(A.sO, 2 * (n - 18), A.pO, A.l); sm1(A.sO, 2 * (n - 18) + 1, A.pO, A.l); }
      if (n == 26) sm1(A.sO, 16, A.pO, A.l);
    }
    gap(n);
    SB0();
  }
  if (POS < 2) {
#pragma unroll
    for (int i = 0; i < 4; ++i) A.pre[i] = f[32 + i];
  }
}
DI void att_redo(Att& A, rsrc_t rkv, unsigned vo) {
  float m = -3.0e38f, l = 0.f;
#pragma unroll
  for (int d = 0; d < 4; ++d)
#pragma unroll
    for (int i = 0; i < 16; ++i) A.o[d][i] = 0.f;
  for (int blk = 0; blk < 16; ++blk) {
    const int kb = (blk ? 2 * blk - 1 : 0) * 8192, vb = (blk < 15 ? 2 * blk + 2 : 31) * 8192;
    f32x16 s;
#pragma unroll
    for (int i = 0; i < 16; ++i) s[i] = 0.f;
#pragma unroll
    for (int n = 0; n < 8; ++n) s = MFMA32(PFRAG(__builtin_amdgcn_raw_buffer_load_b128(rkv, vo + n * 1024, kb, 16)), A.q[n], s);
    const float mn = fmaxf(m, vmax16(s)), fct = __builtin_amdgcn_exp2f(m - mn);
    m = mn;
    l *= fct;
    u32x4 p[2];
#pragma unroll
    for (int i = 0; i < 16; ++i) s[i] -= mn;
#pragma unroll
    for (int i = 0; i < 17; ++i) sm1(s, i, p, l);
#pragma unroll
    for (int d = 0; d < 4; ++d)
#pragma unroll
      for (int i = 0; i < 16; ++i) A.o[d][i] *= fct;
#pragma unroll
    for (int n = 0; n < 8; ++n) A.o[n & 3] = MFMA32(PFRAG(__builtin_amdgcn_raw_buffer_load_b128(rkv, vo + n * 1024, vb, 16)), PFRAG(p[n >> 2]), A.o[n & 3]);
  }
  A.l = l;
}

__global__ __launch_bounds__(512, 2) void k_main(const u16* __restrict__ x, const u16* __restrict__ kc, const u16* __restrict__ vc, const u16* __restrict__ wq, const u16* __restrict__ wo,
                                                 const u16* __restrict__ w1, const u16* __restrict__ b1, const u16* __restrict__ w2,
                                                 const u16* __restrict__ b2, char* __restrict__ ws, u16* __restrict__ out) {
  extern __shared__ __attribute__((aligned(16))) char smem[];
  LAS char* lds = (LAS char*)smem;
  const unsigned lb = (unsigned)(size_t)lds;
  const int tid = threadIdx.x, wid = __builtin_amdgcn_readfirstlane(tid >> 6);
  const int g8 = blockIdx.x & 7, idx = blockIdx.x >> 3;
  STAMP(1, 0);
  STAMP(0, 0);
  const bool hasU = idx < 28;
  const int dt = 4 * (g8 & 1) + (idx & 3), ft = 7 * (g8 >> 1) + (idx >> 2);
  const int kvh = g8 >> 1, head = 8 * kvh + (idx & 7), rb = 4 * (g8 & 1) + (idx >> 3);
  const int dc = idx & 7, tile = rb * 8 + dc;
  const int lw = wid - 4, wi = (wid >> 1) & 1, wj = wid & 1;
  constexpr int QS = 24576;
  constexpr int PARK = 98304;
  const rsrc_t rf = mkrsrc(ws + WS_FLAG, F_END + 256);
  const rsrc_t rkv = mkrsrc(ws + WS_KV + (size_t)kvh * 262144, 262144);
  const rsrc_t rxb = mkrsrc(ws + WS_XB + (size_t)tile * 393216, 393216);
  struct Strm { rsrc_t r; unsigned v0, v1, step, pair, px; };
  auto strm_x = [&](Strm& S, const char* base, unsigned bytes, unsigned step, int xr) {
    const int ln = lane_id();
    S.r = mkrsrc(base, bytes); S.v0 = ln * 16 + (xr & 1) * 1024; S.v1 = S.v0 ^ 1024; S.step = step; S.pair = 2048; S.px = xr >> 1;
  };
  auto strm_w = [&](Strm& S, const char* base, unsigned rows, unsigned ld) {
    const int ln = lane_id(), q4 = ln >> 4;
    const unsigned c0 = ((ln & 15) ^ (q4 << 1)) * 16;
    S.r = mkrsrc(base, (rows - 1) * ld + 256); S.v0 = q4 * ld + c0; S.v1 = (4 + q4) * ld + (c0 ^ 128); S.step = 32 * ld; S.pair = 8 * ld; S.px = 0;
  };
  auto issue = [&](const Strm& S, int k) {
    const unsigned s = lb + (k & 3) * QS + lw * 8192;
#pragma unroll
    for (int i = 0; i < 4; ++i) { const unsigned so = k * S.step + (i ^ S.px) * S.pair; dma16s(S.r, s + i * 2048, S.v0, so); dma16s(S.r, s + i * 2048 + 1024, S.v1, so); }
  };
  constexpr int QW = 98304, HST = 131072, HRS = 2112, RST = HST + 8 * HRS, PKQ = 148480, STT = 151552, CSX = 153600;
  const int lane0 = tid & 63;
  const rsrc_t rq = lw < 2 ? mkrsrc(x + (size_t)(rb * 256 + lw * 128) * DM, 128 * DM * 2) : mkrsrc((const char*)wq + head * 256, (DM - 1) * HD * 2 + 256);
  const unsigned qv0 = lw < 2 ? (lane0 >> 3) * 2048 + (((lane0 & 7) ^ (lane0 >> 4)) * 16) : (lane0 >> 4) * (HD * 2) + (((lane0 & 15) ^ ((lane0 >> 4) << 1)) * 16);
  const unsigned qv1 = lw < 2 ? (qv0 ^ 64) + 8 * 2048 : (qv0 ^ 128) + 8 * (HD * 2);
  const bool qx = lw < 2;
  const unsigned qp1 = qx ? 32768 : 16 * HD * 2, qp2 = qx ? 65536 : 4 * HD * 2;
  const unsigned qlb = lb + (qx ? lw * 16384 : QW), qlw = qx ? 98304 : 32768, qom = qx ? 0x20780 : 0x7c0000;
  unsigned ql = qx ? 65536 : 0, qdl = 8192, qo = rb * (qx ? 256 : 4 * 32 * HD * 2), qdo = qx ? 131072 : 32 * HD * 2;
  auto issq = [&]() {
    const unsigned s = qlb + ql;
    dma16(rq, s, qv0, qo); dma16(rq, s + 1024, qv1, qo);
    dma16(rq, s + 2048, qv0, qo + qp1); dma16(rq, s + 3072, qv1, qo + qp1);
    dma16(rq, s + 4096, qv0, qo + qp2); dma16(rq, s + 5120, qv1, qo + qp2);
    dma16(rq, s + 6144, qv0, qo + qp1 + qp2); dma16(rq, s + 7168, qv1, qo + qp1 + qp2);
    ql += qdl; ql = ql >= qlw ? ql - qlw : ql; qdl ^= qx ? (8192 ^ 24576) : 0;
    qo = (qo + qdo) & qom; qdo ^= qx ? 128 : 0;
  };
  auto h_out = [&](int it) {
    const rsrc_t rh = mkrsrc(ws + WS_HT + (size_t)(blockIdx.x >> 2) * 8192 + (blockIdx.x & 3) * 256, 7 * 524288 + 8192);
    const unsigned a0 = HST + ((lane0 >> 2) & 3) * HRS + (it * 4 + (lane0 >> 4)) * 32 + (lane0 & 3) * 8;
    s16x4 lo = __builtin_amdgcn_ds_read_tr16_b64_v4i16((LAS s16x4*)(lds + a0));
    s16x4 hi = __builtin_amdgcn_ds_read_tr16_b64_v4i16((LAS s16x4*)(lds + a0 + 4 * HRS));
    st16s<0>(__builtin_bit_cast(u32x4, __builtin_shufflevector(lo, hi, 0, 1, 2, 3, 4, 5, 6, 7)), rh, (lane0 >> 4) * 1024 + (lane0 & 15) * 16,
             (it >> 1) * 524288 + (it & 1) * 4096);
  };
  auto st_out = [&]() { st16s<0>(*(LAS u32x4*)(lds + RST + (lane0 & 3) * 16), mkrsrc(ws + WS_ST + blockIdx.x * 64, 64), lane0 * 16, 0); };
  Tag tag;
  {
    const int b = blockIdx.x, lane = lane0;
    u32x4 A, B;
    u32x2 ep;
    asm volatile("global_load_dwordx4 %0, %3, %4\n\tglobal_load_dwordx4 %1, %3, %4 offset:1024\n\tglobal_load_dwordx2 %2, %5, %6 sc1"
                 : "=&v"(A), "=&v"(B), "=&v"(ep) : "v"(lane * 16), "s"(x + (size_t)(8 * b + wid) * DM), "v"(F_EP), "s"(ws + WS_FLAG) : "memory");
    const bool strm = hasU && wid >= 4 && wid < 7;
    if (strm) { issq(); issq(); wait_vm<16>(); } else wait_vm<0>();
    asm volatile("" : "+v"(A), "+v"(B), "+v"(ep));
    unsigned v[8] = {A[0], A[1], A[2], A[3], B[0], B[1], B[2], B[3]};
    const unsigned long long e64 = ((unsigned long long)(unsigned)__builtin_amdgcn_readfirstlane(ep[1]) << 32) | (unsigned)__builtin_amdgcn_readfirstlane(ep[0]);
    const unsigned long long tg = e64 * 0x9E3779B97F4A7C15ull + 0x632BE59BD9B4E019ull;
    tag.lo = (unsigned)tg; tag.hi = (unsigned)(tg >> 32);
    *(LAS f32x2*)(lds + RST + wid * 8) = ln16(v);
    ln16(v);
    *(LAS u32x4*)(lds + HST + wid * HRS + lane * 16) = (u32x4){v[0], v[1], v[2], v[3]};
    *(LAS u32x4*)(lds + HST + wid * HRS + 1024 + lane * 16) = (u32x4){v[4], v[5], v[6], v[7]};
    wait_lgkm0();
  }
  STAMP(0, 1);
  if (!hasU) {
    const int sp = g8 * 4 + (idx - 28), lane = lane0, h = lane >> 5;
    const unsigned long long t_in = __builtin_amdgcn_s_memrealtime();
    bar();
    h_out(2 * wid); h_out(2 * wid + 1);
    if (wid == 0) st_out();
    wait_vm<0>();
    bar();
    if (wid == 0) signal<1>(ws, F_L + blockIdx.x * 8, tag);
    unsigned e[4][8];
#pragma unroll
    for (int i = 0; i < 4; ++i) {
      const int kh = sp >> 3, unit = (sp & 7) * 4 + i, frag = wid;
      const bool isK = unit == 0 || ((unit & 1) && unit < 31);
      const u16* sk = kc + ((size_t)kh * SEQ + 1536 + 32 * ((unit + 1) >> 1) + (lane & 31)) * 128 + 32 * (frag >> 1) + 4 * (2 * (frag & 1) + h);
      const u16* sv = vc + ((size_t)kh * SEQ + 1536 + 32 * ((unit - 1) >> 1) + 16 * (frag >> 2) + 4 * h) * 128 + 32 * (frag & 3) + (lane & 31);
      const u16* src = isK ? sk : sv;
      const int s1 = isK ? 1 : 128, s4 = isK ? 16 : 1024;
#pragma unroll
      for (int j2 = 0; j2 < 8; ++j2) e[i][j2] = src[(j2 >> 2) * s4 + (j2 & 3) * s1];
    }
    unsigned pw[8];
#pragma unroll
    for (int i = 0; i < NSP; ++i) pw[i] = *(const unsigned*)((const char*)w1 + (size_t)i * PFB + (size_t)(sp * 448 + (tid < 448 ? tid : 0)) * 128);
    SB0();
#pragma unroll
    for (int i = 0; i < 4; ++i)
      st16s<0>((u32x4){e[i][0] | (e[i][1] << 16), e[i][2] | (e[i][3] << 16), e[i][4] | (e[i][5] << 16), e[i][6] | (e[i][7] << 16)},
               mkrsrc(ws + WS_KV, 1048576), tid * 16, (sp * 4 + i) * 8192);
#pragma unroll
    for (int i = 0; i < NSP; ++i) asm volatile("" ::"v"(pw[i]));
    wait_vm<0>();
    bar();
    if (wid == 0) signal<1>(ws, F_X + (sp >> 3) * 128 + (sp & 7) * 8, tag);
    for (int i = 0; i < 1000 && __builtin_amdgcn_s_memrealtime() - t_in < SPARE_T; ++i) __builtin_amdgcn_s_sleep(8);
    if (wid >= 4 && wid < 7) { issq(); issq(); }
  }
  bf16x8 qh[8];
  auto kv8 = [&](int t, int slot) {
    dma4k(rkv, lb + slot * 32768 + lw * 8192, lane0 * 16, t * 32768 + lw * 8192);
    dma4k(rkv, lb + slot * 32768 + lw * 8192 + 4096, lane0 * 16, t * 32768 + lw * 8192 + 4096);
  };
  if (wid < 4) {
    f32x4 acc[8][4], cs[2];
    Frags F0, F1;
    Addr a;
    int xs = 2, wsl = 1;
    {
      const int ln = lane_id(), r = ln & 15;
      addr_init(a, 0, lb + QW, wj, 2 * wi, ln);
      a.x = lb + 65536 + wi * 16384 + r * 128 + (((ln >> 4) ^ (r >> 1)) * 16);
    }
    bar();
    load_frags<2048>(F0, a);
    a.x ^= 64;
#pragma unroll
    for (int c = 0; c < 4; ++c) a.w[c] += 8192;
    wait_lgkm0();
    bar();
    STAMP(1, 1);
    {
      const unsigned o1 = (rb & 3) == 3 ? 0 : 0x3f803f80, o2 = (rb & 3) == 2 ? 0x3f803f80 : 0;
      u32x4 ones = {o1, o1, o1, o1};
      qstep<1, true, true>(acc, cs, ones, o1, F1, F0, a, xs, wsl);
      for (int t = 0; t < 4; ++t) {
        const unsigned on = t == 0 ? o1 : t == 3 ? 0 : o2;
        qstep<0, true>(acc, cs, ones, on, F0, F1, a, xs, wsl);
        qstep<1, true>(acc, cs, ones, on, F1, F0, a, xs, wsl);
      }
      const int ln = lane_id();
      const unsigned csa = lb + CSX + ((ln & 15) ? 7168 : (64 * wj + 32 * wi) * 4) + (ln & 48);
      *(LAS f32x4*)(size_t)csa = cs[0];
      *(LAS f32x4*)(size_t)(csa + 64) = cs[1];
      for (int t = 0; t < 11; ++t) { qstep<0, false>(acc, cs, ones, 0, F0, F1, a, xs, wsl); qstep<1, false>(acc, cs, ones, 0, F1, F0, a, xs, wsl); }
      addr_init(a, lb + wi * 16384, lb + wi * 16384 + 8192, wj, 0, lane_id());
      qstep<2, false>(acc, cs, ones, 0, F0, F1, a, xs, wsl);
    }
    STAMP(1, 2);
    f32x4 c0[4];
    {
      const int ln = lane_id(), g = ln >> 4;
      u32x2 bv[4];
#pragma unroll
      for (int j = 0; j < 4; ++j) bv[j] = *(LAS u32x2*)(lds + CSX + 5120 + (64 * wj + 16 * j + 4 * g) * 2);
      f32x4 cq[4];
#pragma unroll
      for (int j = 0; j < 4; ++j) cq[j] = *(LAS f32x4*)(lds + CSX + 512 + (64 * wj + 16 * (j ^ (2 * wi)) + 4 * g) * 4);
      f32x2 st[8];
#pragma unroll
      for (int i = 0; i < 8; ++i) st[i] = *(LAS f32x2*)(lds + STT + (128 * wi + 16 * i + (ln & 15)) * 8);
      wait_lgkm0();
      bar();
      const unsigned qa = lb + (wi ? 98304 : 131072) + (4 * wj + (g >> 1)) * (wi ? 4096 : 1024) + ((ln & 15) + 32 * (g & 1)) * 16;
      const unsigned sq = wi ? 1024 : 8192, m0 = wi ? 8192 : 0, m1 = wi ? 0 : 2048;
#pragma unroll
      for (int i = 0; i < 8; ++i) {
        const float sc = st[i][1] * 0.12751743082f, nm = -st[i][0];
#pragma unroll
        for (int mm = 0; mm < 2; ++mm)
          *(LAS u32x4*)(size_t)(qa + (i >> 1) * sq + (mm ? m1 : m0) + (i & 1) * 256) =
              pack8((acc[i][2 * mm] + cq[2 * mm] * nm) * sc, (acc[i][2 * mm + 1] + cq[2 * mm + 1] * nm) * sc);
      }
#pragma unroll
      for (int j = 0; j < 4; ++j) c0[j] = wi ? (f32x4){0.f, 0.f, 0.f, 0.f} : (f32x4){bflo(bv[j][0]), bfhi(bv[j][0]), bflo(bv[j][1]), bfhi(bv[j][1])};
      wait_lgkm0();
      bar();
    }
    STAMP(1, 3);
    if (hasU) {
      int slot = 1;
      gstep<true, 4, 32768, true>(acc, F1, F0, a, slot, c0);
      gemm_steps<true, 4, 32768>(acc, F0, F1, a, slot, 30);
      gstep<true, 4, 32768>(acc, F0, F1, a, slot);
      STAMP(1, 4);
      const rsrc_t ru = mkrsrc(ws + WS_U + (size_t)(dt * 112 + 4 * ft) * 8192, 32768);
      const int ln = lane_id();
      if (wi) epi_u<1>(acc, lb + 65536 + ln * 16, wid, ru, ln * 16); else epi_u<0>(acc, lb + 65536 + ln * 16, wid, ru, ln * 16);
    }
    STAMP(1, 5);
    const int ln = lane_id();
#pragma unroll
    for (int ks = 0; ks < 8; ++ks) qh[ks] = *(LAS bf16x8*)(lds + 131072 + wid * 8192 + ks * 1024 + ln * 16);
  } else {
    const int lane = lane0;
    LdU L;
    const int lrow = 4 * lw + (lane >> 4);
    L.rx = mkrsrc(ws + WS_HT + (size_t)dt * 524288, 524288);
    L.rw = mkrsrc((const char*)w1 + ft * 256, (SEQ - 1) * FF * 2 + 256);
    L.xv = lw * 1024 + lane * 16; L.wv = (8 * lw + (lane >> 4)) * (FF * 2) + ((lane & 15) ^ ((lrow & 7) << 1)) * 16;
    L.lb = lb + lw * 1024; L.hn = 0; L.hs = 0;
    const unsigned pk = lb + PKQ + lw * 512;
    if (lw < 3) {
      issq();
      if (lw < 2) issq();
      wait_vm<16>();
      bar();
      for (int t = 0; t <= 21; ++t) { issq(); wait_vm<16>(); bar(); }
      peek(rf, pk, F_L + lw * 512); peek(rf, pk + 256, F_L + lw * 512 + 256);
      for (int t = 22; t <= 26; ++t) { issq(); wait_vm<16>(); bar(); }
      issq(); wait_vm<16>();
    } else {
      peek(mkrsrc(b1, FF * 2), lb + CSX + 5120, ft * 256);
      bar();
      if (hasU) {
        for (int t = 0; t <= 7; ++t) { h_out(2 * t); h_out(2 * t + 1); bar(); }
        st_out();
        bar(); bar(); bar();
        st16s<0>(*(LAS u32x4*)(lds + CSX + (lane & 31) * 16), mkrsrc(ws + WS_CS + (head * 8 + rb) * 512, 512), lane * 16, 0);
        bar(); bar(); bar();
        wait_vm<0>();
        signal<1>(ws, F_L + blockIdx.x * 8, tag);
      } else {
        for (int t = 0; t <= 13; ++t) bar();
        spin_until(ws, F_X + kvh * 128, 8, tag);
      }
      const rsrc_t rp = mkrsrc(w1, hasU ? SEQ * FF * 2 : 0);
      for (int t = 14; t <= 21; ++t) {
        if ((t - 14) % PFS == 0 && NSP + (t - 14) / PFS < 8)
          asm volatile("s_mov_b32 m0, %0\n\ts_nop 0\n\tbuffer_load_dword %2, %1, %3 offen lds" ::"s"(lb + 161792), "s"(rp), "v"(((g8 * 28 + idx) * 64 + lane) * 128),
                       "s"((NSP + (t - 14) / PFS) * 1835008) : "memory");
        bar();
      }
      peek(rf, pk, F_L + lw * 512); peek(rf, pk + 256, F_L + lw * 512 + 256);
      for (int t = 22; t <= 26; ++t) bar();
      wait_vm<0>();
    }
    peek_check(ws, pk, F_L + lw * 512, 32, tag); peek_check(ws, pk + 256, F_L + lw * 512 + 256, 32, tag);
    bar();
    if (blockIdx.x == 0 && lw == 3) signal<1>(ws, F_EP, tag);
    if (lw == 2) issq();
    if (lw == 3) {
      const rsrc_t rs = mkrsrc(ws + WS_ST + rb * 2048, 2048);
      dma16s(rs, lb + STT, lane * 16, 0); dma16s(rs, lb + STT + 1024, lane * 16, 1024);
      dma4k(mkrsrc(ws + WS_CS + head * 4096, 4096), lb + CSX + 1024, lane * 16, 0);
    }
    if (hasU) { ldu_half(L); ldu_half(L); } else kv8(0, 0);
    wait_vm<16>(); bar();
    wait_vm<8>(); bar();
    if (hasU) { ldu_half(L); ldu_half(L); } else kv8(1, 1);
    if (lw == 3) {
      const LAS f32x2* cp = (const LAS f32x2*)(lds + CSX + 1024) + lane;
      *(LAS f32x2*)(lds + CSX + 512 + lane * 8) = ((cp[0] + cp[64]) + cp[128]) + ((cp[256] + cp[320]) + cp[384]);
      wait_lgkm0();
    }
    bar();
    wait_vm<8>(); bar();
    if (hasU) { ldu_half(L); ldu_half(L); wait_vm<8>(); } else wait_vm<0>();
    bar();
    bar();
    bar();
#pragma unroll
    for (int ks = 0; ks < 8; ++ks) qh[ks] = *(LAS bf16x8*)(lds + 98304 + ks * 4096 + lw * 1024 + lane * 16);
    wait_lgkm0();
    asm volatile("" : "+v"(qh[0]), "+v"(qh[1]), "+v"(qh[2]), "+v"(qh[3]), "+v"(qh[4]), "+v"(qh[5]), "+v"(qh[6]), "+v"(qh[7])::"memory");
    if (hasU) {
      ldu_half(L); ldu_half(L); ldu_half(L); wait_vm<12>(); bar();
      ldu_half(L); ldu_half(L); ldu_half(L); wait_vm<16>(); bar();
      for (int t = 3; t <= 27; ++t) { ldu_half(L); ldu_half(L); wait_vm<16>(); bar(); }
      ldu_half(L); ldu_half(L); wait_vm<16>();
      if (lw == 3) spin_until(ws, F_X + kvh * 128, 8, tag);
      bar();
      kv8(0, 0); wait_vm<16>(); bar();
      kv8(1, 1); wait_vm<16>(); bar();
      bar(); bar();
      bar();
      wait_vm<0>();
    }
  }
  wait_lgkm0();
  bar();
  STAMP(1, 6);
  unsigned pf = 0;
  Strm Y;
  {
    const int lane = lane_id();
    Att A;
#pragma unroll
    for (int ks = 0; ks < 8; ++ks) A.q[ks] = qh[ks];
#pragma unroll
    for (int d = 0; d < 4; ++d)
#pragma unroll
      for (int i = 0; i < 16; ++i) A.o[d][i] = 0.f;
#pragma unroll
    for (int i = 0; i < 16; ++i) A.negm[i] = 0.f;
    A.l = 0.f;
    const unsigned kvl = lb + lane * 16;
#pragma unroll
    for (int i = 0; i < 4; ++i) A.pre[i] = ldfrag(kvl + i * 1024);
    if (lw == 0) strm_x(Y, ws + WS_U + (size_t)(dc * 112 + 28 * kvh) * 8192, 28 * 8192, 8192, 0);
    else strm_w(Y, (const char*)w2 + (size_t)kvh * 896 * (SEQ * 2) + rb * 512 + ((lw - 1) & 1) * 256, 896, SEQ * 2);
    const bool strm = wid >= 4 && wid < 7, hw = wid >= 4;
    const rsrc_t rp2 = mkrsrc(w2, SEQ * FF * 2), rpo = mkrsrc(wo, HD * DM * 2);
    const unsigned pv = (blockIdx.x * 64 + lane) * 128;
#define PF(W, R, C) pf_line<W>(pf, wid, R, pv, (C) * 2097152u)
    att_tile<0>(A, kvl + 0 * 32768, kvl + 1 * 32768, [&](int n) { if (n == 0 && hw) kv8(2, 2); if (n == 17 && hw) kv8(3, 3); });
    if (hw) wait_vm<8>(); else wait_vm<0>();
    bar();
    if (wid == 0 && hasU) signal<1>(ws, F_U + (dt * 4 + kvh) * 128 + (idx >> 2) * 8, tag);
    att_tile<1>(A, kvl + 1 * 32768, kvl + 2 * 32768, [&](int n) { if (n == 1 && hw) kv8(4, 0); if (n == 17 && hw) kv8(5, 4); if (n == 5) PF(0, rp2, 0); if (n == 14) PF(1, rp2, 1); if (n == 25) PF(2, rp2, 2); });
    if (hw) wait_vm<16>();
    bar();
    att_tile<1>(A, kvl + 2 * 32768, kvl + 3 * 32768, [&](int n) { if (n == 3) PF(3, rp2, 3); if (n == 19) PF(0, rp2, 4); });
    if (hw) wait_vm<8>();
    bar();
    att_tile<1>(A, kvl + 3 * 32768, kvl + 0 * 32768, [&](int n) { if (n == 1 && hw) kv8(7, 2); if (n == 7) PF(1, rp2, 5); if (n == 18) PF(2, rp2, 6); });
    if (hw) wait_vm<8>();
    bar();
    att_tile<1>(A, kvl + 0 * 32768, kvl + 4 * 32768, [&](int n) { if (n == 0 && hw) kv8(6, 3); if (n == 9) PF(3, rpo, 0); if (n == 23) PF(0, rpo, 1); });
    if (hw) wait_vm<0>();
    bar();
    att_tile<1>(A, kvl + 4 * 32768, kvl + 3 * 32768, [&](int n) { if (n == 1 && wid == 4) peek(rf, lb + 49152, F_U + (dc * 4 + kvh) * 128); if (n == 5) PF(1, rpo, 2); if (n == 16) PF(2, rpo, 3); });
    if (wid == 4) { wait_vm<0>(); peek_check(ws, lb + 49152, F_U + (dc * 4 + kvh) * 128, 7, tag); }
    bar();
    att_tile<1>(A, kvl + 3 * 32768, kvl + 2 * 32768, [&](int n) { if (n == 1 && strm) issue(Y, 0); if (n == 17 && strm) issue(Y, 1); });
    if (hw) wait_vm<8>();
    bar();
    att_tile<2>(A, kvl + 2 * 32768, 0, [&](int n) { if (n == 9) bar(); if (n == 10 && strm) issue(Y, 2); });
    STAMP(1, 7);
    if (__builtin_expect(__any(!(A.l <= 1.0e9f)), 0)) att_redo(A, rkv, lane * 16);
    auto sw = __builtin_amdgcn_permlane32_swap(__float_as_uint(A.l), __float_as_uint(A.l), false, false);
    const float inv = __builtin_amdgcn_rcpf(__uint_as_float(sw[0]) + __uint_as_float(sw[1]));
    const unsigned oa = lb + PARK + wid * 2048 + ((lane >> 4) & 1) * 1024 + (lane >> 5) * 256 + (lane & 15) * 16;
#pragma unroll
    for (int d = 0; d < 4; ++d)
#pragma unroll
      for (int u = 0; u < 2; ++u) {
        const int r0 = 4 * u, r1 = 8 + 4 * u;
        *(LAS u32x4*)(size_t)(oa + d * 16384 + u * 512) =
            (u32x4){cvtpk(A.o[d][r0] * inv, A.o[d][r0 + 1] * inv), cvtpk(A.o[d][r0 + 2] * inv, A.o[d][r0 + 3] * inv),
                    cvtpk(A.o[d][r1] * inv, A.o[d][r1 + 1] * inv), cvtpk(A.o[d][r1 + 2] * inv, A.o[d][r1 + 3] * inv)};
      }
    wait_lgkm0();
  }
  STAMP(1, 8);
  if (wid < 4) {
    f32x4 acc[8][4];
    Frags F0, F1;
    Addr a;
    int slot = 1;
    addr_init(a, lb, lb + 8192 + wi * 8192, wj, kvh, lane_id());
    load_frags(F0, a);
    addr_add(a, QS);
    wait_lgkm0();
    bar();
    STAMP(1, 9);
    gstep<false, 4, QS, true>(acc, F1, F0, a, slot);
    gemm_steps<false, 4, QS>(acc, F0, F1, a, slot, 26);
    addr_init(a, lb + wi * 8192, lb + 16384, wj, 0, lane_id());
    gstep<false, 4, QS>(acc, F0, F1, a, slot);
    STAMP(1, 10);
    asm volatile("s_waitcnt vmcnt(0)" : "+v"(pf)::"memory");
    ystep_park(acc, F1, F0, a, slot, lb + PARK + lane_id() * 16 + wid * 4096);
    STAMP(1, 11);
    gemm_steps<true, 4, QS>(acc, F0, F1, a, slot, 30);
    gstep<true, 4, QS>(acc, F0, F1, a, slot);
    STAMP(1, 12);
    const int lt = lane_id(), g = lt >> 4;
    const unsigned wo_ = wi * 8192 + wj * 2048;
#pragma unroll
    for (int dl = 1; dl < 4; ++dl)
#pragma unroll
      for (int h = 0; h < 2; ++h)
#pragma unroll
        for (int mm = 0; mm < 2; ++mm)
          st16s<0>(pack8(acc[4 * h + dl][2 * mm], acc[4 * h + dl][2 * mm + 1]), rxb, lt * 16, (kvh ^ dl) * 98304 + 49152 + (dl - 1) * 16384 + wo_ + h * 4096 + mm * 1024);
    wait_vm<0>();
    signal<14>(ws, F_S + (tile * 4 + (kvh ^ (lt & 3))) * 128 + wid * 32 + ((lt & 3) - 1) * 8, tag);
    STAMP(1, 13);
    const int srow = rb * 256 + (8 * wi + kvh) * 16;
    const rsrc_t rx = mkrsrc(x + (size_t)srow * DM, 80 * DM * 2), rout = mkrsrc(out + (size_t)srow * DM, 80 * DM * 2);
    const unsigned vo = ((lt & 15) * DM + dc * 128 + wj * 64 + 16 * (g & 1) + 8 * (g >> 1)) * 2;
    u32x4 ps[3][4], xv[4], gv[4];
#pragma unroll
    for (int p = 0; p < 4; ++p) xv[p] = __builtin_amdgcn_raw_buffer_load_b128(rx, vo + (p >> 1) * (64 * DM * 2) + (p & 1) * 64, 0, 0);
    bar();
#pragma unroll
    for (int p = 0; p < 4; ++p) gv[p] = ldsr(lb + PARK + lt * 16 + wo_ + (p >> 1) * 4096 + (p & 1) * 1024);
    spin_until(ws, F_S + (tile * 4 + kvh) * 128 + wid * 32, 3, tag);
    STAMP(1, 14);
#pragma unroll
    for (int p = 0; p < 4; ++p)
#pragma unroll
      for (int k = 0; k < 3; ++k)
        ps[k][p] = __builtin_amdgcn_raw_buffer_load_b128(rxb, lt * 16 + (p & 1) * 1024, kvh * 98304 + 49152 + k * 16384 + wo_ + (p >> 1) * 4096, 16);
#pragma unroll
    for (int p = 0; p < 4; ++p) {
      f32x4 s0 = acc[4 * (p >> 1)][2 * (p & 1)], s1 = acc[4 * (p >> 1)][2 * (p & 1) + 1];
#pragma unroll
      for (int k = 0; k < 3; ++k) add8(s0, s1, ps[k][p]);
      s0 *= (f32x4){bflo(gv[p][0]), bfhi(gv[p][0]), bflo(gv[p][1]), bfhi(gv[p][1])};
      s1 *= (f32x4){bflo(gv[p][2]), bfhi(gv[p][2]), bflo(gv[p][3]), bfhi(gv[p][3])};
      const u32x4 tp = pack8(s0, s1);
      const auto w0 = __builtin_amdgcn_permlane16_swap(tp[0], tp[2], false, false), w1 = __builtin_amdgcn_permlane16_swap(tp[1], tp[3], false, false);
      const unsigned t[4] = {w0[0], w1[0], w0[1], w1[1]};
      u32x4 o;
#pragma unroll
      for (int e = 0; e < 4; ++e) o[e] = cvtpk(bflo(xv[p][e]) + bflo(t[e]), bfhi(xv[p][e]) + bfhi(t[e]));
      __builtin_amdgcn_raw_buffer_store_b128(o, rout, vo + (p >> 1) * (64 * DM * 2) + (p & 1) * 64, 0, 0);
    }
    STAMP(1, 15);
  } else {
    const int lane = lane_id();
    const unsigned pl = lb + PARK + lane * 16, pk = lb + 159744 + lw * 256;
    const int fo = F_O + (rb * 4 + kvh) * 128;
    if (lw < 3) {
      Strm S;
      if (lw < 2) strm_x(S, ws + WS_O + (size_t)(rb * 128 + 32 * kvh) * 16384 + lw * 8192, 524288 - lw * 8192, 16384, kvh);
      else strm_w(S, (const char*)wo + (size_t)kvh * 1024 * (DM * 2) + dc * 256, 1024, DM * 2);
      wait_vm<8>();
      bar();
      issue(Y, 3);
      for (int t = 1; t <= 22; ++t) { issue(Y, t + 3); wait_vm<16>(); bar(); }
      peek(rf, pk, fo);
      issue(Y, 26); wait_vm<16>(); bar();
      issue(Y, 27); wait_vm<16>(); bar();
      peek_check(ws, pk, fo, 8, tag);
      for (int t = 25; t <= 56; ++t) { issue(S, t - 25); wait_vm<16>(); bar(); }
      wait_vm<8>(); bar();
      wait_vm<0>(); bar();
    } else {
      bar();
      const rsrc_t ro = mkrsrc(ws + WS_O + (size_t)(rb * 128 + 4 * head) * 16384, 65536);
      for (int t = 1; t <= 16; ++t) {
        const unsigned o = (t - 1) * 4096;
        const u32x4 v0 = ldsr(pl + o), v1 = ldsr(pl + o + 1024), v2 = ldsr(pl + o + 2048), v3 = ldsr(pl + o + 3072);
        st16s<0>(v0, ro, lane * 16, o); st16s<1024>(v1, ro, lane * 16, o);
        st16s<2048>(v2, ro, lane * 16, o); st16s<3072>(v3, ro, lane * 16, o);
        bar();
      }
      bar(); bar();
      wait_vm<0>();
      signal<1>(ws, fo + dc * 8, tag);
      for (int t = 19; t <= 29; ++t) bar();
      for (int t = 30; t <= 45; ++t) {
        const unsigned o = (t - 30) * 1024;
        const u32x4 v0 = ldsr(pl + 16384 + o), v1 = ldsr(pl + 32768 + o), v2 = ldsr(pl + 49152 + o);
        st16s<0>(v0, rxb, lane * 16, (kvh ^ 1) * 98304 + o);
        st16s<0>(v1, rxb, lane * 16, (kvh ^ 2) * 98304 + 16384 + o);
        st16s<0>(v2, rxb, lane * 16, (kvh ^ 3) * 98304 + 32768 + o);
        bar();
      }
      for (int t = 46; t <= 48; ++t) bar();
      wait_vm<0>();
      signal<14>(ws, F_Y + (tile * 4 + (kvh ^ (lane & 3))) * 128 + ((lane & 3) - 1) * 8, tag);
      for (int t = 49; t <= 56; ++t) bar();
      peek(rf, pk, F_Y + (tile * 4 + kvh) * 128);
      bar();
      wait_vm<0>();
      peek_check(ws, pk, F_Y + (tile * 4 + kvh) * 128, 3, tag);
      bar();
    }
    u32x4 py[3][4];
#pragma unroll
    for (int k = 0; k < 3; ++k)
#pragma unroll
      for (int m = 0; m < 4; ++m) py[k][m] = __builtin_amdgcn_raw_buffer_load_b128(rxb, lane * 16 + m * 1024, kvh * 98304 + lw * 4096 + k * 16384, 16);
    const float bb = bflo(b2[rb * 256 + (4 * lw + kvh) * 16 + (lane & 15)]);
    bar();
    bar();
#pragma unroll
    for (int m = 0; m < 4; ++m) {
      f32x4 y0 = {bb, bb, bb, bb}, y1 = y0;
      add8(y0, y1, ldsr(pl + lw * 4096 + m * 1024));
#pragma unroll
      for (int k = 0; k < 3; ++k) add8(y0, y1, py[k][m]);
      *(LAS u32x4*)(size_t)(pl + lw * 4096 + m * 1024) = (u32x4){silu2(y0.xy), silu2(y0.zw), silu2(y1.xy), silu2(y1.zw)};
    }
    wait_lgkm0();
    bar();
  }
}

extern "C" void kernel_launch(void* const* d_in, const int* in_sizes, int n_in, void* d_out, int out_size, void* d_ws, size_t ws_size,
                              hipStream_t stream) {
  const u16* x = (const u16*)d_in[0];
  const u16* kc = (const u16*)d_in[1];
  const u16* vc = (const u16*)d_in[2];
  const u16* wq = (const u16*)d_in[3];
  const u16* wo = (const u16*)d_in[4];
  const u16* w1 = (const u16*)d_in[6];
  const u16* b1 = (const u16*)d_in[7];
  const u16* w2 = (const u16*)d_in[8];
  const u16* b2 = (const u16*)d_in[9];
  char* ws = (char*)d_ws;
  (void)hipFuncSetAttribute((const void*)k_main, hipFuncAttributeMaxDynamicSharedMemorySize, 163840);
  hipLaunchKernelGGL(k_main, dim3(256), dim3(512), 163840, stream, x, kc, vc, wq, wo, w1, b1, w2, b2, ws, (u16*)d_out);
}
```

```cpp
#include <hip/hip_runtime.h>
#include <hip/hip_bf16.h>

#define ROT 2
#define SPARE_SLEEP 8
typedef unsigned short u16;
typedef short bf16x8 __attribute__((ext_vector_type(8)));
typedef short s16x4 __attribute__((ext_vector_type(4)));
typedef float f32x16 __attribute__((ext_vector_type(16)));
typedef float f32x4 __attribute__((ext_vector_type(4)));
typedef float f32x2 __attribute__((ext_vector_type(2)));
typedef unsigned u32x4 __attribute__((ext_vector_type(4)));
typedef unsigned u32x2 __attribute__((ext_vector_type(2)));
typedef __bf16 bf16x2 __attribute__((ext_vector_type(2)));
typedef __amdgpu_buffer_rsrc_t rsrc_t;
#define LAS __attribute__((address_space(3)))
#define GAS __attribute__((address_space(1)))
#define DI __device__ __forceinline__
#define SGB(m, n) __builtin_amdgcn_sched_group_barrier(m, n, 0)
#define SB0() __builtin_amdgcn_sched_barrier(0)

constexpr int SEQ = 2048, DM = 1024, FF = 3584, HD = 4096;
constexpr size_t WS_FLAG = 0;
constexpr int F_EP = 0, F_X = 512, F_L = 2048, F_U = 4096, F_O = 8192, F_Y = 16384, F_S = 49152, F_END = 81920;
constexpr size_t WS_ST = 131072;
constexpr size_t WS_HT = 262144;
constexpr size_t WS_KV = WS_HT + 4194304;
constexpr size_t WS_O = WS_KV + 1048576;
constexpr size_t WS_U = WS_O + 16777216;
constexpr size_t WS_XB = WS_U + 7340032;
constexpr size_t WS_END = WS_XB + 25165824;

#define STAMP(kid, i)
DI unsigned cvtpk(float lo, float hi) { f32x2 v = {lo, hi}; bf16x2 b = __builtin_convertvector(v, bf16x2); return __builtin_bit_cast(unsigned, b); }
DI float bflo(unsigned u) { return __uint_as_float(u << 16); }
DI float bfhi(unsigned u) { return __uint_as_float(u & 0xffff0000u); }
DI float rbf(float x) { return bflo(cvtpk(x, x)); }
template <int N> DI void wait_vm() { asm volatile("s_waitcnt vmcnt(%0)" ::"i"(N) : "memory"); }
DI void wait_lgkm0() { __builtin_amdgcn_s_waitcnt(0xC07F); }
DI void bar() { asm volatile("" ::: "memory"); __builtin_amdgcn_s_barrier(); asm volatile("" ::: "memory"); }
DI rsrc_t mkrsrc(const void* p, unsigned bytes) { return __builtin_amdgcn_make_buffer_rsrc((void*)p, 0, bytes, 0x00020000); }
DI void dma16(rsrc_t r, unsigned lds_addr, unsigned voff, unsigned soff) {
  asm volatile("s_mov_b32 m0, %0\n\ts_nop 0\n\tbuffer_load_dwordx4 %1, %2, %3 offen lds" ::"s"(lds_addr), "v"(voff), "s"(r), "s"(soff) : "memory");
}
DI void dma16s(rsrc_t r, unsigned lds_addr, unsigned voff, unsigned soff) {
  asm volatile("s_mov_b32 m0, %0\n\ts_nop 0\n\tbuffer_load_dwordx4 %1, %2, %3 offen sc1 lds" ::"s"(lds_addr), "v"(voff), "s"(r), "s"(soff) : "memory");
}
template <int OFF> DI void st16s(u32x4 v, rsrc_t r, unsigned voff, unsigned soff) {
  asm volatile("buffer_store_dwordx4 %0, %1, %2, %3 offen offset:%4 sc1\n\ts_nop 1" ::"v"(v), "v"(voff), "s"(r), "s"(soff), "i"(OFF) : "memory");
}
DI int lane_id() { int l; asm volatile("v_mbcnt_lo_u32_b32 %0, -1, 0\n\tv_mbcnt_hi_u32_b32 %0, -1, %0" : "=v"(l)); return l; }
struct Tag { unsigned lo, hi; };
DI void peek(rsrc_t rf, unsigned lds_addr, int off) {
  asm volatile("s_mov_b32 m0, %0\n\ts_nop 0\n\tbuffer_load_dword %3, %1, %2 offen sc1 lds" ::"s"(lds_addr), "s"(rf), "s"(off), "v"(lane_id() * 4) : "memory");
}
template <int GAP = 1> DI void spin_until(char* ws, int off, int n, Tag t) {
  const int ln = lane_id();
  GAS unsigned long long* p = (GAS unsigned long long*)(ws + WS_FLAG + off) + (ln < n ? ln : 0);
  const unsigned long long want = ((unsigned long long)t.hi << 32) | t.lo;
  for (unsigned sp = 0; sp < (1u << 20); ++sp) {
    if (__all(__hip_atomic_load(p, __ATOMIC_RELAXED, __HIP_MEMORY_SCOPE_AGENT) == want)) break;
    __builtin_amdgcn_s_sleep(GAP);
  }
  asm volatile("" ::: "memory");
}
DI void peek_check(char* ws, unsigned lds_addr, int off, int n, Tag t) {
  const int ln = lane_id();
  const unsigned v = *(LAS volatile unsigned*)(size_t)(lds_addr + ln * 4);
  if (!__all(ln >= 2 * n || v == ((ln & 1) ? t.hi : t.lo))) spin_until(ws, off, n, t);
}
template <long MASK> DI void signal(char* ws, int off, Tag t) {
  const u32x2 v = {t.lo, t.hi};
  asm volatile("s_mov_b64 exec, %3\n\tglobal_store_dwordx2 %0, %1, %2 sc1\n\ts_mov_b64 exec, -1" ::"v"(off), "v"(v), "s"(ws + WS_FLAG), "s"(MASK) : "memory");
}
DI void dma4k(rsrc_t r, unsigned lds_addr, unsigned voff, unsigned soff) {
  asm volatile("s_mov_b32 m0, %0\n\ts_nop 0\n\tbuffer_load_dwordx4 %1, %2, %3 offen sc1 lds\n\tbuffer_load_dwordx4 %1, %2, %3 offen offset:1024 sc1 lds\n\t"
               "buffer_load_dwordx4 %1, %2, %3 offen offset:2048 sc1 lds\n\tbuffer_load_dwordx4 %1, %2, %3 offen offset:3072 sc1 lds"
               ::"s"(lds_addr), "v"(voff), "s"(r), "s"(soff) : "memory");
}
template <int W> DI void pf_line(unsigned& sink, int wid, rsrc_t r, unsigned voff, unsigned soff) {
  asm volatile("s_cmp_lg_u32 %1, %5\n\ts_cbranch_scc1 1f\n\tbuffer_load_dword %0, %2, %3, %4 offen\n1:"
               : "+v"(sink) : "s"(wid), "v"(voff), "s"(r), "s"(soff), "i"(W) : "scc", "memory");
}
template <int CTRL> DI float dppf(float x) { return __builtin_bit_cast(float, __builtin_amdgcn_mov_dpp(__builtin_bit_cast(int, x), CTRL, 0xf, 0xf, true)); }
DI float wave_sum(float v) {
  v += dppf<0xB1>(v); v += dppf<0x4E>(v); v += dppf<0x141>(v); v += dppf<0x140>(v);
  auto a = __builtin_amdgcn_permlane16_swap(__float_as_uint(v), __float_as_uint(v), false, false);
  v = __uint_as_float(a[0]) + __uint_as_float(a[1]);
  auto b = __builtin_amdgcn_permlane32_swap(__float_as_uint(v), __float_as_uint(v), false, false);
  return __uint_as_float(b[0]) + __uint_as_float(b[1]);
}

DI f32x2 ln16(unsigned (&w)[8]) {
  f32x2 v[8];
#pragma unroll
  for (int i = 0; i < 8; ++i) v[i] = (f32x2){bflo(w[i]), bfhi(w[i])};
  const f32x2 s = ((v[0] + v[1]) + (v[2] + v[3])) + ((v[4] + v[5]) + (v[6] + v[7]));
  const float mean = wave_sum(s[0] + s[1]) * (1.f / 1024.f);
  f32x2 q0 = {0.f, 0.f}, q1 = q0;
#pragma unroll
  for (int i = 0; i < 8; i += 2) {
    const f32x2 d0 = v[i] - mean, d1 = v[i + 1] - mean;
    q0 = __builtin_elementwise_fma(d0, d0, q0); q1 = __builtin_elementwise_fma(d1, d1, q1);
  }
  q0 += q1;
  const float var = wave_sum(q0[0] + q0[1]) * (1.f / 1024.f);
  const float mb = rbf(mean);
  const float rs = __builtin_amdgcn_rcpf(rbf(__builtin_amdgcn_sqrtf(rbf(rbf(var) + 1e-5f))));
#pragma unroll
  for (int i = 0; i < 8; ++i) {
    const f32x2 d = v[i] - mb;
    const unsigned c = cvtpk(d[0], d[1]);
    const f32x2 r = (f32x2){bflo(c), bfhi(c)} * rs;
    w[i] = cvtpk(r[0], r[1]);
  }
  return (f32x2){mb, rs};
}

struct Frags { bf16x8 x[8]; bf16x8 w[4]; };
struct Addr { unsigned x, w[4]; };
template <int XS = 1024> DI void load_frags(Frags& f, const Addr& a) {
#pragma unroll
  for (int c = 0; c < 4; ++c) {
    s16x4 a0 = __builtin_amdgcn_ds_read_tr16_b64_v4i16((LAS s16x4*)(size_t)(a.w[c]));
    s16x4 a1 = __builtin_amdgcn_ds_read_tr16_b64_v4i16((LAS s16x4*)(size_t)(a.w[c] + 4096));
    f.w[c] = __builtin_shufflevector(a0, a1, 0, 1, 2, 3, 4, 5, 6, 7);
  }
#pragma unroll
  for (int i = 0; i < 8; ++i) f.x[i] = *(LAS bf16x8*)(size_t)(a.x + i * XS);
}
template <bool WA, bool Z> DI void mma_all(f32x4 (&acc)[8][4], const Frags& f, const f32x4* c0) {
  const f32x4 zero = {0.f, 0.f, 0.f, 0.f};
#pragma unroll
  for (int i = 0; i < 8; ++i)
#pragma unroll
    for (int j = 0; j < 4; ++j) {
      if (WA) acc[i][j] = __builtin_amdgcn_mfma_f32_16x16x32_bf16(f.w[j], f.x[i], Z ? (c0 ? c0[j] : zero) : acc[i][j], 0, 0, 0);
      else    acc[i][j] = __builtin_amdgcn_mfma_f32_16x16x32_bf16(f.x[i], f.w[j], Z ? (c0 ? c0[j] : zero) : acc[i][j], 0, 0, 0);
    }
}
DI void addr_add(Addr& a, int d) {
  a.x += d;
#pragma unroll
  for (int c = 0; c < 4; ++c) a.w[c] += d;
}
template <bool WA, int NSLOT, int SLOTB, bool Z = false>
DI void gstep(f32x4 (&acc)[8][4], Frags& Fn, const Frags& Fc, Addr& a, int& slot, const f32x4* c0 = nullptr) {
  SB0();
  load_frags(Fn, a);
  mma_all<WA, Z>(acc, Fc, c0);
  slot = (slot + 1 == NSLOT) ? 0 : slot + 1;
  addr_add(a, (slot == 0) ? -(NSLOT - 1) * SLOTB : SLOTB);
  asm volatile("" : "+v"(a.x), "+v"(a.w[0]), "+v"(a.w[1]), "+v"(a.w[2]), "+v"(a.w[3]));
#pragma unroll
  for (int i = 0; i < 8; ++i) { SGB(0x8, 1); SGB(0x100, 1); SGB(0x8, 1); SGB(0x100, 1); SGB(0x8, 1); }
#pragma unroll
  for (int i = 0; i < 6; ++i) { SGB(0x8, 1); SGB(0x4, 1); SGB(0x2, 1); }
  SGB(0x8, 2);
  SB0();
  wait_lgkm0();
  bar();
}
template <int MODE, bool Z = false>
DI void qstep(f32x4 (&acc)[8][4], f32x4 (&cs)[2], Frags& Fn, const Frags& Fc, Addr& a, int& xs, int& wsl, unsigned csa = 0) {
  const bf16x8 ones = {0x3f80, 0x3f80, 0x3f80, 0x3f80, 0x3f80, 0x3f80, 0x3f80, 0x3f80};
  const f32x4 zero = {0.f, 0.f, 0.f, 0.f};
  SB0();
  load_frags<MODE == 2 ? 1024 : 2048>(Fn, a);
#pragma unroll
  for (int j = 0; j < 2; ++j) cs[j] = __builtin_amdgcn_mfma_f32_16x16x32_bf16(Fc.w[j], ones, Z ? zero : cs[j], 0, 0, 0);
  mma_all<true, Z>(acc, Fc, nullptr);
  if (MODE == 2) {
    addr_add(a, 32768);
    *(LAS f32x4*)(size_t)csa = cs[0]; *(LAS f32x4*)(size_t)(csa + 1024) = cs[1];
  } else {
    wsl = (wsl + 1) & 3;
    const int dw = wsl ? 8192 : -24576;
#pragma unroll
    for (int c = 0; c < 4; ++c) a.w[c] += dw;
    a.x ^= 64;
    if (MODE == 1) { xs = (xs == 2) ? 0 : xs + 1; a.x += xs ? 32768 : -65536; }
  }
  asm volatile("" : "+v"(a.x), "+v"(a.w[0]), "+v"(a.w[1]), "+v"(a.w[2]), "+v"(a.w[3]));
#pragma unroll
  for (int i = 0; i < 8; ++i) { SGB(0x8, 1); SGB(0x100, 1); SGB(0x8, 1); SGB(0x100, 1); SGB(0x8, 1); }
#pragma unroll
  for (int i = 0; i < 7; ++i) { SGB(0x8, 1); SGB(0x4, 1); SGB(0x2, 1); }
  SGB(0x8, 3);
  SB0();
  wait_lgkm0();
  bar();
}
DI void addr_init(Addr& a, unsigned xbase, unsigned wbase, int wj, int cx, int lane) {
  const int g = lane >> 4, q = (lane >> 2) & 3, p = lane & 3, row = 4 * g + q;
  a.x = xbase + lane * 16;
#pragma unroll
  for (int c = 0; c < 4; ++c) a.w[c] = wbase + row * 256 + (((2 * (4 * wj + (c ^ cx)) + (p >> 1)) ^ ((row & 7) << 1)) * 16) + (p & 1) * 8;
}
template <int SLOTB> DI void gemm_start(Frags& F0, Addr& a) {
  bar();
  load_frags(F0, a);
  addr_add(a, SLOTB);
  wait_lgkm0();
  bar();
}
template <bool WA, int NSLOT, int SLOTB> DI void gemm_steps(f32x4 (&acc)[8][4], Frags& F0, Frags& F1, Addr& a, int& slot, int n) {
  for (int t = 0; t < n; t += 2) {
    gstep<WA, NSLOT, SLOTB>(acc, F0, F1, a, slot);
    gstep<WA, NSLOT, SLOTB>(acc, F1, F0, a, slot);
  }
}

struct LdU { rsrc_t rx, rw; unsigned xv, wv, lb; int hn, hs; };
DI void ldu_half(LdU& L) {
  const unsigned s = L.lb + L.hs * 16384, xs = L.hn * 8192, wsf = L.hn * (32 * FF * 2);
  dma16s(L.rx, s, L.xv, xs);
  dma16(L.rw, s + 8192, L.wv, wsf);
  dma16s(L.rx, s + 4096, L.xv, xs + 4096);
  dma16(L.rw, s + 12288, L.wv, wsf + 4 * FF * 2);
  L.hn++; L.hs = (L.hs + 1) & 7;
}
DI u32x4 pack8(const f32x4& a, const f32x4& b) { return (u32x4){cvtpk(a[0], a[1]), cvtpk(a[2], a[3]), cvtpk(b[0], b[1]), cvtpk(b[2], b[3])}; }
DI f32x2 gate2(f32x2 u, f32x2 z) {
  f32x2 e = {__builtin_amdgcn_exp2f(z[0]), __builtin_amdgcn_exp2f(z[1])};
  e += 1.f;
  return u * (f32x2){__builtin_amdgcn_rcpf(e[0]), __builtin_amdgcn_rcpf(e[1])};
}
DI unsigned gelu2(f32x2 u) {
  const f32x2 r = gate2(u, u * __builtin_elementwise_fma(u * u, (f32x2){-0.1029432397f, -0.1029432397f}, (f32x2){-2.302208198f, -2.302208198f}));
  return cvtpk(r[0], r[1]);
}
DI unsigned silu2(f32x2 y) { const f32x2 r = gate2(y, y * -1.4426950409f); return cvtpk(r[0], r[1]); }
template <int KK> DI void epi_u(f32x4 (&acc)[8][4], unsigned pk, int cw, rsrc_t ru, unsigned vo) {
#pragma unroll
  for (int ii = 0; ii < 4; ++ii)
#pragma unroll
    for (int j = 0; j < 4; ++j) *(LAS f32x4*)(size_t)(pk + cw * 16384 + (ii * 4 + j) * 1024) = acc[4 * (1 - KK) + ii][j];
  wait_lgkm0();
  bar();
  const unsigned pp = pk + (cw ^ 2) * 16384;
#pragma unroll
  for (int ii = 0; ii < 4; ++ii)
#pragma unroll
    for (int j = 0; j < 4; ++j) acc[4 * KK + ii][j] += *(LAS f32x4*)(size_t)(pp + (ii * 4 + j) * 1024);
#pragma unroll
  for (int ii = 0; ii < 4; ++ii)
#pragma unroll
    for (int mm = 0; mm < 2; ++mm) {
      const f32x4 a = acc[4 * KK + ii][2 * mm], b = acc[4 * KK + ii][2 * mm + 1];
      st16s<0>((u32x4){gelu2(a.xy), gelu2(a.zw), gelu2(b.xy), gelu2(b.zw)}, ru, vo, (cw & 1) * 16384 + mm * 8192 + (4 * KK + ii) * 1024);
    }
}
DI u32x4 ldsr(unsigned a) { return *(LAS u32x4*)(size_t)a; }
DI void add8(f32x4& a, f32x4& b, const u32x4& w) {
  a += (f32x4){bflo(w[0]), bfhi(w[0]), bflo(w[1]), bfhi(w[1])};
  b += (f32x4){bflo(w[2]), bfhi(w[2]), bflo(w[3]), bfhi(w[3])};
}

struct Att { bf16x8 q[8]; f32x16 o[4]; f32x16 negm, sE, sO; u32x4 pO[2]; bf16x8 pre[4]; float l; };
DI float vmax16(const f32x16& a) {
  float m = fmaxf(fmaxf(a[0], a[1]), a[2]);
#pragma unroll
  for (int i = 3; i < 15; i += 2) m = fmaxf(fmaxf(m, a[i]), a[i + 1]);
  m = fmaxf(m, a[15]);
  auto sw = __builtin_amdgcn_permlane32_swap(__float_as_uint(m), __float_as_uint(m), false, false);
  return fmaxf(__uint_as_float(sw[0]), __uint_as_float(sw[1]));
}
DI bf16x8 ldfrag(unsigned a) { return *(LAS bf16x8*)(size_t)a; }
DI void sm1(f32x16& s, int e, u32x4 (&w)[2], float& l) {
  if (e < 16) {
    float x = s[e];
    x = __builtin_amdgcn_exp2f(x);
    asm volatile("" : "+v"(x));
    s[e] = x;
  }
  if (e >= 1) {
    l += s[e - 1];
    asm volatile("" : "+v"(l));
    if (!(e & 1)) { unsigned c = cvtpk(s[e - 2], s[e - 1]); asm volatile("" : "+v"(c)); w[(e - 1) >> 3][((e - 1) >> 1) & 3] = c; }
  }
}
#define SETPRIO(x) __builtin_amdgcn_s_setprio(x)
#define MFMA32(a, b, c) __builtin_amdgcn_mfma_f32_32x32x16_bf16(a, b, c, 0, 0, 0)
#define PFRAG(w) __builtin_bit_cast(bf16x8, w)
template <int POS, class F> DI void att_tile(Att& A, unsigned kv, unsigned kvn, F&& gap) {
  bf16x8 f[36];
  u32x4 pE[2];
  asm volatile("" : "+v"(kv), "+v"(kvn));
#pragma unroll
  for (int i = 0; i < 4; ++i) f[i] = A.pre[i];
  SB0();
  SETPRIO(3);
#pragma unroll
  for (int n = 0; n < 8; ++n) {
    if (POS == 0) A.sE = MFMA32(f[n], A.q[n], n ? A.sE : A.negm);
    else A.o[n & 3] = MFMA32(f[n], PFRAG(A.pO[n >> 2]), A.o[n & 3]);
    f[n + 4] = ldfrag(kv + (n + 4) * 1024);
    if (POS && n <= 2) sm1(A.sO, 14 + n, A.pO, A.l);
    if (POS && n >= 2) sm1(A.sE, n - 2, pE, A.l);
    gap(n);
    SB0();
  }
  if (POS == 0) {
    const float mx = vmax16(A.sE);
#pragma unroll
    for (int i = 0; i < 16; ++i) { A.sE[i] -= mx; A.negm[i] = -mx; }
    SB0();
  }
  SETPRIO(2);
#pragma unroll
  for (int n = 8; n < 16; ++n) {
    A.sO = MFMA32(f[n], A.q[n - 8], n > 8 ? A.sO : A.negm);
    f[n + 4] = ldfrag(kv + (n + 4) * 1024);
    if (POS) sm1(A.sE, n - 2, pE, A.l);
    else if (n < 15) { sm1(A.sE, 2 * (n - 8), pE, A.l); sm1(A.sE, 2 * (n - 8) + 1, pE, A.l); }
    gap(n);
    SB0();
  }
  SETPRIO(1);
#pragma unroll
  for (int n = 16; n < 24; ++n) {
    A.o[n & 3] = MFMA32(f[n], PFRAG(pE[(n >> 2) & 1]), A.o[n & 3]);
    f[n + 4] = ldfrag(kv + (n + 4) * 1024);
    if (n <= 18) sm1(A.sE, n - 2, pE, A.l);
    if (n >= 18) {
      if (POS < 2) sm1(A.sO, n - 18, A.pO, A.l);
      else { sm1(A.sO, 2 * (n - 18), A.pO, A.l); sm1(A.sO, 2 * (n - 18) + 1, A.pO, A.l); }
    }
    gap(n);
    SB0();
  }
  SETPRIO(0);
#pragma unroll
  for (int n = 24; n < 32; ++n) {
    if (POS < 2) {
      A.sE = MFMA32(f[n], A.q[n - 24], n > 24 ? A.sE : A.negm);
      f[n + 4] = ldfrag(n < 28 ? kv + (n + 4) * 1024 : kvn + (n - 28) * 1024);
      sm1(A.sO, n - 18, A.pO, A.l);
    } else {
      A.o[n & 3] = MFMA32(f[n], PFRAG(A.pO[(n >> 2) & 1]), A.o[n & 3]);
      if (n < 28) f[n + 4] = ldfrag(kv + (n + 4) * 1024);
      if (n < 26) { sm1(A.sO, 2 * (n - 18), A.pO, A.l); sm1(A.sO, 2 * (n - 18) + 1, A.pO, A.l); }
      if (n == 26) sm1(A.sO, 16, A.pO, A.l);
    }
    gap(n);
    SB0();
  }
  if (POS < 2) {
#pragma unroll
    for (int i = 0; i < 4; ++i) A.pre[i] = f[32 + i];
  }
}
DI void att_redo(Att& A, rsrc_t rkv, unsigned vo) {
  float m = -3.0e38f, l = 0.f;
#pragma unroll
  for (int d = 0; d < 4; ++d)
#pragma unroll
    for (int i = 0; i < 16; ++i) A.o[d][i] = 0.f;
  for (int blk = 0; blk < 16; ++blk) {
    const int kb = (blk ? 2 * blk - 1 : 0) * 8192, vb = (blk < 15 ? 2 * blk + 2 : 31) * 8192;
    f32x16 s;
#pragma unroll
    for (int i = 0; i < 16; ++i) s[i] = 0.f;
#pragma unroll
    for (int n = 0; n < 8; ++n) s = MFMA32(PFRAG(__builtin_amdgcn_raw_buffer_load_b128(rkv, vo + n * 1024, kb, 16)), A.q[n], s);
    const float mn = fmaxf(m, vmax16(s)), fct = __builtin_amdgcn_exp2f(m - mn);
    m = mn;
    l *= fct;
    u32x4 p[2];
#pragma unroll
    for (int i = 0; i < 16; ++i) s[i] -= mn;
#pragma unroll
    for (int i = 0; i < 17; ++i) sm1(s, i, p, l);
#pragma unroll
    for (int d = 0; d < 4; ++d)
#pragma unroll
      for (int i = 0; i < 16; ++i) A.o[d][i] *= fct;
#pragma unroll
    for (int n = 0; n < 8; ++n) A.o[n & 3] = MFMA32(PFRAG(__builtin_amdgcn_raw_buffer_load_b128(rkv, vo + n * 1024, vb, 16)), PFRAG(p[n >> 2]), A.o[n & 3]);
  }
  A.l = l;
}

__global__ __launch_bounds__(512, 2) void k_main(const u16* __restrict__ x, const u16* __restrict__ kc, const u16* __restrict__ vc, const u16* __restrict__ wq, const u16* __restrict__ wo,
                                                 const u16* __restrict__ w1, const u16* __restrict__ b1, const u16* __restrict__ w2,
                                                 const u16* __restrict__ b2, char* __restrict__ ws, u16* __restrict__ out) {
  extern __shared__ __attribute__((aligned(16))) char smem[];
  LAS char* lds = (LAS char*)smem;
  const unsigned lb = (unsigned)(size_t)lds;
  const int tid = threadIdx.x, wid = __builtin_amdgcn_readfirstlane(tid >> 6);
  const int g8 = blockIdx.x & 7, idx = blockIdx.x >> 3;
  STAMP(1, 0);
  STAMP(0, 0);
  const bool hasU = idx < 28;
  const int dt = 4 * (g8 & 1) + (idx & 3), ft = 7 * (g8 >> 1) + (idx >> 2);
  const int kvh = g8 >> 1, head = 8 * kvh + (idx & 7), rb = 4 * (g8 & 1) + (idx >> 3);
  const int dc = idx & 7, tile = rb * 8 + dc;
  const int lw = wid - 4, wi = (wid >> 1) & 1, wj = wid & 1;
  constexpr int QS = 24576;
  constexpr int PARK = 98304;
  const rsrc_t rf = mkrsrc(ws + WS_FLAG, F_END + 256);
  const rsrc_t rkv = mkrsrc(ws + WS_KV + (size_t)kvh * 262144, 262144);
  const rsrc_t rxb = mkrsrc(ws + WS_XB + (size_t)tile * 393216, 393216);
  struct Strm { rsrc_t r; unsigned v0, v1, step, pair, px; };
  auto strm_x = [&](Strm& S, const char* base, unsigned bytes, unsigned step, int xr) {
    const int ln = lane_id();
    S.r = mkrsrc(base, bytes); S.v0 = ln * 16 + (xr & 1) * 1024; S.v1 = S.v0 ^ 1024; S.step = step; S.pair = 2048; S.px = xr >> 1;
  };
  auto strm_w = [&](Strm& S, const char* base, unsigned rows, unsigned ld) {
    const int ln = lane_id(), q4 = ln >> 4;
    const unsigned c0 = ((ln & 15) ^ (q4 << 1)) * 16;
    S.r = mkrsrc(base, (rows - 1) * ld + 256); S.v0 = q4 * ld + c0; S.v1 = (4 + q4) * ld + (c0 ^ 128); S.step = 32 * ld; S.pair = 8 * ld; S.px = 0;
  };
  auto issue = [&](const Strm& S, int k) {
    const unsigned s = lb + (k & 3) * QS + lw * 8192;
#pragma unroll
    for (int i = 0; i < 4; ++i) { const unsigned so = k * S.step + (i ^ S.px) * S.pair; dma16s(S.r, s + i * 2048, S.v0, so); dma16s(S.r, s + i * 2048 + 1024, S.v1, so); }
  };
  constexpr int QW = 98304, HST = 131072, HRS = 2112, RST = HST + 8 * HRS, PKQ = 148480, STT = 151552, CSX = 153600;
  const int lane0 = tid & 63;
  const rsrc_t rq = lw < 2 ? mkrsrc(x + (size_t)(rb * 256 + lw * 128) * DM, 128 * DM * 2) : mkrsrc((const char*)wq + head * 256, (DM - 1) * HD * 2 + 256);
  const unsigned qv0 = lw < 2 ? (lane0 >> 3) * 2048 + (((lane0 & 7) ^ (lane0 >> 4)) * 16) : (lane0 >> 4) * (HD * 2) + (((lane0 & 15) ^ ((lane0 >> 4) << 1)) * 16);
  const unsigned qv1 = lw < 2 ? (qv0 ^ 64) + 8 * 2048 : (qv0 ^ 128) + 8 * (HD * 2);
  auto issq = [&](int k) {
    const int p = k >> 1;
    const unsigned s = lw < 2 ? lb + ((p + 2) % 3) * 32768 + lw * 16384 + (k & 1) * 8192 : lb + QW + (k & 3) * 8192;
    const unsigned o = lw < 2 ? ((p + ROT * rb) & 15) * 128 + (k & 1) * 131072 : ((k + 2 * ROT * rb) & 31) * (32 * HD * 2);
#pragma unroll
    for (int i = 0; i < 4; ++i) {
      const unsigned so = o + (lw < 2 ? i * 32768 : (16 * (i & 1) + 4 * (i >> 1)) * (HD * 2));
      dma16(rq, s + i * 2048, qv0, so); dma16(rq, s + i * 2048 + 1024, qv1, so);
    }
  };
  auto h_out = [&](int it) {
    const rsrc_t rh = mkrsrc(ws + WS_HT + (size_t)(blockIdx.x >> 2) * 8192 + (blockIdx.x & 3) * 256, 7 * 524288 + 8192);
    const unsigned a0 = HST + ((lane0 >> 2) & 3) * HRS + (it * 4 + (lane0 >> 4)) * 32 + (lane0 & 3) * 8;
    s16x4 lo = __builtin_amdgcn_ds_read_tr16_b64_v4i16((LAS s16x4*)(lds + a0));
    s16x4 hi = __builtin_amdgcn_ds_read_tr16_b64_v4i16((LAS s16x4*)(lds + a0 + 4 * HRS));
    st16s<0>(__builtin_bit_cast(u32x4, __builtin_shufflevector(lo, hi, 0, 1, 2, 3, 4, 5, 6, 7)), rh, (lane0 >> 4) * 1024 + (lane0 & 15) * 16,
             (it >> 1) * 524288 + (it & 1) * 4096);
  };
  auto st_out = [&]() { st16s<0>(*(LAS u32x4*)(lds + RST + (lane0 & 3) * 16), mkrsrc(ws + WS_ST + blockIdx.x * 64, 64), lane0 * 16, 0); };
  Tag tag;
  {
    const int b = blockIdx.x, lane = lane0;
    u32x4 A, B;
    u32x2 ep;
    asm volatile("global_load_dwordx4 %0, %3, %4\n\tglobal_load_dwordx4 %1, %3, %4 offset:1024\n\tglobal_load_dwordx2 %2, %5, %6 sc1"
                 : "=&v"(A), "=&v"(B), "=&v"(ep) : "v"(lane * 16), "s"(x + (size_t)(8 * b + wid) * DM), "v"(F_EP), "s"(ws + WS_FLAG) : "memory");
    const bool strm = hasU && wid >= 4 && wid < 7;
    if (strm) { issq(0); issq(1); wait_vm<16>(); } else wait_vm<0>();
    asm volatile("" : "+v"(A), "+v"(B), "+v"(ep));
    unsigned v[8] = {A[0], A[1], A[2], A[3], B[0], B[1], B[2], B[3]};
    const unsigned long long e64 = ((unsigned long long)(unsigned)__builtin_amdgcn_readfirstlane(ep[1]) << 32) | (unsigned)__builtin_amdgcn_readfirstlane(ep[0]);
    const unsigned long long tg = e64 * 0x9E3779B97F4A7C15ull + 0x632BE59BD9B4E019ull;
    tag.lo = (unsigned)tg; tag.hi = (unsigned)(tg >> 32);
    *(LAS f32x2*)(lds + RST + wid * 8) = ln16(v);
    ln16(v);
    *(LAS u32x4*)(lds + HST + wid * HRS + lane * 16) = (u32x4){v[0], v[1], v[2], v[3]};
    *(LAS u32x4*)(lds + HST + wid * HRS + 1024 + lane * 16) = (u32x4){v[4], v[5], v[6], v[7]};
    wait_lgkm0();
  }
  STAMP(0, 1);
  if (!hasU) {
    const int sp = g8 * 4 + (idx - 28), lane = lane0, h = lane >> 5;
    bar();
    h_out(2 * wid); h_out(2 * wid + 1);
    if (wid == 0) st_out();
    wait_vm<0>();
    bar();
    if (wid == 0) signal<1>(ws, F_L + blockIdx.x * 8, tag);
    unsigned e[4][8];
#pragma unroll
    for (int i = 0; i < 4; ++i) {
      const int kh = sp >> 3, unit = (sp & 7) * 4 + i, frag = wid;
      const bool isK = unit == 0 || ((unit & 1) && unit < 31);
      const u16* sk = kc + ((size_t)kh * SEQ + 1536 + 32 * ((unit + 1) >> 1) + (lane & 31)) * 128 + 32 * (frag >> 1) + 4 * (2 * (frag & 1) + h);
      const u16* sv = vc + ((size_t)kh * SEQ + 1536 + 32 * ((unit - 1) >> 1) + 16 * (frag >> 2) + 4 * h) * 128 + 32 * (frag & 3) + (lane & 31);
      const u16* src = isK ? sk : sv;
      const int s1 = isK ? 1 : 128, s4 = isK ? 16 : 1024;
#pragma unroll
      for (int j2 = 0; j2 < 8; ++j2) e[i][j2] = src[(j2 >> 2) * s4 + (j2 & 3) * s1];
    }
#pragma unroll
    for (int i = 0; i < 4; ++i)
      st16s<0>((u32x4){e[i][0] | (e[i][1] << 16), e[i][2] | (e[i][3] << 16), e[i][4] | (e[i][5] << 16), e[i][6] | (e[i][7] << 16)},
               mkrsrc(ws + WS_KV, 1048576), tid * 16, (sp * 4 + i) * 8192);
    wait_vm<0>();
    bar();
    if (wid == 0) signal<1>(ws, F_X + (sp >> 3) * 128 + (sp & 7) * 8, tag);
    for (int i = 0; i < 7; ++i) {
      const unsigned v = *(const unsigned*)((const char*)w1 + (size_t)i * 2097152 + (size_t)(sp * 512 + tid) * 128);
      asm volatile("" ::"v"(v));
      __builtin_amdgcn_s_sleep(SPARE_SLEEP);
    }
    if (wid >= 4 && wid < 7) { issq(0); issq(1); }
  }
  bf16x8 qh[8];
  auto kv8 = [&](int t, int slot) {
    dma4k(rkv, lb + slot * 32768 + lw * 8192, lane0 * 16, t * 32768 + lw * 8192);
    dma4k(rkv, lb + slot * 32768 + lw * 8192 + 4096, lane0 * 16, t * 32768 + lw * 8192 + 4096);
  };
  if (wid < 4) {
    f32x4 acc[8][4], cs[2];
    Frags F0, F1;
    Addr a;
    int xs = 2, wsl = 1;
    {
      const int ln = lane_id(), r = ln & 15;
      addr_init(a, 0, lb + QW, wj, 2 * wi, ln);
      a.x = lb + 65536 + wi * 16384 + r * 128 + (((ln >> 4) ^ (r >> 1)) * 16);
    }
    bar();
    load_frags<2048>(F0, a);
    a.x ^= 64;
#pragma unroll
    for (int c = 0; c < 4; ++c) a.w[c] += 8192;
    wait_lgkm0();
    bar();
    STAMP(1, 1);
    qstep<1, true>(acc, cs, F1, F0, a, xs, wsl);
    for (int t = 0; t < 15; ++t) { qstep<0>(acc, cs, F0, F1, a, xs, wsl); qstep<1>(acc, cs, F1, F0, a, xs, wsl); }
    addr_init(a, lb + wi * 16384, lb + wi * 16384 + 8192, wj, 0, lane_id());
    qstep<2>(acc, cs, F0, F1, a, xs, wsl, lb + CSX + wid * 2048 + lane_id() * 16);
    STAMP(1, 2);
    f32x4 c0[4];
    {
      wait_vm<0>();
      const int ln = lane_id(), g = ln >> 4;
      u32x2 bv[4];
#pragma unroll
      for (int j = 0; j < 4; ++j) bv[j] = *(const u32x2*)(b1 + (hasU ? ft : 0) * 128 + 64 * wj + 16 * j + 4 * g);
      f32x4 cq[4] = {cs[0], cs[1], *(LAS f32x4*)(lds + CSX + (wid ^ 2) * 2048 + ln * 16), *(LAS f32x4*)(lds + CSX + (wid ^ 2) * 2048 + 1024 + ln * 16)};
      f32x2 st[8];
#pragma unroll
      for (int i = 0; i < 8; ++i) st[i] = *(LAS f32x2*)(lds + STT + (128 * wi + 16 * i + (ln & 15)) * 8);
      wait_lgkm0();
      bar();
      const unsigned qa = lb + (wi ? 98304 : 131072) + (4 * wj + (g >> 1)) * (wi ? 4096 : 1024) + ((ln & 15) + 32 * (g & 1)) * 16;
      const unsigned sq = wi ? 1024 : 8192, m0 = wi ? 8192 : 0, m1 = wi ? 0 : 2048;
#pragma unroll
      for (int i = 0; i < 8; ++i) {
        const float sc = st[i][1] * 0.12751743082f, nm = -st[i][0];
#pragma unroll
        for (int mm = 0; mm < 2; ++mm)
          *(LAS u32x4*)(size_t)(qa + (i >> 1) * sq + (mm ? m1 : m0) + (i & 1) * 256) =
              pack8((acc[i][2 * mm] + cq[2 * mm] * nm) * sc, (acc[i][2 * mm + 1] + cq[2 * mm + 1] * nm) * sc);
      }
#pragma unroll
      for (int j = 0; j < 4; ++j) c0[j] = wi ? (f32x4){0.f, 0.f, 0.f, 0.f} : (f32x4){bflo(bv[j][0]), bfhi(bv[j][0]), bflo(bv[j][1]), bfhi(bv[j][1])};
      wait_lgkm0();
      bar();
    }
    STAMP(1, 3);
    if (hasU) {
      int slot = 1;
      gstep<true, 4, 32768, true>(acc, F1, F0, a, slot, c0);
      gemm_steps<true, 4, 32768>(acc, F0, F1, a, slot, 30);
      gstep<true, 4, 32768>(acc, F0, F1, a, slot);
      STAMP(1, 4);
      const rsrc_t ru = mkrsrc(ws + WS_U + (size_t)(dt * 112 + 4 * ft) * 8192, 32768);
      const int ln = lane_id();
      if (wi) epi_u<1>(acc, lb + 65536 + ln * 16, wid, ru, ln * 16); else epi_u<0>(acc, lb + 65536 + ln * 16, wid, ru, ln * 16);
    }
    STAMP(1, 5);
    const int ln = lane_id();
#pragma unroll
    for (int ks = 0; ks < 8; ++ks) qh[ks] = *(LAS bf16x8*)(lds + 131072 + wid * 8192 + ks * 1024 + ln * 16);
  } else {
    const int lane = lane0;
    LdU L;
    const int lrow = 4 * lw + (lane >> 4);
    L.rx = mkrsrc(ws + WS_HT + (size_t)dt * 524288, 524288);
    L.rw = mkrsrc((const char*)w1 + ft * 256, (SEQ - 1) * FF * 2 + 256);
    L.xv = lw * 1024 + lane * 16; L.wv = (8 * lw + (lane >> 4)) * (FF * 2) + ((lane & 15) ^ ((lrow & 7) << 1)) * 16;
    L.lb = lb + lw * 1024; L.hn = 0; L.hs = 0;
    const unsigned pk = lb + PKQ + lw * 512;
    if (lw < 3) {
      const int ahead = lw < 2 ? 4 : 3;
      issq(2);
      if (lw < 2) issq(3);
      wait_vm<16>();
      bar();
      for (int t = 0; t <= 21; ++t) { issq(t + ahead); wait_vm<16>(); bar(); }
      peek(rf, pk, F_L + lw * 512); peek(rf, pk + 256, F_L + lw * 512 + 256);
      for (int t = 22; t <= 26; ++t) { issq(t + ahead); wait_vm<16>(); bar(); }
      issq(27 + ahead); wait_vm<16>();
    } else {
      bar();
      if (hasU) {
        for (int t = 0; t <= 7; ++t) { h_out(2 * t); h_out(2 * t + 1); bar(); }
        st_out();
        bar(); bar(); bar();
        wait_vm<0>();
        signal<1>(ws, F_L + blockIdx.x * 8, tag);
      } else {
        for (int t = 0; t <= 10; ++t) bar();
        spin_until(ws, F_X + kvh * 128, 8, tag);
      }
      for (int t = 11; t <= 21; ++t) bar();
      peek(rf, pk, F_L + lw * 512); peek(rf, pk + 256, F_L + lw * 512 + 256);
      for (int t = 22; t <= 26; ++t) bar();
      wait_vm<0>();
    }
    peek_check(ws, pk, F_L + lw * 512, 32, tag); peek_check(ws, pk + 256, F_L + lw * 512 + 256, 32, tag);
    bar();
    if (blockIdx.x == 0 && lw == 3) signal<1>(ws, F_EP, tag);
    if (lw == 2) issq(31);
    if (lw == 3) {
      const rsrc_t rs = mkrsrc(ws + WS_ST + rb * 2048, 2048);
      dma16s(rs, lb + STT, lane * 16, 0); dma16s(rs, lb + STT + 1024, lane * 16, 1024);
    }
    if (hasU) { ldu_half(L); ldu_half(L); } else kv8(0, 0);
    wait_vm<16>(); bar();
    wait_vm<8>(); bar();
    if (hasU) { ldu_half(L); ldu_half(L); } else kv8(1, 1);
    bar();
    wait_vm<8>(); bar();
    if (hasU) { ldu_half(L); ldu_half(L); wait_vm<8>(); } else wait_vm<0>();
    bar();
    bar();
    bar();
#pragma unroll
    for (int ks = 0; ks < 8; ++ks) qh[ks] = *(LAS bf16x8*)(lds + 98304 + ks * 4096 + lw * 1024 + lane * 16);
    wait_lgkm0();
    asm volatile("" : "+v"(qh[0]), "+v"(qh[1]), "+v"(qh[2]), "+v"(qh[3]), "+v"(qh[4]), "+v"(qh[5]), "+v"(qh[6]), "+v"(qh[7])::"memory");
    if (hasU) {
      ldu_half(L); ldu_half(L); ldu_half(L); wait_vm<12>(); bar();
      ldu_half(L); ldu_half(L); ldu_half(L); wait_vm<16>(); bar();
      for (int t = 3; t <= 27; ++t) { ldu_half(L); ldu_half(L); wait_vm<16>(); bar(); }
      ldu_half(L); ldu_half(L); wait_vm<16>();
      if (lw == 3) spin_until(ws, F_X + kvh * 128, 8, tag);
      bar();
      kv8(0, 0); wait_vm<16>(); bar();
      kv8(1, 1); wait_vm<16>(); bar();
      bar(); bar();
      bar();
      wait_vm<0>();
    }
  }
  wait_lgkm0();
  bar();
  STAMP(1, 6);
  unsigned pf = 0;
  Strm Y;
  {
    const int lane = lane_id();
    Att A;
#pragma unroll
    for (int ks = 0; ks < 8; ++ks) A.q[ks] = qh[ks];
#pragma unroll
    for (int d = 0; d < 4; ++d)
#pragma unroll
      for (int i = 0; i < 16; ++i) A.o[d][i] = 0.f;
#pragma unroll
    for (int i = 0; i < 16; ++i) A.negm[i] = 0.f;
    A.l = 0.f;
    const unsigned kvl = lb + lane * 16;
#pragma unroll
    for (int i = 0; i < 4; ++i) A.pre[i] = ldfrag(kvl + i * 1024);
    if (lw == 0) strm_x(Y, ws + WS_U + (size_t)(dc * 112 + 28 * kvh) * 8192, 28 * 8192, 8192, 0);
    else strm_w(Y, (const char*)w2 + (size_t)kvh * 896 * (SEQ * 2) + rb * 512 + ((lw - 1) & 1) * 256, 896, SEQ * 2);
    const bool strm = wid >= 4 && wid < 7, hw = wid >= 4;
    const rsrc_t rp2 = mkrsrc(w2, SEQ * FF * 2), rpo = mkrsrc(wo, HD * DM * 2);
    const unsigned pv = (blockIdx.x * 64 + lane) * 128;
#define PF(W, R, C) pf_line<W>(pf, wid, R, pv, (C) * 2097152u)
    att_tile<0>(A, kvl + 0 * 32768, kvl + 1 * 32768, [&](int n) { if (n == 0 && hw) kv8(2, 2); if (n == 17 && hw) kv8(3, 3); });
    if (hw) wait_vm<8>(); else wait_vm<0>();
    bar();
    if (wid == 0 && hasU) signal<1>(ws, F_U + (dt * 4 + kvh) * 128 + (idx >> 2) * 8, tag);
    att_tile<1>(A, kvl + 1 * 32768, kvl + 2 * 32768, [&](int n) { if (n == 1 && hw) kv8(4, 0); if (n == 17 && hw) kv8(5, 4); if (n == 5) PF(0, rp2, 0); if (n == 14) PF(1, rp2, 1); if (n == 25) PF(2, rp2, 2); });
    if (hw) wait_vm<16>();
    bar();
    att_tile<1>(A, kvl + 2 * 32768, kvl + 3 * 32768, [&](int n) { if (n == 3) PF(3, rp2, 3); if (n == 19) PF(0, rp2, 4); });
    if (hw) wait_vm<8>();
    bar();
    att_tile<1>(A, kvl + 3 * 32768, kvl + 0 * 32768, [&](int n) { if (n == 1 && hw) kv8(7, 2); if (n == 7) PF(1, rp2, 5); if (n == 18) PF(2, rp2, 6); });
    if (hw) wait_vm<8>();
    bar();
    att_tile<1>(A, kvl + 0 * 32768, kvl + 4 * 32768, [&](int n) { if (n == 0 && hw) kv8(6, 3); if (n == 9) PF(3, rpo, 0); if (n == 23) PF(0, rpo, 1); });
    if (hw) wait_vm<0>();
    bar();
    att_tile<1>(A, kvl + 4 * 32768, kvl + 3 * 32768, [&](int n) { if (n == 1 && wid == 4) peek(rf, lb + 49152, F_U + (dc * 4 + kvh) * 128); if (n == 5) PF(1, rpo, 2); if (n == 21) PF(2, rpo, 3); });
    if (wid == 4) { wait_vm<0>(); peek_check(ws, lb + 49152, F_U + (dc * 4 + kvh) * 128, 7, tag); }
    bar();
    att_tile<1>(A, kvl + 3 * 32768, kvl + 2 * 32768, [&](int n) { if (n == 4 && strm) issue(Y, 0); });
    bar();
    att_tile<2>(A, kvl + 2 * 32768, 0, [&](int n) { if (n == 4 && strm) issue(Y, 1); });
    STAMP(1, 7);
    if (__builtin_expect(__any(!(A.l <= 1.0e9f)), 0)) att_redo(A, rkv, lane * 16);
    auto sw = __builtin_amdgcn_permlane32_swap(__float_as_uint(A.l), __float_as_uint(A.l), false, false);
    const float inv = __builtin_amdgcn_rcpf(__uint_as_float(sw[0]) + __uint_as_float(sw[1]));
    const unsigned oa = lb + PARK + wid * 2048 + ((lane >> 4) & 1) * 1024 + (lane >> 5) * 256 + (lane & 15) * 16;
#pragma unroll
    for (int d = 0; d < 4; ++d)
#pragma unroll
      for (int u = 0; u < 2; ++u) {
        const int r0 = 4 * u, r1 = 8 + 4 * u;
        *(LAS u32x4*)(size_t)(oa + d * 16384 + u * 512) =
            (u32x4){cvtpk(A.o[d][r0] * inv, A.o[d][r0 + 1] * inv), cvtpk(A.o[d][r0 + 2] * inv, A.o[d][r0 + 3] * inv),
                    cvtpk(A.o[d][r1] * inv, A.o[d][r1 + 1] * inv), cvtpk(A.o[d][r1 + 2] * inv, A.o[d][r1 + 3] * inv)};
      }
    wait_lgkm0();
  }
  STAMP(1, 8);
  if (wid < 4) {
    f32x4 acc[8][4];
    Frags F0, F1;
    Addr a;
    int slot = 1;
    addr_init(a, lb, lb + 8192 + wi * 8192, wj, kvh, lane_id());
    gemm_start<QS>(F0, a);
    STAMP(1, 9);
    gstep<false, 4, QS, true>(acc, F1, F0, a, slot);
    gemm_steps<false, 4, QS>(acc, F0, F1, a, slot, 26);
    addr_init(a, lb + wi * 8192, lb + 16384, wj, 0, lane_id());
    gstep<false, 4, QS>(acc, F0, F1, a, slot);
    STAMP(1, 10);
    asm volatile("s_waitcnt vmcnt(0)" : "+v"(pf)::"memory");
    const unsigned pm = lb + PARK + lane_id() * 16 + wid * 4096;
#pragma unroll
    for (int j = 0; j < 4; ++j)
#pragma unroll
      for (int m = 0; m < 4; ++m) *(LAS u32x4*)(size_t)(pm + j * 16384 + m * 1024) = pack8(acc[2 * m][j], acc[2 * m + 1][j]);
    STAMP(1, 11);
    gstep<true, 4, QS, true>(acc, F1, F0, a, slot);
    gemm_steps<true, 4, QS>(acc, F0, F1, a, slot, 30);
    gstep<true, 4, QS>(acc, F0, F1, a, slot);
    STAMP(1, 12);
    const int lt = lane_id(), g = lt >> 4;
    const unsigned wo_ = wi * 8192 + wj * 2048;
#pragma unroll
    for (int dl = 1; dl < 4; ++dl)
#pragma unroll
      for (int h = 0; h < 2; ++h)
#pragma unroll
        for (int mm = 0; mm < 2; ++mm)
          st16s<0>(pack8(acc[4 * h + dl][2 * mm], acc[4 * h + dl][2 * mm + 1]), rxb, lt * 16, (kvh ^ dl) * 98304 + 49152 + (dl - 1) * 16384 + wo_ + h * 4096 + mm * 1024);
    wait_vm<0>();
    signal<14>(ws, F_S + (tile * 4 + (kvh ^ (lt & 3))) * 128 + wid * 32 + ((lt & 3) - 1) * 8, tag);
    STAMP(1, 13);
    const int srow = rb * 256 + (8 * wi + kvh) * 16;
    const rsrc_t rx = mkrsrc(x + (size_t)srow * DM, 80 * DM * 2), rout = mkrsrc(out + (size_t)srow * DM, 80 * DM * 2);
    const unsigned vo = ((lt & 15) * DM + dc * 128 + wj * 64 + 16 * (g & 1) + 8 * (g >> 1)) * 2;
    u32x4 ps[3][4], xv[4], gv[4];
#pragma unroll
    for (int p = 0; p < 4; ++p) xv[p] = __builtin_amdgcn_raw_buffer_load_b128(rx, vo + (p >> 1) * (64 * DM * 2) + (p & 1) * 64, 0, 0);
    bar();
#pragma unroll
    for (int p = 0; p < 4; ++p) gv[p] = ldsr(lb + PARK + lt * 16 + wo_ + (p >> 1) * 4096 + (p & 1) * 1024);
    spin_until(ws, F_S + (tile * 4 + kvh) * 128 + wid * 32, 3, tag);
    STAMP(1, 14);
#pragma unroll
    for (int p = 0; p < 4; ++p)
#pragma unroll
      for (int k = 0; k < 3; ++k)
        ps[k][p] = __builtin_amdgcn_raw_buffer_load_b128(rxb, lt * 16 + (p & 1) * 1024, kvh * 98304 + 49152 + k * 16384 + wo_ + (p >> 1) * 4096, 16);
#pragma unroll
    for (int p = 0; p < 4; ++p) {
      f32x4 s0 = acc[4 * (p >> 1)][2 * (p & 1)], s1 = acc[4 * (p >> 1)][2 * (p & 1) + 1];
#pragma unroll
      for (int k = 0; k < 3; ++k) add8(s0, s1, ps[k][p]);
      s0 *= (f32x4){bflo(gv[p][0]), bfhi(gv[p][0]), bflo(gv[p][1]), bfhi(gv[p][1])};
      s1 *= (f32x4){bflo(gv[p][2]), bfhi(gv[p][2]), bflo(gv[p][3]), bfhi(gv[p][3])};
      const u32x4 tp = pack8(s0, s1);
      const auto w0 = __builtin_amdgcn_permlane16_swap(tp[0], tp[2], false, false), w1 = __builtin_amdgcn_permlane16_swap(tp[1], tp[3], false, false);
      const unsigned t[4] = {w0[0], w1[0], w0[1], w1[1]};
      u32x4 o;
#pragma unroll
      for (int e = 0; e < 4; ++e) o[e] = cvtpk(bflo(xv[p][e]) + bflo(t[e]), bfhi(xv[p][e]) + bfhi(t[e]));
      __builtin_amdgcn_raw_buffer_store_b128(o, rout, vo + (p >> 1) * (64 * DM * 2) + (p & 1) * 64, 0, 0);
    }
    STAMP(1, 15);
  } else {
    const int lane = lane_id();
    const unsigned pl = lb + PARK + lane * 16, pk = lb + 159744 + lw * 256;
    const int fo = F_O + (rb * 4 + kvh) * 128;
    if (lw < 3) {
      Strm S;
      if (lw < 2) strm_x(S, ws + WS_O + (size_t)(rb * 128 + 32 * kvh) * 16384 + lw * 8192, 524288 - lw * 8192, 16384, kvh);
      else strm_w(S, (const char*)wo + (size_t)kvh * 1024 * (DM * 2) + dc * 256, 1024, DM * 2);
      wait_vm<8>();
      bar();
      issue(Y, 2); issue(Y, 3);
      wait_vm<16>();
      bar();
      for (int t = 1; t <= 22; ++t) { issue(Y, t + 3); wait_vm<16>(); bar(); }
      peek(rf, pk, fo);
      issue(Y, 26); wait_vm<16>(); bar();
      issue(Y, 27); wait_vm<16>(); bar();
      peek_check(ws, pk, fo, 8, tag);
      for (int t = 25; t <= 56; ++t) { issue(S, t - 25); wait_vm<16>(); bar(); }
      wait_vm<8>(); bar();
      wait_vm<0>(); bar();
    } else {
      bar();
      const rsrc_t ro = mkrsrc(ws + WS_O + (size_t)(rb * 128 + 4 * head) * 16384, 65536);
      for (int t = 0; t <= 15; ++t) {
        const unsigned o = t * 4096;
        const u32x4 v0 = ldsr(pl + o), v1 = ldsr(pl + o + 1024), v2 = ldsr(pl + o + 2048), v3 = ldsr(pl + o + 3072);
        st16s<0>(v0, ro, lane * 16, o); st16s<1024>(v1, ro, lane * 16, o);
        st16s<2048>(v2, ro, lane * 16, o); st16s<3072>(v3, ro, lane * 16, o);
        bar();
      }
      bar(); bar();
      wait_vm<0>();
      signal<1>(ws, fo + dc * 8, tag);
      for (int t = 18; t <= 29; ++t) bar();
      for (int t = 30; t <= 45; ++t) {
        const unsigned o = (t - 30) * 1024;
        const u32x4 v0 = ldsr(pl + 16384 + o), v1 = ldsr(pl + 32768 + o), v2 = ldsr(pl + 49152 + o);
        st16s<0>(v0, rxb, lane * 16, (kvh ^ 1) * 98304 + o);
        st16s<0>(v1, rxb, lane * 16, (kvh ^ 2) * 98304 + 16384 + o);
        st16s<0>(v2, rxb, lane * 16, (kvh ^ 3) * 98304 + 32768 + o);
        bar();
      }
      for (int t = 46; t <= 48; ++t) bar();
      wait_vm<0>();
      signal<14>(ws, F_Y + (tile * 4 + (kvh ^ (lane & 3))) * 128 + ((lane & 3) - 1) * 8, tag);
      for (int t = 49; t <= 56; ++t) bar();
      peek(rf, pk, F_Y + (tile * 4 + kvh) * 128);
      bar();
      wait_vm<0>();
      peek_check(ws, pk, F_Y + (tile * 4 + kvh) * 128, 3, tag);
      bar();
    }
    u32x4 py[3][4];
#pragma unroll
    for (int k = 0; k < 3; ++k)
#pragma unroll
      for (int m = 0; m < 4; ++m) py[k][m] = __builtin_amdgcn_raw_buffer_load_b128(rxb, lane * 16 + m * 1024, kvh * 98304 + lw * 4096 + k * 16384, 16);
    const float bb = bflo(b2[rb * 256 + (4 * lw + kvh) * 16 + (lane & 15)]);
    bar();
    bar();
#pragma unroll
    for (int m = 0; m < 4; ++m) {
      f32x4 y0 = {bb, bb, bb, bb}, y1 = y0;
      add8(y0, y1, ldsr(pl + lw * 4096 + m * 1024));
#pragma unroll
      for (int k = 0; k < 3; ++k) add8(y0, y1, py[k][m]);
      *(LAS u32x4*)(size_t)(pl + lw * 4096 + m * 1024) = (u32x4){silu2(y0.xy), silu2(y0.zw), silu2(y1.xy), silu2(y1.zw)};
    }
    wait_lgkm0();
    bar();
  }
}

extern "C" void kernel_launch(void* const* d_in, const int* in_sizes, int n_in, void* d_out, int out_size, void* d_ws, size_t ws_size,
                              hipStream_t stream) {
  const u16* x = (const u16*)d_in[0];
  const u16* kc = (const u16*)d_in[1];
  const u16* vc = (const u16*)d_in[2];
  const u16* wq = (const u16*)d_in[3];
  const u16* wo = (const u16*)d_in[4];
  const u16* w1 = (const u16*)d_in[6];
  const u16* b1 = (const u16*)d_in[7];
  const u16* w2 = (const u16*)d_in[8];
  const u16* b2 = (const u16*)d_in[9];
  char* ws = (char*)d_ws;
  (void)hipFuncSetAttribute((const void*)k_main, hipFuncAttributeMaxDynamicSharedMemorySize, 163840);
  hipLaunchKernelGGL(k_main, dim3(256), dim3(512), 163840, stream, x, kc, vc, wq, wo, w1, b1, w2, b2, ws, (u16*)d_out);
}
```

```cpp
#include <hip/hip_runtime.h>
#include <hip/hip_bf16.h>

#define PFB 1835008
#define UROT 8
#define NSP 6
#define PFS 4
#define SPARE_T 1150
typedef unsigned short u16;
typedef short bf16x8 __attribute__((ext_vector_type(8)));
typedef short s16x4 __attribute__((ext_vector_type(4)));
typedef float f32x16 __attribute__((ext_vector_type(16)));
typedef float f32x4 __attribute__((ext_vector_type(4)));
typedef float f32x2 __attribute__((ext_vector_type(2)));
typedef unsigned u32x4 __attribute__((ext_vector_type(4)));
typedef unsigned u32x2 __attribute__((ext_vector_type(2)));
typedef __bf16 bf16x2 __attribute__((ext_vector_type(2)));
typedef __amdgpu_buffer_rsrc_t rsrc_t;
#define LAS __attribute__((address_space(3)))
#define GAS __attribute__((address_space(1)))
#define DI __device__ __forceinline__
#define SGB(m, n) __builtin_amdgcn_sched_group_barrier(m, n, 0)
#define SB0() __builtin_amdgcn_sched_barrier(0)

constexpr int SEQ = 2048, DM = 1024, FF = 3584, HD = 4096;
constexpr size_t WS_FLAG = 0;
constexpr int F_EP = 0, F_X = 512, F_L = 2048, F_U = 4096, F_O = 12288, F_Y = 16384, F_S = 49152, F_P = 81920, F_END = 96256;
constexpr size_t WS_ST = 98304;
constexpr size_t WS_CS = 131072;
constexpr size_t WS_HT = 262144;
constexpr size_t WS_KV = WS_HT + 4194304;
constexpr size_t WS_O = WS_KV + 1048576;
constexpr size_t WS_U = WS_O + 16777216;
constexpr size_t WS_XB = WS_U + 7340032;
constexpr size_t WS_UX = WS_XB + 25165824;
constexpr size_t WS_END = WS_UX + 7340032;

#define STAMP(kid, i)
#define STAMPF(i)
DI unsigned cvtpk(float lo, float hi) { f32x2 v = {lo, hi}; bf16x2 b = __builtin_convertvector(v, bf16x2); return __builtin_bit_cast(unsigned, b); }
DI float bflo(unsigned u) { return __uint_as_float(u << 16); }
DI float bfhi(unsigned u) { return __uint_as_float(u & 0xffff0000u); }
DI float rbf(float x) { return bflo(cvtpk(x, x)); }
template <int N> DI void wait_vm() { asm volatile("s_waitcnt vmcnt(%0)" ::"i"(N) : "memory"); }
DI void wait_lgkm0() { __builtin_amdgcn_s_waitcnt(0xC07F); }
DI void bar() { asm volatile("" ::: "memory"); __builtin_amdgcn_s_barrier(); asm volatile("" ::: "memory"); }
DI rsrc_t mkrsrc(const void* p, unsigned bytes) { return __builtin_amdgcn_make_buffer_rsrc((void*)p, 0, bytes, 0x00020000); }
DI void dma16(rsrc_t r, unsigned lds_addr, unsigned voff, unsigned soff) {
  asm volatile("s_mov_b32 m0, %0\n\ts_nop 0\n\tbuffer_load_dwordx4 %1, %2, %3 offen lds" ::"s"(lds_addr), "v"(voff), "s"(r), "s"(soff) : "memory");
}
DI void dma16s(rsrc_t r, unsigned lds_addr, unsigned voff, unsigned soff) {
  asm volatile("s_mov_b32 m0, %0\n\ts_nop 0\n\tbuffer_load_dwordx4 %1, %2, %3 offen sc1 lds" ::"s"(lds_addr), "v"(voff), "s"(r), "s"(soff) : "memory");
}
template <int OFF> DI void st16s(u32x4 v, rsrc_t r, unsigned voff, unsigned soff) {
  asm volatile("buffer_store_dwordx4 %0, %1, %2, %3 offen offset:%4 sc1\n\ts_nop 1" ::"v"(v), "v"(voff), "s"(r), "s"(soff), "i"(OFF) : "memory");
}
DI int lane_id() { int l; asm volatile("v_mbcnt_lo_u32_b32 %0, -1, 0\n\tv_mbcnt_hi_u32_b32 %0, -1, %0" : "=v"(l)); return l; }
struct Tag { unsigned lo, hi; };
DI void peek(rsrc_t rf, unsigned lds_addr, int off) {
  asm volatile("s_mov_b32 m0, %0\n\ts_nop 0\n\tbuffer_load_dword %3, %1, %2 offen sc1 lds" ::"s"(lds_addr), "s"(rf), "s"(off), "v"(lane_id() * 4) : "memory");
}
template <int GAP = 1> DI void spin_until(char* ws, int off, int n, Tag t) {
  const int ln = lane_id();
  GAS unsigned long long* p = (GAS unsigned long long*)(ws + WS_FLAG + off) + (ln < n ? ln : 0);
  const unsigned long long want = ((unsigned long long)t.hi << 32) | t.lo;
  for (unsigned sp = 0; sp < (1u << 20); ++sp) {
    if (__all(__hip_atomic_load(p, __ATOMIC_RELAXED, __HIP_MEMORY_SCOPE_AGENT) == want)) break;
    __builtin_amdgcn_s_sleep(GAP);
  }
  asm volatile("" ::: "memory");
}
DI void peek_check(char* ws, unsigned lds_addr, int off, int n, Tag t) {
  const int ln = lane_id();
  const unsigned v = *(LAS volatile unsigned*)(size_t)(lds_addr + ln * 4);
  if (!__all(ln >= 2 * n || v == ((ln & 1) ? t.hi : t.lo))) spin_until(ws, off, n, t);
}
template <long MASK> DI void signal(char* ws, int off, Tag t) {
  const u32x2 v = {t.lo, t.hi};
  asm volatile("s_mov_b64 exec, %3\n\tglobal_store_dwordx2 %0, %1, %2 sc1\n\ts_mov_b64 exec, -1" ::"v"(off), "v"(v), "s"(ws + WS_FLAG), "s"(MASK) : "memory");
}
DI void dma4k(rsrc_t r, unsigned lds_addr, unsigned voff, unsigned soff) {
  asm volatile("s_mov_b32 m0, %0\n\ts_nop 0\n\tbuffer_load_dwordx4 %1, %2, %3 offen sc1 lds\n\tbuffer_load_dwordx4 %1, %2, %3 offen offset:1024 sc1 lds\n\t"
               "buffer_load_dwordx4 %1, %2, %3 offen offset:2048 sc1 lds\n\tbuffer_load_dwordx4 %1, %2, %3 offen offset:3072 sc1 lds"
               ::"s"(lds_addr), "v"(voff), "s"(r), "s"(soff) : "memory");
}
template <int W> DI void pf_line(unsigned& sink, int wid, rsrc_t r, unsigned voff, unsigned soff) {
  asm volatile("s_cmp_lg_u32 %1, %5\n\ts_cbranch_scc1 1f\n\tbuffer_load_dword %0, %2, %3, %4 offen\n1:"
               : "+v"(sink) : "s"(wid), "v"(voff), "s"(r), "s"(soff), "i"(W) : "scc", "memory");
}
template <int CTRL> DI float dppf(float x) { return __builtin_bit_cast(float, __builtin_amdgcn_mov_dpp(__builtin_bit_cast(int, x), CTRL, 0xf, 0xf, true)); }
DI float wave_sum(float v) {
  v += dppf<0xB1>(v); v += dppf<0x4E>(v); v += dppf<0x141>(v); v += dppf<0x140>(v);
  auto a = __builtin_amdgcn_permlane16_swap(__float_as_uint(v), __float_as_uint(v), false, false);
  v = __uint_as_float(a[0]) + __uint_as_float(a[1]);
  auto b = __builtin_amdgcn_permlane32_swap(__float_as_uint(v), __float_as_uint(v), false, false);
  return __uint_as_float(b[0]) + __uint_as_float(b[1]);
}

DI f32x2 ln16(unsigned (&w)[8]) {
  f32x2 v[8];
#pragma unroll
  for (int i = 0; i < 8; ++i) v[i] = (f32x2){bflo(w[i]), bfhi(w[i])};
  const f32x2 s = ((v[0] + v[1]) + (v[2] + v[3])) + ((v[4] + v[5]) + (v[6] + v[7]));
  const float mean = wave_sum(s[0] + s[1]) * (1.f / 1024.f);
  f32x2 q0 = {0.f, 0.f}, q1 = q0;
#pragma unroll
  for (int i = 0; i < 8; i += 2) {
    const f32x2 d0 = v[i] - mean, d1 = v[i + 1] - mean;
    q0 = __builtin_elementwise_fma(d0, d0, q0); q1 = __builtin_elementwise_fma(d1, d1, q1);
  }
  q0 += q1;
  const float var = wave_sum(q0[0] + q0[1]) * (1.f / 1024.f);
  const float mb = rbf(mean);
  const float rs = __builtin_amdgcn_rcpf(rbf(__builtin_amdgcn_sqrtf(rbf(rbf(var) + 1e-5f))));
#pragma unroll
  for (int i = 0; i < 8; ++i) {
    const f32x2 d = v[i] - mb;
    const unsigned c = cvtpk(d[0], d[1]);
    const f32x2 r = (f32x2){bflo(c), bfhi(c)} * rs;
    w[i] = cvtpk(r[0], r[1]);
  }
  return (f32x2){mb, rs};
}

struct Frags { bf16x8 x[8]; bf16x8 w[4]; };
struct Addr { unsigned x, w[4]; };
template <int XS = 1024> DI void load_frags(Frags& f, const Addr& a) {
#pragma unroll
  for (int c = 0; c < 4; ++c) {
    s16x4 a0 = __builtin_amdgcn_ds_read_tr16_b64_v4i16((LAS s16x4*)(size_t)(a.w[c]));
    s16x4 a1 = __builtin_amdgcn_ds_read_tr16_b64_v4i16((LAS s16x4*)(size_t)(a.w[c] + 4096));
    f.w[c] = __builtin_shufflevector(a0, a1, 0, 1, 2, 3, 4, 5, 6, 7);
  }
#pragma unroll
  for (int i = 0; i < 8; ++i) f.x[i] = *(LAS bf16x8*)(size_t)(a.x + i * XS);
}
template <bool WA, bool Z> DI void mma_all(f32x4 (&acc)[8][4], const Frags& f, const f32x4* c0) {
  const f32x4 zero = {0.f, 0.f, 0.f, 0.f};
#pragma unroll
  for (int i = 0; i < 8; ++i)
#pragma unroll
    for (int jj = 0; jj < 4; ++jj) {
      const int j = (i & 1) ? 3 - jj : jj;
      if (WA) acc[i][j] = __builtin_amdgcn_mfma_f32_16x16x32_bf16(f.w[j], f.x[i], Z ? (c0 ? c0[j] : zero) : acc[i][j], 0, 0, 0);
      else    acc[i][j] = __builtin_amdgcn_mfma_f32_16x16x32_bf16(f.x[i], f.w[j], Z ? (c0 ? c0[j] : zero) : acc[i][j], 0, 0, 0);
    }
}
DI void addr_add(Addr& a, int d) {
  a.x += d;
#pragma unroll
  for (int c = 0; c < 4; ++c) a.w[c] += d;
}
template <bool WA, int NSLOT, int SLOTB, bool Z = false>
DI void gstep(f32x4 (&acc)[8][4], Frags& Fn, const Frags& Fc, Addr& a, int& slot, const f32x4* c0 = nullptr) {
  SB0();
  load_frags(Fn, a);
  mma_all<WA, Z>(acc, Fc, c0);
  slot = (slot + 1 == NSLOT) ? 0 : slot + 1;
  addr_add(a, (slot == 0) ? -(NSLOT - 1) * SLOTB : SLOTB);
  asm volatile("" : "+v"(a.x), "+v"(a.w[0]), "+v"(a.w[1]), "+v"(a.w[2]), "+v"(a.w[3]));
#pragma unroll
  for (int i = 0; i < 8; ++i) { SGB(0x8, 1); SGB(0x100, 1); SGB(0x8, 1); SGB(0x100, 1); SGB(0x8, 1); }
#pragma unroll
  for (int i = 0; i < 6; ++i) { SGB(0x8, 1); SGB(0x4, 1); SGB(0x2, 1); }
  SGB(0x8, 2);
  SB0();
  wait_lgkm0();
  bar();
}
template <int MODE, bool CS, bool Z = false>
DI void qstep(f32x4 (&acc)[8][4], f32x4 (&cs)[2], u32x4& ones, unsigned onext, Frags& Fn, const Frags& Fc, Addr& a, int& xs, int& wsl) {
  const f32x4 zero = {0.f, 0.f, 0.f, 0.f};
  SB0();
  load_frags<MODE == 2 ? 1024 : 2048>(Fn, a);
  if (CS) {
#pragma unroll
    for (int j = 0; j < 2; ++j) cs[j] = __builtin_amdgcn_mfma_f32_16x16x32_bf16(Fc.w[j], __builtin_bit_cast(bf16x8, ones), Z ? zero : cs[j], 0, 0, 0);
    ones = (u32x4){onext, onext, onext, onext};
    asm volatile("" : "+v"(ones));
  }
  mma_all<true, Z>(acc, Fc, nullptr);
  if (MODE == 2) addr_add(a, 24576);
  else {
    wsl = (wsl + 1) & 3;
    const int dw = wsl ? 8192 : -24576;
#pragma unroll
    for (int c = 0; c < 4; ++c) a.w[c] += dw;
    a.x ^= 64;
    if (MODE == 1) { xs = (xs == 2) ? 0 : xs + 1; a.x += xs ? 32768 : -65536; }
  }
  asm volatile("" : "+v"(a.x), "+v"(a.w[0]), "+v"(a.w[1]), "+v"(a.w[2]), "+v"(a.w[3]));
#pragma unroll
  for (int i = 0; i < 8; ++i) { SGB(0x8, 1); SGB(0x100, 1); SGB(0x8, 1); SGB(0x100, 1); SGB(0x8, 1); }
#pragma unroll
  for (int i = 0; i < 7; ++i) { SGB(0x8, 1); SGB(0x4, 1); SGB(0x2, 1); }
  if (CS) { SGB(0x8, 1); SGB(0x2, 2); SGB(0x8, 1); SGB(0x2, 2); }
  SGB(0x8, 1);
  SB0();
  wait_lgkm0();
  bar();
}
DI void addr_init(Addr& a, unsigned xbase, unsigned wbase, int wj, int cx, int lane) {
  const int g = lane >> 4, q = (lane >> 2) & 3, p = lane & 3, row = 4 * g + q;
  a.x = xbase + lane * 16;
#pragma unroll
  for (int c = 0; c < 4; ++c) a.w[c] = wbase + row * 256 + (((2 * (4 * wj + (c ^ cx)) + (p >> 1)) ^ ((row & 7) << 1)) * 16) + (p & 1) * 8;
}
template <bool WA, int NSLOT, int SLOTB> DI void gemm_steps(f32x4 (&acc)[8][4], Frags& F0, Frags& F1, Addr& a, int& slot, int n) {
  for (int t = 0; t < n; t += 2) {
    gstep<WA, NSLOT, SLOTB>(acc, F0, F1, a, slot);
    gstep<WA, NSLOT, SLOTB>(acc, F1, F0, a, slot);
  }
}

DI u32x4 pack8(const f32x4& a, const f32x4& b) { return (u32x4){cvtpk(a[0], a[1]), cvtpk(a[2], a[3]), cvtpk(b[0], b[1]), cvtpk(b[2], b[3])}; }
DI void ystep_park(f32x4 (&acc)[8][4], Frags& Fn, const Frags& Fc, Addr& a, int& slot, unsigned pm) {
  s16x4 h[8];
  SB0();
#pragma unroll
  for (int k = 0; k < 16; ++k) {
    const int m = k >> 2, j = k & 3;
    *(LAS u32x4*)(size_t)(pm + j * 16384 + m * 1024) = pack8(acc[2 * m][j], acc[2 * m + 1][j]);
    asm volatile("v_mfma_f32_16x16x32_bf16 %0, %1, %2, 0" : "+v"(acc[2 * m][j]) : "v"(Fc.w[j]), "v"(Fc.x[2 * m]));
    if (k < 8) h[k] = __builtin_amdgcn_ds_read_tr16_b64_v4i16((LAS s16x4*)(size_t)(a.w[k >> 1] + (k & 1) * 4096));
    else Fn.x[k - 8] = *(LAS bf16x8*)(size_t)(a.x + (k - 8) * 1024);
    asm volatile("v_mfma_f32_16x16x32_bf16 %0, %1, %2, 0" : "+v"(acc[2 * m + 1][j]) : "v"(Fc.w[j]), "v"(Fc.x[2 * m + 1]));
    SB0();
  }
#pragma unroll
  for (int c = 0; c < 4; ++c) Fn.w[c] = __builtin_shufflevector(h[2 * c], h[2 * c + 1], 0, 1, 2, 3, 4, 5, 6, 7);
  slot = 2;
  addr_add(a, 24576);
  wait_lgkm0();
  bar();
}
DI f32x2 gate2(f32x2 u, f32x2 z) {
  f32x2 e = {__builtin_amdgcn_exp2f(z[0]), __builtin_amdgcn_exp2f(z[1])};
  e += 1.f;
  return u * (f32x2){__builtin_amdgcn_rcpf(e[0]), __builtin_amdgcn_rcpf(e[1])};
}
DI unsigned gelu2(f32x2 u) {
  const f32x2 r = gate2(u, u * __builtin_elementwise_fma(u * u, (f32x2){-0.1029432397f, -0.1029432397f}, (f32x2){-2.302208198f, -2.302208198f}));
  return cvtpk(r[0], r[1]);
}
DI unsigned silu2(f32x2 y) { const f32x2 r = gate2(y, y * -1.4426950409f); return cvtpk(r[0], r[1]); }
DI u32x4 ldsr(unsigned a) { return *(LAS u32x4*)(size_t)a; }
DI void add8(f32x4& a, f32x4& b, const u32x4& w) {
  a += (f32x4){bflo(w[0]), bfhi(w[0]), bflo(w[1]), bfhi(w[1])};
  b += (f32x4){bflo(w[2]), bfhi(w[2]), bflo(w[3]), bfhi(w[3])};
}

struct Att { bf16x8 q[8]; f32x16 o[4]; f32x16 negm, sE, sO; u32x4 pO[2]; bf16x8 pre[4]; float l; };
DI float vmax16(const f32x16& a) {
  float m = fmaxf(fmaxf(a[0], a[1]), a[2]);
#pragma unroll
  for (int i = 3; i < 15; i += 2) m = fmaxf(fmaxf(m, a[i]), a[i + 1]);
  m = fmaxf(m, a[15]);
  auto sw = __builtin_amdgcn_permlane32_swap(__float_as_uint(m), __float_as_uint(m), false, false);
  return fmaxf(__uint_as_float(sw[0]), __uint_as_float(sw[1]));
}
DI bf16x8 ldfrag(unsigned a) { return *(LAS bf16x8*)(size_t)a; }
DI void sm1(f32x16& s, int e, u32x4 (&w)[2], float& l) {
  if (e < 16) {
    float x = s[e];
    x = __builtin_amdgcn_exp2f(x);
    asm volatile("" : "+v"(x));
    s[e] = x;
  }
  if (e >= 1) {
    l += s[e - 1];
    asm volatile("" : "+v"(l));
    if (!(e & 1)) { unsigned c = cvtpk(s[e - 2], s[e - 1]); asm volatile("" : "+v"(c)); w[(e - 1) >> 3][((e - 1) >> 1) & 3] = c; }
  }
}
#define SETPRIO(x) __builtin_amdgcn_s_setprio(x)
#define MFMA32(a, b, c) __builtin_amdgcn_mfma_f32_32x32x16_bf16(a, b, c, 0, 0, 0)
#define PFRAG(w) __builtin_bit_cast(bf16x8, w)
template <int POS, class F> DI void att_tile(Att& A, unsigned kv, unsigned kvn, F&& gap) {
  bf16x8 f[36];
  u32x4 pE[2];
  asm volatile("" : "+v"(kv), "+v"(kvn));
#pragma unroll
  for (int i = 0; i < 4; ++i) f[i] = A.pre[i];
  SB0();
  SETPRIO(3);
#pragma unroll
  for (int n = 0; n < 8; ++n) {
    if (POS == 0) A.sE = MFMA32(f[n], A.q[n], n ? A.sE : A.negm);
    else A.o[n & 3] = MFMA32(f[n], PFRAG(A.pO[n >> 2]), A.o[n & 3]);
    f[n + 4] = ldfrag(kv + (n + 4) * 1024);
    if (POS && n <= 2) sm1(A.sO, 14 + n, A.pO, A.l);
    if (POS && n >= 2) sm1(A.sE, n - 2, pE, A.l);
    gap(n);
    SB0();
  }
  if (POS == 0) {
    const float mx = vmax16(A.sE);
#pragma unroll
    for (int i = 0; i < 16; ++i) { A.sE[i] -= mx; A.negm[i] = -mx; }
    SB0();
  }
  SETPRIO(2);
#pragma unroll
  for (int n = 8; n < 16; ++n) {
    A.sO = MFMA32(f[n], A.q[n - 8], n > 8 ? A.sO : A.negm);
    f[n + 4] = ldfrag(kv + (n + 4) * 1024);
    if (POS) sm1(A.sE, n - 2, pE, A.l);
    else if (n < 15) { sm1(A.sE, 2 * (n - 8), pE, A.l); sm1(A.sE, 2 * (n - 8) + 1, pE, A.l); }
    gap(n);
    SB0();
  }
  SETPRIO(1);
#pragma unroll
  for (int n = 16; n < 24; ++n) {
    A.o[n & 3] = MFMA32(f[n], PFRAG(pE[(n >> 2) & 1]), A.o[n & 3]);
    f[n + 4] = ldfrag(kv + (n + 4) * 1024);
    if (n <= 18) sm1(A.sE, n - 2, pE, A.l);
    if (n >= 18) {
      if (POS < 2) sm1(A.sO, n - 18, A.pO, A.l);
      else { sm1(A.sO, 2 * (n - 18), A.pO, A.l); sm1(A.sO, 2 * (n - 18) + 1, A.pO, A.l); }
    }
    gap(n);
    SB0();
  }
  SETPRIO(0);
#pragma unroll
  for (int n = 24; n < 32; ++n) {
    if (POS < 2) {
      A.sE = MFMA32(f[n], A.q[n - 24], n > 24 ? A.sE : A.negm);
      f[n + 4] = ldfrag(n < 28 ? kv + (n + 4) * 1024 : kvn + (n - 28) * 1024);
      sm1(A.sO, n - 18, A.pO, A.l);
    } else {
      A.o[n & 3] = MFMA32(f[n], PFRAG(A.pO[(n >> 2) & 1]), A.o[n & 3]);
      if (n < 28) f[n + 4] = ldfrag(kv + (n + 4) * 1024);
      if (n < 26) { sm1(A.sO, 2 * (n - 18), A.pO, A.l); sm1(A.sO, 2 * (n - 18) + 1, A.pO, A.l); }
      if (n == 26) sm1(A.sO, 16, A.pO, A.l);
    }
    gap(n);
    SB0();
  }
  if (POS < 2) {
#pragma unroll
    for (int i = 0; i < 4; ++i) A.pre[i] = f[32 + i];
  }
}
DI void att_redo(Att& A, rsrc_t rkv, unsigned vo) {
  float m = -3.0e38f, l = 0.f;
#pragma unroll
  for (int d = 0; d < 4; ++d)
#pragma unroll
    for (int i = 0; i < 16; ++i) A.o[d][i] = 0.f;
  for (int blk = 0; blk < 16; ++blk) {
    const int kb = (blk ? 2 * blk - 1 : 0) * 8192, vb = (blk < 15 ? 2 * blk + 2 : 31) * 8192;
    f32x16 s;
#pragma unroll
    for (int i = 0; i < 16; ++i) s[i] = 0.f;
#pragma unroll
    for (int n = 0; n < 8; ++n) s = MFMA32(PFRAG(__builtin_amdgcn_raw_buffer_load_b128(rkv, vo + n * 1024, kb, 16)), A.q[n], s);
    const float mn = fmaxf(m, vmax16(s)), fct = __builtin_amdgcn_exp2f(m - mn);
    m = mn;
    l *= fct;
    u32x4 p[2];
#pragma unroll
    for (int i = 0; i < 16; ++i) s[i] -= mn;
#pragma unroll
    for (int i = 0; i < 17; ++i) sm1(s, i, p, l);
#pragma unroll
    for (int d = 0; d < 4; ++d)
#pragma unroll
      for (int i = 0; i < 16; ++i) A.o[d][i] *= fct;
#pragma unroll
    for (int n = 0; n < 8; ++n) A.o[n & 3] = MFMA32(PFRAG(__builtin_amdgcn_raw_buffer_load_b128(rkv, vo + n * 1024, vb, 16)), PFRAG(p[n >> 2]), A.o[n & 3]);
  }
  A.l = l;
}

__global__ __launch_bounds__(512, 2) void k_main(const u16* __restrict__ x, const u16* __restrict__ kc, const u16* __restrict__ vc, const u16* __restrict__ wq, const u16* __restrict__ wo,
                                                 const u16* __restrict__ w1, const u16* __restrict__ b1, const u16* __restrict__ w2,
                                                 const u16* __restrict__ b2, char* __restrict__ ws, u16* __restrict__ out) {
  extern __shared__ __attribute__((aligned(16))) char smem[];
  LAS char* lds = (LAS char*)smem;
  const unsigned lb = (unsigned)(size_t)lds;
  const int tid = threadIdx.x, wid = __builtin_amdgcn_readfirstlane(tid >> 6);
  const int g8 = blockIdx.x & 7, idx = blockIdx.x >> 3;
  STAMP(1, 0);
  STAMP(0, 0);
  const bool hasU = idx < 28;
  const int uh = g8 & 1, dq = idx & 3, ft = 7 * (g8 >> 1) + (idx >> 2), ut = ft * 4 + dq;
  const int kvh = g8 >> 1, head = 8 * kvh + (idx & 7), rb = 4 * (g8 & 1) + (idx >> 3);
  const int dc = idx & 7, tile = rb * 8 + dc;
  const int lw = wid - 4, wi = (wid >> 1) & 1, wj = wid & 1;
  constexpr int QS = 24576;
  constexpr int PARK = 98304;
  const rsrc_t rf = mkrsrc(ws + WS_FLAG, F_END + 256);
  const rsrc_t rkv = mkrsrc(ws + WS_KV + (size_t)kvh * 262144, 262144);
  const rsrc_t rxb = mkrsrc(ws + WS_XB + (size_t)tile * 393216, 393216);
  struct Strm { rsrc_t r; unsigned v0, v1, step, p0, p1, p2; int rot; };
  auto strm_x = [&](Strm& S, const char* base, unsigned bytes, unsigned step, int xr) {
    const int ln = lane_id();
    S.r = mkrsrc(base, bytes); S.v0 = ln * 16 + (xr & 1) * 1024; S.v1 = S.v0 ^ 1024; S.step = step;
    S.p0 = (xr >> 1) * 2048; S.p1 = S.p0 ^ 2048; S.p2 = 4096; S.rot = 0;
  };
  auto strm_w = [&](Strm& S, const char* base, unsigned rows, unsigned ld, bool nat) {
    const int ln = lane_id(), q4 = ln >> 4;
    const unsigned c0 = ((ln & 15) ^ (q4 << 1)) * 16;
    S.r = mkrsrc(base, (rows - 1) * ld + 256); S.v0 = q4 * ld + c0; S.v1 = ((nat ? 8 : 4) + q4) * ld + (c0 ^ 128); S.step = 32 * ld;
    S.p0 = 0; S.p1 = (nat ? 16 : 8) * ld; S.p2 = (nat ? 4 : 16) * ld; S.rot = 0;
  };
  auto issue = [&](const Strm& S, int k) {
    const unsigned s = lb + (k & 3) * QS + lw * 8192;
#pragma unroll
    for (int i = 0; i < 4; ++i) {
      const unsigned so = ((k + S.rot) & 31) * S.step + ((i & 1) ? S.p1 : S.p0) + (i >> 1) * S.p2;
      dma16s(S.r, s + i * 2048, S.v0, so); dma16s(S.r, s + i * 2048 + 1024, S.v1, so);
    }
  };
  constexpr int QW = 98304, HST = 131072, HRS = 2112, RST = HST + 8 * HRS, PKQ = 148480, STT = 151552, CSX = 153600;
  const int lane0 = tid & 63;
  const rsrc_t rq = lw < 2 ? mkrsrc(x + (size_t)(rb * 256 + lw * 128) * DM, 128 * DM * 2) : mkrsrc((const char*)wq + head * 256, (DM - 1) * HD * 2 + 256);
  const unsigned qv0 = lw < 2 ? (lane0 >> 3) * 2048 + (((lane0 & 7) ^ (lane0 >> 4)) * 16) : (lane0 >> 4) * (HD * 2) + (((lane0 & 15) ^ ((lane0 >> 4) << 1)) * 16);
  const unsigned qv1 = lw < 2 ? (qv0 ^ 64) + 8 * 2048 : (qv0 ^ 128) + 8 * (HD * 2);
  const bool qx = lw < 2;
  const unsigned qp1 = qx ? 32768 : 16 * HD * 2, qp2 = qx ? 65536 : 4 * HD * 2;
  const unsigned qlb = lb + (qx ? lw * 16384 : QW), qlw = qx ? 98304 : 32768, qom = qx ? 0x20780 : 0x7c0000;
  unsigned ql = qx ? 65536 : 0, qdl = 8192, qo = rb * (qx ? 256 : 4 * 32 * HD * 2), qdo = qx ? 131072 : 32 * HD * 2;
  auto issq = [&]() {
    const unsigned s = qlb + ql;
    dma16(rq, s, qv0, qo); dma16(rq, s + 1024, qv1, qo);
    dma16(rq, s + 2048, qv0, qo + qp1); dma16(rq, s + 3072, qv1, qo + qp1);
    dma16(rq, s + 4096, qv0, qo + qp2); dma16(rq, s + 5120, qv1, qo + qp2);
    dma16(rq, s + 6144, qv0, qo + qp1 + qp2); dma16(rq, s + 7168, qv1, qo + qp1 + qp2);
    ql += qdl; ql = ql >= qlw ? ql - qlw : ql; qdl ^= qx ? (8192 ^ 24576) : 0;
    qo = (qo + qdo) & qom; qdo ^= qx ? 128 : 0;
  };
  auto h_out = [&](int it) {
    const rsrc_t rh = mkrsrc(ws + WS_HT + (size_t)(blockIdx.x >> 2) * 16384 + (blockIdx.x & 3) * 256, 3 * 1048576 + 16384);
    const unsigned a0 = HST + ((lane0 >> 2) & 3) * HRS + (it * 4 + (lane0 >> 4)) * 32 + (lane0 & 3) * 8;
    s16x4 lo = __builtin_amdgcn_ds_read_tr16_b64_v4i16((LAS s16x4*)(lds + a0));
    s16x4 hi = __builtin_amdgcn_ds_read_tr16_b64_v4i16((LAS s16x4*)(lds + a0 + 4 * HRS));
    st16s<0>(__builtin_bit_cast(u32x4, __builtin_shufflevector(lo, hi, 0, 1, 2, 3, 4, 5, 6, 7)), rh, (lane0 >> 4) * 1024 + (lane0 & 15) * 16,
             (it >> 2) * 1048576 + (it & 3) * 4096);
  };
  auto st_out = [&]() { st16s<0>(*(LAS u32x4*)(lds + RST + (lane0 & 3) * 16), mkrsrc(ws + WS_ST + blockIdx.x * 64, 64), lane0 * 16, 0); };
  Tag tag;
  {
    const int b = blockIdx.x, lane = lane0;
    u32x4 A, B;
    u32x2 ep;
    asm volatile("global_load_dwordx4 %0, %3, %4\n\tglobal_load_dwordx4 %1, %3, %4 offset:1024\n\tglobal_load_dwordx2 %2, %5, %6 sc1"
                 : "=&v"(A), "=&v"(B), "=&v"(ep) : "v"(lane * 16), "s"(x + (size_t)(8 * b + wid) * DM), "v"(F_EP), "s"(ws + WS_FLAG) : "memory");
    const bool strm = hasU && wid >= 4 && wid < 7;
    if (strm) { issq(); issq(); wait_vm<16>(); } else wait_vm<0>();
    asm volatile("" : "+v"(A), "+v"(B), "+v"(ep));
    unsigned v[8] = {A[0], A[1], A[2], A[3], B[0], B[1], B[2], B[3]};
    const unsigned long long e64 = ((unsigned long long)(unsigned)__builtin_amdgcn_readfirstlane(ep[1]) << 32) | (unsigned)__builtin_amdgcn_readfirstlane(ep[0]);
    const unsigned long long tg = e64 * 0x9E3779B97F4A7C15ull + 0x632BE59BD9B4E019ull;
    tag.lo = (unsigned)tg; tag.hi = (unsigned)(tg >> 32);
    *(LAS f32x2*)(lds + RST + wid * 8) = ln16(v);
    ln16(v);
    *(LAS u32x4*)(lds + HST + wid * HRS + lane * 16) = (u32x4){v[0], v[1], v[2], v[3]};
    *(LAS u32x4*)(lds + HST + wid * HRS + 1024 + lane * 16) = (u32x4){v[4], v[5], v[6], v[7]};
    wait_lgkm0();
  }
  STAMP(0, 1);
  if (!hasU) {
    const int sp = g8 * 4 + (idx - 28), lane = lane0, h = lane >> 5;
    const unsigned long long t_in = __builtin_amdgcn_s_memrealtime();
    bar();
    h_out(2 * wid); h_out(2 * wid + 1);
    if (wid == 0) st_out();
    wait_vm<0>();
    bar();
    if (wid == 0) signal<1>(ws, F_L + blockIdx.x * 8, tag);
    unsigned e[4][8];
#pragma unroll
    for (int i = 0; i < 4; ++i) {
      const int kh = sp >> 3, unit = (sp & 7) * 4 + i, frag = wid;
      const bool isK = unit == 0 || ((unit & 1) && unit < 31);
      const u16* sk = kc + ((size_t)kh * SEQ + 1536 + 32 * ((unit + 1) >> 1) + (lane & 31)) * 128 + 32 * (frag >> 1) + 4 * (2 * (frag & 1) + h);
      const u16* sv = vc + ((size_t)kh * SEQ + 1536 + 32 * ((unit - 1) >> 1) + 16 * (frag >> 2) + 4 * h) * 128 + 32 * (frag & 3) + (lane & 31);
      const u16* src = isK ? sk : sv;
      const int s1 = isK ? 1 : 128, s4 = isK ? 16 : 1024;
#pragma unroll
      for (int j2 = 0; j2 < 8; ++j2) e[i][j2] = src[(j2 >> 2) * s4 + (j2 & 3) * s1];
    }
    unsigned pw[8];
#pragma unroll
    for (int i = 0; i < NSP; ++i) pw[i] = *(const unsigned*)((const char*)w1 + (UROT ? (size_t)(sp >> 2) * PFB + (size_t)i * (PFB / 8) + (size_t)((sp & 3) * 448 + (tid < 448 ? tid : 0)) * 128
                                                                          : (size_t)((i >> 1) + 4 * (i & 1)) * PFB + (size_t)(sp * 448 + (tid < 448 ? tid : 0)) * 128));
    SB0();
#pragma unroll
    for (int i = 0; i < 4; ++i)
      st16s<0>((u32x4){e[i][0] | (e[i][1] << 16), e[i][2] | (e[i][3] << 16), e[i][4] | (e[i][5] << 16), e[i][6] | (e[i][7] << 16)},
               mkrsrc(ws + WS_KV, 1048576), tid * 16, (sp * 4 + i) * 8192);
#pragma unroll
    for (int i = 0; i < NSP; ++i) asm volatile("" ::"v"(pw[i]));
    wait_vm<0>();
    bar();
    if (wid == 0) signal<1>(ws, F_X + (sp >> 3) * 128 + (sp & 7) * 8, tag);
    for (int i = 0; i < 1000 && __builtin_amdgcn_s_memrealtime() - t_in < SPARE_T; ++i) __builtin_amdgcn_s_sleep(8);
    if (wid >= 4 && wid < 7) { issq(); issq(); }
  }
  bf16x8 qh[8];
  f32x4 uk[4][4];
  auto kv8 = [&](int t, int slot) {
    const unsigned v = lane_id() * 16;
    dma4k(rkv, lb + slot * 32768 + lw * 8192, v, t * 32768 + lw * 8192);
    dma4k(rkv, lb + slot * 32768 + lw * 8192 + 4096, v, t * 32768 + lw * 8192 + 4096);
  };
  if (wid < 4) {
    f32x4 acc[8][4], cs[2];
    Frags F0, F1;
    Addr a;
    int xs = 2, wsl = 1;
    {
      const int ln = lane_id(), r = ln & 15;
      addr_init(a, 0, lb + QW, wj, 2 * wi, ln);
      a.x = lb + 65536 + wi * 16384 + r * 128 + (((ln >> 4) ^ (r >> 1)) * 16);
    }
    bar();
    load_frags<2048>(F0, a);
    a.x ^= 64;
#pragma unroll
    for (int c = 0; c < 4; ++c) a.w[c] += 8192;
    wait_lgkm0();
    bar();
    STAMP(1, 1);
    {
      const unsigned o1 = (rb & 3) == 3 ? 0 : 0x3f803f80, o2 = (rb & 3) == 2 ? 0x3f803f80 : 0;
      u32x4 ones = {o1, o1, o1, o1};
      qstep<1, true, true>(acc, cs, ones, o1, F1, F0, a, xs, wsl);
      for (int t = 0; t < 4; ++t) {
        const unsigned on = t == 0 ? o1 : t == 3 ? 0 : o2;
        qstep<0, true>(acc, cs, ones, on, F0, F1, a, xs, wsl);
        qstep<1, true>(acc, cs, ones, on, F1, F0, a, xs, wsl);
      }
      const int ln = lane_id();
      const unsigned csa = lb + CSX + ((ln & 15) ? 7168 : (64 * wj + 32 * wi) * 4) + (ln & 48);
      *(LAS f32x4*)(size_t)csa = cs[0];
      *(LAS f32x4*)(size_t)(csa + 64) = cs[1];
      for (int t = 0; t < 11; ++t) { qstep<0, false>(acc, cs, ones, 0, F0, F1, a, xs, wsl); qstep<1, false>(acc, cs, ones, 0, F1, F0, a, xs, wsl); }
      addr_init(a, lb + wi * 8192, lb + 16384, wj, 0, lane_id());
      qstep<2, false>(acc, cs, ones, 0, F0, F1, a, xs, wsl);
    }
    STAMP(1, 2);
    f32x4 c0[4];
    {
      const int ln = lane_id(), g = ln >> 4;
      u32x2 bv[4];
#pragma unroll
      for (int j = 0; j < 4; ++j) bv[j] = *(LAS u32x2*)(lds + CSX + 5120 + (64 * wj + 16 * j + 4 * g) * 2);
      f32x4 cq[4];
#pragma unroll
      for (int j = 0; j < 4; ++j) cq[j] = *(LAS f32x4*)(lds + CSX + 512 + (64 * wj + 16 * (j ^ (2 * wi)) + 4 * g) * 4);
      f32x2 st[8];
#pragma unroll
      for (int i = 0; i < 8; ++i) st[i] = *(LAS f32x2*)(lds + STT + (128 * wi + 16 * i + (ln & 15)) * 8);
      wait_lgkm0();
      bar();
      const unsigned qa = lb + (wi ? 98304 : 131072) + (4 * wj + (g >> 1)) * (wi ? 4096 : 1024) + ((ln & 15) + 32 * (g & 1)) * 16;
      const unsigned sq = wi ? 1024 : 8192, m0 = wi ? 8192 : 0, m1 = wi ? 0 : 2048;
#pragma unroll
      for (int i = 0; i < 8; ++i) {
        const float sc = st[i][1] * 0.12751743082f, nm = -st[i][0];
#pragma unroll
        for (int mm = 0; mm < 2; ++mm)
          *(LAS u32x4*)(size_t)(qa + (i >> 1) * sq + (mm ? m1 : m0) + (i & 1) * 256) =
              pack8((acc[i][2 * mm] + cq[2 * mm] * nm) * sc, (acc[i][2 * mm + 1] + cq[2 * mm + 1] * nm) * sc);
      }
#pragma unroll
      for (int j = 0; j < 4; ++j) c0[j] = uh ? (f32x4){0.f, 0.f, 0.f, 0.f} : (f32x4){bflo(bv[j][0]), bfhi(bv[j][0]), bflo(bv[j][1]), bfhi(bv[j][1])};
    }
    STAMP(1, 3);
    if (hasU) {
      int slot = 1;
      gstep<true, 4, QS, true>(acc, F1, F0, a, slot, c0);
      gemm_steps<true, 4, QS>(acc, F0, F1, a, slot, 30);
      gstep<true, 4, QS>(acc, F0, F1, a, slot);
      STAMP(1, 4);
      const rsrc_t rux = mkrsrc(ws + WS_UX + (size_t)(ut * 2 + (uh ^ 1)) * 32768 + wid * 8192, 8192);
      const int ln = lane_id();
#pragma unroll
      for (int ii = 0; ii < 4; ++ii) {
        st16s<0>(pack8(acc[2 * ii + 1][0], acc[2 * ii + 1][1]), rux, ln * 16, ii * 2048);
        st16s<1024>(pack8(acc[2 * ii + 1][2], acc[2 * ii + 1][3]), rux, ln * 16, ii * 2048);
#pragma unroll
        for (int j = 0; j < 4; ++j) uk[ii][j] = acc[2 * ii][j];
      }
    } else {
      wait_lgkm0();
      bar();
    }
    STAMP(1, 5);
    const int ln = lane_id();
#pragma unroll
    for (int ks = 0; ks < 8; ++ks) qh[ks] = *(LAS bf16x8*)(lds + 131072 + wid * 8192 + ks * 1024 + ln * 16);
  } else {
    const int lane = lane0;
    Strm U;
    if (lw < 2) strm_x(U, ws + WS_HT + (size_t)dq * 1048576 + uh * 524288 + lw * 8192, 524288 - lw * 8192, 16384, uh);
    else strm_w(U, (const char*)w1 + (size_t)uh * 1024 * (FF * 2) + ft * 256, 1024, FF * 2, true);
    U.rot = UROT * dq;
    const unsigned pk = lb + PKQ + lw * 512;
    if (lw < 3) {
      if (lw < 2) wait_vm<0>(); else wait_vm<8>();
      bar();
      issq();
      if (lw < 2) issq(); else wait_vm<8>();
      bar();
      issq();
      for (int t = 1; t <= 21; ++t) { issq(); wait_vm<16>(); bar(); }
      peek(rf, pk, F_L + lw * 512); peek(rf, pk + 256, F_L + lw * 512 + 256);
      for (int t = 22; t <= 26; ++t) { issq(); wait_vm<16>(); bar(); }
      issq(); wait_vm<16>();
    } else {
      peek(mkrsrc(b1, FF * 2), lb + CSX + 5120, ft * 256);
      bar();
      if (hasU) {
        for (int t = 0; t <= 7; ++t) { h_out(2 * t); h_out(2 * t + 1); bar(); }
        st_out();
        bar(); bar(); bar();
        st16s<0>(*(LAS u32x4*)(lds + CSX + (lane & 31) * 16), mkrsrc(ws + WS_CS + (head * 8 + rb) * 512, 512), lane * 16, 0);
        bar(); bar(); bar();
        wait_vm<0>();
        signal<1>(ws, F_L + blockIdx.x * 8, tag);
      } else {
        for (int t = 0; t <= 13; ++t) bar();
        spin_until(ws, F_X + kvh * 128, 8, tag);
      }
      const rsrc_t rp = mkrsrc(w1, hasU ? SEQ * FF * 2 : 0);
      for (int t = 14; t <= 21; ++t) {
        if (!UROT && (t - 14) % PFS == 0 && NSP + (t - 14) / PFS < 8)
          asm volatile("s_mov_b32 m0, %0\n\ts_nop 0\n\tbuffer_load_dword %2, %1, %3 offen lds" ::"s"(lb + 161792), "s"(rp), "v"(((g8 * 28 + idx) * 64 + lane) * 128),
                       "s"((((NSP + (t - 14) / PFS) >> 1) + 4 * ((NSP + (t - 14) / PFS) & 1)) * 1835008) : "memory");
        bar();
      }
      peek(rf, pk, F_L + lw * 512); peek(rf, pk + 256, F_L + lw * 512 + 256);
      for (int t = 22; t <= 26; ++t) bar();
      wait_vm<0>();
    }
    peek_check(ws, pk, F_L + lw * 512, 32, tag); peek_check(ws, pk + 256, F_L + lw * 512 + 256, 32, tag);
    bar();
    if (blockIdx.x == 0 && lw == 3) signal<1>(ws, F_EP, tag);
    if (lw == 2) issq();
    if (lw == 3) {
      const rsrc_t rs = mkrsrc(ws + WS_ST + rb * 2048, 2048);
      dma16s(rs, lb + STT, lane * 16, 0); dma16s(rs, lb + STT + 1024, lane * 16, 1024);
      dma4k(mkrsrc(ws + WS_CS + head * 4096, 4096), lb + CSX + 1024, lane * 16, 0);
    }
    const bool us = hasU && lw < 3;
    if (us) issue(U, 0); else if (!hasU) kv8(0, 0);
    wait_vm<16>(); bar();
    if (lw == 3 && hasU) wait_vm<0>(); else wait_vm<8>();
    bar();
    if (us) issue(U, 1); else if (!hasU) kv8(1, 1);
    if (lw == 3) {
      const LAS f32x2* cp = (const LAS f32x2*)(lds + CSX + 1024) + lane;
      *(LAS f32x2*)(lds + CSX + 512 + lane * 8) = ((cp[0] + cp[64]) + cp[128]) + ((cp[256] + cp[320]) + cp[384]);
      wait_lgkm0();
    }
    bar();
    wait_vm<8>(); bar();
    if (us) issue(U, 2); else if (!hasU) kv8(2, 2);
    wait_vm<8>();
    bar();
    if (us) issue(U, 3);
    bar();
    if (hasU) {
      if (lw < 3) { for (int t = 1; t <= 28; ++t) { issue(U, t + 3); wait_vm<16>(); bar(); } }
      else {
        for (int t = 1; t <= 20; ++t) bar();
        spin_until(ws, F_X + kvh * 128, 8, tag);
        for (int t = 21; t <= 28; ++t) bar();
      }
      wait_vm<8>(); bar();
      kv8(0, 0); wait_vm<8>(); bar();
      kv8(1, 1); bar();
      kv8(2, 2); bar();
    } else bar();
#pragma unroll
    for (int ks = 0; ks < 8; ++ks) qh[ks] = *(LAS bf16x8*)(lds + 98304 + ks * 4096 + lw * 1024 + lane * 16);
    wait_vm<8>();
  }
  wait_lgkm0();
  bar();
  STAMP(1, 6);
  unsigned pf = 0;
  Strm Y;
  {
    const int lane = lane_id();
    Att A;
#pragma unroll
    for (int ks = 0; ks < 8; ++ks) A.q[ks] = qh[ks];
#pragma unroll
    for (int d = 0; d < 4; ++d)
#pragma unroll
      for (int i = 0; i < 16; ++i) A.o[d][i] = 0.f;
#pragma unroll
    for (int i = 0; i < 16; ++i) A.negm[i] = 0.f;
    A.l = 0.f;
    const unsigned kvl = lb + lane * 16;
#pragma unroll
    for (int i = 0; i < 4; ++i) A.pre[i] = ldfrag(kvl + i * 1024);
    const bool strm = wid >= 4 && wid < 7, hw = wid >= 4;
    const rsrc_t rp2 = mkrsrc(w2, SEQ * FF * 2), rpo = mkrsrc(wo, HD * DM * 2);
    const unsigned pv = (blockIdx.x * 64 + lane) * 128;
#define PF(W, R, C) pf_line<W>(pf, wid, R, pv, (C) * 2097152u)
    const int fp = F_P + (ut * 2 + uh) * 64 + (wid & 3) * 8;
    const bool cu = !hw && hasU;
    STAMPF(0);
    att_tile<0>(A, kvl + 0 * 32768, kvl + 1 * 32768, [&](int n) { if (n == 1 && hw) kv8(3, 3); if (n == 20 && cu) { wait_vm<0>(); signal<1>(ws, fp ^ 64, tag); }
      if (n == 23) PF(0, rp2, 0); });
    if (hw) wait_vm<8>();
    bar();
    STAMPF(1);
    att_tile<1>(A, kvl + 1 * 32768, kvl + 2 * 32768, [&](int n) {
      if (n == 1 && hw) kv8(4, 0); if (n == 28 && hw) peek(rf, lb + 131072 + lw * 256, fp); if (n == 3) PF(1, rp2, 1); if (n == 10) PF(2, rp2, 2); });
    if (hw) wait_vm<9>();
    bar();
    STAMPF(2);
    att_tile<1>(A, kvl + 2 * 32768, kvl + 3 * 32768, [&](int n) {
      if (n == 8 && hw) {
        wait_vm<0>();
        if (hasU) peek_check(ws, lb + 131072 + lw * 256, fp, 1, tag);
        const rsrc_t rux = mkrsrc(ws + WS_UX + (size_t)(ut * 2 + uh) * 32768, hasU ? 32768 : 0);
        dma4k(rux, lb + 32768 + lw * 8192, lane * 16, lw * 8192); dma4k(rux, lb + 32768 + lw * 8192 + 4096, lane * 16, lw * 8192 + 4096);
      }
      if (n == 13) PF(3, rp2, 3); if (n == 21) PF(0, rp2, 4); if (n == 29) PF(1, rp2, 5); });
    if (hw) wait_vm<0>();
    bar();
    if (hw) { kv8(5, 4); kv8(7, 2); }
    STAMPF(3);
    if (cu) {
      u32x4 p[8];
      p[0] = ldsr(kvl + 32768 + wid * 8192);
#pragma unroll
      for (int k = 0; k < 8; ++k) {
        if (k < 7) p[k + 1] = ldsr(kvl + 32768 + wid * 8192 + (k + 1) * 1024);
        f32x4 a = uk[k >> 1][2 * (k & 1)], b = uk[k >> 1][2 * (k & 1) + 1];
        add8(a, b, p[k]);
        p[k] = (u32x4){gelu2(a.xy), gelu2(a.zw), gelu2(b.xy), gelu2(b.zw)};
        asm volatile("" : "+v"(p[k]));
        SB0();
      }
      bar();
      const rsrc_t ru = mkrsrc(ws + WS_U + (size_t)((2 * dq + wi) * 112 + 4 * ft + 2 * wj) * 8192 + uh * 1024, 16384);
#pragma unroll
      for (int k = 0; k < 8; ++k) st16s<0>(p[k], ru, lane * 16, (k & 1) * 8192 + (k >> 1) * 2048);
    } else bar();
    STAMPF(4);
    att_tile<1>(A, kvl + 3 * 32768, kvl + 0 * 32768, [&](int n) {
      if (n == 20 && cu) { wait_vm<0>(); signal<1>(ws, F_U + ((2 * dq + wi) * 4 + kvh) * 256 + (((idx >> 2) * 2 + uh) * 2 + wj) * 8, tag); }
      if (n == 23) PF(2, rp2, 6); });
    if (hw) wait_vm<0>();
    bar();
    STAMPF(5);
    att_tile<1>(A, kvl + 0 * 32768, kvl + 4 * 32768, [&](int n) { if (n == 0 && hw) kv8(6, 3); if (n == 5) PF(3, rpo, 0); if (n == 18) PF(0, rpo, 1); });
    if (hw) wait_vm<0>();
    bar();
    STAMPF(6);
    att_tile<1>(A, kvl + 4 * 32768, kvl + 3 * 32768, [&](int n) { if (n == 1) PF(1, rpo, 2); if (n == 13) PF(2, rpo, 3); });
    if (lw == 0) strm_x(Y, ws + WS_U + (size_t)(dc * 112 + 28 * kvh) * 8192, 28 * 8192, 8192, 0);
    else strm_w(Y, (const char*)w2 + (size_t)kvh * 896 * (SEQ * 2) + rb * 512 + ((lw - 1) & 1) * 256, 896, SEQ * 2, false);
    bar();
    STAMPF(7);
    att_tile<1>(A, kvl + 3 * 32768, kvl + 2 * 32768, [&](int n) {
      if (n == 4 && wid == 4) peek(rf, lb + 49152, F_U + (dc * 4 + kvh) * 256);
      if (n == 18 && wid == 4) { wait_vm<0>(); peek_check(ws, lb + 49152, F_U + (dc * 4 + kvh) * 256, 28, tag); }
      if (n == 19 && strm) issue(Y, 0); if (n == 27 && strm) issue(Y, 1); });
    bar();
    STAMPF(8);
    att_tile<2>(A, kvl + 2 * 32768, 0, [&](int n) {
      if (n == 9) { if (hw) wait_vm<8>(); bar(); }
      if (n == 10 && strm) issue(Y, 2); });
    STAMP(1, 7);
    STAMPF(9);
    if (__builtin_expect(__any(!(A.l <= 1.0e9f)), 0)) att_redo(A, rkv, lane * 16);
    auto sw = __builtin_amdgcn_permlane32_swap(__float_as_uint(A.l), __float_as_uint(A.l), false, false);
    const float inv = __builtin_amdgcn_rcpf(__uint_as_float(sw[0]) + __uint_as_float(sw[1]));
    const unsigned oa = lb + PARK + wid * 2048 + ((lane >> 4) & 1) * 1024 + (lane >> 5) * 256 + (lane & 15) * 16;
#pragma unroll
    for (int d = 0; d < 4; ++d)
#pragma unroll
      for (int u = 0; u < 2; ++u) {
        const int r0 = 4 * u, r1 = 8 + 4 * u;
        *(LAS u32x4*)(size_t)(oa + d * 16384 + u * 512) =
            (u32x4){cvtpk(A.o[d][r0] * inv, A.o[d][r0 + 1] * inv), cvtpk(A.o[d][r0 + 2] * inv, A.o[d][r0 + 3] * inv),
                    cvtpk(A.o[d][r1] * inv, A.o[d][r1 + 1] * inv), cvtpk(A.o[d][r1 + 2] * inv, A.o[d][r1 + 3] * inv)};
      }
    wait_lgkm0();
  }
  STAMP(1, 8);
  if (wid < 4) {
    f32x4 acc[8][4];
    Frags F0, F1;
    Addr a;
    int slot = 1;
    addr_init(a, lb, lb + 8192 + wi * 8192, wj, kvh, lane_id());
    load_frags(F0, a);
    addr_add(a, QS);
    wait_lgkm0();
    bar();
    STAMP(1, 9);
    gstep<false, 4, QS, true>(acc, F1, F0, a, slot);
    gemm_steps<false, 4, QS>(acc, F0, F1, a, slot, 26);
    addr_init(a, lb + wi * 8192, lb + 16384, wj, 0, lane_id());
    gstep<false, 4, QS>(acc, F0, F1, a, slot);
    STAMP(1, 10);
    asm volatile("s_waitcnt vmcnt(0)" : "+v"(pf)::"memory");
    ystep_park(acc, F1, F0, a, slot, lb + PARK + lane_id() * 16 + wid * 4096);
    STAMP(1, 11);
    gemm_steps<true, 4, QS>(acc, F0, F1, a, slot, 30);
    gstep<true, 4, QS>(acc, F0, F1, a, slot);
    STAMP(1, 12);
    const int lt = lane_id(), g = lt >> 4;
    const unsigned wo_ = wi * 8192 + wj * 2048;
#pragma unroll
    for (int dl = 1; dl < 4; ++dl)
#pragma unroll
      for (int h = 0; h < 2; ++h)
#pragma unroll
        for (int mm = 0; mm < 2; ++mm)
          st16s<0>(pack8(acc[4 * h + dl][2 * mm], acc[4 * h + dl][2 * mm + 1]), rxb, lt * 16, (kvh ^ dl) * 98304 + 49152 + (dl - 1) * 16384 + wo_ + h * 4096 + mm * 1024);
    wait_vm<0>();
    signal<14>(ws, F_S + (tile * 4 + (kvh ^ (lt & 3))) * 128 + wid * 32 + ((lt & 3) - 1) * 8, tag);
    STAMP(1, 13);
    const int srow = rb * 256 + (8 * wi + kvh) * 16;
    const rsrc_t rx = mkrsrc(x + (size_t)srow * DM, 80 * DM * 2), rout = mkrsrc(out + (size_t)srow * DM, 80 * DM * 2);
    const unsigned vo = ((lt & 15) * DM + dc * 128 + wj * 64 + 16 * (g & 1) + 8 * (g >> 1)) * 2;
    u32x4 ps[3][4], xv[4], gv[4];
#pragma unroll
    for (int p = 0; p < 4; ++p) xv[p] = __builtin_amdgcn_raw_buffer_load_b128(rx, vo + (p >> 1) * (64 * DM * 2) + (p & 1) * 64, 0, 0);
    bar();
#pragma unroll
    for (int p = 0; p < 4; ++p) gv[p] = ldsr(lb + PARK + lt * 16 + wo_ + (p >> 1) * 4096 + (p & 1) * 1024);
    spin_until(ws, F_S + (tile * 4 + kvh) * 128 + wid * 32, 3, tag);
    STAMP(1, 14);
#pragma unroll
    for (int p = 0; p < 4; ++p)
#pragma unroll
      for (int k = 0; k < 3; ++k)
        ps[k][p] = __builtin_amdgcn_raw_buffer_load_b128(rxb, lt * 16 + (p & 1) * 1024, kvh * 98304 + 49152 + k * 16384 + wo_ + (p >> 1) * 4096, 16);
#pragma unroll
    for (int p = 0; p < 4; ++p) {
      f32x4 s0 = acc[4 * (p >> 1)][2 * (p & 1)], s1 = acc[4 * (p >> 1)][2 * (p & 1) + 1];
#pragma unroll
      for (int k = 0; k < 3; ++k) add8(s0, s1, ps[k][p]);
      s0 *= (f32x4){bflo(gv[p][0]), bfhi(gv[p][0]), bflo(gv[p][1]), bfhi(gv[p][1])};
      s1 *= (f32x4){bflo(gv[p][2]), bfhi(gv[p][2]), bflo(gv[p][3]), bfhi(gv[p][3])};
      const u32x4 tp = pack8(s0, s1);
      const auto w0 = __builtin_amdgcn_permlane16_swap(tp[0], tp[2], false, false), w1 = __builtin_amdgcn_permlane16_swap(tp[1], tp[3], false, false);
      const unsigned t[4] = {w0[0], w1[0], w0[1], w1[1]};
      u32x4 o;
#pragma unroll
      for (int e = 0; e < 4; ++e) o[e] = cvtpk(bflo(xv[p][e]) + bflo(t[e]), bfhi(xv[p][e]) + bfhi(t[e]));
      __builtin_amdgcn_raw_buffer_store_b128(o, rout, vo + (p >> 1) * (64 * DM * 2) + (p & 1) * 64, 0, 0);
    }
    STAMP(1, 15);
  } else {
    const int lane = lane_id();
    const unsigned pl = lb + PARK + lane * 16, pk = lb + 159744 + lw * 256;
    const int fo = F_O + (rb * 4 + kvh) * 128;
    if (lw < 3) {
      Strm S;
      if (lw < 2) strm_x(S, ws + WS_O + (size_t)(rb * 128 + 32 * kvh) * 16384 + lw * 8192, 524288 - lw * 8192, 16384, kvh);
      else strm_w(S, (const char*)wo + (size_t)kvh * 1024 * (DM * 2) + dc * 256, 1024, DM * 2, false);
      wait_vm<8>();
      bar();
      issue(Y, 3);
      for (int t = 1; t <= 22; ++t) { issue(Y, t + 3); wait_vm<16>(); bar(); }
      peek(rf, pk, fo);
      issue(Y, 26); wait_vm<16>(); bar();
      issue(Y, 27); wait_vm<16>(); bar();
      peek_check(ws, pk, fo, 8, tag);
      for (int t = 25; t <= 56; ++t) { issue(S, t - 25); wait_vm<16>(); bar(); }
      wait_vm<8>(); bar();
      wait_vm<0>(); bar();
    } else {
      bar();
      const rsrc_t ro = mkrsrc(ws + WS_O + (size_t)(rb * 128 + 4 * head) * 16384, 65536);
      for (int t = 1; t <= 16; ++t) {
        const unsigned o = (t - 1) * 4096;
        const u32x4 v0 = ldsr(pl + o), v1 = ldsr(pl + o + 1024), v2 = ldsr(pl + o + 2048), v3 = ldsr(pl + o + 3072);
        st16s<0>(v0, ro, lane * 16, o); st16s<1024>(v1, ro, lane * 16, o);
        st16s<2048>(v2, ro, lane * 16, o); st16s<3072>(v3, ro, lane * 16, o);
        bar();
      }
      bar(); bar();
      wait_vm<0>();
      signal<1>(ws, fo + dc * 8, tag);
      for (int t = 19; t <= 29; ++t) bar();
      for (int t = 30; t <= 45; ++t) {
        const unsigned o = (t - 30) * 1024;
        const u32x4 v0 = ldsr(pl + 16384 + o), v1 = ldsr(pl + 32768 + o), v2 = ldsr(pl + 49152 + o);
        st16s<0>(v0, rxb, lane * 16, (kvh ^ 1) * 98304 + o);
        st16s<0>(v1, rxb, lane * 16, (kvh ^ 2) * 98304 + 16384 + o);
        st16s<0>(v2, rxb, lane * 16, (kvh ^ 3) * 98304 + 32768 + o);
        bar();
      }
      for (int t = 46; t <= 48; ++t) bar();
      wait_vm<0>();
      signal<14>(ws, F_Y + (tile * 4 + (kvh ^ (lane & 3))) * 128 + ((lane & 3) - 1) * 8, tag);
      for (int t = 49; t <= 56; ++t) bar();
      peek(rf, pk, F_Y + (tile * 4 + kvh) * 128);
      bar();
      wait_vm<0>();
      peek_check(ws, pk, F_Y + (tile * 4 + kvh) * 128, 3, tag);
      bar();
    }
    u32x4 py[3][4];
#pragma unroll
    for (int k = 0; k < 3; ++k)
#pragma unroll
      for (int m = 0; m < 4; ++m) py[k][m] = __builtin_amdgcn_raw_buffer_load_b128(rxb, lane * 16 + m * 1024, kvh * 98304 + lw * 4096 + k * 16384, 16);
    const float bb = bflo(b2[rb * 256 + (4 * lw + kvh) * 16 + (lane & 15)]);
    bar();
    bar();
#pragma unroll
    for (int m = 0; m < 4; ++m) {
      f32x4 y0 = {bb, bb, bb, bb}, y1 = y0;
      add8(y0, y1, ldsr(pl + lw * 4096 + m * 1024));
#pragma unroll
      for (int k = 0; k < 3; ++k) add8(y0, y1, py[k][m]);
      *(LAS u32x4*)(size_t)(pl + lw * 4096 + m * 1024) = (u32x4){silu2(y0.xy), silu2(y0.zw), silu2(y1.xy), silu2(y1.zw)};
    }
    wait_lgkm0();
    bar();
  }
}

extern "C" void kernel_launch(void* const* d_in, const int* in_sizes, int n_in, void* d_out, int out_size, void* d_ws, size_t ws_size,
                              hipStream_t stream) {
  const u16* x = (const u16*)d_in[0];
  const u16* kc = (const u16*)d_in[1];
  const u16* vc = (const u16*)d_in[2];
  const u16* wq = (const u16*)d_in[3];
  const u16* wo = (const u16*)d_in[4];
  const u16* w1 = (const u16*)d_in[6];
  const u16* b1 = (const u16*)d_in[7];
  const u16* w2 = (const u16*)d_in[8];
  const u16* b2 = (const u16*)d_in[9];
  char* ws = (char*)d_ws;
  (void)hipFuncSetAttribute((const void*)k_main, hipFuncAttributeMaxDynamicSharedMemorySize, 163840);
  hipLaunchKernelGGL(k_main, dim3(256), dim3(512), 163840, stream, x, kc, vc, wq, wo, w1, b1, w2, b2, ws, (u16*)d_out);
}
```

```cpp
#include <hip/hip_runtime.h>
#include <hip/hip_bf16.h>

#define PFB 1835008
#define UROT 8
#define NSP 6
#define PFS 4
#define SPARE_T 1150
typedef unsigned short u16;
typedef short bf16x8 __attribute__((ext_vector_type(8)));
typedef short s16x4 __attribute__((ext_vector_type(4)));
typedef float f32x16 __attribute__((ext_vector_type(16)));
typedef float f32x4 __attribute__((ext_vector_type(4)));
typedef float f32x2 __attribute__((ext_vector_type(2)));
typedef unsigned u32x4 __attribute__((ext_vector_type(4)));
typedef unsigned u32x2 __attribute__((ext_vector_type(2)));
typedef __bf16 bf16x2 __attribute__((ext_vector_type(2)));
typedef __amdgpu_buffer_rsrc_t rsrc_t;
#define LAS __attribute__((address_space(3)))
#define GAS __attribute__((address_space(1)))
#define DI __device__ __forceinline__
#define SGB(m, n) __builtin_amdgcn_sched_group_barrier(m, n, 0)
#define SB0() __builtin_amdgcn_sched_barrier(0)

constexpr int SEQ = 2048, DM = 1024, FF = 3584, HD = 4096;
constexpr size_t WS_FLAG = 0;
constexpr int F_EP = 0, F_X = 512, F_L = 2048, F_U = 4096, F_O = 12288, F_Y = 16384, F_S = 49152, F_P = 81920, F_END = 96256;
constexpr size_t WS_ST = 98304;
constexpr size_t WS_CS = 131072;
constexpr size_t WS_HT = 262144;
constexpr size_t WS_KV = WS_HT + 4194304;
constexpr size_t WS_O = WS_KV + 1048576;
constexpr size_t WS_U = WS_O + 16777216;
constexpr size_t WS_XB = WS_U + 7340032;
constexpr size_t WS_UX = WS_XB + 25165824;
constexpr size_t WS_END = WS_UX + 7340032;

#define STAMP(kid, i) do { if ((kid) == 1 && (i) == 0) { dgA = __builtin_amdgcn_s_memrealtime(); __builtin_amdgcn_s_waitcnt(0xC07F); } if ((kid) == 1 && (i) == 0) { dgB = __builtin_amdgcn_s_memrealtime(); __builtin_amdgcn_s_waitcnt(0xC07F); } if ((kid) == 1 && (i) == 15 && blockIdx.x == 3 && wid == 0) { const unsigned long long e = dg0 + 10000 + (dgB - dgA) * 0; for (int z = 0; z < 100000 && __builtin_amdgcn_s_memrealtime() < e; ++z) __builtin_amdgcn_s_sleep(1); } } while (0)
#define STAMPF(i)
DI unsigned cvtpk(float lo, float hi) { f32x2 v = {lo, hi}; bf16x2 b = __builtin_convertvector(v, bf16x2); return __builtin_bit_cast(unsigned, b); }
DI float bflo(unsigned u) { return __uint_as_float(u << 16); }
DI float bfhi(unsigned u) { return __uint_as_float(u & 0xffff0000u); }
DI float rbf(float x) { return bflo(cvtpk(x, x)); }
template <int N> DI void wait_vm() { asm volatile("s_waitcnt vmcnt(%0)" ::"i"(N) : "memory"); }
DI void wait_lgkm0() { __builtin_amdgcn_s_waitcnt(0xC07F); }
DI void bar() { asm volatile("" ::: "memory"); __builtin_amdgcn_s_barrier(); asm volatile("" ::: "memory"); }
DI rsrc_t mkrsrc(const void* p, unsigned bytes) { return __builtin_amdgcn_make_buffer_rsrc((void*)p, 0, bytes, 0x00020000); }
DI void dma16(rsrc_t r, unsigned lds_addr, unsigned voff, unsigned soff) {
  asm volatile("s_mov_b32 m0, %0\n\ts_nop 0\n\tbuffer_load_dwordx4 %1, %2, %3 offen lds" ::"s"(lds_addr), "v"(voff), "s"(r), "s"(soff) : "memory");
}
DI void dma16s(rsrc_t r, unsigned lds_addr, unsigned voff, unsigned soff) {
  asm volatile("s_mov_b32 m0, %0\n\ts_nop 0\n\tbuffer_load_dwordx4 %1, %2, %3 offen sc1 lds" ::"s"(lds_addr), "v"(voff), "s"(r), "s"(soff) : "memory");
}
template <int OFF> DI void st16s(u32x4 v, rsrc_t r, unsigned voff, unsigned soff) {
  asm volatile("buffer_store_dwordx4 %0, %1, %2, %3 offen offset:%4 sc1\n\ts_nop 1" ::"v"(v), "v"(voff), "s"(r), "s"(soff), "i"(OFF) : "memory");
}
DI int lane_id() { int l; asm volatile("v_mbcnt_lo_u32_b32 %0, -1, 0\n\tv_mbcnt_hi_u32_b32 %0, -1, %0" : "=v"(l)); return l; }
struct Tag { unsigned lo, hi; };
DI void peek(rsrc_t rf, unsigned lds_addr, int off) {
  asm volatile("s_mov_b32 m0, %0\n\ts_nop 0\n\tbuffer_load_dword %3, %1, %2 offen sc1 lds" ::"s"(lds_addr), "s"(rf), "s"(off), "v"(lane_id() * 4) : "memory");
}
template <int GAP = 1> DI void spin_until(char* ws, int off, int n, Tag t) {
  const int ln = lane_id();
  GAS unsigned long long* p = (GAS unsigned long long*)(ws + WS_FLAG + off) + (ln < n ? ln : 0);
  const unsigned long long want = ((unsigned long long)t.hi << 32) | t.lo;
  for (unsigned sp = 0; sp < (1u << 20); ++sp) {
    if (__all(__hip_atomic_load(p, __ATOMIC_RELAXED, __HIP_MEMORY_SCOPE_AGENT) == want)) break;
    __builtin_amdgcn_s_sleep(GAP);
  }
  asm volatile("" ::: "memory");
}
DI void peek_check(char* ws, unsigned lds_addr, int off, int n, Tag t) {
  const int ln = lane_id();
  const unsigned v = *(LAS volatile unsigned*)(size_t)(lds_addr + ln * 4);
  if (!__all(ln >= 2 * n || v == ((ln & 1) ? t.hi : t.lo))) spin_until(ws, off, n, t);
}
template <long MASK> DI void signal(char* ws, int off, Tag t) {
  const u32x2 v = {t.lo, t.hi};
  asm volatile("s_mov_b64 exec, %3\n\tglobal_store_dwordx2 %0, %1, %2 sc1\n\ts_mov_b64 exec, -1" ::"v"(off), "v"(v), "s"(ws + WS_FLAG), "s"(MASK) : "memory");
}
DI void dma4k(rsrc_t r, unsigned lds_addr, unsigned voff, unsigned soff) {
  asm volatile("s_mov_b32 m0, %0\n\ts_nop 0\n\tbuffer_load_dwordx4 %1, %2, %3 offen sc1 lds\n\tbuffer_load_dwordx4 %1, %2, %3 offen offset:1024 sc1 lds\n\t"
               "buffer_load_dwordx4 %1, %2, %3 offen offset:2048 sc1 lds\n\tbuffer_load_dwordx4 %1, %2, %3 offen offset:3072 sc1 lds"
               ::"s"(lds_addr), "v"(voff), "s"(r), "s"(soff) : "memory");
}
template <int W> DI void pf_line(unsigned& sink, int wid, rsrc_t r, unsigned voff, unsigned soff) {
  asm volatile("s_cmp_lg_u32 %1, %5\n\ts_cbranch_scc1 1f\n\tbuffer_load_dword %0, %2, %3, %4 offen\n1:"
               : "+v"(sink) : "s"(wid), "v"(voff), "s"(r), "s"(soff), "i"(W) : "scc", "memory");
}
template <int CTRL> DI float dppf(float x) { return __builtin_bit_cast(float, __builtin_amdgcn_mov_dpp(__builtin_bit_cast(int, x), CTRL, 0xf, 0xf, true)); }
DI float wave_sum(float v) {
  v += dppf<0xB1>(v); v += dppf<0x4E>(v); v += dppf<0x141>(v); v += dppf<0x140>(v);
  auto a = __builtin_amdgcn_permlane16_swap(__float_as_uint(v), __float_as_uint(v), false, false);
  v = __uint_as_float(a[0]) + __uint_as_float(a[1]);
  auto b = __builtin_amdgcn_permlane32_swap(__float_as_uint(v), __float_as_uint(v), false, false);
  return __uint_as_float(b[0]) + __uint_as_float(b[1]);
}

DI f32x2 ln16(unsigned (&w)[8]) {
  f32x2 v[8];
#pragma unroll
  for (int i = 0; i < 8; ++i) v[i] = (f32x2){bflo(w[i]), bfhi(w[i])};
  const f32x2 s = ((v[0] + v[1]) + (v[2] + v[3])) + ((v[4] + v[5]) + (v[6] + v[7]));
  const float mean = wave_sum(s[0] + s[1]) * (1.f / 1024.f);
  f32x2 q0 = {0.f, 0.f}, q1 = q0;
#pragma unroll
  for (int i = 0; i < 8; i += 2) {
    const f32x2 d0 = v[i] - mean, d1 = v[i + 1] - mean;
    q0 = __builtin_elementwise_fma(d0, d0, q0); q1 = __builtin_elementwise_fma(d1, d1, q1);
  }
  q0 += q1;
  const float var = wave_sum(q0[0] + q0[1]) * (1.f / 1024.f);
  const float mb = rbf(mean);
  const float rs = __builtin_amdgcn_rcpf(rbf(__builtin_amdgcn_sqrtf(rbf(rbf(var) + 1e-5f))));
#pragma unroll
  for (int i = 0; i < 8; ++i) {
    const f32x2 d = v[i] - mb;
    const unsigned c = cvtpk(d[0], d[1]);
    const f32x2 r = (f32x2){bflo(c), bfhi(c)} * rs;
    w[i] = cvtpk(r[0], r[1]);
  }
  return (f32x2){mb, rs};
}

struct Frags { bf16x8 x[8]; bf16x8 w[4]; };
struct Addr { unsigned x, w[4]; };
template <int XS = 1024> DI void load_frags(Frags& f, const Addr& a) {
#pragma unroll
  for (int c = 0; c < 4; ++c) {
    s16x4 a0 = __builtin_amdgcn_ds_read_tr16_b64_v4i16((LAS s16x4*)(size_t)(a.w[c]));
    s16x4 a1 = __builtin_amdgcn_ds_read_tr16_b64_v4i16((LAS s16x4*)(size_t)(a.w[c] + 4096));
    f.w[c] = __builtin_shufflevector(a0, a1, 0, 1, 2, 3, 4, 5, 6, 7);
  }
#pragma unroll
  for (int i = 0; i < 8; ++i) f.x[i] = *(LAS bf16x8*)(size_t)(a.x + i * XS);
}
template <bool WA, bool Z> DI void mma_all(f32x4 (&acc)[8][4], const Frags& f, const f32x4* c0) {
  const f32x4 zero = {0.f, 0.f, 0.f, 0.f};
#pragma unroll
  for (int i = 0; i < 8; ++i)
#pragma unroll
    for (int jj = 0; jj < 4; ++jj) {
      const int j = (i & 1) ? 3 - jj : jj;
      if (WA) acc[i][j] = __builtin_amdgcn_mfma_f32_16x16x32_bf16(f.w[j], f.x[i], Z ? (c0 ? c0[j] : zero) : acc[i][j], 0, 0, 0);
      else    acc[i][j] = __builtin_amdgcn_mfma_f32_16x16x32_bf16(f.x[i], f.w[j], Z ? (c0 ? c0[j] : zero) : acc[i][j], 0, 0, 0);
    }
}
DI void addr_add(Addr& a, int d) {
  a.x += d;
#pragma unroll
  for (int c = 0; c < 4; ++c) a.w[c] += d;
}
template <bool WA, int NSLOT, int SLOTB, bool Z = false>
DI void gstep(f32x4 (&acc)[8][4], Frags& Fn, const Frags& Fc, Addr& a, int& slot, const f32x4* c0 = nullptr) {
  SB0();
  load_frags(Fn, a);
  mma_all<WA, Z>(acc, Fc, c0);
  slot = (slot + 1 == NSLOT) ? 0 : slot + 1;
  addr_add(a, (slot == 0) ? -(NSLOT - 1) * SLOTB : SLOTB);
  asm volatile("" : "+v"(a.x), "+v"(a.w[0]), "+v"(a.w[1]), "+v"(a.w[2]), "+v"(a.w[3]));
#pragma unroll
  for (int i = 0; i < 8; ++i) { SGB(0x8, 1); SGB(0x100, 1); SGB(0x8, 1); SGB(0x100, 1); SGB(0x8, 1); }
#pragma unroll
  for (int i = 0; i < 6; ++i) { SGB(0x8, 1); SGB(0x4, 1); SGB(0x2, 1); }
  SGB(0x8, 2);
  SB0();
  wait_lgkm0();
  bar();
}
template <int MODE, bool CS, bool Z = false>
DI void qstep(f32x4 (&acc)[8][4], f32x4 (&cs)[2], u32x4& ones, unsigned onext, Frags& Fn, const Frags& Fc, Addr& a, int& xs, int& wsl) {
  const f32x4 zero = {0.f, 0.f, 0.f, 0.f};
  SB0();
  load_frags<MODE == 2 ? 1024 : 2048>(Fn, a);
  if (CS) {
#pragma unroll
    for (int j = 0; j < 2; ++j) cs[j] = __builtin_amdgcn_mfma_f32_16x16x32_bf16(Fc.w[j], __builtin_bit_cast(bf16x8, ones), Z ? zero : cs[j], 0, 0, 0);
    ones = (u32x4){onext, onext, onext, onext};
    asm volatile("" : "+v"(ones));
  }
  mma_all<true, Z>(acc, Fc, nullptr);
  if (MODE == 2) addr_add(a, 24576);
  else {
    wsl = (wsl + 1) & 3;
    const int dw = wsl ? 8192 : -24576;
#pragma unroll
    for (int c = 0; c < 4; ++c) a.w[c] += dw;
    a.x ^= 64;
    if (MODE == 1) { xs = (xs == 2) ? 0 : xs + 1; a.x += xs ? 32768 : -65536; }
  }
  asm volatile("" : "+v"(a.x), "+v"(a.w[0]), "+v"(a.w[1]), "+v"(a.w[2]), "+v"(a.w[3]));
#pragma unroll
  for (int i = 0; i < 8; ++i) { SGB(0x8, 1); SGB(0x100, 1); SGB(0x8, 1); SGB(0x100, 1); SGB(0x8, 1); }
#pragma unroll
  for (int i = 0; i < 7; ++i) { SGB(0x8, 1); SGB(0x4, 1); SGB(0x2, 1); }
  if (CS) { SGB(0x8, 1); SGB(0x2, 2); SGB(0x8, 1); SGB(0x2, 2); }
  SGB(0x8, 1);
  SB0();
  wait_lgkm0();
  bar();
}
DI void addr_init(Addr& a, unsigned xbase, unsigned wbase, int wj, int cx, int lane) {
  const int g = lane >> 4, q = (lane >> 2) & 3, p = lane & 3, row = 4 * g + q;
  a.x = xbase + lane * 16;
#pragma unroll
  for (int c = 0; c < 4; ++c) a.w[c] = wbase + row * 256 + (((2 * (4 * wj + (c ^ cx)) + (p >> 1)) ^ ((row & 7) << 1)) * 16) + (p & 1) * 8;
}
template <bool WA, int NSLOT, int SLOTB> DI void gemm_steps(f32x4 (&acc)[8][4], Frags& F0, Frags& F1, Addr& a, int& slot, int n) {
  for (int t = 0; t < n; t += 2) {
    gstep<WA, NSLOT, SLOTB>(acc, F0, F1, a, slot);
    gstep<WA, NSLOT, SLOTB>(acc, F1, F0, a, slot);
  }
}

DI u32x4 pack8(const f32x4& a, const f32x4& b) { return (u32x4){cvtpk(a[0], a[1]), cvtpk(a[2], a[3]), cvtpk(b[0], b[1]), cvtpk(b[2], b[3])}; }
DI void ystep_park(f32x4 (&acc)[8][4], Frags& Fn, const Frags& Fc, Addr& a, int& slot, unsigned pm) {
  s16x4 h[8];
  SB0();
#pragma unroll
  for (int k = 0; k < 16; ++k) {
    const int m = k >> 2, j = k & 3;
    *(LAS u32x4*)(size_t)(pm + j * 16384 + m * 1024) = pack8(acc[2 * m][j], acc[2 * m + 1][j]);
    asm volatile("v_mfma_f32_16x16x32_bf16 %0, %1, %2, 0" : "+v"(acc[2 * m][j]) : "v"(Fc.w[j]), "v"(Fc.x[2 * m]));
    if (k < 8) h[k] = __builtin_amdgcn_ds_read_tr16_b64_v4i16((LAS s16x4*)(size_t)(a.w[k >> 1] + (k & 1) * 4096));
    else Fn.x[k - 8] = *(LAS bf16x8*)(size_t)(a.x + (k - 8) * 1024);
    asm volatile("v_mfma_f32_16x16x32_bf16 %0, %1, %2, 0" : "+v"(acc[2 * m + 1][j]) : "v"(Fc.w[j]), "v"(Fc.x[2 * m + 1]));
    SB0();
  }
#pragma unroll
  for (int c = 0; c < 4; ++c) Fn.w[c] = __builtin_shufflevector(h[2 * c], h[2 * c + 1], 0, 1, 2, 3, 4, 5, 6, 7);
  slot = 2;
  addr_add(a, 24576);
  wait_lgkm0();
  bar();
}
DI f32x2 gate2(f32x2 u, f32x2 z) {
  f32x2 e = {__builtin_amdgcn_exp2f(z[0]), __builtin_amdgcn_exp2f(z[1])};
  e += 1.f;
  return u * (f32x2){__builtin_amdgcn_rcpf(e[0]), __builtin_amdgcn_rcpf(e[1])};
}
DI unsigned gelu2(f32x2 u) {
  const f32x2 r = gate2(u, u * __builtin_elementwise_fma(u * u, (f32x2){-0.1029432397f, -0.1029432397f}, (f32x2){-2.302208198f, -2.302208198f}));
  return cvtpk(r[0], r[1]);
}
DI unsigned silu2(f32x2 y) { const f32x2 r = gate2(y, y * -1.4426950409f); return cvtpk(r[0], r[1]); }
DI u32x4 ldsr(unsigned a) { return *(LAS u32x4*)(size_t)a; }
DI void add8(f32x4& a, f32x4& b, const u32x4& w) {
  a += (f32x4){bflo(w[0]), bfhi(w[0]), bflo(w[1]), bfhi(w[1])};
  b += (f32x4){bflo(w[2]), bfhi(w[2]), bflo(w[3]), bfhi(w[3])};
}

struct Att { bf16x8 q[8]; f32x16 o[4]; f32x16 negm, sE, sO; u32x4 pO[2]; bf16x8 pre[4]; float l; };
DI float vmax16(const f32x16& a) {
  float m = fmaxf(fmaxf(a[0], a[1]), a[2]);
#pragma unroll
  for (int i = 3; i < 15; i += 2) m = fmaxf(fmaxf(m, a[i]), a[i + 1]);
  m = fmaxf(m, a[15]);
  auto sw = __builtin_amdgcn_permlane32_swap(__float_as_uint(m), __float_as_uint(m), false, false);
  return fmaxf(__uint_as_float(sw[0]), __uint_as_float(sw[1]));
}
DI bf16x8 ldfrag(unsigned a) { return *(LAS bf16x8*)(size_t)a; }
DI void sm1(f32x16& s, int e, u32x4 (&w)[2], float& l) {
  if (e < 16) {
    float x = s[e];
    x = __builtin_amdgcn_exp2f(x);
    asm volatile("" : "+v"(x));
    s[e] = x;
  }
  if (e >= 1) {
    l += s[e - 1];
    asm volatile("" : "+v"(l));
    if (!(e & 1)) { unsigned c = cvtpk(s[e - 2], s[e - 1]); asm volatile("" : "+v"(c)); w[(e - 1) >> 3][((e - 1) >> 1) & 3] = c; }
  }
}
#define SETPRIO(x) __builtin_amdgcn_s_setprio(x)
#define MFMA32(a, b, c) __builtin_amdgcn_mfma_f32_32x32x16_bf16(a, b, c, 0, 0, 0)
#define PFRAG(w) __builtin_bit_cast(bf16x8, w)
template <int POS, class F> DI void att_tile(Att& A, unsigned kv, unsigned kvn, F&& gap) {
  bf16x8 f[36];
  u32x4 pE[2];
  asm volatile("" : "+v"(kv), "+v"(kvn));
#pragma unroll
  for (int i = 0; i < 4; ++i) f[i] = A.pre[i];
  SB0();
  SETPRIO(3);
#pragma unroll
  for (int n = 0; n < 8; ++n) {
    if (POS == 0) A.sE = MFMA32(f[n], A.q[n], n ? A.sE : A.negm);
    else A.o[n & 3] = MFMA32(f[n], PFRAG(A.pO[n >> 2]), A.o[n & 3]);
    f[n + 4] = ldfrag(kv + (n + 4) * 1024);
    if (POS && n <= 2) sm1(A.sO, 14 + n, A.pO, A.l);
    if (POS && n >= 2) sm1(A.sE, n - 2, pE, A.l);
    gap(n);
    SB0();
  }
  if (POS == 0) {
    const float mx = vmax16(A.sE);
#pragma unroll
    for (int i = 0; i < 16; ++i) { A.sE[i] -= mx; A.negm[i] = -mx; }
    SB0();
  }
  SETPRIO(2);
#pragma unroll
  for (int n = 8; n < 16; ++n) {
    A.sO = MFMA32(f[n], A.q[n - 8], n > 8 ? A.sO : A.negm);
    f[n + 4] = ldfrag(kv + (n + 4) * 1024);
    if (POS) sm1(A.sE, n - 2, pE, A.l);
    else if (n < 15) { sm1(A.sE, 2 * (n - 8), pE, A.l); sm1(A.sE, 2 * (n - 8) + 1, pE, A.l); }
    gap(n);
    SB0();
  }
  SETPRIO(1);
#pragma unroll
  for (int n = 16; n < 24; ++n) {
    A.o[n & 3] = MFMA32(f[n], PFRAG(pE[(n >> 2) & 1]), A.o[n & 3]);
    f[n + 4] = ldfrag(kv + (n + 4) * 1024);
    if (n <= 18) sm1(A.sE, n - 2, pE, A.l);
    if (n >= 18) {
      if (POS < 2) sm1(A.sO, n - 18, A.pO, A.l);
      else { sm1(A.sO, 2 * (n - 18), A.pO, A.l); sm1(A.sO, 2 * (n - 18) + 1, A.pO, A.l); }
    }
    gap(n);
    SB0();
  }
  SETPRIO(0);
#pragma unroll
  for (int n = 24; n < 32; ++n) {
    if (POS < 2) {
      A.sE = MFMA32(f[n], A.q[n - 24], n > 24 ? A.sE : A.negm);
      f[n + 4] = ldfrag(n < 28 ? kv + (n + 4) * 1024 : kvn + (n - 28) * 1024);
      sm1(A.sO, n - 18, A.pO, A.l);
    } else {
      A.o[n & 3] = MFMA32(f[n], PFRAG(A.pO[(n >> 2) & 1]), A.o[n & 3]);
      if (n < 28) f[n + 4] = ldfrag(kv + (n + 4) * 1024);
      if (n < 26) { sm1(A.sO, 2 * (n - 18), A.pO, A.l); sm1(A.sO, 2 * (n - 18) + 1, A.pO, A.l); }
      if (n == 26) sm1(A.sO, 16, A.pO, A.l);
    }
    gap(n);
    SB0();
  }
  if (POS < 2) {
#pragma unroll
    for (int i = 0; i < 4; ++i) A.pre[i] = f[32 + i];
  }
}
DI void att_redo(Att& A, rsrc_t rkv, unsigned vo) {
  float m = -3.0e38f, l = 0.f;
#pragma unroll
  for (int d = 0; d < 4; ++d)
#pragma unroll
    for (int i = 0; i < 16; ++i) A.o[d][i] = 0.f;
  for (int blk = 0; blk < 16; ++blk) {
    const int kb = (blk ? 2 * blk - 1 : 0) * 8192, vb = (blk < 15 ? 2 * blk + 2 : 31) * 8192;
    f32x16 s;
#pragma unroll
    for (int i = 0; i < 16; ++i) s[i] = 0.f;
#pragma unroll
    for (int n = 0; n < 8; ++n) s = MFMA32(PFRAG(__builtin_amdgcn_raw_buffer_load_b128(rkv, vo + n * 1024, kb, 16)), A.q[n], s);
    const float mn = fmaxf(m, vmax16(s)), fct = __builtin_amdgcn_exp2f(m - mn);
    m = mn;
    l *= fct;
    u32x4 p[2];
#pragma unroll
    for (int i = 0; i < 16; ++i) s[i] -= mn;
#pragma unroll
    for (int i = 0; i < 17; ++i) sm1(s, i, p, l);
#pragma unroll
    for (int d = 0; d < 4; ++d)
#pragma unroll
      for (int i = 0; i < 16; ++i) A.o[d][i] *= fct;
#pragma unroll
    for (int n = 0; n < 8; ++n) A.o[n & 3] = MFMA32(PFRAG(__builtin_amdgcn_raw_buffer_load_b128(rkv, vo + n * 1024, vb, 16)), PFRAG(p[n >> 2]), A.o[n & 3]);
  }
  A.l = l;
}

__global__ __launch_bounds__(512, 2) void k_main(const u16* __restrict__ x, const u16* __restrict__ kc, const u16* __restrict__ vc, const u16* __restrict__ wq, const u16* __restrict__ wo,
                                                 const u16* __restrict__ w1, const u16* __restrict__ b1, const u16* __restrict__ w2,
                                                 const u16* __restrict__ b2, char* __restrict__ ws, u16* __restrict__ out) {
  extern __shared__ __attribute__((aligned(16))) char smem[];
  LAS char* lds = (LAS char*)smem;
  const unsigned lb = (unsigned)(size_t)lds;
  const int tid = threadIdx.x, wid = __builtin_amdgcn_readfirstlane(tid >> 6);
  const int g8 = blockIdx.x & 7, idx = blockIdx.x >> 3;
  unsigned long long dgA = 0, dgB = 0, dg0 = __builtin_amdgcn_s_memrealtime();
  STAMP(1, 0);
  STAMP(0, 0);
  const bool hasU = idx < 28;
  const int uh = g8 & 1, dq = idx & 3, ft = 7 * (g8 >> 1) + (idx >> 2), ut = ft * 4 + dq;
  const int kvh = g8 >> 1, head = 8 * kvh + (idx & 7), rb = 4 * (g8 & 1) + (idx >> 3);
  const int dc = idx & 7, tile = rb * 8 + dc;
  const int lw = wid - 4, wi = (wid >> 1) & 1, wj = wid & 1;
  constexpr int QS = 24576;
  constexpr int PARK = 98304;
  const rsrc_t rf = mkrsrc(ws + WS_FLAG, F_END + 256);
  const rsrc_t rkv = mkrsrc(ws + WS_KV + (size_t)kvh * 262144, 262144);
  const rsrc_t rxb = mkrsrc(ws + WS_XB + (size_t)tile * 393216, 393216);
  struct Strm { rsrc_t r; unsigned v0, v1, step, p0, p1, p2; int rot; };
  auto strm_x = [&](Strm& S, const char* base, unsigned bytes, unsigned step, int xr) {
    const int ln = lane_id();
    S.r = mkrsrc(base, bytes); S.v0 = ln * 16 + (xr & 1) * 1024; S.v1 = S.v0 ^ 1024; S.step = step;
    S.p0 = (xr >> 1) * 2048; S.p1 = S.p0 ^ 2048; S.p2 = 4096; S.rot = 0;
  };
  auto strm_w = [&](Strm& S, const char* base, unsigned rows, unsigned ld, bool nat) {
    const int ln = lane_id(), q4 = ln >> 4;
    const unsigned c0 = ((ln & 15) ^ (q4 << 1)) * 16;
    S.r = mkrsrc(base, (rows - 1) * ld + 256); S.v0 = q4 * ld + c0; S.v1 = ((nat ? 8 : 4) + q4) * ld + (c0 ^ 128); S.step = 32 * ld;
    S.p0 = 0; S.p1 = (nat ? 16 : 8) * ld; S.p2 = (nat ? 4 : 16) * ld; S.rot = 0;
  };
  auto issue = [&](const Strm& S, int k, int src = -1) {
    const unsigned s = lb + (k & 3) * QS + lw * 8192;
    if (src < 0) src = (k + S.rot) & 31;
#pragma unroll
    for (int i = 0; i < 4; ++i) {
      const unsigned so = src * S.step + ((i & 1) ? S.p1 : S.p0) + (i >> 1) * S.p2;
      dma16s(S.r, s + i * 2048, S.v0, so); dma16s(S.r, s + i * 2048 + 1024, S.v1, so);
    }
  };
  constexpr int QW = 98304, HST = 131072, HRS = 2112, RST = HST + 8 * HRS, PKQ = 148480, STT = 151552, CSX = 153600;
  const int lane0 = tid & 63;
  const rsrc_t rq = lw < 2 ? mkrsrc(x + (size_t)(rb * 256 + lw * 128) * DM, 128 * DM * 2) : mkrsrc((const char*)wq + head * 256, (DM - 1) * HD * 2 + 256);
  const unsigned qv0 = lw < 2 ? (lane0 >> 3) * 2048 + (((lane0 & 7) ^ (lane0 >> 4)) * 16) : (lane0 >> 4) * (HD * 2) + (((lane0 & 15) ^ ((lane0 >> 4) << 1)) * 16);
  const unsigned qv1 = lw < 2 ? (qv0 ^ 64) + 8 * 2048 : (qv0 ^ 128) + 8 * (HD * 2);
  const bool qx = lw < 2;
  const unsigned qp1 = qx ? 32768 : 16 * HD * 2, qp2 = qx ? 65536 : 4 * HD * 2;
  const unsigned qlb = lb + (qx ? lw * 16384 : QW), qlw = qx ? 98304 : 32768, qom = qx ? 0x20780 : 0x7c0000;
  unsigned ql = qx ? 65536 : 0, qdl = 8192, qo = rb * (qx ? 256 : 4 * 32 * HD * 2), qdo = qx ? 131072 : 32 * HD * 2;
  auto issq = [&]() {
    const unsigned s = qlb + ql;
    dma16(rq, s, qv0, qo); dma16(rq, s + 1024, qv1, qo);
    dma16(rq, s + 2048, qv0, qo + qp1); dma16(rq, s + 3072, qv1, qo + qp1);
    dma16(rq, s + 4096, qv0, qo + qp2); dma16(rq, s + 5120, qv1, qo + qp2);
    dma16(rq, s + 6144, qv0, qo + qp1 + qp2); dma16(rq, s + 7168, qv1, qo + qp1 + qp2);
    ql += qdl; ql = ql >= qlw ? ql - qlw : ql; qdl ^= qx ? (8192 ^ 24576) : 0;
    qo = (qo + qdo) & qom; qdo ^= qx ? 128 : 0;
  };
  auto h_out = [&](int it) {
    const rsrc_t rh = mkrsrc(ws + WS_HT + (size_t)(blockIdx.x >> 2) * 16384 + (blockIdx.x & 3) * 256, 3 * 1048576 + 16384);
    const unsigned a0 = HST + ((lane0 >> 2) & 3) * HRS + (it * 4 + (lane0 >> 4)) * 32 + (lane0 & 3) * 8;
    s16x4 lo = __builtin_amdgcn_ds_read_tr16_b64_v4i16((LAS s16x4*)(lds + a0));
    s16x4 hi = __builtin_amdgcn_ds_read_tr16_b64_v4i16((LAS s16x4*)(lds + a0 + 4 * HRS));
    st16s<0>(__builtin_bit_cast(u32x4, __builtin_shufflevector(lo, hi, 0, 1, 2, 3, 4, 5, 6, 7)), rh, (lane0 >> 4) * 1024 + (lane0 & 15) * 16,
             (it >> 2) * 1048576 + (it & 3) * 4096);
  };
  auto st_out = [&]() { st16s<0>(*(LAS u32x4*)(lds + RST + (lane0 & 3) * 16), mkrsrc(ws + WS_ST + blockIdx.x * 64, 64), lane0 * 16, 0); };
  Tag tag;
  {
    const int b = blockIdx.x, lane = lane0;
    u32x4 A, B;
    u32x2 ep;
    asm volatile("global_load_dwordx4 %0, %3, %4\n\tglobal_load_dwordx4 %1, %3, %4 offset:1024\n\tglobal_load_dwordx2 %2, %5, %6 sc1"
                 : "=&v"(A), "=&v"(B), "=&v"(ep) : "v"(lane * 16), "s"(x + (size_t)(8 * b + wid) * DM), "v"(F_EP), "s"(ws + WS_FLAG) : "memory");
    const bool strm = hasU && wid >= 4 && wid < 7;
    if (strm || wid == 6) { issq(); issq(); if (lw == 2) { issq(); issq(); wait_vm<32>(); } else wait_vm<16>(); } else wait_vm<0>();
    asm volatile("" : "+v"(A), "+v"(B), "+v"(ep));
    unsigned v[8] = {A[0], A[1], A[2], A[3], B[0], B[1], B[2], B[3]};
    const unsigned long long e64 = ((unsigned long long)(unsigned)__builtin_amdgcn_readfirstlane(ep[1]) << 32) | (unsigned)__builtin_amdgcn_readfirstlane(ep[0]);
    const unsigned long long tg = e64 * 0x9E3779B97F4A7C15ull + 0x632BE59BD9B4E019ull;
    tag.lo = (unsigned)tg; tag.hi = (unsigned)(tg >> 32);
    *(LAS f32x2*)(lds + RST + wid * 8) = ln16(v);
    ln16(v);
    *(LAS u32x4*)(lds + HST + wid * HRS + lane * 16) = (u32x4){v[0], v[1], v[2], v[3]};
    *(LAS u32x4*)(lds + HST + wid * HRS + 1024 + lane * 16) = (u32x4){v[4], v[5], v[6], v[7]};
    wait_lgkm0();
  }
  STAMP(0, 1);
  if (!hasU) {
    const int sp = g8 * 4 + (idx - 28), lane = lane0, h = lane >> 5;
    const unsigned long long t_in = __builtin_amdgcn_s_memrealtime();
    bar();
    h_out(2 * wid); h_out(2 * wid + 1);
    if (wid == 0) st_out();
    wait_vm<0>();
    bar();
    if (wid == 0) signal<1>(ws, F_L + blockIdx.x * 8, tag);
    unsigned e[4][8];
#pragma unroll
    for (int i = 0; i < 4; ++i) {
      const int kh = sp >> 3, unit = (sp & 7) * 4 + i, frag = wid;
      const bool isK = unit == 0 || ((unit & 1) && unit < 31);
      const u16* sk = kc + ((size_t)kh * SEQ + 1536 + 32 * ((unit + 1) >> 1) + (lane & 31)) * 128 + 32 * (frag >> 1) + 4 * (2 * (frag & 1) + h);
      const u16* sv = vc + ((size_t)kh * SEQ + 1536 + 32 * ((unit - 1) >> 1) + 16 * (frag >> 2) + 4 * h) * 128 + 32 * (frag & 3) + (lane & 31);
      const u16* src = isK ? sk : sv;
      const int s1 = isK ? 1 : 128, s4 = isK ? 16 : 1024;
#pragma unroll
      for (int j2 = 0; j2 < 8; ++j2) e[i][j2] = src[(j2 >> 2) * s4 + (j2 & 3) * s1];
    }
    unsigned pw[8];
#pragma unroll
    for (int i = 0; i < NSP; ++i) pw[i] = *(const unsigned*)((const char*)w1 + (UROT ? (size_t)(sp >> 2) * PFB + (size_t)i * (PFB / 8) + (size_t)((sp & 3) * 448 + (tid < 448 ? tid : 0)) * 128
                                                                          : (size_t)((i >> 1) + 4 * (i & 1)) * PFB + (size_t)(sp * 448 + (tid < 448 ? tid : 0)) * 128));
    SB0();
#pragma unroll
    for (int i = 0; i < 4; ++i)
      st16s<0>((u32x4){e[i][0] | (e[i][1] << 16), e[i][2] | (e[i][3] << 16), e[i][4] | (e[i][5] << 16), e[i][6] | (e[i][7] << 16)},
               mkrsrc(ws + WS_KV, 1048576), tid * 16, (sp * 4 + i) * 8192);
#pragma unroll
    for (int i = 0; i < NSP; ++i) asm volatile("" ::"v"(pw[i]));
    wait_vm<0>();
    bar();
    if (wid == 0) signal<1>(ws, F_X + (sp >> 3) * 128 + (sp & 7) * 8, tag);
    for (int i = 0; i < 1000 && __builtin_amdgcn_s_memrealtime() - t_in < SPARE_T; ++i) __builtin_amdgcn_s_sleep(8);
    if (wid >= 4 && wid < 6) { issq(); issq(); }
  }
  bf16x8 qh[8];
  f32x4 uk[4][4];
  auto kv8 = [&](int t, int slot) {
    const unsigned v = lane_id() * 16;
    dma4k(rkv, lb + slot * 32768 + lw * 8192, v, t * 32768 + lw * 8192);
    dma4k(rkv, lb + slot * 32768 + lw * 8192 + 4096, v, t * 32768 + lw * 8192 + 4096);
  };
  if (wid < 4) {
    f32x4 acc[8][4], cs[2];
    Frags F0, F1;
    Addr a;
    int xs = 2, wsl = 1;
    {
      const int ln = lane_id(), r = ln & 15;
      addr_init(a, 0, lb + QW, wj, 2 * wi, ln);
      a.x = lb + 65536 + wi * 16384 + r * 128 + (((ln >> 4) ^ (r >> 1)) * 16);
    }
    bar();
    load_frags<2048>(F0, a);
    a.x ^= 64;
#pragma unroll
    for (int c = 0; c < 4; ++c) a.w[c] += 8192;
    wait_lgkm0();
    bar();
    STAMP(1, 1);
    {
      const unsigned o1 = (rb & 3) == 3 ? 0 : 0x3f803f80, o2 = (rb & 3) == 2 ? 0x3f803f80 : 0;
      u32x4 ones = {o1, o1, o1, o1};
      qstep<1, true, true>(acc, cs, ones, o1, F1, F0, a, xs, wsl);
      for (int t = 0; t < 4; ++t) {
        const unsigned on = t == 0 ? o1 : t == 3 ? 0 : o2;
        qstep<0, true>(acc, cs, ones, on, F0, F1, a, xs, wsl);
        qstep<1, true>(acc, cs, ones, on, F1, F0, a, xs, wsl);
      }
      const int ln = lane_id();
      const unsigned csa = lb + CSX + ((ln & 15) ? 7168 : (64 * wj + 32 * wi) * 4) + (ln & 48);
      *(LAS f32x4*)(size_t)csa = cs[0];
      *(LAS f32x4*)(size_t)(csa + 64) = cs[1];
      for (int t = 0; t < 11; ++t) { qstep<0, false>(acc, cs, ones, 0, F0, F1, a, xs, wsl); qstep<1, false>(acc, cs, ones, 0, F1, F0, a, xs, wsl); }
      addr_init(a, lb + wi * 8192, lb + 16384, wj, 0, lane_id());
      qstep<2, false>(acc, cs, ones, 0, F0, F1, a, xs, wsl);
    }
    STAMP(1, 2);
    f32x4 c0[4];
    {
      const int ln = lane_id(), g = ln >> 4;
      u32x2 bv[4];
#pragma unroll
      for (int j = 0; j < 4; ++j) bv[j] = *(LAS u32x2*)(lds + CSX + 5120 + (64 * wj + 16 * j + 4 * g) * 2);
      f32x4 cq[4];
#pragma unroll
      for (int j = 0; j < 4; ++j) cq[j] = *(LAS f32x4*)(lds + CSX + 512 + (64 * wj + 16 * (j ^ (2 * wi)) + 4 * g) * 4);
      f32x2 st[8];
#pragma unroll
      for (int i = 0; i < 8; ++i) st[i] = *(LAS f32x2*)(lds + STT + (128 * wi + 16 * i + (ln & 15)) * 8);
      wait_lgkm0();
      bar();
      const unsigned qa = lb + (wi ? 98304 : 131072) + (4 * wj + (g >> 1)) * (wi ? 4096 : 1024) + ((ln & 15) + 32 * (g & 1)) * 16;
      const unsigned sq = wi ? 1024 : 8192, m0 = wi ? 8192 : 0, m1 = wi ? 0 : 2048;
#pragma unroll
      for (int i = 0; i < 8; ++i) {
        const float sc = st[i][1] * 0.12751743082f, nm = -st[i][0];
#pragma unroll
        for (int mm = 0; mm < 2; ++mm)
          *(LAS u32x4*)(size_t)(qa + (i >> 1) * sq + (mm ? m1 : m0) + (i & 1) * 256) =
              pack8((acc[i][2 * mm] + cq[2 * mm] * nm) * sc, (acc[i][2 * mm + 1] + cq[2 * mm + 1] * nm) * sc);
      }
#pragma unroll
      for (int j = 0; j < 4; ++j) c0[j] = uh ? (f32x4){0.f, 0.f, 0.f, 0.f} : (f32x4){bflo(bv[j][0]), bfhi(bv[j][0]), bflo(bv[j][1]), bfhi(bv[j][1])};
    }
    STAMP(1, 3);
    if (hasU) {
      int slot = 1;
      gstep<true, 4, QS, true>(acc, F1, F0, a, slot, c0);
      gemm_steps<true, 4, QS>(acc, F0, F1, a, slot, 30);
      gstep<true, 4, QS>(acc, F0, F1, a, slot);
      STAMP(1, 4);
      const rsrc_t rux = mkrsrc(ws + WS_UX + (size_t)(ut * 2 + (uh ^ 1)) * 32768 + wid * 8192, 8192);
      const int ln = lane_id();
#pragma unroll
      for (int ii = 0; ii < 4; ++ii) {
        st16s<0>(pack8(acc[2 * ii + 1][0], acc[2 * ii + 1][1]), rux, ln * 16, ii * 2048);
        st16s<1024>(pack8(acc[2 * ii + 1][2], acc[2 * ii + 1][3]), rux, ln * 16, ii * 2048);
#pragma unroll
        for (int j = 0; j < 4; ++j) uk[ii][j] = acc[2 * ii][j];
      }
    } else {
      wait_lgkm0();
      bar();
    }
    STAMP(1, 5);
    const int ln = lane_id();
#pragma unroll
    for (int ks = 0; ks < 8; ++ks) qh[ks] = *(LAS bf16x8*)(lds + 131072 + wid * 8192 + ks * 1024 + ln * 16);
  } else {
    const int lane = lane0;
    Strm U;
    if (lw < 2) strm_x(U, ws + WS_HT + (size_t)dq * 1048576 + uh * 524288 + lw * 8192, 524288 - lw * 8192, 16384, uh);
    else strm_w(U, (const char*)w1 + (size_t)uh * 1024 * (FF * 2) + ft * 256, 1024, FF * 2, true);
    U.rot = UROT * dq;
    const unsigned pk = lb + PKQ + lw * 512;
    if (lw < 3) {
      if (lw < 2) wait_vm<0>(); else wait_vm<24>();
      bar();
      if (lw < 2) { issq(); issq(); } else wait_vm<16>();
      bar();
      if (lw < 2) issq();
      for (int t = 1; t <= 21; ++t) { issq(); wait_vm<16>(); bar(); }
      peek(rf, pk, F_L + lw * 512); peek(rf, pk + 256, F_L + lw * 512 + 256);
      for (int t = 22; t <= 26; ++t) { issq(); wait_vm<16>(); bar(); }
      issq(); wait_vm<16>();
    } else {
      peek(mkrsrc(b1, FF * 2), lb + CSX + 5120, ft * 256);
      bar();
      if (hasU) {
        for (int t = 0; t <= 7; ++t) { h_out(2 * t); h_out(2 * t + 1); bar(); }
        st_out();
        bar(); bar(); bar();
        st16s<0>(*(LAS u32x4*)(lds + CSX + (lane & 31) * 16), mkrsrc(ws + WS_CS + (head * 8 + rb) * 512, 512), lane * 16, 0);
        bar(); bar(); bar();
        wait_vm<0>();
        signal<1>(ws, F_L + blockIdx.x * 8, tag);
      } else {
        for (int t = 0; t <= 13; ++t) bar();
        spin_until(ws, F_X + kvh * 128, 8, tag);
      }
      const rsrc_t rp = mkrsrc(w1, hasU ? SEQ * FF * 2 : 0);
      for (int t = 14; t <= 21; ++t) {
        if (!UROT && (t - 14) % PFS == 0 && NSP + (t - 14) / PFS < 8)
          asm volatile("s_mov_b32 m0, %0\n\ts_nop 0\n\tbuffer_load_dword %2, %1, %3 offen lds" ::"s"(lb + 161792), "s"(rp), "v"(((g8 * 28 + idx) * 64 + lane) * 128),
                       "s"((((NSP + (t - 14) / PFS) >> 1) + 4 * ((NSP + (t - 14) / PFS) & 1)) * 1835008) : "memory");
        bar();
      }
      peek(rf, pk, F_L + lw * 512); peek(rf, pk + 256, F_L + lw * 512 + 256);
      for (int t = 22; t <= 26; ++t) bar();
      wait_vm<0>();
    }
    peek_check(ws, pk, F_L + lw * 512, 32, tag); peek_check(ws, pk + 256, F_L + lw * 512 + 256, 32, tag);
    bar();
    if (blockIdx.x == 0 && lw == 3) signal<1>(ws, F_EP, tag);
    if (lw == 2) issq();
    if (lw == 3) {
      const rsrc_t rs = mkrsrc(ws + WS_ST + rb * 2048, 2048);
      dma16s(rs, lb + STT, lane * 16, 0); dma16s(rs, lb + STT + 1024, lane * 16, 1024);
      dma4k(mkrsrc(ws + WS_CS + head * 4096, 4096), lb + CSX + 1024, lane * 16, 0);
    }
    const bool us = hasU && lw < 3;
    if (us) issue(U, 0); else if (!hasU) kv8(0, 0);
    wait_vm<16>(); bar();
    if (lw == 3 && hasU) wait_vm<0>(); else wait_vm<8>();
    bar();
    if (us) issue(U, 1); else if (!hasU) kv8(1, 1);
    if (lw == 3) {
      const LAS f32x2* cp = (const LAS f32x2*)(lds + CSX + 1024) + lane;
      *(LAS f32x2*)(lds + CSX + 512 + lane * 8) = ((cp[0] + cp[64]) + cp[128]) + ((cp[256] + cp[320]) + cp[384]);
      wait_lgkm0();
    }
    bar();
    wait_vm<8>(); bar();
    if (us) issue(U, 2); else if (!hasU) kv8(2, 2);
    wait_vm<8>();
    bar();
    if (us) issue(U, 3);
    bar();
    if (hasU) {
      if (lw < 3) { for (int t = 1; t <= 28; ++t) { issue(U, t + 3); wait_vm<16>(); bar(); } }
      else {
        for (int t = 1; t <= 20; ++t) bar();
        spin_until(ws, F_X + kvh * 128, 8, tag);
        for (int t = 21; t <= 28; ++t) bar();
      }
      wait_vm<8>(); bar();
      kv8(0, 0); wait_vm<8>(); bar();
      kv8(1, 1); bar();
      kv8(2, 2); bar();
    } else bar();
#pragma unroll
    for (int ks = 0; ks < 8; ++ks) qh[ks] = *(LAS bf16x8*)(lds + 98304 + ks * 4096 + lw * 1024 + lane * 16);
    wait_vm<8>();
  }
  wait_lgkm0();
  bar();
  STAMP(1, 6);
  unsigned pf = 0;
  Strm Y;
  {
    const int lane = lane_id();
    Att A;
#pragma unroll
    for (int ks = 0; ks < 8; ++ks) A.q[ks] = qh[ks];
#pragma unroll
    for (int d = 0; d < 4; ++d)
#pragma unroll
      for (int i = 0; i < 16; ++i) A.o[d][i] = 0.f;
#pragma unroll
    for (int i = 0; i < 16; ++i) A.negm[i] = 0.f;
    A.l = 0.f;
    const unsigned kvl = lb + lane * 16;
#pragma unroll
    for (int i = 0; i < 4; ++i) A.pre[i] = ldfrag(kvl + i * 1024);
    const bool strm = wid >= 4 && wid < 7, hw = wid >= 4;
    const rsrc_t rp2 = mkrsrc(w2, SEQ * FF * 2), rpo = mkrsrc(wo, HD * DM * 2);
    const unsigned pv = (blockIdx.x * 64 + lane) * 128;
#define PF(W, R, C) pf_line<W>(pf, wid, R, pv, (C) * 2097152u)
    const int fp = F_P + (ut * 2 + uh) * 64 + (wid & 3) * 8;
    const bool cu = !hw && hasU;
    att_tile<0>(A, kvl + 0 * 32768, kvl + 1 * 32768, [&](int n) { if (n == 1 && hw) kv8(3, 3); if (n == 20 && cu) { wait_vm<0>(); signal<1>(ws, fp ^ 64, tag); }
      if (n == 23) PF(0, rp2, 0); });
    if (hw) wait_vm<8>();
    bar();
    att_tile<1>(A, kvl + 1 * 32768, kvl + 2 * 32768, [&](int n) {
      if (n == 1 && hw) kv8(4, 0); if (n == 28 && hw) peek(rf, lb + 131072 + lw * 256, fp); if (n == 3) PF(1, rp2, 1); if (n == 10) PF(2, rp2, 2); });
    if (hw) wait_vm<9>();
    bar();
    att_tile<1>(A, kvl + 2 * 32768, kvl + 3 * 32768, [&](int n) {
      if (n == 8 && hw) {
        wait_vm<0>();
        if (hasU) peek_check(ws, lb + 131072 + lw * 256, fp, 1, tag);
        const rsrc_t rux = mkrsrc(ws + WS_UX + (size_t)(ut * 2 + uh) * 32768, hasU ? 32768 : 0);
        dma4k(rux, lb + 32768 + lw * 8192, lane * 16, lw * 8192); dma4k(rux, lb + 32768 + lw * 8192 + 4096, lane * 16, lw * 8192 + 4096);
      }
      if (n == 13) PF(3, rp2, 3); if (n == 21) PF(0, rp2, 4); if (n == 29) PF(1, rp2, 5); });
    if (hw) wait_vm<0>();
    bar();
    if (hw) { kv8(5, 4); kv8(7, 2); }
    if (cu) {
      u32x4 p[8];
      p[0] = ldsr(kvl + 32768 + wid * 8192);
#pragma unroll
      for (int k = 0; k < 8; ++k) {
        if (k < 7) p[k + 1] = ldsr(kvl + 32768 + wid * 8192 + (k + 1) * 1024);
        f32x4 a = uk[k >> 1][2 * (k & 1)], b = uk[k >> 1][2 * (k & 1) + 1];
        add8(a, b, p[k]);
        p[k] = (u32x4){gelu2(a.xy), gelu2(a.zw), gelu2(b.xy), gelu2(b.zw)};
        asm volatile("" : "+v"(p[k]));
        SB0();
      }
      bar();
      const rsrc_t ru = mkrsrc(ws + WS_U + (size_t)((2 * dq + wi) * 112 + 4 * ft + 2 * wj) * 8192 + uh * 1024, 16384);
#pragma unroll
      for (int k = 0; k < 8; ++k) st16s<0>(p[k], ru, lane * 16, (k & 1) * 8192 + (k >> 1) * 2048);
    } else bar();
    att_tile<1>(A, kvl + 3 * 32768, kvl + 0 * 32768, [&](int n) {
      if (n == 20 && cu) { wait_vm<0>(); signal<1>(ws, F_U + ((2 * dq + wi) * 4 + kvh) * 256 + (((idx >> 2) * 2 + uh) * 2 + wj) * 8, tag); }
      if (n == 23) PF(2, rp2, 6); });
    if (hw) wait_vm<0>();
    bar();
    att_tile<1>(A, kvl + 0 * 32768, kvl + 4 * 32768, [&](int n) { if (n == 0 && hw) kv8(6, 3); if (n == 5) PF(3, rpo, 0); if (n == 18) PF(0, rpo, 1); });
    if (hw) wait_vm<0>();
    bar();
    att_tile<1>(A, kvl + 4 * 32768, kvl + 3 * 32768, [&](int n) { if (n == 1) PF(1, rpo, 2); if (n == 13) PF(2, rpo, 3); });
    if (lw == 0) strm_x(Y, ws + WS_U + (size_t)(dc * 112 + 28 * kvh) * 8192, 28 * 8192, 8192, 0);
    else strm_w(Y, (const char*)w2 + (size_t)kvh * 896 * (SEQ * 2) + rb * 512 + ((lw - 1) & 1) * 256, 896, SEQ * 2, false);
    bar();
    att_tile<1>(A, kvl + 3 * 32768, kvl + 2 * 32768, [&](int n) {
      if (n == 4 && wid == 4) peek(rf, lb + 49152, F_U + (dc * 4 + kvh) * 256);
      if (n == 18 && wid == 4) { wait_vm<0>(); peek_check(ws, lb + 49152, F_U + (dc * 4 + kvh) * 256, 28, tag); }
      if (n == 19 && strm) issue(Y, 0); if (n == 27 && strm) issue(Y, 1); });
    bar();
    att_tile<2>(A, kvl + 2 * 32768, 0, [&](int n) {
      if (n == 9) { if (hw) wait_vm<8>(); bar(); }
      if (n == 10 && strm) issue(Y, 2); });
    STAMP(1, 7);
    if (__builtin_expect(__any(!(A.l <= 1.0e9f)), 0)) att_redo(A, rkv, lane * 16);
    auto sw = __builtin_amdgcn_permlane32_swap(__float_as_uint(A.l), __float_as_uint(A.l), false, false);
    const float inv = __builtin_amdgcn_rcpf(__uint_as_float(sw[0]) + __uint_as_float(sw[1]));
    const unsigned oa = lb + PARK + wid * 2048 + ((lane >> 4) & 1) * 1024 + (lane >> 5) * 256 + (lane & 15) * 16;
#pragma unroll
    for (int d = 0; d < 4; ++d)
#pragma unroll
      for (int u = 0; u < 2; ++u) {
        const int r0 = 4 * u, r1 = 8 + 4 * u;
        *(LAS u32x4*)(size_t)(oa + d * 16384 + u * 512) =
            (u32x4){cvtpk(A.o[d][r0] * inv, A.o[d][r0 + 1] * inv), cvtpk(A.o[d][r0 + 2] * inv, A.o[d][r0 + 3] * inv),
                    cvtpk(A.o[d][r1] * inv, A.o[d][r1 + 1] * inv), cvtpk(A.o[d][r1 + 2] * inv, A.o[d][r1 + 3] * inv)};
      }
    wait_lgkm0();
  }
  STAMP(1, 8);
  if (wid < 4) {
    f32x4 acc[8][4];
    Frags F0, F1;
    Addr a;
    int slot = 1;
    addr_init(a, lb, lb + 8192 + wi * 8192, wj, kvh, lane_id());
    load_frags(F0, a);
    addr_add(a, QS);
    wait_lgkm0();
    bar();
    STAMP(1, 9);
    gstep<false, 4, QS, true>(acc, F1, F0, a, slot);
    gemm_steps<false, 4, QS>(acc, F0, F1, a, slot, 26);
    addr_init(a, lb + wi * 8192, lb + 16384, wj, 0, lane_id());
    gstep<false, 4, QS>(acc, F0, F1, a, slot);
    STAMP(1, 10);
    asm volatile("s_waitcnt vmcnt(0)" : "+v"(pf)::"memory");
    ystep_park(acc, F1, F0, a, slot, lb + PARK + lane_id() * 16 + wid * 4096);
    STAMP(1, 11);
    gemm_steps<true, 4, QS>(acc, F0, F1, a, slot, 30);
    gstep<true, 4, QS>(acc, F0, F1, a, slot);
    STAMP(1, 12);
    const int lt = lane_id(), g = lt >> 4;
    const unsigned wo_ = wi * 8192 + wj * 2048;
#pragma unroll
    for (int dl = 1; dl < 4; ++dl)
#pragma unroll
      for (int h = 0; h < 2; ++h)
#pragma unroll
        for (int mm = 0; mm < 2; ++mm)
          st16s<0>(pack8(acc[4 * h + dl][2 * mm], acc[4 * h + dl][2 * mm + 1]), rxb, lt * 16, (kvh ^ dl) * 98304 + 49152 + (dl - 1) * 16384 + wo_ + h * 4096 + mm * 1024);
    wait_vm<0>();
    signal<14>(ws, F_S + (tile * 4 + (kvh ^ (lt & 3))) * 128 + wid * 32 + ((lt & 3) - 1) * 8, tag);
    STAMP(1, 13);
    const int srow = rb * 256 + (8 * wi + kvh) * 16;
    const rsrc_t rx = mkrsrc(x + (size_t)srow * DM, 80 * DM * 2), rout = mkrsrc(out + (size_t)srow * DM, 80 * DM * 2);
    const unsigned vo = ((lt & 15) * DM + dc * 128 + wj * 64 + 16 * (g & 1) + 8 * (g >> 1)) * 2;
    u32x4 ps[3][4], xv[4], gv[4];
#pragma unroll
    for (int p = 0; p < 4; ++p) xv[p] = __builtin_amdgcn_raw_buffer_load_b128(rx, vo + (p >> 1) * (64 * DM * 2) + (p & 1) * 64, 0, 0);
    bar();
#pragma unroll
    for (int p = 0; p < 4; ++p) gv[p] = ldsr(lb + PARK + lt * 16 + wo_ + (p >> 1) * 4096 + (p & 1) * 1024);
    spin_until(ws, F_S + (tile * 4 + kvh) * 128 + wid * 32, 3, tag);
    STAMP(1, 14);
#pragma unroll
    for (int p = 0; p < 4; ++p)
#pragma unroll
      for (int k = 0; k < 3; ++k)
        ps[k][p] = __builtin_amdgcn_raw_buffer_load_b128(rxb, lt * 16 + (p & 1) * 1024, kvh * 98304 + 49152 + k * 16384 + wo_ + (p >> 1) * 4096, 16);
#pragma unroll
    for (int p = 0; p < 4; ++p) {
      f32x4 s0 = acc[4 * (p >> 1)][2 * (p & 1)], s1 = acc[4 * (p >> 1)][2 * (p & 1) + 1];
#pragma unroll
      for (int k = 0; k < 3; ++k) add8(s0, s1, ps[k][p]);
      s0 *= (f32x4){bflo(gv[p][0]), bfhi(gv[p][0]), bflo(gv[p][1]), bfhi(gv[p][1])};
      s1 *= (f32x4){bflo(gv[p][2]), bfhi(gv[p][2]), bflo(gv[p][3]), bfhi(gv[p][3])};
      const u32x4 tp = pack8(s0, s1);
      const auto w0 = __builtin_amdgcn_permlane16_swap(tp[0], tp[2], false, false), w1 = __builtin_amdgcn_permlane16_swap(tp[1], tp[3], false, false);
      const unsigned t[4] = {w0[0], w1[0], w0[1], w1[1]};
      u32x4 o;
#pragma unroll
      for (int e = 0; e < 4; ++e) o[e] = cvtpk(bflo(xv[p][e]) + bflo(t[e]), bfhi(xv[p][e]) + bfhi(t[e]));
      __builtin_amdgcn_raw_buffer_store_b128(o, rout, vo + (p >> 1) * (64 * DM * 2) + (p & 1) * 64, 0, 0);
    }
    STAMP(1, 15);
  } else {
    const int lane = lane_id();
    const unsigned pl = lb + PARK + lane * 16, pk = lb + 159744 + lw * 256;
    const int fo = F_O + (rb * 4 + kvh) * 128;
    if (lw < 3) {
      Strm S;
      if (lw < 2) strm_x(S, ws + WS_O + (size_t)(rb * 128 + 32 * kvh) * 16384 + lw * 8192, 524288 - lw * 8192, 16384, kvh);
      else strm_w(S, (const char*)wo + (size_t)kvh * 1024 * (DM * 2) + dc * 256, 1024, DM * 2, false);
      wait_vm<8>();
      bar();
      issue(Y, 3);
      for (int t = 1; t <= 22; ++t) { issue(Y, t + 3); wait_vm<16>(); bar(); }
      peek(rf, lb + PARK + lw * 256, fo);
      issue(Y, 26); wait_vm<16>(); bar();
      issue(Y, 27); wait_vm<16>(); bar();
      peek_check(ws, lb + PARK + lw * 256, fo, 8, tag);
      auto iss = [&](int k) { issue(S, k, 4 * (k & 7) + (k >> 3)); wait_vm<16>(); bar(); };
      for (int t = 25; t <= 36; ++t) iss(t - 25);
      peek(rf, lb + PARK + 16384 + lw * 256, fo + 64);
      for (int t = 37; t <= 40; ++t) iss(t - 25);
      peek_check(ws, lb + PARK + 16384 + lw * 256, fo + 64, 8, tag);
      for (int t = 41; t <= 56; ++t) iss(t - 25);
      wait_vm<8>(); bar();
      wait_vm<0>(); bar();
    } else {
      bar();
      const rsrc_t ro = mkrsrc(ws + WS_O + (size_t)(rb * 128 + 4 * head) * 16384, 65536);
      for (int t = 1; t <= 25; t += 2) {
        const unsigned o = (t >> 1) * 5120;
        const u32x4 v0 = ldsr(pl + o), v1 = ldsr(pl + o + 1024), v2 = ldsr(pl + o + 2048), v3 = ldsr(pl + o + 3072), v4 = ldsr(pl + (t < 25 ? o + 4096 : 0));
        st16s<0>(v0, ro, lane * 16, o); st16s<1024>(v1, ro, lane * 16, o); st16s<2048>(v2, ro, lane * 16, o);
        if (t == 17) { wait_vm<11>(); signal<1>(ws, fo + dc * 8, tag); }
        bar();
        st16s<3072>(v3, ro, lane * 16, o); st16s<0>(v4, ro, lane * 16, o + 4096);
        bar();
      }
      bar(); bar(); bar();
      wait_vm<0>();
      signal<1>(ws, fo + 64 + dc * 8, tag);
      for (int t = 30; t <= 49; t += 2) {
        u32x4 v[5];
        unsigned so[5];
#pragma unroll
        for (int i = 0; i < 5; ++i) {
          const int p = (t - 30) / 2 * 5 + i, dl = 1 + (p >> 4);
          v[i] = ldsr(pl + (p < 48 ? 16384 + p * 1024 : 0));
          so[i] = p < 48 ? (kvh ^ dl) * 98304 + p * 1024 : 393216;
        }
        st16s<0>(v[0], rxb, lane * 16, so[0]); st16s<0>(v[1], rxb, lane * 16, so[1]); st16s<0>(v[2], rxb, lane * 16, so[2]);
        bar();
        st16s<0>(v[3], rxb, lane * 16, so[3]); st16s<0>(v[4], rxb, lane * 16, so[4]);
        bar();
      }
      bar(); bar();
      wait_vm<0>();
      signal<14>(ws, F_Y + (tile * 4 + (kvh ^ (lane & 3))) * 128 + ((lane & 3) - 1) * 8, tag);
      for (int t = 52; t <= 56; ++t) bar();
      peek(rf, pk, F_Y + (tile * 4 + kvh) * 128);
      bar();
      wait_vm<0>();
      peek_check(ws, pk, F_Y + (tile * 4 + kvh) * 128, 3, tag);
      bar();
    }
    u32x4 py[3][4];
#pragma unroll
    for (int k = 0; k < 3; ++k)
#pragma unroll
      for (int m = 0; m < 4; ++m) py[k][m] = __builtin_amdgcn_raw_buffer_load_b128(rxb, lane * 16 + m * 1024, kvh * 98304 + lw * 4096 + k * 16384, 16);
    const float bb = bflo(b2[rb * 256 + (4 * lw + kvh) * 16 + (lane & 15)]);
    bar();
    bar();
#pragma unroll
    for (int m = 0; m < 4; ++m) {
      f32x4 y0 = {bb, bb, bb, bb}, y1 = y0;
      add8(y0, y1, ldsr(pl + lw * 4096 + m * 1024));
#pragma unroll
      for (int k = 0; k < 3; ++k) add8(y0, y1, py[k][m]);
      *(LAS u32x4*)(size_t)(pl + lw * 4096 + m * 1024) = (u32x4){silu2(y0.xy), silu2(y0.zw), silu2(y1.xy), silu2(y1.zw)};
    }
    wait_lgkm0();
    bar();
  }
}

extern "C" void kernel_launch(void* const* d_in, const int* in_sizes, int n_in, void* d_out, int out_size, void* d_ws, size_t ws_size,
                              hipStream_t stream) {
  const u16* x = (const u16*)d_in[0];
  const u16* kc = (const u16*)d_in[1];
  const u16* vc = (const u16*)d_in[2];
  const u16* wq = (const u16*)d_in[3];
  const u16* wo = (const u16*)d_in[4];
  const u16* w1 = (const u16*)d_in[6];
  const u16* b1 = (const u16*)d_in[7];
  const u16* w2 = (const u16*)d_in[8];
  const u16* b2 = (const u16*)d_in[9];
  char* ws = (char*)d_ws;
  (void)hipFuncSetAttribute((const void*)k_main, hipFuncAttributeMaxDynamicSharedMemorySize, 163840);
  hipLaunchKernelGGL(k_main, dim3(256), dim3(512), 163840, stream, x, kc, vc, wq, wo, w1, b1, w2, b2, ws, (u16*)d_out);
}
```

```cpp
#include <hip/hip_runtime.h>
#include <hip/hip_bf16.h>

#define PFB 1835008
#define CODE_BYTES 71168
#define CODE2 0
#define UROT 8
#define NSP 6
#define PFS 4
#define SPARE_T 1150
typedef unsigned short u16;
typedef short bf16x8 __attribute__((ext_vector_type(8)));
typedef short s16x4 __attribute__((ext_vector_type(4)));
typedef float f32x16 __attribute__((ext_vector_type(16)));
typedef float f32x4 __attribute__((ext_vector_type(4)));
typedef float f32x2 __attribute__((ext_vector_type(2)));
typedef unsigned u32x4 __attribute__((ext_vector_type(4)));
typedef unsigned u32x2 __attribute__((ext_vector_type(2)));
typedef __bf16 bf16x2 __attribute__((ext_vector_type(2)));
typedef __amdgpu_buffer_rsrc_t rsrc_t;
#define LAS __attribute__((address_space(3)))
#define GAS __attribute__((address_space(1)))
#define DI __device__ __forceinline__
#define SGB(m, n) __builtin_amdgcn_sched_group_barrier(m, n, 0)
#define SB0() __builtin_amdgcn_sched_barrier(0)

constexpr int SEQ = 2048, DM = 1024, FF = 3584, HD = 4096;
constexpr size_t WS_FLAG = 0;
constexpr int F_EP = 0, F_X = 512, F_L = 2048, F_U = 4096, F_O = 12288, F_Y = 16384, F_S = 49152, F_P = 81920, F_END = 96256;
constexpr size_t WS_ST = 98304;
constexpr size_t WS_CS = 131072;
constexpr size_t WS_HT = 262144;
constexpr size_t WS_KV = WS_HT + 4194304;
constexpr size_t WS_O = WS_KV + 1048576;
constexpr size_t WS_U = WS_O + 16777216;
constexpr size_t WS_XB = WS_U + 7340032;
constexpr size_t WS_UX = WS_XB + 25165824;
constexpr size_t WS_END = WS_UX + 7340032;

#define STAMP(kid, i)
#define STAMPF(i)
DI unsigned cvtpk(float lo, float hi) { f32x2 v = {lo, hi}; bf16x2 b = __builtin_convertvector(v, bf16x2); return __builtin_bit_cast(unsigned, b); }
DI float bflo(unsigned u) { return __uint_as_float(u << 16); }
DI float bfhi(unsigned u) { return __uint_as_float(u & 0xffff0000u); }
DI float rbf(float x) { return bflo(cvtpk(x, x)); }
template <int N> DI void wait_vm() { asm volatile("s_waitcnt vmcnt(%0)" ::"i"(N) : "memory"); }
DI void wait_lgkm0() { __builtin_amdgcn_s_waitcnt(0xC07F); }
DI void bar() { asm volatile("" ::: "memory"); __builtin_amdgcn_s_barrier(); asm volatile("" ::: "memory"); }
DI rsrc_t mkrsrc(const void* p, unsigned bytes) { return __builtin_amdgcn_make_buffer_rsrc((void*)p, 0, bytes, 0x00020000); }
DI void dma16(rsrc_t r, unsigned lds_addr, unsigned voff, unsigned soff) {
  asm volatile("s_mov_b32 m0, %0\n\ts_nop 0\n\tbuffer_load_dwordx4 %1, %2, %3 offen lds" ::"s"(lds_addr), "v"(voff), "s"(r), "s"(soff) : "memory");
}
DI void dma16s(rsrc_t r, unsigned lds_addr, unsigned voff, unsigned soff) {
  asm volatile("s_mov_b32 m0, %0\n\ts_nop 0\n\tbuffer_load_dwordx4 %1, %2, %3 offen sc1 lds" ::"s"(lds_addr), "v"(voff), "s"(r), "s"(soff) : "memory");
}
template <int OFF> DI void st16s(u32x4 v, rsrc_t r, unsigned voff, unsigned soff) {
  asm volatile("buffer_store_dwordx4 %0, %1, %2, %3 offen offset:%4 sc1\n\ts_nop 1" ::"v"(v), "v"(voff), "s"(r), "s"(soff), "i"(OFF) : "memory");
}
DI int lane_id() { int l; asm volatile("v_mbcnt_lo_u32_b32 %0, -1, 0\n\tv_mbcnt_hi_u32_b32 %0, -1, %0" : "=v"(l)); return l; }
struct Tag { unsigned lo, hi; };
DI void peek(rsrc_t rf, unsigned lds_addr, int off) {
  asm volatile("s_mov_b32 m0, %0\n\ts_nop 0\n\tbuffer_load_dword %3, %1, %2 offen sc1 lds" ::"s"(lds_addr), "s"(rf), "s"(off), "v"(lane_id() * 4) : "memory");
}
template <int GAP = 1> DI void spin_until(char* ws, int off, int n, Tag t) {
  const int ln = lane_id();
  GAS unsigned long long* p = (GAS unsigned long long*)(ws + WS_FLAG + off) + (ln < n ? ln : 0);
  const unsigned long long want = ((unsigned long long)t.hi << 32) | t.lo;
  for (unsigned sp = 0; sp < (1u << 20); ++sp) {
    if (__all(__hip_atomic_load(p, __ATOMIC_RELAXED, __HIP_MEMORY_SCOPE_AGENT) == want)) break;
    __builtin_amdgcn_s_sleep(GAP);
  }
  asm volatile("" ::: "memory");
}
DI void peek_check(char* ws, unsigned lds_addr, int off, int n, Tag t) {
  const int ln = lane_id();
  const unsigned v = *(LAS volatile unsigned*)(size_t)(lds_addr + ln * 4);
  if (!__all(ln >= 2 * n || v == ((ln & 1) ? t.hi : t.lo))) spin_until(ws, off, n, t);
}
template <long MASK> DI void signal(char* ws, int off, Tag t) {
  const u32x2 v = {t.lo, t.hi};
  asm volatile("s_mov_b64 exec, %3\n\tglobal_store_dwordx2 %0, %1, %2 sc1\n\ts_mov_b64 exec, -1" ::"v"(off), "v"(v), "s"(ws + WS_FLAG), "s"(MASK) : "memory");
}
DI void dma4k(rsrc_t r, unsigned lds_addr, unsigned voff, unsigned soff) {
  asm volatile("s_mov_b32 m0, %0\n\ts_nop 0\n\tbuffer_load_dwordx4 %1, %2, %3 offen sc1 lds\n\tbuffer_load_dwordx4 %1, %2, %3 offen offset:1024 sc1 lds\n\t"
               "buffer_load_dwordx4 %1, %2, %3 offen offset:2048 sc1 lds\n\tbuffer_load_dwordx4 %1, %2, %3 offen offset:3072 sc1 lds"
               ::"s"(lds_addr), "v"(voff), "s"(r), "s"(soff) : "memory");
}
template <int W> DI void pf_line(unsigned& sink, int wid, rsrc_t r, unsigned voff, unsigned soff) {
  asm volatile("s_cmp_lg_u32 %1, %5\n\ts_cbranch_scc1 1f\n\tbuffer_load_dword %0, %2, %3, %4 offen\n1:"
               : "+v"(sink) : "s"(wid), "v"(voff), "s"(r), "s"(soff), "i"(W) : "scc", "memory");
}
template <int CTRL> DI float dppf(float x) { return __builtin_bit_cast(float, __builtin_amdgcn_mov_dpp(__builtin_bit_cast(int, x), CTRL, 0xf, 0xf, true)); }
DI float wave_sum(float v) {
  v += dppf<0xB1>(v); v += dppf<0x4E>(v); v += dppf<0x141>(v); v += dppf<0x140>(v);
  auto a = __builtin_amdgcn_permlane16_swap(__float_as_uint(v), __float_as_uint(v), false, false);
  v = __uint_as_float(a[0]) + __uint_as_float(a[1]);
  auto b = __builtin_amdgcn_permlane32_swap(__float_as_uint(v), __float_as_uint(v), false, false);
  return __uint_as_float(b[0]) + __uint_as_float(b[1]);
}

DI f32x2 ln16(unsigned (&w)[8]) {
  f32x2 v[8];
#pragma unroll
  for (int i = 0; i < 8; ++i) v[i] = (f32x2){bflo(w[i]), bfhi(w[i])};
  const f32x2 s = ((v[0] + v[1]) + (v[2] + v[3])) + ((v[4] + v[5]) + (v[6] + v[7]));
  const float mean = wave_sum(s[0] + s[1]) * (1.f / 1024.f);
  f32x2 q0 = {0.f, 0.f}, q1 = q0;
#pragma unroll
  for (int i = 0; i < 8; i += 2) {
    const f32x2 d0 = v[i] - mean, d1 = v[i + 1] - mean;
    q0 = __builtin_elementwise_fma(d0, d0, q0); q1 = __builtin_elementwise_fma(d1, d1, q1);
  }
  q0 += q1;
  const float var = wave_sum(q0[0] + q0[1]) * (1.f / 1024.f);
  const float mb = rbf(mean);
  const float rs = __builtin_amdgcn_rcpf(rbf(__builtin_amdgcn_sqrtf(rbf(rbf(var) + 1e-5f))));
#pragma unroll
  for (int i = 0; i < 8; ++i) {
    const f32x2 d = v[i] - mb;
    const unsigned c = cvtpk(d[0], d[1]);
    const f32x2 r = (f32x2){bflo(c), bfhi(c)} * rs;
    w[i] = cvtpk(r[0], r[1]);
  }
  return (f32x2){mb, rs};
}

struct Frags { bf16x8 x[8]; bf16x8 w[4]; };
struct Addr { unsigned x, w[4]; };
template <int XS = 1024> DI void load_frags(Frags& f, const Addr& a) {
#pragma unroll
  for (int c = 0; c < 4; ++c) {
    s16x4 a0 = __builtin_amdgcn_ds_read_tr16_b64_v4i16((LAS s16x4*)(size_t)(a.w[c]));
    s16x4 a1 = __builtin_amdgcn_ds_read_tr16_b64_v4i16((LAS s16x4*)(size_t)(a.w[c] + 4096));
    f.w[c] = __builtin_shufflevector(a0, a1, 0, 1, 2, 3, 4, 5, 6, 7);
  }
#pragma unroll
  for (int i = 0; i < 8; ++i) f.x[i] = *(LAS bf16x8*)(size_t)(a.x + i * XS);
}
template <bool WA, bool Z> DI void mma_all(f32x4 (&acc)[8][4], const Frags& f, const f32x4* c0) {
  const f32x4 zero = {0.f, 0.f, 0.f, 0.f};
#pragma unroll
  for (int i = 0; i < 8; ++i)
#pragma unroll
    for (int jj = 0; jj < 4; ++jj) {
      const int j = (i & 1) ? 3 - jj : jj;
      if (WA) acc[i][j] = __builtin_amdgcn_mfma_f32_16x16x32_bf16(f.w[j], f.x[i], Z ? (c0 ? c0[j] : zero) : acc[i][j], 0, 0, 0);
      else    acc[i][j] = __builtin_amdgcn_mfma_f32_16x16x32_bf16(f.x[i], f.w[j], Z ? (c0 ? c0[j] : zero) : acc[i][j], 0, 0, 0);
    }
}
DI void addr_add(Addr& a, int d) {
  a.x += d;
#pragma unroll
  for (int c = 0; c < 4; ++c) a.w[c] += d;
}
template <bool WA, int NSLOT, int SLOTB, bool Z = false>
DI void gstep(f32x4 (&acc)[8][4], Frags& Fn, const Frags& Fc, Addr& a, int& slot, const f32x4* c0 = nullptr) {
  SB0();
  load_frags(Fn, a);
  mma_all<WA, Z>(acc, Fc, c0);
  slot = (slot + 1 == NSLOT) ? 0 : slot + 1;
  addr_add(a, (slot == 0) ? -(NSLOT - 1) * SLOTB : SLOTB);
  asm volatile("" : "+v"(a.x), "+v"(a.w[0]), "+v"(a.w[1]), "+v"(a.w[2]), "+v"(a.w[3]));
#pragma unroll
  for (int i = 0; i < 8; ++i) { SGB(0x8, 1); SGB(0x100, 1); SGB(0x8, 1); SGB(0x100, 1); SGB(0x8, 1); }
#pragma unroll
  for (int i = 0; i < 6; ++i) { SGB(0x8, 1); SGB(0x4, 1); SGB(0x2, 1); }
  SGB(0x8, 2);
  SB0();
  wait_lgkm0();
  bar();
}
template <int MODE, bool CS, bool Z = false>
DI void qstep(f32x4 (&acc)[8][4], f32x4 (&cs)[2], u32x4& ones, unsigned onext, Frags& Fn, const Frags& Fc, Addr& a, int& xs, int& wsl) {
  const f32x4 zero = {0.f, 0.f, 0.f, 0.f};
  SB0();
  load_frags<MODE == 2 ? 1024 : 2048>(Fn, a);
  if (CS) {
#pragma unroll
    for (int j = 0; j < 2; ++j) cs[j] = __builtin_amdgcn_mfma_f32_16x16x32_bf16(Fc.w[j], __builtin_bit_cast(bf16x8, ones), Z ? zero : cs[j], 0, 0, 0);
    ones = (u32x4){onext, onext, onext, onext};
    asm volatile("" : "+v"(ones));
  }
  mma_all<true, Z>(acc, Fc, nullptr);
  if (MODE == 2) addr_add(a, 24576);
  else {
    wsl = (wsl + 1) & 3;
    const int dw = wsl ? 8192 : -24576;
#pragma unroll
    for (int c = 0; c < 4; ++c) a.w[c] += dw;
    a.x ^= 64;
    if (MODE == 1) { xs = (xs == 2) ? 0 : xs + 1; a.x += xs ? 32768 : -65536; }
  }
  asm volatile("" : "+v"(a.x), "+v"(a.w[0]), "+v"(a.w[1]), "+v"(a.w[2]), "+v"(a.w[3]));
#pragma unroll
  for (int i = 0; i < 8; ++i) { SGB(0x8, 1); SGB(0x100, 1); SGB(0x8, 1); SGB(0x100, 1); SGB(0x8, 1); }
#pragma unroll
  for (int i = 0; i < 7; ++i) { SGB(0x8, 1); SGB(0x4, 1); SGB(0x2, 1); }
  if (CS) { SGB(0x8, 1); SGB(0x2, 2); SGB(0x8, 1); SGB(0x2, 2); }
  SGB(0x8, 1);
  SB0();
  wait_lgkm0();
  bar();
}
DI void addr_init(Addr& a, unsigned xbase, unsigned wbase, int wj, int cx, int lane) {
  const int g = lane >> 4, q = (lane >> 2) & 3, p = lane & 3, row = 4 * g + q;
  a.x = xbase + lane * 16;
#pragma unroll
  for (int c = 0; c < 4; ++c) a.w[c] = wbase + row * 256 + (((2 * (4 * wj + (c ^ cx)) + (p >> 1)) ^ ((row & 7) << 1)) * 16) + (p & 1) * 8;
}
template <bool WA, int NSLOT, int SLOTB> DI void gemm_steps(f32x4 (&acc)[8][4], Frags& F0, Frags& F1, Addr& a, int& slot, int n) {
  for (int t = 0; t < n; t += 2) {
    gstep<WA, NSLOT, SLOTB>(acc, F0, F1, a, slot);
    gstep<WA, NSLOT, SLOTB>(acc, F1, F0, a, slot);
  }
}

DI u32x4 pack8(const f32x4& a, const f32x4& b) { return (u32x4){cvtpk(a[0], a[1]), cvtpk(a[2], a[3]), cvtpk(b[0], b[1]), cvtpk(b[2], b[3])}; }
DI void ystep_park(f32x4 (&acc)[8][4], Frags& Fn, const Frags& Fc, Addr& a, int& slot, unsigned pm) {
  s16x4 h[8];
  SB0();
#pragma unroll
  for (int k = 0; k < 16; ++k) {
    const int m = k >> 2, j = k & 3;
    *(LAS u32x4*)(size_t)(pm + j * 16384 + m * 1024) = pack8(acc[2 * m][j], acc[2 * m + 1][j]);
    asm volatile("v_mfma_f32_16x16x32_bf16 %0, %1, %2, 0" : "+v"(acc[2 * m][j]) : "v"(Fc.w[j]), "v"(Fc.x[2 * m]));
    if (k < 8) h[k] = __builtin_amdgcn_ds_read_tr16_b64_v4i16((LAS s16x4*)(size_t)(a.w[k >> 1] + (k & 1) * 4096));
    else Fn.x[k - 8] = *(LAS bf16x8*)(size_t)(a.x + (k - 8) * 1024);
    asm volatile("v_mfma_f32_16x16x32_bf16 %0, %1, %2, 0" : "+v"(acc[2 * m + 1][j]) : "v"(Fc.w[j]), "v"(Fc.x[2 * m + 1]));
    SB0();
  }
#pragma unroll
  for (int c = 0; c < 4; ++c) Fn.w[c] = __builtin_shufflevector(h[2 * c], h[2 * c + 1], 0, 1, 2, 3, 4, 5, 6, 7);
  slot = 2;
  addr_add(a, 24576);
  wait_lgkm0();
  bar();
}
DI f32x2 gate2(f32x2 u, f32x2 z) {
  f32x2 e = {__builtin_amdgcn_exp2f(z[0]), __builtin_amdgcn_exp2f(z[1])};
  e += 1.f;
  return u * (f32x2){__builtin_amdgcn_rcpf(e[0]), __builtin_amdgcn_rcpf(e[1])};
}
DI unsigned gelu2(f32x2 u) {
  const f32x2 r = gate2(u, u * __builtin_elementwise_fma(u * u, (f32x2){-0.1029432397f, -0.1029432397f}, (f32x2){-2.302208198f, -2.302208198f}));
  return cvtpk(r[0], r[1]);
}
DI unsigned silu2(f32x2 y) { const f32x2 r = gate2(y, y * -1.4426950409f); return cvtpk(r[0], r[1]); }
DI u32x4 ldsr(unsigned a) { return *(LAS u32x4*)(size_t)a; }
DI void add8(f32x4& a, f32x4& b, const u32x4& w) {
  a += (f32x4){bflo(w[0]), bfhi(w[0]), bflo(w[1]), bfhi(w[1])};
  b += (f32x4){bflo(w[2]), bfhi(w[2]), bflo(w[3]), bfhi(w[3])};
}

struct Att { bf16x8 q[8]; f32x16 o[4]; f32x16 negm, sE, sO; u32x4 pO[2]; bf16x8 pre[4]; float l; };
DI float vmax16(const f32x16& a) {
  float m = fmaxf(fmaxf(a[0], a[1]), a[2]);
#pragma unroll
  for (int i = 3; i < 15; i += 2) m = fmaxf(fmaxf(m, a[i]), a[i + 1]);
  m = fmaxf(m, a[15]);
  auto sw = __builtin_amdgcn_permlane32_swap(__float_as_uint(m), __float_as_uint(m), false, false);
  return fmaxf(__uint_as_float(sw[0]), __uint_as_float(sw[1]));
}
DI bf16x8 ldfrag(unsigned a) { return *(LAS bf16x8*)(size_t)a; }
DI void sm1(f32x16& s, int e, u32x4 (&w)[2], float& l) {
  if (e < 16) {
    float x = s[e];
    x = __builtin_amdgcn_exp2f(x);
    asm volatile("" : "+v"(x));
    s[e] = x;
  }
  if (e >= 1) {
    l += s[e - 1];
    asm volatile("" : "+v"(l));
    if (!(e & 1)) { unsigned c = cvtpk(s[e - 2], s[e - 1]); asm volatile("" : "+v"(c)); w[(e - 1) >> 3][((e - 1) >> 1) & 3] = c; }
  }
}
#define SETPRIO(x) __builtin_amdgcn_s_setprio(x)
#define MFMA32(a, b, c) __builtin_amdgcn_mfma_f32_32x32x16_bf16(a, b, c, 0, 0, 0)
#define PFRAG(w) __builtin_bit_cast(bf16x8, w)
template <int POS, class F> DI void att_tile(Att& A, unsigned kv, unsigned kvn, F&& gap) {
  bf16x8 f[36];
  u32x4 pE[2];
  const f32x16 zero = {};
  asm volatile("" : "+v"(kv), "+v"(kvn));
#pragma unroll
  for (int i = 0; i < 4; ++i) f[i] = A.pre[i];
  SB0();
  if (POS < 2) SETPRIO(3);
#pragma unroll
  for (int n = 0; n < 8; ++n) {
    if (POS == 0) A.sE = MFMA32(f[n], A.q[n], n ? A.sE : zero);
    else A.o[n & 3] = MFMA32(f[n], PFRAG(A.pO[n >> 2]), A.o[n & 3]);
    f[n + 4] = ldfrag(kv + (n + 4) * 1024);
    if (POS && n <= 2) sm1(A.sO, 14 + n, A.pO, A.l);
    if (POS && n >= 2) sm1(A.sE, n - 2, pE, A.l);
    gap(n);
    SB0();
  }
  if (POS == 0) {
    const float mx = vmax16(A.sE);
#pragma unroll
    for (int i = 0; i < 16; ++i) { A.sE[i] -= mx; A.negm[i] = -mx; }
    SB0();
  }
  if (POS < 2) SETPRIO(2);
#pragma unroll
  for (int n = 8; n < 16; ++n) {
    A.sO = MFMA32(f[n], A.q[n - 8], n > 8 ? A.sO : A.negm);
    f[n + 4] = ldfrag(kv + (n + 4) * 1024);
    if (POS) sm1(A.sE, n - 2, pE, A.l);
    else if (n < 15) { sm1(A.sE, 2 * (n - 8), pE, A.l); sm1(A.sE, 2 * (n - 8) + 1, pE, A.l); }
    gap(n);
    SB0();
  }
  if (POS < 2) SETPRIO(1);
#pragma unroll
  for (int n = 16; n < 24; ++n) {
    A.o[n & 3] = MFMA32(f[n], PFRAG(pE[(n >> 2) & 1]), (POS == 0 && n < 20) ? zero : A.o[n & 3]);
    f[n + 4] = ldfrag(kv + (n + 4) * 1024);
    if (n <= 18) sm1(A.sE, n - 2, pE, A.l);
    if (n >= 18) {
      if (POS < 2) sm1(A.sO, n - 18, A.pO, A.l);
      else { sm1(A.sO, 2 * (n - 18), A.pO, A.l); sm1(A.sO, 2 * (n - 18) + 1, A.pO, A.l); }
    }
    gap(n);
    SB0();
  }
  if (POS < 2) SETPRIO(0);
#pragma unroll
  for (int n = 24; n < 32; ++n) {
    if (POS < 2) {
      A.sE = MFMA32(f[n], A.q[n - 24], n > 24 ? A.sE : A.negm);
      f[n + 4] = ldfrag(n < 28 ? kv + (n + 4) * 1024 : kvn + (n - 28) * 1024);
      sm1(A.sO, n - 18, A.pO, A.l);
    } else {
      A.o[n & 3] = MFMA32(f[n], PFRAG(A.pO[(n >> 2) & 1]), A.o[n & 3]);
      if (n < 28) f[n + 4] = ldfrag(kv + (n + 4) * 1024);
      if (n < 26) { sm1(A.sO, 2 * (n - 18), A.pO, A.l); sm1(A.sO, 2 * (n - 18) + 1, A.pO, A.l); }
      if (n == 26) sm1(A.sO, 16, A.pO, A.l);
    }
    gap(n);
    SB0();
  }
  if (POS < 2) {
#pragma unroll
    for (int i = 0; i < 4; ++i) A.pre[i] = f[32 + i];
  }
}
DI void att_redo(Att& A, rsrc_t rkv, unsigned vo) {
  float m = -3.0e38f, l = 0.f;
#pragma unroll
  for (int d = 0; d < 4; ++d)
#pragma unroll
    for (int i = 0; i < 16; ++i) A.o[d][i] = 0.f;
  for (int blk = 0; blk < 16; ++blk) {
    const int kb = (blk ? 2 * blk - 1 : 0) * 8192, vb = (blk < 15 ? 2 * blk + 2 : 31) * 8192;
    f32x16 s;
#pragma unroll
    for (int i = 0; i < 16; ++i) s[i] = 0.f;
#pragma unroll
    for (int n = 0; n < 8; ++n) s = MFMA32(PFRAG(__builtin_amdgcn_raw_buffer_load_b128(rkv, vo + n * 1024, kb, 16)), A.q[n], s);
    const float mn = fmaxf(m, vmax16(s)), fct = __builtin_amdgcn_exp2f(m - mn);
    m = mn;
    l *= fct;
    u32x4 p[2];
#pragma unroll
    for (int i = 0; i < 16; ++i) s[i] -= mn;
#pragma unroll
    for (int i = 0; i < 17; ++i) sm1(s, i, p, l);
#pragma unroll
    for (int d = 0; d < 4; ++d)
#pragma unroll
      for (int i = 0; i < 16; ++i) A.o[d][i] *= fct;
#pragma unroll
    for (int n = 0; n < 8; ++n) A.o[n & 3] = MFMA32(PFRAG(__builtin_amdgcn_raw_buffer_load_b128(rkv, vo + n * 1024, vb, 16)), PFRAG(p[n >> 2]), A.o[n & 3]);
  }
  A.l = l;
}

__global__ __launch_bounds__(512, 2) void k_main(const u16* __restrict__ x, const u16* __restrict__ kc, const u16* __restrict__ vc, const u16* __restrict__ wq, const u16* __restrict__ wo,
                                                 const u16* __restrict__ w1, const u16* __restrict__ b1, const u16* __restrict__ w2,
                                                 const u16* __restrict__ b2, char* __restrict__ ws, u16* __restrict__ out) {
  extern __shared__ __attribute__((aligned(16))) char smem[];
  LAS char* lds = (LAS char*)smem;
  const unsigned lb = (unsigned)(size_t)lds;
  const int tid = threadIdx.x, wid = __builtin_amdgcn_readfirstlane(tid >> 6);
  const int g8 = blockIdx.x & 7, idx = blockIdx.x >> 3;
  const rsrc_t rcode = mkrsrc((const void*)(__builtin_amdgcn_s_getpc() & ~255ull), CODE_BYTES);
  auto code_pf = [&](unsigned lds_addr) {
    asm volatile("s_mov_b32 m0, %0\n\ts_nop 0\n\tbuffer_load_dword %3, %1, %2 offen lds" ::"s"(lds_addr), "s"(rcode), "s"((idx % 9) * 8192), "v"(lane_id() * 128) : "memory");
  };
  if (wid == 7) code_pf(lb + 161792);
  STAMP(1, 0);
  STAMP(0, 0);
  const bool hasU = idx < 28;
  const int uh = g8 & 1, dq = idx & 3, ft = 7 * (g8 >> 1) + (idx >> 2), ut = ft * 4 + dq;
  const int kvh = g8 >> 1, head = 8 * kvh + (idx & 7), rb = 4 * (g8 & 1) + (idx >> 3);
  const int dc = idx & 7, tile = rb * 8 + dc;
  const int lw = wid - 4, wi = (wid >> 1) & 1, wj = wid & 1;
  constexpr int QS = 24576;
  constexpr int PARK = 98304;
  const rsrc_t rf = mkrsrc(ws + WS_FLAG, F_END + 256);
  const rsrc_t rkv = mkrsrc(ws + WS_KV + (size_t)kvh * 262144, 262144);
  const rsrc_t rxb = mkrsrc(ws + WS_XB + (size_t)tile * 393216, 393216);
  struct Strm { rsrc_t r; unsigned v0, v1, step, p0, p1, p2; int rot; };
  auto strm_x = [&](Strm& S, const char* base, unsigned bytes, unsigned step, int xr) {
    const int ln = lane_id();
    S.r = mkrsrc(base, bytes); S.v0 = ln * 16 + (xr & 1) * 1024; S.v1 = S.v0 ^ 1024; S.step = step;
    S.p0 = (xr >> 1) * 2048; S.p1 = S.p0 ^ 2048; S.p2 = 4096; S.rot = 0;
  };
  auto strm_w = [&](Strm& S, const char* base, unsigned rows, unsigned ld, bool nat) {
    const int ln = lane_id(), q4 = ln >> 4;
    const unsigned c0 = ((ln & 15) ^ (q4 << 1)) * 16;
    S.r = mkrsrc(base, (rows - 1) * ld + 256); S.v0 = q4 * ld + c0; S.v1 = ((nat ? 8 : 4) + q4) * ld + (c0 ^ 128); S.step = 32 * ld;
    S.p0 = 0; S.p1 = (nat ? 16 : 8) * ld; S.p2 = (nat ? 4 : 16) * ld; S.rot = 0;
  };
  auto issue = [&](const Strm& S, int k, int src = -1) {
    const unsigned s = lb + (k & 3) * QS + lw * 8192;
    if (src < 0) src = (k + S.rot) & 31;
#pragma unroll
    for (int i = 0; i < 4; ++i) {
      const unsigned so = src * S.step + ((i & 1) ? S.p1 : S.p0) + (i >> 1) * S.p2;
      dma16s(S.r, s + i * 2048, S.v0, so); dma16s(S.r, s + i * 2048 + 1024, S.v1, so);
    }
  };
  constexpr int QW = 98304, HST = 131072, HRS = 2112, RST = HST + 8 * HRS, PKQ = 148480, STT = 151552, CSX = 153600;
  const int lane0 = tid & 63;
  const rsrc_t rq = lw < 2 ? mkrsrc(x + (size_t)(rb * 256 + lw * 128) * DM, 128 * DM * 2) : mkrsrc((const char*)wq + head * 256, (DM - 1) * HD * 2 + 256);
  const unsigned qv0 = lw < 2 ? (lane0 >> 3) * 2048 + (((lane0 & 7) ^ (lane0 >> 4)) * 16) : (lane0 >> 4) * (HD * 2) + (((lane0 & 15) ^ ((lane0 >> 4) << 1)) * 16);
  const unsigned qv1 = lw < 2 ? (qv0 ^ 64) + 8 * 2048 : (qv0 ^ 128) + 8 * (HD * 2);
  const bool qx = lw < 2;
  const unsigned qp1 = qx ? 32768 : 16 * HD * 2, qp2 = qx ? 65536 : 4 * HD * 2;
  const unsigned qlb = lb + (qx ? lw * 16384 : QW), qlw = qx ? 98304 : 32768, qom = qx ? 0x20780 : 0x7c0000;
  unsigned ql = qx ? 65536 : 0, qdl = 8192, qo = rb * (qx ? 256 : 4 * 32 * HD * 2), qdo = qx ? 131072 : 32 * HD * 2;
  auto issq = [&]() {
    const unsigned s = qlb + ql;
    dma16(rq, s, qv0, qo); dma16(rq, s + 1024, qv1, qo);
    dma16(rq, s + 2048, qv0, qo + qp1); dma16(rq, s + 3072, qv1, qo + qp1);
    dma16(rq, s + 4096, qv0, qo + qp2); dma16(rq, s + 5120, qv1, qo + qp2);
    dma16(rq, s + 6144, qv0, qo + qp1 + qp2); dma16(rq, s + 7168, qv1, qo + qp1 + qp2);
    ql += qdl; ql = ql >= qlw ? ql - qlw : ql; qdl ^= qx ? (8192 ^ 24576) : 0;
    qo = (qo + qdo) & qom; qdo ^= qx ? 128 : 0;
  };
  auto h_out = [&](int it) {
    const rsrc_t rh = mkrsrc(ws + WS_HT + (size_t)(blockIdx.x >> 2) * 16384 + (blockIdx.x & 3) * 256, 3 * 1048576 + 16384);
    const unsigned a0 = HST + ((lane0 >> 2) & 3) * HRS + (it * 4 + (lane0 >> 4)) * 32 + (lane0 & 3) * 8;
    s16x4 lo = __builtin_amdgcn_ds_read_tr16_b64_v4i16((LAS s16x4*)(lds + a0));
    s16x4 hi = __builtin_amdgcn_ds_read_tr16_b64_v4i16((LAS s16x4*)(lds + a0 + 4 * HRS));
    st16s<0>(__builtin_bit_cast(u32x4, __builtin_shufflevector(lo, hi, 0, 1, 2, 3, 4, 5, 6, 7)), rh, (lane0 >> 4) * 1024 + (lane0 & 15) * 16,
             (it >> 2) * 1048576 + (it & 3) * 4096);
  };
  auto st_out = [&]() { st16s<0>(*(LAS u32x4*)(lds + RST + (lane0 & 3) * 16), mkrsrc(ws + WS_ST + blockIdx.x * 64, 64), lane0 * 16, 0); };
  Tag tag;
  {
    const int b = blockIdx.x, lane = lane0;
    u32x4 A, B;
    u32x2 ep;
    asm volatile("global_load_dwordx4 %0, %3, %4\n\tglobal_load_dwordx4 %1, %3, %4 offset:1024\n\tglobal_load_dwordx2 %2, %5, %6 sc1"
                 : "=&v"(A), "=&v"(B), "=&v"(ep) : "v"(lane * 16), "s"(x + (size_t)(8 * b + wid) * DM), "v"(F_EP), "s"(ws + WS_FLAG) : "memory");
    const bool strm = hasU && wid >= 4 && wid < 7;
    if (strm || wid == 6) { issq(); issq(); if (lw == 2) { issq(); issq(); wait_vm<32>(); } else wait_vm<16>(); } else wait_vm<0>();
    asm volatile("" : "+v"(A), "+v"(B), "+v"(ep));
    unsigned v[8] = {A[0], A[1], A[2], A[3], B[0], B[1], B[2], B[3]};
    const unsigned long long e64 = ((unsigned long long)(unsigned)__builtin_amdgcn_readfirstlane(ep[1]) << 32) | (unsigned)__builtin_amdgcn_readfirstlane(ep[0]);
    const unsigned long long tg = e64 * 0x9E3779B97F4A7C15ull + 0x632BE59BD9B4E019ull;
    tag.lo = (unsigned)tg; tag.hi = (unsigned)(tg >> 32);
    *(LAS f32x2*)(lds + RST + wid * 8) = ln16(v);
    ln16(v);
    *(LAS u32x4*)(lds + HST + wid * HRS + lane * 16) = (u32x4){v[0], v[1], v[2], v[3]};
    *(LAS u32x4*)(lds + HST + wid * HRS + 1024 + lane * 16) = (u32x4){v[4], v[5], v[6], v[7]};
    wait_lgkm0();
  }
  STAMP(0, 1);
  if (!hasU) {
    const int sp = g8 * 4 + (idx - 28), lane = lane0, h = lane >> 5;
    const unsigned long long t_in = __builtin_amdgcn_s_memrealtime();
    bar();
    h_out(2 * wid); h_out(2 * wid + 1);
    if (wid == 0) st_out();
    wait_vm<0>();
    bar();
    if (wid == 0) signal<1>(ws, F_L + blockIdx.x * 8, tag);
    unsigned e[4][8];
#pragma unroll
    for (int i = 0; i < 4; ++i) {
      const int kh = sp >> 3, unit = (sp & 7) * 4 + i, frag = wid;
      const bool isK = unit == 0 || ((unit & 1) && unit < 31);
      const u16* sk = kc + ((size_t)kh * SEQ + 1536 + 32 * ((unit + 1) >> 1) + (lane & 31)) * 128 + 32 * (frag >> 1) + 4 * (2 * (frag & 1) + h);
      const u16* sv = vc + ((size_t)kh * SEQ + 1536 + 32 * ((unit - 1) >> 1) + 16 * (frag >> 2) + 4 * h) * 128 + 32 * (frag & 3) + (lane & 31);
      const u16* src = isK ? sk : sv;
      const int s1 = isK ? 1 : 128, s4 = isK ? 16 : 1024;
#pragma unroll
      for (int j2 = 0; j2 < 8; ++j2) e[i][j2] = src[(j2 >> 2) * s4 + (j2 & 3) * s1];
    }
    unsigned pw[8];
#pragma unroll
    for (int i = 0; i < NSP; ++i) pw[i] = *(const unsigned*)((const char*)w1 + (UROT ? (size_t)(sp >> 2) * PFB + (size_t)i * (PFB / 8) + (size_t)((sp & 3) * 448 + (tid < 448 ? tid : 0)) * 128
                                                                          : (size_t)((i >> 1) + 4 * (i & 1)) * PFB + (size_t)(sp * 448 + (tid < 448 ? tid : 0)) * 128));
    SB0();
#pragma unroll
    for (int i = 0; i < 4; ++i)
      st16s<0>((u32x4){e[i][0] | (e[i][1] << 16), e[i][2] | (e[i][3] << 16), e[i][4] | (e[i][5] << 16), e[i][6] | (e[i][7] << 16)},
               mkrsrc(ws + WS_KV, 1048576), tid * 16, (sp * 4 + i) * 8192);
#pragma unroll
    for (int i = 0; i < NSP; ++i) asm volatile("" ::"v"(pw[i]));
    wait_vm<0>();
    bar();
    if (wid == 0) signal<1>(ws, F_X + (sp >> 3) * 128 + (sp & 7) * 8, tag);
    for (int i = 0; i < 1000 && __builtin_amdgcn_s_memrealtime() - t_in < SPARE_T; ++i) __builtin_amdgcn_s_sleep(8);
    if (wid >= 4 && wid < 6) { issq(); issq(); }
  }
  bf16x8 qh[8];
  f32x4 uk[4][4];
  auto kv8 = [&](int t, int slot) {
    const unsigned v = lane_id() * 16;
    dma4k(rkv, lb + slot * 32768 + lw * 8192, v, t * 32768 + lw * 8192);
    dma4k(rkv, lb + slot * 32768 + lw * 8192 + 4096, v, t * 32768 + lw * 8192 + 4096);
  };
  if (wid < 4) {
    f32x4 acc[8][4], cs[2];
    Frags F0, F1;
    Addr a;
    int xs = 2, wsl = 1;
    {
      const int ln = lane_id(), r = ln & 15;
      addr_init(a, 0, lb + QW, wj, 2 * wi, ln);
      a.x = lb + 65536 + wi * 16384 + r * 128 + (((ln >> 4) ^ (r >> 1)) * 16);
    }
    bar();
    load_frags<2048>(F0, a);
    a.x ^= 64;
#pragma unroll
    for (int c = 0; c < 4; ++c) a.w[c] += 8192;
    wait_lgkm0();
    bar();
    STAMP(1, 1);
    {
      const unsigned o1 = (rb & 3) == 3 ? 0 : 0x3f803f80, o2 = (rb & 3) == 2 ? 0x3f803f80 : 0;
      u32x4 ones = {o1, o1, o1, o1};
      qstep<1, true, true>(acc, cs, ones, o1, F1, F0, a, xs, wsl);
      for (int t = 0; t < 4; ++t) {
        const unsigned on = t == 0 ? o1 : t == 3 ? 0 : o2;
        qstep<0, true>(acc, cs, ones, on, F0, F1, a, xs, wsl);
        qstep<1, true>(acc, cs, ones, on, F1, F0, a, xs, wsl);
      }
      const int ln = lane_id();
      const unsigned csa = lb + CSX + ((ln & 15) ? 7168 : (64 * wj + 32 * wi) * 4) + (ln & 48);
      *(LAS f32x4*)(size_t)csa = cs[0];
      *(LAS f32x4*)(size_t)(csa + 64) = cs[1];
      for (int t = 0; t < 11; ++t) { qstep<0, false>(acc, cs, ones, 0, F0, F1, a, xs, wsl); qstep<1, false>(acc, cs, ones, 0, F1, F0, a, xs, wsl); }
      addr_init(a, lb + wi * 8192, lb + 16384, wj, 0, lane_id());
      qstep<2, false>(acc, cs, ones, 0, F0, F1, a, xs, wsl);
    }
    STAMP(1, 2);
    f32x4 c0[4];
    {
      const int ln = lane_id(), g = ln >> 4;
      u32x2 bv[4];
#pragma unroll
      for (int j = 0; j < 4; ++j) bv[j] = *(LAS u32x2*)(lds + CSX + 5120 + (64 * wj + 16 * j + 4 * g) * 2);
      f32x4 cq[4];
#pragma unroll
      for (int j = 0; j < 4; ++j) cq[j] = *(LAS f32x4*)(lds + CSX + 512 + (64 * wj + 16 * (j ^ (2 * wi)) + 4 * g) * 4);
      f32x2 st[8];
#pragma unroll
      for (int i = 0; i < 8; ++i) st[i] = *(LAS f32x2*)(lds + STT + (128 * wi + 16 * i + (ln & 15)) * 8);
      wait_lgkm0();
      bar();
      const unsigned qa = lb + (wi ? 98304 : 131072) + (4 * wj + (g >> 1)) * (wi ? 4096 : 1024) + ((ln & 15) + 32 * (g & 1)) * 16;
      const unsigned sq = wi ? 1024 : 8192, m0 = wi ? 8192 : 0, m1 = wi ? 0 : 2048;
#pragma unroll
      for (int i = 0; i < 8; ++i) {
        const float sc = st[i][1] * 0.12751743082f, nm = -st[i][0];
#pragma unroll
        for (int mm = 0; mm < 2; ++mm)
          *(LAS u32x4*)(size_t)(qa + (i >> 1) * sq + (mm ? m1 : m0) + (i & 1) * 256) =
              pack8((acc[i][2 * mm] + cq[2 * mm] * nm) * sc, (acc[i][2 * mm + 1] + cq[2 * mm + 1] * nm) * sc);
      }
#pragma unroll
      for (int j = 0; j < 4; ++j) c0[j] = uh ? (f32x4){0.f, 0.f, 0.f, 0.f} : (f32x4){bflo(bv[j][0]), bfhi(bv[j][0]), bflo(bv[j][1]), bfhi(bv[j][1])};
    }
    STAMP(1, 3);
    if (hasU) {
      int slot = 1;
      gstep<true, 4, QS, true>(acc, F1, F0, a, slot, c0);
      gemm_steps<true, 4, QS>(acc, F0, F1, a, slot, 30);
      gstep<true, 4, QS>(acc, F0, F1, a, slot);
      STAMP(1, 4);
      const rsrc_t rux = mkrsrc(ws + WS_UX + (size_t)(ut * 2 + (uh ^ 1)) * 32768 + wid * 8192, 8192);
      const int ln = lane_id();
#pragma unroll
      for (int ii = 0; ii < 4; ++ii) {
        st16s<0>(pack8(acc[2 * ii + 1][0], acc[2 * ii + 1][1]), rux, ln * 16, ii * 2048);
        st16s<1024>(pack8(acc[2 * ii + 1][2], acc[2 * ii + 1][3]), rux, ln * 16, ii * 2048);
#pragma unroll
        for (int j = 0; j < 4; ++j) uk[ii][j] = acc[2 * ii][j];
      }
    } else {
      wait_lgkm0();
      bar();
    }
    STAMP(1, 5);
    const int ln = lane_id();
#pragma unroll
    for (int ks = 0; ks < 8; ++ks) qh[ks] = *(LAS bf16x8*)(lds + 131072 + wid * 8192 + ks * 1024 + ln * 16);
  } else {
    const int lane = lane0;
    Strm U;
    if (lw < 2) strm_x(U, ws + WS_HT + (size_t)dq * 1048576 + uh * 524288 + lw * 8192, 524288 - lw * 8192, 16384, uh);
    else strm_w(U, (const char*)w1 + (size_t)uh * 1024 * (FF * 2) + ft * 256, 1024, FF * 2, true);
    U.rot = UROT * dq;
    const unsigned pk = lb + PKQ + lw * 512;
    if (lw < 3) {
      if (lw < 2) wait_vm<0>(); else wait_vm<24>();
      bar();
      if (lw < 2) { issq(); issq(); } else wait_vm<16>();
      bar();
      if (lw < 2) issq();
      for (int t = 1; t <= 21; ++t) { issq(); wait_vm<16>(); bar(); }
      peek(rf, pk, F_L + lw * 512); peek(rf, pk + 256, F_L + lw * 512 + 256);
      for (int t = 22; t <= 26; ++t) { issq(); wait_vm<16>(); bar(); }
      issq(); wait_vm<16>();
    } else {
      peek(mkrsrc(b1, FF * 2), lb + CSX + 5120, ft * 256);
      bar();
      if (hasU) {
        for (int t = 0; t <= 7; ++t) { h_out(2 * t); h_out(2 * t + 1); bar(); }
        st_out();
        bar(); bar(); bar();
        st16s<0>(*(LAS u32x4*)(lds + CSX + (lane & 31) * 16), mkrsrc(ws + WS_CS + (head * 8 + rb) * 512, 512), lane * 16, 0);
        bar(); bar(); bar();
        wait_vm<0>();
        signal<1>(ws, F_L + blockIdx.x * 8, tag);
      } else {
        for (int t = 0; t <= 13; ++t) bar();
        spin_until(ws, F_X + kvh * 128, 8, tag);
      }
      const rsrc_t rp = mkrsrc(w1, hasU ? SEQ * FF * 2 : 0);
      for (int t = 14; t <= 21; ++t) {
        if (!UROT && (t - 14) % PFS == 0 && NSP + (t - 14) / PFS < 8)
          asm volatile("s_mov_b32 m0, %0\n\ts_nop 0\n\tbuffer_load_dword %2, %1, %3 offen lds" ::"s"(lb + 161792), "s"(rp), "v"(((g8 * 28 + idx) * 64 + lane) * 128),
                       "s"((((NSP + (t - 14) / PFS) >> 1) + 4 * ((NSP + (t - 14) / PFS) & 1)) * 1835008) : "memory");
        bar();
      }
      peek(rf, pk, F_L + lw * 512); peek(rf, pk + 256, F_L + lw * 512 + 256);
      for (int t = 22; t <= 26; ++t) bar();
      wait_vm<0>();
    }
    peek_check(ws, pk, F_L + lw * 512, 32, tag); peek_check(ws, pk + 256, F_L + lw * 512 + 256, 32, tag);
    bar();
    if (blockIdx.x == 0 && lw == 3) signal<1>(ws, F_EP, tag);
    if (lw == 2) issq();
    if (lw == 3) {
      const rsrc_t rs = mkrsrc(ws + WS_ST + rb * 2048, 2048);
      dma16s(rs, lb + STT, lane * 16, 0); dma16s(rs, lb + STT + 1024, lane * 16, 1024);
      dma4k(mkrsrc(ws + WS_CS + head * 4096, 4096), lb + CSX + 1024, lane * 16, 0);
    }
    const bool us = hasU && lw < 3;
    if (us) issue(U, 0); else if (!hasU) kv8(0, 0);
    wait_vm<16>(); bar();
    if (lw == 3 && hasU) wait_vm<0>(); else wait_vm<8>();
    bar();
    if (us) issue(U, 1); else if (!hasU) kv8(1, 1);
    if (lw == 3) {
      const LAS f32x2* cp = (const LAS f32x2*)(lds + CSX + 1024) + lane;
      *(LAS f32x2*)(lds + CSX + 512 + lane * 8) = ((cp[0] + cp[64]) + cp[128]) + ((cp[256] + cp[320]) + cp[384]);
      wait_lgkm0();
    }
    bar();
    wait_vm<8>(); bar();
    if (us) issue(U, 2); else if (!hasU) kv8(2, 2);
    wait_vm<8>();
    bar();
    bar();
    if (us) issue(U, 3);
    if (hasU) {
      if (lw < 3) { for (int t = 1; t <= 28; ++t) { issue(U, t + 3); wait_vm<16>(); bar(); } }
      else {
        for (int t = 1; t <= 20; ++t) bar();
        spin_until(ws, F_X + kvh * 128, 8, tag);
        for (int t = 21; t <= 27; ++t) bar();
        bar();
        if (CODE2) { code_pf(lb); wait_vm<0>(); }
      }
      wait_vm<8>(); bar();
      kv8(0, 0); wait_vm<8>(); bar();
      kv8(1, 1); bar();
      kv8(2, 2); bar();
    } else bar();
#pragma unroll
    for (int ks = 0; ks < 8; ++ks) qh[ks] = *(LAS bf16x8*)(lds + 98304 + ks * 4096 + lw * 1024 + lane * 16);
    wait_vm<8>();
  }
  wait_lgkm0();
  bar();
  STAMP(1, 6);
  unsigned pf = 0;
  Strm Y;
  Frags FY;
  Addr aY;
  {
    const int lane = lane_id();
    Att A;
#pragma unroll
    for (int ks = 0; ks < 8; ++ks) A.q[ks] = qh[ks];
    A.l = 0.f;
    const unsigned kvl = lb + lane * 16;
#pragma unroll
    for (int i = 0; i < 4; ++i) A.pre[i] = ldfrag(kvl + i * 1024);
    const bool strm = wid >= 4 && wid < 7, hw = wid >= 4;
    const rsrc_t rp2 = mkrsrc(w2, SEQ * FF * 2), rpo = mkrsrc(wo, HD * DM * 2);
    const unsigned pv = (blockIdx.x * 64 + lane) * 128;
#define PF(W, R, C) pf_line<W>(pf, wid, R, pv, (C) * 2097152u)
    const int fp = F_P + (ut * 2 + uh) * 64 + (wid & 3) * 8;
    const bool cu = !hw && hasU;
    att_tile<0>(A, kvl + 0 * 32768, kvl + 1 * 32768, [&](int n) { if (n == 1 && hw) kv8(3, 3); if (n == 20 && cu) { wait_vm<0>(); signal<1>(ws, fp ^ 64, tag); }
      if (n == 23) PF(0, rp2, 0); });
    if (hw) wait_vm<8>();
    bar();
    att_tile<1>(A, kvl + 1 * 32768, kvl + 2 * 32768, [&](int n) {
      if (n == 1 && hw) kv8(4, 0); if (n == 28 && hw) peek(rf, lb + 131072 + lw * 256, fp); if (n == 3) PF(1, rp2, 1); if (n == 10) PF(2, rp2, 2); });
    if (hw) wait_vm<9>();
    bar();
    att_tile<1>(A, kvl + 2 * 32768, kvl + 3 * 32768, [&](int n) {
      if (n == 8 && hw) {
        wait_vm<0>();
        if (hasU) peek_check(ws, lb + 131072 + lw * 256, fp, 1, tag);
        const rsrc_t rux = mkrsrc(ws + WS_UX + (size_t)(ut * 2 + uh) * 32768, hasU ? 32768 : 0);
        dma4k(rux, lb + 32768 + lw * 8192, lane * 16, lw * 8192); dma4k(rux, lb + 32768 + lw * 8192 + 4096, lane * 16, lw * 8192 + 4096);
      }
      if (n == 13) PF(3, rp2, 3); if (n == 21) PF(0, rp2, 4); if (n == 29) PF(1, rp2, 5); });
    if (hw) wait_vm<0>();
    bar();
    if (hw) { kv8(5, 4); kv8(7, 2); }
    if (cu) {
      u32x4 p[8];
      p[0] = ldsr(kvl + 32768 + wid * 8192);
#pragma unroll
      for (int k = 0; k < 8; ++k) {
        if (k < 7) p[k + 1] = ldsr(kvl + 32768 + wid * 8192 + (k + 1) * 1024);
        f32x4 a = uk[k >> 1][2 * (k & 1)], b = uk[k >> 1][2 * (k & 1) + 1];
        add8(a, b, p[k]);
        p[k] = (u32x4){gelu2(a.xy), gelu2(a.zw), gelu2(b.xy), gelu2(b.zw)};
        asm volatile("" : "+v"(p[k]));
        SB0();
      }
      bar();
      const rsrc_t ru = mkrsrc(ws + WS_U + (size_t)((2 * dq + wi) * 112 + 4 * ft + 2 * wj) * 8192 + uh * 1024, 16384);
#pragma unroll
      for (int k = 0; k < 8; ++k) st16s<0>(p[k], ru, lane * 16, (k & 1) * 8192 + (k >> 1) * 2048);
    } else bar();
    att_tile<1>(A, kvl + 3 * 32768, kvl + 0 * 32768, [&](int n) {
      if (n == 20 && cu) { wait_vm<0>(); signal<1>(ws, F_U + ((2 * dq + wi) * 4 + kvh) * 256 + (((idx >> 2) * 2 + uh) * 2 + wj) * 8, tag); }
      if (n == 23) PF(2, rp2, 6); });
    if (hw) wait_vm<0>();
    bar();
    att_tile<1>(A, kvl + 0 * 32768, kvl + 4 * 32768, [&](int n) { if (n == 0 && hw) kv8(6, 3); if (n == 5) PF(3, rpo, 0); if (n == 18) PF(0, rpo, 1); });
    if (hw) wait_vm<0>();
    bar();
    att_tile<1>(A, kvl + 4 * 32768, kvl + 3 * 32768, [&](int n) { if (n == 1) PF(1, rpo, 2); if (n == 13) PF(2, rpo, 3); if (CODE2 && n == 24 && wid == 7) code_pf(lb + 50176); });
    if (wid == 7) wait_vm<0>();
    if (lw == 0) strm_x(Y, ws + WS_U + (size_t)(dc * 112 + 28 * kvh) * 8192, 28 * 8192, 8192, 0);
    else strm_w(Y, (const char*)w2 + (size_t)kvh * 896 * (SEQ * 2) + rb * 512 + ((lw - 1) & 1) * 256, 896, SEQ * 2, false);
    bar();
    att_tile<1>(A, kvl + 3 * 32768, kvl + 2 * 32768, [&](int n) {
      if (n == 4 && wid == 4) peek(rf, lb + 49152, F_U + (dc * 4 + kvh) * 256);
      if (n == 18 && wid == 4) { wait_vm<0>(); peek_check(ws, lb + 49152, F_U + (dc * 4 + kvh) * 256, 28, tag); }
      if (n == 19 && strm) issue(Y, 0); if (n == 27 && strm) issue(Y, 1); });
    bar();
    att_tile<2>(A, kvl + 2 * 32768, 0, [&](int n) {
      if (n == 9) { if (hw) wait_vm<8>(); bar(); if (hw) SETPRIO(3); }
      if (n == 10 && strm) issue(Y, 2); });
    STAMP(1, 7);
    if (__builtin_expect(__any(!(A.l <= 1.0e9f)), 0)) att_redo(A, rkv, lane * 16);
    addr_init(aY, lb, lb + 8192 + wi * 8192, wj, kvh, lane);
    if (!hw) load_frags(FY, aY);
    auto sw = __builtin_amdgcn_permlane32_swap(__float_as_uint(A.l), __float_as_uint(A.l), false, false);
    const float inv = __builtin_amdgcn_rcpf(__uint_as_float(sw[0]) + __uint_as_float(sw[1]));
    const unsigned oa = lb + PARK + wid * 2048 + ((lane >> 4) & 1) * 1024 + (lane >> 5) * 256 + (lane & 15) * 16;
#pragma unroll
    for (int d = 0; d < 4; ++d)
#pragma unroll
      for (int u = 0; u < 2; ++u) {
        const int r0 = 4 * u, r1 = 8 + 4 * u;
        *(LAS u32x4*)(size_t)(oa + d * 16384 + u * 512) =
            (u32x4){cvtpk(A.o[d][r0] * inv, A.o[d][r0 + 1] * inv), cvtpk(A.o[d][r0 + 2] * inv, A.o[d][r0 + 3] * inv),
                    cvtpk(A.o[d][r1] * inv, A.o[d][r1 + 1] * inv), cvtpk(A.o[d][r1 + 2] * inv, A.o[d][r1 + 3] * inv)};
      }
    wait_lgkm0();
  }
  STAMP(1, 8);
  if (wid < 4) {
    f32x4 acc[8][4];
    Frags F0 = FY, F1;
    Addr a = aY;
    int slot = 1;
    addr_add(a, QS);
    bar();
    STAMP(1, 9);
    gstep<false, 4, QS, true>(acc, F1, F0, a, slot);
    gemm_steps<false, 4, QS>(acc, F0, F1, a, slot, 26);
    addr_init(a, lb + wi * 8192, lb + 16384, wj, 0, lane_id());
    gstep<false, 4, QS>(acc, F0, F1, a, slot);
    STAMP(1, 10);
    asm volatile("s_waitcnt vmcnt(0)" : "+v"(pf)::"memory");
    ystep_park(acc, F1, F0, a, slot, lb + PARK + lane_id() * 16 + wid * 4096);
    STAMP(1, 11);
    gemm_steps<true, 4, QS>(acc, F0, F1, a, slot, 30);
    gstep<true, 4, QS>(acc, F0, F1, a, slot);
    STAMP(1, 12);
    const int lt = lane_id(), g = lt >> 4;
    const unsigned wo_ = wi * 8192 + wj * 2048;
#pragma unroll
    for (int dl = 1; dl < 4; ++dl)
#pragma unroll
      for (int h = 0; h < 2; ++h)
#pragma unroll
        for (int mm = 0; mm < 2; ++mm)
          st16s<0>(pack8(acc[4 * h + dl][2 * mm], acc[4 * h + dl][2 * mm + 1]), rxb, lt * 16, (kvh ^ dl) * 98304 + 49152 + (dl - 1) * 16384 + wo_ + h * 4096 + mm * 1024);
    const int fs = F_S + tile * 512 + wid * 32;
    wait_vm<8>(); signal<1>(ws, fs + (kvh ^ 1) * 128, tag);
    wait_vm<5>(); signal<1>(ws, fs + (kvh ^ 2) * 128 + 8, tag);
    wait_vm<2>(); signal<1>(ws, fs + (kvh ^ 3) * 128 + 16, tag);
    STAMP(1, 13);
    const int srow = rb * 256 + (8 * wi + kvh) * 16;
    const rsrc_t rx = mkrsrc(x + (size_t)srow * DM, 80 * DM * 2), rout = mkrsrc(out + (size_t)srow * DM, 80 * DM * 2);
    const unsigned vo = ((lt & 15) * DM + dc * 128 + wj * 64 + 16 * (g & 1) + 8 * (g >> 1)) * 2;
    u32x4 ps[3][4], xv[4], gv[4];
#pragma unroll
    for (int p = 0; p < 4; ++p) xv[p] = __builtin_amdgcn_raw_buffer_load_b128(rx, vo + (p >> 1) * (64 * DM * 2) + (p & 1) * 64, 0, 0);
    bar();
#pragma unroll
    for (int p = 0; p < 4; ++p) gv[p] = ldsr(lb + PARK + lt * 16 + wo_ + (p >> 1) * 4096 + (p & 1) * 1024);
    {
      GAS unsigned long long* fp = (GAS unsigned long long*)(ws + WS_FLAG + fs + kvh * 128) + (lt & 3);
      const unsigned long long want = ((unsigned long long)tag.hi << 32) | tag.lo;
      unsigned got = 0;
      for (unsigned sp = 0; got != 7; ++sp) {
        unsigned now = (unsigned)__ballot(__hip_atomic_load(fp, __ATOMIC_RELAXED, __HIP_MEMORY_SCOPE_AGENT) == want) & 7;
        if (sp >= (1u << 20)) now = 7;
        asm volatile("" ::: "memory");
#pragma unroll
        for (int k = 0; k < 3; ++k)
          if ((now & ~got) >> k & 1) {
#pragma unroll
            for (int p = 0; p < 4; ++p)
              ps[k][p] = __builtin_amdgcn_raw_buffer_load_b128(rxb, lt * 16 + (p & 1) * 1024, kvh * 98304 + 49152 + k * 16384 + wo_ + (p >> 1) * 4096, 16);
          }
        got |= now;
        if (got != 7) __builtin_amdgcn_s_sleep(1);
      }
    }
    STAMP(1, 14);
#pragma unroll
    for (int p = 0; p < 4; ++p) {
      f32x4 s0 = acc[4 * (p >> 1)][2 * (p & 1)], s1 = acc[4 * (p >> 1)][2 * (p & 1) + 1];
#pragma unroll
      for (int k = 0; k < 3; ++k) add8(s0, s1, ps[k][p]);
      s0 *= (f32x4){bflo(gv[p][0]), bfhi(gv[p][0]), bflo(gv[p][1]), bfhi(gv[p][1])};
      s1 *= (f32x4){bflo(gv[p][2]), bfhi(gv[p][2]), bflo(gv[p][3]), bfhi(gv[p][3])};
      const u32x4 tp = pack8(s0, s1);
      const auto w0 = __builtin_amdgcn_permlane16_swap(tp[0], tp[2], false, false), w1 = __builtin_amdgcn_permlane16_swap(tp[1], tp[3], false, false);
      const unsigned t[4] = {w0[0], w1[0], w0[1], w1[1]};
      u32x4 o;
#pragma unroll
      for (int e = 0; e < 4; ++e) o[e] = cvtpk(bflo(xv[p][e]) + bflo(t[e]), bfhi(xv[p][e]) + bfhi(t[e]));
      __builtin_amdgcn_raw_buffer_store_b128(o, rout, vo + (p >> 1) * (64 * DM * 2) + (p & 1) * 64, 0, 0);
    }
    STAMP(1, 15);
  } else {
    SETPRIO(0);
    const int lane = lane_id();
    const unsigned pl = lb + PARK + lane * 16, pk = lb + 159744 + lw * 256;
    const int fo = F_O + (rb * 4 + kvh) * 128;
    if (lw < 3) {
      Strm S;
      if (lw < 2) strm_x(S, ws + WS_O + (size_t)(rb * 128 + 32 * kvh) * 16384 + lw * 8192, 524288 - lw * 8192, 16384, kvh);
      else strm_w(S, (const char*)wo + (size_t)kvh * 1024 * (DM * 2) + dc * 256, 1024, DM * 2, false);
      wait_vm<8>();
      bar();
      issue(Y, 3);
      for (int t = 1; t <= 22; ++t) { issue(Y, t + 3); wait_vm<16>(); bar(); }
      peek(rf, lb + PARK + lw * 256, fo);
      issue(Y, 26); wait_vm<16>(); bar();
      issue(Y, 27); wait_vm<16>(); bar();
      peek_check(ws, lb + PARK + lw * 256, fo, 8, tag);
      auto iss = [&](int k) { issue(S, k, 4 * (k & 7) + (k >> 3)); wait_vm<16>(); bar(); };
      for (int t = 25; t <= 36; ++t) iss(t - 25);
      peek(rf, lb + PARK + 16384 + lw * 256, fo + 64);
      for (int t = 37; t <= 40; ++t) iss(t - 25);
      peek_check(ws, lb + PARK + 16384 + lw * 256, fo + 64, 8, tag);
      for (int t = 41; t <= 56; ++t) iss(t - 25);
      wait_vm<8>(); bar();
      wait_vm<0>(); bar();
    } else {
      bar();
      const rsrc_t ro = mkrsrc(ws + WS_O + (size_t)(rb * 128 + 4 * head) * 16384, 65536);
      for (int t = 1; t <= 25; t += 2) {
        const unsigned o = (t >> 1) * 5120;
        const u32x4 v0 = ldsr(pl + o), v1 = ldsr(pl + o + 1024), v2 = ldsr(pl + o + 2048), v3 = ldsr(pl + o + 3072), v4 = ldsr(pl + (t < 25 ? o + 4096 : 0));
        st16s<0>(v0, ro, lane * 16, o); st16s<1024>(v1, ro, lane * 16, o); st16s<2048>(v2, ro, lane * 16, o);
        if (t == 17) { wait_vm<11>(); signal<1>(ws, fo + dc * 8, tag); }
        bar();
        st16s<3072>(v3, ro, lane * 16, o); st16s<0>(v4, ro, lane * 16, o + 4096);
        bar();
      }
      bar(); bar(); bar();
      wait_vm<0>();
      signal<1>(ws, fo + 64 + dc * 8, tag);
      for (int t = 30; t <= 49; t += 2) {
        u32x4 v[5];
        unsigned so[5];
#pragma unroll
        for (int i = 0; i < 5; ++i) {
          const int p = (t - 30) / 2 * 5 + i, dl = 1 + (p >> 4);
          v[i] = ldsr(pl + (p < 48 ? 16384 + p * 1024 : 0));
          so[i] = p < 48 ? (kvh ^ dl) * 98304 + p * 1024 : 393216;
        }
        st16s<0>(v[0], rxb, lane * 16, so[0]); st16s<0>(v[1], rxb, lane * 16, so[1]); st16s<0>(v[2], rxb, lane * 16, so[2]);
        bar();
        st16s<0>(v[3], rxb, lane * 16, so[3]); st16s<0>(v[4], rxb, lane * 16, so[4]);
        bar();
      }
      bar(); bar();
      wait_vm<0>();
      signal<14>(ws, F_Y + (tile * 4 + (kvh ^ (lane & 3))) * 128 + ((lane & 3) - 1) * 8, tag);
      for (int t = 52; t <= 56; ++t) bar();
      peek(rf, pk, F_Y + (tile * 4 + kvh) * 128);
      bar();
      wait_vm<0>();
      peek_check(ws, pk, F_Y + (tile * 4 + kvh) * 128, 3, tag);
      bar();
    }
    u32x4 py[3][4];
#pragma unroll
    for (int k = 0; k < 3; ++k)
#pragma unroll
      for (int m = 0; m < 4; ++m) py[k][m] = __builtin_amdgcn_raw_buffer_load_b128(rxb, lane * 16 + m * 1024, kvh * 98304 + lw * 4096 + k * 16384, 16);
    const float bb = bflo(b2[rb * 256 + (4 * lw + kvh) * 16 + (lane & 15)]);
    bar();
    bar();
#pragma unroll
    for (int m = 0; m < 4; ++m) {
      f32x4 y0 = {bb, bb, bb, bb}, y1 = y0;
      add8(y0, y1, ldsr(pl + lw * 4096 + m * 1024));
#pragma unroll
      for (int k = 0; k < 3; ++k) add8(y0, y1, py[k][m]);
      *(LAS u32x4*)(size_t)(pl + lw * 4096 + m * 1024) = (u32x4){silu2(y0.xy), silu2(y0.zw), silu2(y1.xy), silu2(y1.zw)};
    }
    wait_lgkm0();
    bar();
  }
}

extern "C" void kernel_launch(void* const* d_in, const int* in_sizes, int n_in, void* d_out, int out_size, void* d_ws, size_t ws_size,
                              hipStream_t stream) {
  const u16* x = (const u16*)d_in[0];
  const u16* kc = (const u16*)d_in[1];
  const u16* vc = (const u16*)d_in[2];
  const u16* wq = (const u16*)d_in[3];
  const u16* wo = (const u16*)d_in[4];
  const u16* w1 = (const u16*)d_in[6];
  const u16* b1 = (const u16*)d_in[7];
  const u16* w2 = (const u16*)d_in[8];
  const u16* b2 = (const u16*)d_in[9];
  char* ws = (char*)d_ws;
  (void)hipFuncSetAttribute((const void*)k_main, hipFuncAttributeMaxDynamicSharedMemorySize, 163840);
  hipLaunchKernelGGL(k_main, dim3(256), dim3(512), 163840, stream, x, kc, vc, wq, wo, w1, b1, w2, b2, ws, (u16*)d_out);
}
```

```cpp
#include <hip/hip_runtime.h>
#include <hip/hip_bf16.h>

#define PFB 2097152
#define CODE_BYTES 67584
#define YROT 7
#define SROT 4
#define NSP 3
typedef unsigned short u16;
typedef short bf16x8 __attribute__((ext_vector_type(8)));
typedef short s16x4 __attribute__((ext_vector_type(4)));
typedef float f32x16 __attribute__((ext_vector_type(16)));
typedef float f32x4 __attribute__((ext_vector_type(4)));
typedef float f32x2 __attribute__((ext_vector_type(2)));
typedef unsigned u32x4 __attribute__((ext_vector_type(4)));
typedef unsigned u32x2 __attribute__((ext_vector_type(2)));
typedef __bf16 bf16x2 __attribute__((ext_vector_type(2)));
typedef __amdgpu_buffer_rsrc_t rsrc_t;
#define LAS __attribute__((address_space(3)))
#define GAS __attribute__((address_space(1)))
#define DI __device__ __forceinline__
#define SGB(m, n) __builtin_amdgcn_sched_group_barrier(m, n, 0)
#define SB0() __builtin_amdgcn_sched_barrier(0)

constexpr int SEQ = 2048, DM = 1024, FF = 3584, HD = 4096;
constexpr size_t WS_FLAG = 0;
constexpr int F_EP = 0, F_X = 512, F_L = 2048, F_U = 4096, F_O = 12288, F_Y = 16384, F_S = 49152, F_P = 81920, F_END = 96256;
constexpr size_t WS_ST = 98304;
constexpr size_t WS_CS = 131072;
constexpr size_t WS_HT = 262144;
constexpr size_t WS_KV = WS_HT + 4194304;
constexpr size_t WS_O = WS_KV + 1048576;
constexpr size_t WS_U = WS_O + 16777216;
constexpr size_t WS_XB = WS_U + 7340032;
constexpr size_t WS_END = WS_XB + 25165824;

#define STAMP(kid, i)
#define STAMPA(i)
#define STAMPF(i)
DI unsigned cvtpk(float lo, float hi) { f32x2 v = {lo, hi}; bf16x2 b = __builtin_convertvector(v, bf16x2); return __builtin_bit_cast(unsigned, b); }
DI float bflo(unsigned u) { return __uint_as_float(u << 16); }
DI float bfhi(unsigned u) { return __uint_as_float(u & 0xffff0000u); }
DI float rbf(float x) { return bflo(cvtpk(x, x)); }
template <int N> DI void wait_vm() { asm volatile("s_waitcnt vmcnt(%0)" ::"i"(N) : "memory"); }
DI void wait_lgkm0() { __builtin_amdgcn_s_waitcnt(0xC07F); }
DI void bar() { asm volatile("" ::: "memory"); __builtin_amdgcn_s_barrier(); asm volatile("" ::: "memory"); }
DI rsrc_t mkrsrc(const void* p, unsigned bytes) { return __builtin_amdgcn_make_buffer_rsrc((void*)p, 0, bytes, 0x00020000); }
DI void dma16(rsrc_t r, unsigned lds_addr, unsigned voff, unsigned soff) {
  asm volatile("s_mov_b32 m0, %0\n\ts_nop 0\n\tbuffer_load_dwordx4 %1, %2, %3 offen lds" ::"s"(lds_addr), "v"(voff), "s"(r), "s"(soff) : "memory");
}
DI void dma16s(rsrc_t r, unsigned lds_addr, unsigned voff, unsigned soff) {
  asm volatile("s_mov_b32 m0, %0\n\ts_nop 0\n\tbuffer_load_dwordx4 %1, %2, %3 offen sc1 lds" ::"s"(lds_addr), "v"(voff), "s"(r), "s"(soff) : "memory");
}
template <int OFF> DI void st16s(u32x4 v, rsrc_t r, unsigned voff, unsigned soff) {
  asm volatile("buffer_store_dwordx4 %0, %1, %2, %3 offen offset:%4 sc1\n\ts_nop 1" ::"v"(v), "v"(voff), "s"(r), "s"(soff), "i"(OFF) : "memory");
}
DI int lane_id() { int l; asm volatile("v_mbcnt_lo_u32_b32 %0, -1, 0\n\tv_mbcnt_hi_u32_b32 %0, -1, %0" : "=v"(l)); return l; }
struct Tag { unsigned lo, hi; };
DI void peek(rsrc_t rf, unsigned lds_addr, int off) {
  asm volatile("s_mov_b32 m0, %0\n\ts_nop 0\n\tbuffer_load_dword %3, %1, %2 offen sc1 lds" ::"s"(lds_addr), "s"(rf), "s"(off), "v"(lane_id() * 4) : "memory");
}
template <int GAP = 1> DI void spin_until(char* ws, int off, int n, Tag t) {
  const int ln = lane_id();
  GAS unsigned long long* p = (GAS unsigned long long*)(ws + WS_FLAG + off) + (ln < n ? ln : 0);
  const unsigned long long want = ((unsigned long long)t.hi << 32) | t.lo;
  for (unsigned sp = 0; sp < (1u << 20); ++sp) {
    if (__all(__hip_atomic_load(p, __ATOMIC_RELAXED, __HIP_MEMORY_SCOPE_AGENT) == want)) break;
    __builtin_amdgcn_s_sleep(GAP);
  }
  asm volatile("" ::: "memory");
}
DI void peek_check(char* ws, unsigned lds_addr, int off, int n, Tag t) {
  const int ln = lane_id();
  const unsigned v = *(LAS volatile unsigned*)(size_t)(lds_addr + ln * 4);
  if (!__all(ln >= 2 * n || v == ((ln & 1) ? t.hi : t.lo))) spin_until(ws, off, n, t);
}
template <long MASK> DI void signal(char* ws, int off, Tag t) {
  const u32x2 v = {t.lo, t.hi};
  asm volatile("s_mov_b64 exec, %3\n\tglobal_store_dwordx2 %0, %1, %2 sc1\n\ts_mov_b64 exec, -1" ::"v"(off), "v"(v), "s"(ws + WS_FLAG), "s"(MASK) : "memory");
}
DI void dma4k(rsrc_t r, unsigned lds_addr, unsigned voff, unsigned soff) {
  asm volatile("s_mov_b32 m0, %0\n\ts_nop 0\n\tbuffer_load_dwordx4 %1, %2, %3 offen sc1 lds\n\tbuffer_load_dwordx4 %1, %2, %3 offen offset:1024 sc1 lds\n\t"
               "buffer_load_dwordx4 %1, %2, %3 offen offset:2048 sc1 lds\n\tbuffer_load_dwordx4 %1, %2, %3 offen offset:3072 sc1 lds"
               ::"s"(lds_addr), "v"(voff), "s"(r), "s"(soff) : "memory");
}
template <int W> DI void pf_line(int wid, rsrc_t r, unsigned voff, unsigned soff) {
  asm volatile("s_cmp_lg_u32 %0, %4\n\ts_cbranch_scc1 1f\n\tbuffer_load_dword v255, %1, %2, %3 offen\n1:"
               ::"s"(wid), "v"(voff), "s"(r), "s"(soff), "i"(W) : "scc", "memory", "v255");
}
template <int CTRL> DI float dppf(float x) { return __builtin_bit_cast(float, __builtin_amdgcn_mov_dpp(__builtin_bit_cast(int, x), CTRL, 0xf, 0xf, true)); }
DI float wave_sum(float v) {
  v += dppf<0xB1>(v); v += dppf<0x4E>(v); v += dppf<0x141>(v); v += dppf<0x140>(v);
  auto a = __builtin_amdgcn_permlane16_swap(__float_as_uint(v), __float_as_uint(v), false, false);
  v = __uint_as_float(a[0]) + __uint_as_float(a[1]);
  auto b = __builtin_amdgcn_permlane32_swap(__float_as_uint(v), __float_as_uint(v), false, false);
  return __uint_as_float(b[0]) + __uint_as_float(b[1]);
}

DI f32x2 ln16(unsigned (&w)[8]) {
  f32x2 v[8];
#pragma unroll
  for (int i = 0; i < 8; ++i) v[i] = (f32x2){bflo(w[i]), bfhi(w[i])};
  const f32x2 s = ((v[0] + v[1]) + (v[2] + v[3])) + ((v[4] + v[5]) + (v[6] + v[7]));
  const float mean = wave_sum(s[0] + s[1]) * (1.f / 1024.f);
  f32x2 q0 = {0.f, 0.f}, q1 = q0;
#pragma unroll
  for (int i = 0; i < 8; i += 2) {
    const f32x2 d0 = v[i] - mean, d1 = v[i + 1] - mean;
    q0 = __builtin_elementwise_fma(d0, d0, q0); q1 = __builtin_elementwise_fma(d1, d1, q1);
  }
  q0 += q1;
  const float var = wave_sum(q0[0] + q0[1]) * (1.f / 1024.f);
  const float mb = rbf(mean);
  const float rs = __builtin_amdgcn_rcpf(rbf(__builtin_amdgcn_sqrtf(rbf(rbf(var) + 1e-5f))));
#pragma unroll
  for (int i = 0; i < 8; ++i) {
    const f32x2 d = v[i] - mb;
    const unsigned c = cvtpk(d[0], d[1]);
    const f32x2 r = (f32x2){bflo(c), bfhi(c)} * rs;
    w[i] = cvtpk(r[0], r[1]);
  }
  return (f32x2){mb, rs};
}

struct Frags { bf16x8 x[8]; bf16x8 w[4]; };
struct Addr { unsigned x, w[4]; };
template <int XS = 1024> DI void load_frags(Frags& f, const Addr& a) {
#pragma unroll
  for (int c = 0; c < 4; ++c) {
    s16x4 a0 = __builtin_amdgcn_ds_read_tr16_b64_v4i16((LAS s16x4*)(size_t)(a.w[c]));
    s16x4 a1 = __builtin_amdgcn_ds_read_tr16_b64_v4i16((LAS s16x4*)(size_t)(a.w[c] + 4096));
    f.w[c] = __builtin_shufflevector(a0, a1, 0, 1, 2, 3, 4, 5, 6, 7);
  }
#pragma unroll
  for (int i = 0; i < 8; ++i) f.x[i] = *(LAS bf16x8*)(size_t)(a.x + i * XS);
}
template <bool WA, bool Z> DI void mma_all(f32x4 (&acc)[8][4], const Frags& f, const f32x4* c0) {
  const f32x4 zero = {0.f, 0.f, 0.f, 0.f};
#pragma unroll
  for (int i = 0; i < 8; ++i)
#pragma unroll
    for (int jj = 0; jj < 4; ++jj) {
      const int j = (i & 1) ? 3 - jj : jj;
      if (WA) acc[i][j] = __builtin_amdgcn_mfma_f32_16x16x32_bf16(f.w[j], f.x[i], Z ? (c0 ? c0[j] : zero) : acc[i][j], 0, 0, 0);
      else    acc[i][j] = __builtin_amdgcn_mfma_f32_16x16x32_bf16(f.x[i], f.w[j], Z ? (c0 ? c0[j] : zero) : acc[i][j], 0, 0, 0);
    }
}
DI void addr_add(Addr& a, int d) {
  a.x += d;
#pragma unroll
  for (int c = 0; c < 4; ++c) a.w[c] += d;
}
template <bool WA, int NSLOT, int SLOTB, bool Z = false>
DI void gstep(f32x4 (&acc)[8][4], Frags& Fn, const Frags& Fc, Addr& a, int& slot, const f32x4* c0 = nullptr) {
  SB0();
  load_frags(Fn, a);
  mma_all<WA, Z>(acc, Fc, c0);
  slot = (slot + 1 == NSLOT) ? 0 : slot + 1;
  addr_add(a, (slot == 0) ? -(NSLOT - 1) * SLOTB : SLOTB);
  asm volatile("" : "+v"(a.x), "+v"(a.w[0]), "+v"(a.w[1]), "+v"(a.w[2]), "+v"(a.w[3]));
#pragma unroll
  for (int i = 0; i < 8; ++i) { SGB(0x8, 1); SGB(0x100, 1); SGB(0x8, 1); SGB(0x100, 1); SGB(0x8, 1); }
#pragma unroll
  for (int i = 0; i < 6; ++i) { SGB(0x8, 1); SGB(0x4, 1); SGB(0x2, 1); }
  SGB(0x8, 2);
  SB0();
  wait_lgkm0();
  bar();
}
template <int MODE, bool CS, bool Z = false>
DI void qstep(f32x4 (&acc)[8][4], f32x4 (&cs)[2], u32x4& ones, unsigned onext, Frags& Fn, const Frags& Fc, Addr& a, int& xs, int& wsl) {
  const f32x4 zero = {0.f, 0.f, 0.f, 0.f};
  SB0();
  load_frags<MODE == 2 ? 1024 : 2048>(Fn, a);
  if (CS) {
#pragma unroll
    for (int j = 0; j < 2; ++j) cs[j] = __builtin_amdgcn_mfma_f32_16x16x32_bf16(Fc.w[j], __builtin_bit_cast(bf16x8, ones), Z ? zero : cs[j], 0, 0, 0);
    ones = (u32x4){onext, onext, onext, onext};
    asm volatile("" : "+v"(ones));
  }
  mma_all<true, Z>(acc, Fc, nullptr);
  if (MODE == 2) addr_add(a, 24576);
  else {
    wsl = (wsl + 1) & 3;
    const int dw = wsl ? 8192 : -24576;
#pragma unroll
    for (int c = 0; c < 4; ++c) a.w[c] += dw;
    a.x ^= 64;
    if (MODE == 1) { xs = (xs == 2) ? 0 : xs + 1; a.x += xs ? 32768 : -65536; }
  }
  asm volatile("" : "+v"(a.x), "+v"(a.w[0]), "+v"(a.w[1]), "+v"(a.w[2]), "+v"(a.w[3]));
#pragma unroll
  for (int i = 0; i < 8; ++i) { SGB(0x8, 1); SGB(0x100, 1); SGB(0x8, 1); SGB(0x100, 1); SGB(0x8, 1); }
#pragma unroll
  for (int i = 0; i < 7; ++i) { SGB(0x8, 1); SGB(0x4, 1); SGB(0x2, 1); }
  if (CS) { SGB(0x8, 1); SGB(0x2, 2); SGB(0x8, 1); SGB(0x2, 2); }
  SGB(0x8, 1);
  SB0();
  wait_lgkm0();
  bar();
}
DI void addr_init(Addr& a, unsigned xbase, unsigned wbase, int wj, int cx, int lane) {
  const int g = lane >> 4, q = (lane >> 2) & 3, p = lane & 3, row = 4 * g + q;
  a.x = xbase + lane * 16;
#pragma unroll
  for (int c = 0; c < 4; ++c) a.w[c] = wbase + row * 256 + (((2 * (4 * wj + (c ^ cx)) + (p >> 1)) ^ ((row & 7) << 1)) * 16) + (p & 1) * 8;
}
template <bool WA, int NSLOT, int SLOTB> DI void gemm_steps(f32x4 (&acc)[8][4], Frags& F0, Frags& F1, Addr& a, int& slot, int n) {
  for (int t = 0; t < n; t += 2) {
    gstep<WA, NSLOT, SLOTB>(acc, F0, F1, a, slot);
    gstep<WA, NSLOT, SLOTB>(acc, F1, F0, a, slot);
  }
}

DI u32x4 pack8(const f32x4& a, const f32x4& b) { return (u32x4){cvtpk(a[0], a[1]), cvtpk(a[2], a[3]), cvtpk(b[0], b[1]), cvtpk(b[2], b[3])}; }
DI void ystep_park(f32x4 (&acc)[8][4], Frags& Fn, const Frags& Fc, Addr& a, int& slot, unsigned pm) {
  s16x4 h[8];
  SB0();
#pragma unroll
  for (int k = 0; k < 16; ++k) {
    const int m = k >> 2, j = k & 3;
    *(LAS u32x4*)(size_t)(pm + j * 16384 + m * 1024) = pack8(acc[2 * m][j], acc[2 * m + 1][j]);
    asm volatile("v_mfma_f32_16x16x32_bf16 %0, %1, %2, 0" : "+v"(acc[2 * m][j]) : "v"(Fc.w[j]), "v"(Fc.x[2 * m]));
    if (k < 8) h[k] = __builtin_amdgcn_ds_read_tr16_b64_v4i16((LAS s16x4*)(size_t)(a.w[k >> 1] + (k & 1) * 4096));
    else Fn.x[k - 8] = *(LAS bf16x8*)(size_t)(a.x + (k - 8) * 1024);
    asm volatile("v_mfma_f32_16x16x32_bf16 %0, %1, %2, 0" : "+v"(acc[2 * m + 1][j]) : "v"(Fc.w[j]), "v"(Fc.x[2 * m + 1]));
    SB0();
  }
#pragma unroll
  for (int c = 0; c < 4; ++c) Fn.w[c] = __builtin_shufflevector(h[2 * c], h[2 * c + 1], 0, 1, 2, 3, 4, 5, 6, 7);
  slot = 2;
  addr_add(a, 24576);
  wait_lgkm0();
  bar();
}
DI f32x2 gate2(f32x2 u, f32x2 z) {
  f32x2 e = {__builtin_amdgcn_exp2f(z[0]), __builtin_amdgcn_exp2f(z[1])};
  e += 1.f;
  return u * (f32x2){__builtin_amdgcn_rcpf(e[0]), __builtin_amdgcn_rcpf(e[1])};
}
DI unsigned gelu2(f32x2 u) {
  const f32x2 r = gate2(u, u * __builtin_elementwise_fma(u * u, (f32x2){-0.1029432397f, -0.1029432397f}, (f32x2){-2.302208198f, -2.302208198f}));
  return cvtpk(r[0], r[1]);
}
DI unsigned silu2(f32x2 y) { const f32x2 r = gate2(y, y * -1.4426950409f); return cvtpk(r[0], r[1]); }
DI u32x4 ldsr(unsigned a) { return *(LAS u32x4*)(size_t)a; }
DI void add8(f32x4& a, f32x4& b, const u32x4& w) {
  a += (f32x4){bflo(w[0]), bfhi(w[0]), bflo(w[1]), bfhi(w[1])};
  b += (f32x4){bflo(w[2]), bfhi(w[2]), bflo(w[3]), bfhi(w[3])};
}

struct Att { bf16x8 q[8]; f32x16 o[4]; f32x16 negm, sE, sO; u32x4 pO[2]; bf16x8 pre[4]; float l; };
DI float vmax16(const f32x16& a) {
  float m = fmaxf(fmaxf(a[0], a[1]), a[2]);
#pragma unroll
  for (int i = 3; i < 15; i += 2) m = fmaxf(fmaxf(m, a[i]), a[i + 1]);
  m = fmaxf(m, a[15]);
  auto sw = __builtin_amdgcn_permlane32_swap(__float_as_uint(m), __float_as_uint(m), false, false);
  return fmaxf(__uint_as_float(sw[0]), __uint_as_float(sw[1]));
}
DI bf16x8 ldfrag(unsigned a) { return *(LAS bf16x8*)(size_t)a; }
DI void sm1(f32x16& s, int e, u32x4 (&w)[2], float& l) {
  if (e < 16) {
    float x = s[e];
    x = __builtin_amdgcn_exp2f(x);
    asm volatile("" : "+v"(x));
    s[e] = x;
  }
  if (e >= 1) {
    l += s[e - 1];
    asm volatile("" : "+v"(l));
    if (!(e & 1)) { unsigned c = cvtpk(s[e - 2], s[e - 1]); asm volatile("" : "+v"(c)); w[(e - 1) >> 3][((e - 1) >> 1) & 3] = c; }
  }
}
#define SETPRIO(x) __builtin_amdgcn_s_setprio(x)
#define MFMA32(a, b, c) __builtin_amdgcn_mfma_f32_32x32x16_bf16(a, b, c, 0, 0, 0)
#define PFRAG(w) __builtin_bit_cast(bf16x8, w)
template <int POS, class F> DI void att_tile(Att& A, unsigned kv, unsigned kvn, F&& gap) {
  bf16x8 f[36];
  u32x4 pE[2];
  const f32x16 zero = {};
  asm volatile("" : "+v"(kv), "+v"(kvn));
#pragma unroll
  for (int i = 0; i < 4; ++i) f[i] = A.pre[i];
  SB0();
  SETPRIO(3);
#pragma unroll
  for (int n = 0; n < 8; ++n) {
    if (POS == 0) A.sE = MFMA32(f[n], A.q[n], n ? A.sE : zero);
    else A.o[n & 3] = MFMA32(f[n], PFRAG(A.pO[n >> 2]), A.o[n & 3]);
    f[n + 4] = ldfrag(kv + (n + 4) * 1024);
    if (POS && n <= 2) sm1(A.sO, 14 + n, A.pO, A.l);
    if (POS && n >= 2) sm1(A.sE, n - 2, pE, A.l);
    gap(n);
    SB0();
  }
  if (POS == 0) {
    const float mx = vmax16(A.sE);
#pragma unroll
    for (int i = 0; i < 16; ++i) { A.sE[i] -= mx; A.negm[i] = -mx; }
    SB0();
  }
  SETPRIO(2);
#pragma unroll
  for (int n = 8; n < 16; ++n) {
    A.sO = MFMA32(f[n], A.q[n - 8], n > 8 ? A.sO : A.negm);
    f[n + 4] = ldfrag(kv + (n + 4) * 1024);
    if (POS) sm1(A.sE, n - 2, pE, A.l);
    else if (n < 15) { sm1(A.sE, 2 * (n - 8), pE, A.l); sm1(A.sE, 2 * (n - 8) + 1, pE, A.l); }
    gap(n);
    SB0();
  }
  SETPRIO(1);
#pragma unroll
  for (int n = 16; n < 24; ++n) {
    A.o[n & 3] = MFMA32(f[n], PFRAG(pE[(n >> 2) & 1]), (POS == 0 && n < 20) ? zero : A.o[n & 3]);
    f[n + 4] = ldfrag(kv + (n + 4) * 1024);
    if (n <= 18) sm1(A.sE, n - 2, pE, A.l);
    if (n >= 18) {
      if (POS < 2) sm1(A.sO, n - 18, A.pO, A.l);
      else { sm1(A.sO, 2 * (n - 18), A.pO, A.l); sm1(A.sO, 2 * (n - 18) + 1, A.pO, A.l); }
    }
    gap(n);
    SB0();
  }
  SETPRIO(0);
#pragma unroll
  for (int n = 24; n < 32; ++n) {
    if (POS < 2) {
      A.sE = MFMA32(f[n], A.q[n - 24], n > 24 ? A.sE : A.negm);
      f[n + 4] = ldfrag(n < 28 ? kv + (n + 4) * 1024 : kvn + (n - 28) * 1024);
      sm1(A.sO, n - 18, A.pO, A.l);
    } else {
      A.o[n & 3] = MFMA32(f[n], PFRAG(A.pO[(n >> 2) & 1]), A.o[n & 3]);
      if (n < 28) f[n + 4] = ldfrag(kv + (n + 4) * 1024);
      if (n < 26) { sm1(A.sO, 2 * (n - 18), A.pO, A.l); sm1(A.sO, 2 * (n - 18) + 1, A.pO, A.l); }
      if (n == 26) sm1(A.sO, 16, A.pO, A.l);
    }
    gap(n);
    SB0();
  }
  if (POS < 2) {
#pragma unroll
    for (int i = 0; i < 4; ++i) A.pre[i] = f[32 + i];
  }
}
DI void att_redo(Att& A, rsrc_t rkv, unsigned vo) {
  float m = -3.0e38f, l = 0.f;
#pragma unroll
  for (int d = 0; d < 4; ++d)
#pragma unroll
    for (int i = 0; i < 16; ++i) A.o[d][i] = 0.f;
  for (int blk = 0; blk < 16; ++blk) {
    const int kb = (blk ? 2 * blk - 1 : 0) * 8192, vb = (blk < 15 ? 2 * blk + 2 : 31) * 8192;
    f32x16 s;
#pragma unroll
    for (int i = 0; i < 16; ++i) s[i] = 0.f;
#pragma unroll
    for (int n = 0; n < 8; ++n) s = MFMA32(PFRAG(__builtin_amdgcn_raw_buffer_load_b128(rkv, vo + n * 1024, kb, 16)), A.q[n], s);
    const float mn = fmaxf(m, vmax16(s)), fct = __builtin_amdgcn_exp2f(m - mn);
    m = mn;
    l *= fct;
    u32x4 p[2];
#pragma unroll
    for (int i = 0; i < 16; ++i) s[i] -= mn;
#pragma unroll
    for (int i = 0; i < 17; ++i) sm1(s, i, p, l);
#pragma unroll
    for (int d = 0; d < 4; ++d)
#pragma unroll
      for (int i = 0; i < 16; ++i) A.o[d][i] *= fct;
#pragma unroll
    for (int n = 0; n < 8; ++n) A.o[n & 3] = MFMA32(PFRAG(__builtin_amdgcn_raw_buffer_load_b128(rkv, vo + n * 1024, vb, 16)), PFRAG(p[n >> 2]), A.o[n & 3]);
  }
  A.l = l;
}

__global__ __launch_bounds__(512, 2) __attribute__((amdgpu_num_vgpr(254))) void k_main(const u16* __restrict__ x, const u16* __restrict__ kc, const u16* __restrict__ vc, const u16* __restrict__ wq, const u16* __restrict__ wo,
                                                 const u16* __restrict__ w1, const u16* __restrict__ b1, const u16* __restrict__ w2,
                                                 const u16* __restrict__ b2, char* __restrict__ ws, u16* __restrict__ out) {
  extern __shared__ __attribute__((aligned(16))) char smem[];
  LAS char* lds = (LAS char*)smem;
  const unsigned lb = (unsigned)(size_t)lds;
  const int tid = threadIdx.x, wid = __builtin_amdgcn_readfirstlane(tid >> 6);
  const int g8 = blockIdx.x & 7, idx = blockIdx.x >> 3;
  const rsrc_t rcode = mkrsrc((const void*)(__builtin_amdgcn_s_getpc() & ~255ull), CODE_BYTES);
  auto code_pf = [&](unsigned lds_addr) {
    asm volatile("s_mov_b32 m0, %0\n\ts_nop 0\n\tbuffer_load_dword %3, %1, %2 offen lds" ::"s"(lds_addr), "s"(rcode), "s"((idx % ((CODE_BYTES + 8191) / 8192)) * 8192), "v"(lane_id() * 128) : "memory");
  };
  if (wid == 7) code_pf(lb + 161792);
  STAMP(1, 0);
  STAMP(0, 0);
  const bool hasU = idx < 28;
  const int uh = g8 & 1, dq = idx & 3, ft = 7 * (g8 >> 1) + (idx >> 2), ut = ft * 4 + dq;
  const int kvh = g8 >> 1, head = 8 * kvh + (idx & 7), rb = 4 * (g8 & 1) + (idx >> 3);
  const int dc = idx & 7, tile = rb * 8 + dc;
  const int lw = wid - 4, wi = (wid >> 1) & 1, wj = wid & 1;
  constexpr int QS = 24576;
  constexpr int PARK = 98304;
  const rsrc_t rf = mkrsrc(ws + WS_FLAG, F_END + 256);
  const rsrc_t rkv = mkrsrc(ws + WS_KV + (size_t)kvh * 262144, 262144);
  const rsrc_t rxb = mkrsrc(ws + WS_XB + (size_t)tile * 393216, 393216);
  struct Strm { rsrc_t r; unsigned v0, v1, step, p0, p1, p2; int rot; };
  auto strm_x = [&](Strm& S, const char* base, unsigned bytes, unsigned step, int xr) {
    const int ln = lane_id();
    S.r = mkrsrc(base, bytes); S.v0 = ln * 16 + (xr & 1) * 1024; S.v1 = S.v0 ^ 1024; S.step = step;
    S.p0 = (xr >> 1) * 2048; S.p1 = S.p0 ^ 2048; S.p2 = 4096; S.rot = 0;
  };
  auto strm_w = [&](Strm& S, const char* base, unsigned rows, unsigned ld, bool nat) {
    const int ln = lane_id(), q4 = ln >> 4;
    const unsigned c0 = ((ln & 15) ^ (q4 << 1)) * 16;
    S.r = mkrsrc(base, (rows - 1) * ld + 256); S.v0 = q4 * ld + c0; S.v1 = ((nat ? 8 : 4) + q4) * ld + (c0 ^ 128); S.step = 32 * ld;
    S.p0 = 0; S.p1 = (nat ? 16 : 8) * ld; S.p2 = (nat ? 4 : 16) * ld; S.rot = 0;
  };
  auto issue = [&](const Strm& S, int k, int src = -1) {
    const unsigned s = lb + (k & 3) * QS + lw * 8192;
    if (src < 0) src = (k + S.rot) & 31;
#pragma unroll
    for (int i = 0; i < 4; ++i) {
      const unsigned so = src * S.step + ((i & 1) ? S.p1 : S.p0) + (i >> 1) * S.p2;
      dma16s(S.r, s + i * 2048, S.v0, so); dma16s(S.r, s + i * 2048 + 1024, S.v1, so);
    }
  };
  constexpr int QW = 98304, HST = 131072, HRS = 2112, RST = HST + 8 * HRS, PKQ = 148480, STT = 151552, CSX = 153600;
  const int lane0 = tid & 63;
  const rsrc_t rq = lw < 2 ? mkrsrc(x + (size_t)(rb * 256 + lw * 128) * DM, 128 * DM * 2) : mkrsrc((const char*)wq + head * 256, (DM - 1) * HD * 2 + 256);
  const unsigned qv0 = lw < 2 ? (lane0 >> 3) * 2048 + (((lane0 & 7) ^ (lane0 >> 4)) * 16) : (lane0 >> 4) * (HD * 2) + (((lane0 & 15) ^ ((lane0 >> 4) << 1)) * 16);
  const unsigned qv1 = lw < 2 ? (qv0 ^ 64) + 8 * 2048 : (qv0 ^ 128) + 8 * (HD * 2);
  const bool qx = lw < 2;
  const unsigned qp1 = qx ? 32768 : 16 * HD * 2, qp2 = qx ? 65536 : 4 * HD * 2;
  const unsigned qlb = lb + (qx ? lw * 16384 : QW), qlw = qx ? 98304 : 32768, qom = qx ? 0x20780 : 0x7c0000;
  unsigned ql = qx ? 65536 : 0, qdl = 8192, qo = rb * (qx ? 256 : 4 * 32 * HD * 2), qdo = qx ? 131072 : 32 * HD * 2;
  auto issq = [&]() {
    const unsigned s = qlb + ql;
    dma16(rq, s, qv0, qo); dma16(rq, s + 1024, qv1, qo);
    dma16(rq, s + 2048, qv0, qo + qp1); dma16(rq, s + 3072, qv1, qo + qp1);
    dma16(rq, s + 4096, qv0, qo + qp2); dma16(rq, s + 5120, qv1, qo + qp2);
    dma16(rq, s + 6144, qv0, qo + qp1 + qp2); dma16(rq, s + 7168, qv1, qo + qp1 + qp2);
    ql += qdl; ql = ql >= qlw ? ql - qlw : ql; qdl ^= qx ? (8192 ^ 24576) : 0;
    qo = (qo + qdo) & qom; qdo ^= qx ? 128 : 0;
  };
  auto h_out = [&](int it) {
    const rsrc_t rh = mkrsrc(ws + WS_HT + (size_t)(blockIdx.x >> 2) * 16384 + (blockIdx.x & 3) * 256, 3 * 1048576 + 16384);
    const unsigned a0 = HST + ((lane0 >> 2) & 3) * HRS + (it * 4 + (lane0 >> 4)) * 32 + (lane0 & 3) * 8;
    s16x4 lo = __builtin_amdgcn_ds_read_tr16_b64_v4i16((LAS s16x4*)(lds + a0));
    s16x4 hi = __builtin_amdgcn_ds_read_tr16_b64_v4i16((LAS s16x4*)(lds + a0 + 4 * HRS));
    st16s<0>(__builtin_bit_cast(u32x4, __builtin_shufflevector(lo, hi, 0, 1, 2, 3, 4, 5, 6, 7)), rh, (lane0 >> 4) * 1024 + (lane0 & 15) * 16,
             (it >> 2) * 1048576 + (it & 3) * 4096);
  };
  auto st_out = [&]() { st16s<0>(*(LAS u32x4*)(lds + RST + (lane0 & 3) * 16), mkrsrc(ws + WS_ST + blockIdx.x * 64, 64), lane0 * 16, 0); };
  Tag tag;
  {
    const int b = blockIdx.x, lane = lane0;
    u32x4 A, B;
    u32x2 ep;
    asm volatile("global_load_dwordx4 %0, %3, %4\n\tglobal_load_dwordx4 %1, %3, %4 offset:1024\n\tglobal_load_dwordx2 %2, %5, %6 sc1"
                 : "=&v"(A), "=&v"(B), "=&v"(ep) : "v"(lane * 16), "s"(x + (size_t)(8 * b + wid) * DM), "v"(F_EP), "s"(ws + WS_FLAG) : "memory");
    if (wid >= 4 && wid < 7) { issq(); issq(); if (lw == 2) { issq(); issq(); wait_vm<32>(); } else wait_vm<16>(); } else wait_vm<0>();
    asm volatile("" : "+v"(A), "+v"(B), "+v"(ep));
    unsigned v[8] = {A[0], A[1], A[2], A[3], B[0], B[1], B[2], B[3]};
    const unsigned long long e64 = ((unsigned long long)(unsigned)__builtin_amdgcn_readfirstlane(ep[1]) << 32) | (unsigned)__builtin_amdgcn_readfirstlane(ep[0]);
    const unsigned long long tg = e64 * 0x9E3779B97F4A7C15ull + 0x632BE59BD9B4E019ull;
    tag.lo = (unsigned)tg; tag.hi = (unsigned)(tg >> 32);
    *(LAS f32x2*)(lds + RST + wid * 8) = ln16(v);
    ln16(v);
    *(LAS u32x4*)(lds + HST + wid * HRS + lane * 16) = (u32x4){v[0], v[1], v[2], v[3]};
    *(LAS u32x4*)(lds + HST + wid * HRS + 1024 + lane * 16) = (u32x4){v[4], v[5], v[6], v[7]};
    wait_lgkm0();
  }
  STAMP(0, 1);
  bf16x8 qh[8];
  f32x4 uk[4][4];
  auto kv8 = [&](int t, int slot) {
    const unsigned v = lane_id() * 16;
    dma4k(rkv, lb + slot * 32768 + lw * 8192, v, t * 32768 + lw * 8192);
    dma4k(rkv, lb + slot * 32768 + lw * 8192 + 4096, v, t * 32768 + lw * 8192 + 4096);
  };
  if (wid < 4) {
    f32x4 acc[8][4], cs[2];
    Frags F0, F1;
    Addr a;
    int xs = 2, wsl = 1;
    {
      const int ln = lane_id(), r = ln & 15;
      addr_init(a, 0, lb + QW, wj, 2 * wi, ln);
      a.x = lb + 65536 + wi * 16384 + r * 128 + (((ln >> 4) ^ (r >> 1)) * 16);
    }
    bar();
    load_frags<2048>(F0, a);
    a.x ^= 64;
#pragma unroll
    for (int c = 0; c < 4; ++c) a.w[c] += 8192;
    wait_lgkm0();
    bar();
    STAMP(1, 1);
    {
      const unsigned o1 = 0x3f803f80;
      u32x4 ones = {o1, o1, o1, o1};
      qstep<1, true, true>(acc, cs, ones, o1, F1, F0, a, xs, wsl);
      for (int t = 0; t < 2; ++t) {
        const unsigned on = t == 0 ? o1 : 0;
        qstep<0, true>(acc, cs, ones, on, F0, F1, a, xs, wsl);
        qstep<1, true>(acc, cs, ones, on, F1, F0, a, xs, wsl);
      }
      const int ln = lane_id();
      const unsigned csa = lb + CSX + ((ln & 15) ? 7168 : (64 * wj + 32 * wi) * 4) + (ln & 48);
      *(LAS f32x4*)(size_t)csa = cs[0];
      *(LAS f32x4*)(size_t)(csa + 64) = cs[1];
      for (int t = 0; t < 13; ++t) { qstep<0, false>(acc, cs, ones, 0, F0, F1, a, xs, wsl); qstep<1, false>(acc, cs, ones, 0, F1, F0, a, xs, wsl); }
      addr_init(a, lb + wi * 8192, lb + 16384, wj, 0, lane_id());
      qstep<2, false>(acc, cs, ones, 0, F0, F1, a, xs, wsl);
    }
    STAMP(1, 2);
    f32x4 c0[4];
    {
      const int ln = lane_id(), g = ln >> 4;
      u32x2 bv[4];
#pragma unroll
      for (int j = 0; j < 4; ++j) bv[j] = *(LAS u32x2*)(lds + CSX + 5120 + (64 * wj + 16 * j + 4 * g) * 2);
      f32x4 cq[4];
#pragma unroll
      for (int j = 0; j < 4; ++j) cq[j] = *(LAS f32x4*)(lds + CSX + 512 + (64 * wj + 16 * (j ^ (2 * wi)) + 4 * g) * 4);
      f32x2 st[8];
#pragma unroll
      for (int i = 0; i < 8; ++i) st[i] = *(LAS f32x2*)(lds + STT + (128 * wi + 16 * i + (ln & 15)) * 8);
      wait_lgkm0();
      bar();
      const unsigned qa = lb + 98304 + wi * 32768 + (4 * wj + 2 * wi + (g >> 1)) * 1024 + ((ln & 15) + 32 * (g & 1)) * 16, qb = qa ^ 2048;
#pragma unroll
      for (int i = 0; i < 8; ++i) {
        const float sc = st[i][1] * 0.12751743082f, nm = -st[i][0];
#pragma unroll
        for (int mm = 0; mm < 2; ++mm)
          *(LAS u32x4*)(size_t)((mm ? qb : qa) + (i >> 1) * 8192 + (i & 1) * 256) =
              pack8((acc[i][2 * mm] + cq[2 * mm] * nm) * sc, (acc[i][2 * mm + 1] + cq[2 * mm + 1] * nm) * sc);
      }
#pragma unroll
      for (int j = 0; j < 4; ++j) c0[j] = uh ? (f32x4){0.f, 0.f, 0.f, 0.f} : (f32x4){bflo(bv[j][0]), bfhi(bv[j][0]), bflo(bv[j][1]), bfhi(bv[j][1])};
    }
    STAMP(1, 3);
    if (hasU) {
      int slot = 1;
      gstep<true, 4, QS, true>(acc, F1, F0, a, slot, c0);
      gemm_steps<true, 4, QS>(acc, F0, F1, a, slot, 30);
      gstep<true, 4, QS>(acc, F0, F1, a, slot);
      STAMP(1, 4);
      const rsrc_t rux = mkrsrc(ws + WS_XB + (size_t)((tile ^ 32) * 4 + kvh) * 98304 + 49152 + wid * 8192, 8192);
      const int ln = lane_id();
#pragma unroll
      for (int ii = 0; ii < 4; ++ii) {
        st16s<0>(pack8(acc[2 * ii + 1][0], acc[2 * ii + 1][1]), rux, ln * 16, ii * 2048);
        st16s<1024>(pack8(acc[2 * ii + 1][2], acc[2 * ii + 1][3]), rux, ln * 16, ii * 2048);
#pragma unroll
        for (int j = 0; j < 4; ++j) uk[ii][j] = acc[2 * ii][j];
      }
    }
    STAMP(1, 5);
  } else {
    const int lane = lane0;
    Strm U;
    if (lw < 2) strm_x(U, ws + WS_HT + (size_t)dq * 1048576 + uh * 524288 + lw * 8192, 524288 - lw * 8192, 16384, uh);
    else strm_w(U, (const char*)w1 + (size_t)uh * 1024 * (FF * 2) + ft * 256, 1024, FF * 2, true);
    if (!hasU) U.r = mkrsrc(ws, 0);
    U.rot = 8 * dq;
    const unsigned pk = lb + PKQ + lw * 512;
    if (lw < 3) {
      if (lw < 2) wait_vm<0>(); else wait_vm<24>();
      bar();
      if (lw < 2) { issq(); issq(); } else wait_vm<16>();
      bar();
      if (lw < 2) issq();
      for (int t = 1; t <= 21; ++t) { issq(); wait_vm<16>(); bar(); }
      peek(rf, pk, F_L + lw * 512); peek(rf, pk + 256, F_L + lw * 512 + 256);
      for (int t = 22; t <= 26; ++t) { issq(); wait_vm<16>(); bar(); }
      issq(); wait_vm<16>();
    } else {
      peek(mkrsrc(b1, FF * 2), lb + CSX + 5120, ft * 256);
      bar();
      for (int t = 0; t <= 7; ++t) { h_out(2 * t); h_out(2 * t + 1); bar(); }
      st_out();
      st16s<0>(*(LAS u32x4*)(lds + CSX + (lane & 31) * 16), mkrsrc(ws + WS_CS + (head * 8 + rb) * 512, 512), lane * 16, 0);
      bar(); bar(); bar();
      wait_vm<0>();
      signal<1>(ws, F_L + blockIdx.x * 8, tag);
      for (int t = 11; t <= 21; ++t) bar();
      peek(rf, pk, F_L + lw * 512); peek(rf, pk + 256, F_L + lw * 512 + 256);
      for (int t = 22; t <= 26; ++t) bar();
      wait_vm<0>();
    }
    peek_check(ws, pk, F_L + lw * 512, 32, tag); peek_check(ws, pk + 256, F_L + lw * 512 + 256, 32, tag);
    bar();
    if (blockIdx.x == 0 && lw == 3) signal<1>(ws, F_EP, tag);
    if (lw == 2) issq();
    if (lw == 3) {
      const rsrc_t rs = mkrsrc(ws + WS_ST + rb * 2048, 2048);
      dma16s(rs, lb + STT, lane * 16, 0); dma16s(rs, lb + STT + 1024, lane * 16, 1024);
      dma4k(mkrsrc(ws + WS_CS + head * 4096, 4096), lb + CSX + 1024, lane * 16, 0);
    }
    const bool us = lw < 3;
    if (us) issue(U, 0);
    wait_vm<16>(); bar();
    if (us) wait_vm<8>(); else wait_vm<0>();
    bar();
    if (us) issue(U, 1);
    else {
      const LAS f32x2* cp = (const LAS f32x2*)(lds + CSX + 1024) + lane;
      *(LAS f32x2*)(lds + CSX + 512 + lane * 8) = ((cp[0] + cp[64]) + (cp[128] + cp[192])) + ((cp[256] + cp[320]) + (cp[384] + cp[448]));
      wait_lgkm0();
    }
    bar();
    wait_vm<8>(); bar();
    if (us) issue(U, 2);
    wait_vm<8>();
    bar();
    bar();
    if (hasU) {
      if (us) { issue(U, 3); for (int t = 1; t <= 28; ++t) { issue(U, t + 3); wait_vm<16>(); bar(); } }
      else {
        for (int t = 1; t <= 26; ++t) bar();
        spin_until(ws, F_X + kvh * 128, 8, tag);
        bar(); bar();
      }
      wait_vm<8>(); bar();
      kv8(0, 0); wait_vm<8>(); bar();
      kv8(1, 1); bar();
      kv8(2, 2); wait_vm<8>(); bar();
    }
  }
  if (!hasU) {
    const int sp = g8 * 4 + (idx - 28), lane = lane_id(), h = lane >> 5;
    unsigned e[4][8];
#pragma unroll
    for (int i = 0; i < 4; ++i) {
      const int kh = sp >> 3, unit = (sp & 7) * 4 + i, frag = wid;
      const bool isK = unit == 0 || ((unit & 1) && unit < 31);
      const u16* sk = kc + ((size_t)kh * SEQ + 1536 + 32 * ((unit + 1) >> 1) + (lane & 31)) * 128 + 32 * (frag >> 1) + 4 * (2 * (frag & 1) + h);
      const u16* sv = vc + ((size_t)kh * SEQ + 1536 + 32 * ((unit - 1) >> 1) + 16 * (frag >> 2) + 4 * h) * 128 + 32 * (frag & 3) + (lane & 31);
      const u16* src = isK ? sk : sv;
      const int s1 = isK ? 1 : 128, s4 = isK ? 16 : 1024;
#pragma unroll
      for (int j2 = 0; j2 < 8; ++j2) e[i][j2] = src[(j2 >> 2) * s4 + (j2 & 3) * s1];
    }
#pragma unroll
    for (int i = 0; i < 4; ++i)
      st16s<0>((u32x4){e[i][0] | (e[i][1] << 16), e[i][2] | (e[i][3] << 16), e[i][4] | (e[i][5] << 16), e[i][6] | (e[i][7] << 16)},
               mkrsrc(ws + WS_KV, 1048576), (wid * 64 + lane) * 16, (sp * 4 + i) * 8192);
    wait_vm<0>();
    wait_lgkm0();
    bar();
    if (wid == 0) signal<1>(ws, F_X + (sp >> 3) * 128 + (sp & 7) * 8, tag);
    unsigned pw[NSP];
#pragma unroll
    for (int i = 0; i < NSP; ++i) pw[i] = *(const unsigned*)((const char*)w2 + (size_t)i * PFB + (size_t)(sp * 512 + wid * 64 + lane) * 128);
#pragma unroll
    for (int i = 0; i < NSP; ++i) asm volatile("" ::"v"(pw[i]));
    if (wid >= 4) { spin_until(ws, F_X + kvh * 128, 8, tag); kv8(0, 0); kv8(1, 1); kv8(2, 2); wait_vm<8>(); }
    bar();
  }
  {
    const int ln = lane_id();
#pragma unroll
    for (int ks = 0; ks < 8; ++ks) qh[ks] = *(LAS bf16x8*)(lds + 98304 + wid * 8192 + ks * 1024 + ln * 16);
  }
  STAMP(1, 6);
  Strm Y;
  auto y_iss = [&](int k) { const int r = k + YROT * (dc & 3); issue(Y, k, r >= 28 ? r - 28 : r); };
  Frags FY;
  Addr aY;
  {
    const int lane = lane_id();
    Att A;
#pragma unroll
    for (int ks = 0; ks < 8; ++ks) A.q[ks] = qh[ks];
    A.l = 0.f;
    const unsigned kvl = lb + lane * 16;
#pragma unroll
    for (int i = 0; i < 4; ++i) A.pre[i] = ldfrag(kvl + i * 1024);
    const bool strm = wid >= 4 && wid < 7, hw = wid >= 4;
    const rsrc_t rp2 = mkrsrc(w2, SEQ * FF * 2), rpo = mkrsrc(wo, HD * DM * 2);
    const unsigned pv = (blockIdx.x * 64 + lane) * 128;
#define PF(W, R, C) do { if (&R != &rp2 || (C) >= NSP) pf_line<W>(wid, R, pv, (C) * 2097152u); } while (0)
    const int fp = F_P + (ut * 2 + uh) * 64 + (wid & 3) * 8;
    const bool cu = !hw && hasU;
    STAMPA(0);
    att_tile<0>(A, kvl + 0 * 32768, kvl + 1 * 32768, [&](int n) { if (n == 20 && cu) { wait_vm<0>(); signal<1>(ws, fp ^ 64, tag); }
      if (n == 23) PF(0, rp2, 0); });
    if (hw) wait_vm<0>();
    bar();
    STAMPA(1);
    att_tile<1>(A, kvl + 1 * 32768, kvl + 2 * 32768, [&](int n) {
      if (n == 0 && hw) kv8(3, 3); if (n == 12 && hw) kv8(4, 0); if (n == 28 && hw) peek(rf, lb + 131072 + lw * 256, fp); if (n == 3) PF(1, rp2, 1); if (n == 10) PF(2, rp2, 2); });
    if (hw) wait_vm<9>();
    bar();
    STAMPA(2);
    att_tile<1>(A, kvl + 2 * 32768, kvl + 3 * 32768, [&](int n) {
      if (n == 8 && hw) {
        wait_vm<0>();
        if (hasU) peek_check(ws, lb + 131072 + lw * 256, fp, 1, tag);
        const rsrc_t rux = mkrsrc(ws + WS_XB + (size_t)(tile * 4 + kvh) * 98304 + 49152, hasU ? 32768 : 0);
        dma4k(rux, lb + 32768 + lw * 8192, lane * 16, lw * 8192); dma4k(rux, lb + 32768 + lw * 8192 + 4096, lane * 16, lw * 8192 + 4096);
      }
      if (n == 27) PF(3, rp2, 3); });
    if (hw) wait_vm<0>();
    bar();
    if (hw) { kv8(5, 4); kv8(7, 2); }
    STAMPA(3);
    if (cu) {
      u32x4 p[8];
      p[0] = ldsr(kvl + 32768 + wid * 8192);
#pragma unroll
      for (int k = 0; k < 8; ++k) {
        if (k < 7) p[k + 1] = ldsr(kvl + 32768 + wid * 8192 + (k + 1) * 1024);
        f32x4 a = uk[k >> 1][2 * (k & 1)], b = uk[k >> 1][2 * (k & 1) + 1];
        add8(a, b, p[k]);
        p[k] = (u32x4){gelu2(a.xy), gelu2(a.zw), gelu2(b.xy), gelu2(b.zw)};
        asm volatile("" : "+v"(p[k]));
        if (k == 1) PF(0, rp2, 4);
        if (k == 4) PF(1, rp2, 5);
        SB0();
      }
      bar();
      const rsrc_t ru = mkrsrc(ws + WS_U + (size_t)((2 * dq + wi) * 112 + 4 * ft + 2 * wj) * 8192 + uh * 1024, 16384);
#pragma unroll
      for (int k = 0; k < 8; ++k) st16s<0>(p[k], ru, lane * 16, (k & 1) * 8192 + (k >> 1) * 2048);
    } else { PF(0, rp2, 4); PF(1, rp2, 5); bar(); }
    STAMPA(4);
    att_tile<1>(A, kvl + 3 * 32768, kvl + 0 * 32768, [&](int n) {
      if (n == 20 && cu) { wait_vm<0>(); signal<1>(ws, F_U + ((2 * dq + wi) * 4 + kvh) * 256 + (((idx >> 2) * 2 + uh) * 2 + wj) * 8, tag); }
      if (n == 23) PF(2, rp2, 6); });
    if (hw) wait_vm<0>();
    bar();
    STAMPA(5);
    att_tile<1>(A, kvl + 0 * 32768, kvl + 4 * 32768, [&](int n) { if (n == 0 && hw) kv8(6, 3); if (n == 5) PF(3, rpo, 0); if (n == 18) PF(0, rpo, 1); });
    if (hw) wait_vm<0>();
    bar();
    STAMPA(6);
    att_tile<1>(A, kvl + 4 * 32768, kvl + 3 * 32768, [&](int n) { if (n == 1) PF(1, rpo, 2); if (n == 13) PF(2, rpo, 3); });
    if (lw == 0) strm_x(Y, ws + WS_U + (size_t)(dc * 112 + 28 * kvh) * 8192, 28 * 8192, 8192, 0);
    else strm_w(Y, (const char*)w2 + (size_t)kvh * 896 * (SEQ * 2) + rb * 512 + ((lw - 1) & 1) * 256, 896, SEQ * 2, false);
    bar();
    STAMPA(7);
    att_tile<1>(A, kvl + 3 * 32768, kvl + 2 * 32768, [&](int n) {
      if (n == 4 && wid == 4) peek(rf, lb + 49152, F_U + (dc * 4 + kvh) * 256);
      if (n == 18 && wid == 4) { wait_vm<0>(); peek_check(ws, lb + 49152, F_U + (dc * 4 + kvh) * 256, 28, tag); }
      if (n == 19 && strm) y_iss(0); if (n == 27 && strm) y_iss(1); });
    bar();
    STAMPA(8);
    att_tile<2>(A, kvl + 2 * 32768, 0, [&](int n) {
      if (n == 9) { if (hw) wait_vm<8>(); bar(); }
      if (n == 10 && strm) y_iss(2); });
    STAMP(1, 7);
    STAMPA(9);
    if (__builtin_expect(__any(!(A.l <= 1.0e9f)), 0)) att_redo(A, rkv, lane * 16);
    addr_init(aY, lb, lb + 8192 + wi * 8192, wj, kvh, lane);
    if (!hw) load_frags(FY, aY);
    auto sw = __builtin_amdgcn_permlane32_swap(__float_as_uint(A.l), __float_as_uint(A.l), false, false);
    const float inv = __builtin_amdgcn_rcpf(__uint_as_float(sw[0]) + __uint_as_float(sw[1]));
    const unsigned oa = lb + PARK + wid * 2048 + ((lane >> 4) & 1) * 1024 + (lane >> 5) * 256 + (lane & 15) * 16;
#pragma unroll
    for (int d = 0; d < 4; ++d)
#pragma unroll
      for (int u = 0; u < 2; ++u) {
        const int r0 = 4 * u, r1 = 8 + 4 * u;
        *(LAS u32x4*)(size_t)(oa + d * 16384 + u * 512) =
            (u32x4){cvtpk(A.o[d][r0] * inv, A.o[d][r0 + 1] * inv), cvtpk(A.o[d][r0 + 2] * inv, A.o[d][r0 + 3] * inv),
                    cvtpk(A.o[d][r1] * inv, A.o[d][r1 + 1] * inv), cvtpk(A.o[d][r1 + 2] * inv, A.o[d][r1 + 3] * inv)};
      }
    wait_lgkm0();
  }
  STAMP(1, 8);
  if (wid < 4) {
    f32x4 acc[8][4];
    Frags F0 = FY, F1;
    Addr a = aY;
    int slot = 1;
    addr_add(a, QS);
    bar();
    STAMP(1, 9);
    gstep<false, 4, QS, true>(acc, F1, F0, a, slot);
    gemm_steps<false, 4, QS>(acc, F0, F1, a, slot, 26);
    addr_init(a, lb + wi * 8192, lb + 16384, wj, 0, lane_id());
    gstep<false, 4, QS>(acc, F0, F1, a, slot);
    STAMP(1, 10);
    ystep_park(acc, F1, F0, a, slot, lb + PARK + lane_id() * 16 + wid * 4096);
    STAMP(1, 11);
    gemm_steps<true, 4, QS>(acc, F0, F1, a, slot, 30);
    gstep<true, 4, QS>(acc, F0, F1, a, slot);
    STAMP(1, 12);
    const int lt = lane_id(), g = lt >> 4;
    const unsigned wo_ = wi * 8192 + wj * 2048;
#pragma unroll
    for (int dl = 1; dl < 4; ++dl)
#pragma unroll
      for (int h = 0; h < 2; ++h)
#pragma unroll
        for (int mm = 0; mm < 2; ++mm)
          st16s<0>(pack8(acc[4 * h + dl][2 * mm], acc[4 * h + dl][2 * mm + 1]), rxb, lt * 16, (kvh ^ dl) * 98304 + 49152 + (dl - 1) * 16384 + wo_ + h * 4096 + mm * 1024);
    const int fs = F_S + tile * 512 + wid * 32;
    wait_vm<8>(); signal<1>(ws, fs + (kvh ^ 1) * 128, tag);
    wait_vm<5>(); signal<1>(ws, fs + (kvh ^ 2) * 128 + 8, tag);
    wait_vm<2>(); signal<1>(ws, fs + (kvh ^ 3) * 128 + 16, tag);
    STAMP(1, 13);
    const int srow = rb * 256 + (8 * wi + kvh) * 16;
    const rsrc_t rx = mkrsrc(x + (size_t)srow * DM, 80 * DM * 2), rout = mkrsrc(out + (size_t)srow * DM, 80 * DM * 2);
    const unsigned vo = ((lt & 15) * DM + dc * 128 + wj * 64 + 16 * (g & 1) + 8 * (g >> 1)) * 2;
    u32x4 ps[3][4], xv[4], gv[4];
#pragma unroll
    for (int p = 0; p < 4; ++p) xv[p] = __builtin_amdgcn_raw_buffer_load_b128(rx, vo + (p >> 1) * (64 * DM * 2) + (p & 1) * 64, 0, 0);
    bar();
#pragma unroll
    for (int p = 0; p < 4; ++p) gv[p] = ldsr(lb + PARK + lt * 16 + wo_ + (p >> 1) * 4096 + (p & 1) * 1024);
    {
      GAS unsigned long long* fp = (GAS unsigned long long*)(ws + WS_FLAG + fs + kvh * 128) + (lt & 3);
      const unsigned long long want = ((unsigned long long)tag.hi << 32) | tag.lo;
      unsigned got = 0;
      for (unsigned sp = 0; got != 7; ++sp) {
        unsigned now = (unsigned)__ballot(__hip_atomic_load(fp, __ATOMIC_RELAXED, __HIP_MEMORY_SCOPE_AGENT) == want) & 7;
        if (sp >= (1u << 20)) now = 7;
        asm volatile("" ::: "memory");
#pragma unroll
        for (int k = 0; k < 3; ++k)
          if ((now & ~got) >> k & 1) {
#pragma unroll
            for (int p = 0; p < 4; ++p)
              ps[k][p] = __builtin_amdgcn_raw_buffer_load_b128(rxb, lt * 16 + (p & 1) * 1024, kvh * 98304 + 49152 + k * 16384 + wo_ + (p >> 1) * 4096, 16);
          }
        got |= now;
        if (got != 7) __builtin_amdgcn_s_sleep(1);
      }
    }
    STAMP(1, 14);
#pragma unroll
    for (int p = 0; p < 4; ++p) {
      f32x4 s0 = acc[4 * (p >> 1)][2 * (p & 1)], s1 = acc[4 * (p >> 1)][2 * (p & 1) + 1];
#pragma unroll
      for (int k = 0; k < 3; ++k) add8(s0, s1, ps[k][p]);
      s0 *= (f32x4){bflo(gv[p][0]), bfhi(gv[p][0]), bflo(gv[p][1]), bfhi(gv[p][1])};
      s1 *= (f32x4){bflo(gv[p][2]), bfhi(gv[p][2]), bflo(gv[p][3]), bfhi(gv[p][3])};
      const u32x4 tp = pack8(s0, s1);
      const auto w0 = __builtin_amdgcn_permlane16_swap(tp[0], tp[2], false, false), w1 = __builtin_amdgcn_permlane16_swap(tp[1], tp[3], false, false);
      const unsigned t[4] = {w0[0], w1[0], w0[1], w1[1]};
      u32x4 o;
#pragma unroll
      for (int e = 0; e < 4; ++e) o[e] = cvtpk(bflo(xv[p][e]) + bflo(t[e]), bfhi(xv[p][e]) + bfhi(t[e]));
      __builtin_amdgcn_raw_buffer_store_b128(o, rout, vo + (p >> 1) * (64 * DM * 2) + (p & 1) * 64, 0, 0);
    }
    STAMP(1, 15);
  } else {
    const int lane = lane_id();
    const unsigned pl = lb + PARK + lane * 16, pk = lb + 159744 + lw * 256;
    const int fo = F_O + (rb * 4 + kvh) * 128;
    if (lw < 3) {
      Strm S;
      if (lw < 2) strm_x(S, ws + WS_O + (size_t)(rb * 128 + 32 * kvh) * 16384 + lw * 8192, 524288 - lw * 8192, 16384, kvh);
      else strm_w(S, (const char*)wo + (size_t)kvh * 1024 * (DM * 2) + dc * 256, 1024, DM * 2, false);
      wait_vm<8>();
      bar();
      y_iss(3);
      for (int t = 1; t <= 22; ++t) { y_iss(t + 3); wait_vm<16>(); bar(); }
      peek(rf, lb + PARK + lw * 256, fo);
      y_iss(26); wait_vm<16>(); bar();
      y_iss(27); wait_vm<16>(); bar();
      peek_check(ws, lb + PARK + lw * 256, fo, 8, tag);
      auto iss = [&](int k) { const int r = (k & 16) | ((k + SROT * (rb & 3)) & 15); issue(S, k, 4 * (r & 7) + (r >> 3)); wait_vm<16>(); bar(); };
      for (int t = 25; t <= 36; ++t) iss(t - 25);
      peek(rf, lb + PARK + 16384 + lw * 256, fo + 64);
      for (int t = 37; t <= 40; ++t) iss(t - 25);
      peek_check(ws, lb + PARK + 16384 + lw * 256, fo + 64, 8, tag);
      for (int t = 41; t <= 56; ++t) iss(t - 25);
      wait_vm<8>(); bar();
      wait_vm<0>(); bar();
    } else {
      bar();
      const rsrc_t ro = mkrsrc(ws + WS_O + (size_t)(rb * 128 + 4 * head) * 16384, 65536);
      for (int t = 1; t <= 25; t += 2) {
        const unsigned o = (t >> 1) * 5120;
        const u32x4 v0 = ldsr(pl + o), v1 = ldsr(pl + o + 1024), v2 = ldsr(pl + o + 2048), v3 = ldsr(pl + o + 3072), v4 = ldsr(pl + (t < 25 ? o + 4096 : 0));
        st16s<0>(v0, ro, lane * 16, o); st16s<1024>(v1, ro, lane * 16, o); st16s<2048>(v2, ro, lane * 16, o);
        if (t == 17) { wait_vm<11>(); signal<1>(ws, fo + dc * 8, tag); }
        bar();
        st16s<3072>(v3, ro, lane * 16, o); st16s<0>(v4, ro, lane * 16, o + 4096);
        bar();
      }
      bar(); bar(); bar();
      wait_vm<0>();
      signal<1>(ws, fo + 64 + dc * 8, tag);
      for (int t = 30; t <= 49; t += 2) {
        u32x4 v[5];
        unsigned so[5];
#pragma unroll
        for (int i = 0; i < 5; ++i) {
          const int p = (t - 30) / 2 * 5 + i, dl = 1 + (p >> 4);
          v[i] = ldsr(pl + (p < 48 ? 16384 + p * 1024 : 0));
          so[i] = p < 48 ? (kvh ^ dl) * 98304 + p * 1024 : 393216;
        }
        st16s<0>(v[0], rxb, lane * 16, so[0]); st16s<0>(v[1], rxb, lane * 16, so[1]); st16s<0>(v[2], rxb, lane * 16, so[2]);
        bar();
        st16s<0>(v[3], rxb, lane * 16, so[3]); st16s<0>(v[4], rxb, lane * 16, so[4]);
        bar();
      }
      bar(); bar();
      wait_vm<0>();
      signal<14>(ws, F_Y + (tile * 4 + (kvh ^ (lane & 3))) * 128 + ((lane & 3) - 1) * 8, tag);
      for (int t = 52; t <= 56; ++t) bar();
      peek(rf, pk, F_Y + (tile * 4 + kvh) * 128);
      bar();
      wait_vm<0>();
      peek_check(ws, pk, F_Y + (tile * 4 + kvh) * 128, 3, tag);
      bar();
    }
    u32x4 py[3][4];
#pragma unroll
    for (int k = 0; k < 3; ++k)
#pragma unroll
      for (int m = 0; m < 4; ++m) py[k][m] = __builtin_amdgcn_raw_buffer_load_b128(rxb, lane * 16 + m * 1024, kvh * 98304 + lw * 4096 + k * 16384, 16);
    const float bb = bflo(b2[rb * 256 + (4 * lw + kvh) * 16 + (lane & 15)]);
    bar();
    bar();
#pragma unroll
    for (int m = 0; m < 4; ++m) {
      f32x4 y0 = {bb, bb, bb, bb}, y1 = y0;
      add8(y0, y1, ldsr(pl + lw * 4096 + m * 1024));
#pragma unroll
      for (int k = 0; k < 3; ++k) add8(y0, y1, py[k][m]);
      *(LAS u32x4*)(size_t)(pl + lw * 4096 + m * 1024) = (u32x4){silu2(y0.xy), silu2(y0.zw), silu2(y1.xy), silu2(y1.zw)};
    }
    wait_lgkm0();
    bar();
  }
}

extern "C" void kernel_launch(void* const* d_in, const int* in_sizes, int n_in, void* d_out, int out_size, void* d_ws, size_t ws_size,
                              hipStream_t stream) {
  const u16* x = (const u16*)d_in[0];
  const u16* kc = (const u16*)d_in[1];
  const u16* vc = (const u16*)d_in[2];
  const u16* wq = (const u16*)d_in[3];
  const u16* wo = (const u16*)d_in[4];
  const u16* w1 = (const u16*)d_in[6];
  const u16* b1 = (const u16*)d_in[7];
  const u16* w2 = (const u16*)d_in[8];
  const u16* b2 = (const u16*)d_in[9];
  char* ws = (char*)d_ws;
  (void)hipFuncSetAttribute((const void*)k_main, hipFuncAttributeMaxDynamicSharedMemorySize, 163840);
  hipLaunchKernelGGL(k_main, dim3(256), dim3(512), 163840, stream, x, kc, vc, wq, wo, w1, b1, w2, b2, ws, (u16*)d_out);
}
```

```cpp
#include <hip/hip_runtime.h>
#include <hip/hip_bf16.h>

#define PFB 2097152
#define CODE_BYTES 73728
#define YROT 7
#define SROT 4
#define NSP 3
typedef unsigned short u16;
typedef short bf16x8 __attribute__((ext_vector_type(8)));
typedef short s16x4 __attribute__((ext_vector_type(4)));
typedef float f32x16 __attribute__((ext_vector_type(16)));
typedef float f32x4 __attribute__((ext_vector_type(4)));
typedef float f32x2 __attribute__((ext_vector_type(2)));
typedef unsigned u32x4 __attribute__((ext_vector_type(4)));
typedef unsigned u32x2 __attribute__((ext_vector_type(2)));
typedef __bf16 bf16x2 __attribute__((ext_vector_type(2)));
typedef __amdgpu_buffer_rsrc_t rsrc_t;
#define LAS __attribute__((address_space(3)))
#define GAS __attribute__((address_space(1)))
#define DI __device__ __forceinline__
#define SGB(m, n) __builtin_amdgcn_sched_group_barrier(m, n, 0)
#define SB0() __builtin_amdgcn_sched_barrier(0)

constexpr int SEQ = 2048, DM = 1024, FF = 3584, HD = 4096;
constexpr size_t WS_FLAG = 0;
constexpr int F_EP = 0, F_X = 512, F_L = 2048, F_U = 4096, F_O = 12288, F_Y = 16384, F_S = 49152, F_P = 81920, F_END = 96256;
constexpr size_t WS_ST = 98304;
constexpr size_t WS_CS = 131072;
constexpr size_t WS_HT = 262144;
constexpr size_t WS_KV = WS_HT + 4194304;
constexpr size_t WS_O = WS_KV + 1048576;
constexpr size_t WS_U = WS_O + 16777216;
constexpr size_t WS_XB = WS_U + 7340032;
constexpr size_t WS_END = WS_XB + 25165824;

#define STAMP(kid, i)
#define STAMPA(i)
#define STAMPF(i)
DI unsigned cvtpk(float lo, float hi) { f32x2 v = {lo, hi}; bf16x2 b = __builtin_convertvector(v, bf16x2); return __builtin_bit_cast(unsigned, b); }
DI float bflo(unsigned u) { return __uint_as_float(u << 16); }
DI float bfhi(unsigned u) { return __uint_as_float(u & 0xffff0000u); }
DI float rbf(float x) { return bflo(cvtpk(x, x)); }
template <int N> DI void wait_vm() { asm volatile("s_waitcnt vmcnt(%0)" ::"i"(N) : "memory"); }
DI void wait_lgkm0() { __builtin_amdgcn_s_waitcnt(0xC07F); }
DI void bar() { asm volatile("" ::: "memory"); __builtin_amdgcn_s_barrier(); asm volatile("" ::: "memory"); }
DI rsrc_t mkrsrc(const void* p, unsigned bytes) { return __builtin_amdgcn_make_buffer_rsrc((void*)p, 0, bytes, 0x00020000); }
DI void dma16(rsrc_t r, unsigned lds_addr, unsigned voff, unsigned soff) {
  asm volatile("s_mov_b32 m0, %0\n\ts_nop 0\n\tbuffer_load_dwordx4 %1, %2, %3 offen lds" ::"s"(lds_addr), "v"(voff), "s"(r), "s"(soff) : "memory");
}
DI void dma16s(rsrc_t r, unsigned lds_addr, unsigned voff, unsigned soff) {
  asm volatile("s_mov_b32 m0, %0\n\ts_nop 0\n\tbuffer_load_dwordx4 %1, %2, %3 offen sc1 lds" ::"s"(lds_addr), "v"(voff), "s"(r), "s"(soff) : "memory");
}
template <int OFF> DI void st16s(u32x4 v, rsrc_t r, unsigned voff, unsigned soff) {
  asm volatile("buffer_store_dwordx4 %0, %1, %2, %3 offen offset:%4 sc1\n\ts_nop 1" ::"v"(v), "v"(voff), "s"(r), "s"(soff), "i"(OFF) : "memory");
}
DI int lane_id() { int l; asm volatile("v_mbcnt_lo_u32_b32 %0, -1, 0\n\tv_mbcnt_hi_u32_b32 %0, -1, %0" : "=v"(l)); return l; }
struct Tag { unsigned lo, hi; };
DI void peek(rsrc_t rf, unsigned lds_addr, int off) {
  asm volatile("s_mov_b32 m0, %0\n\ts_nop 0\n\tbuffer_load_dword %3, %1, %2 offen sc1 lds" ::"s"(lds_addr), "s"(rf), "s"(off), "v"(lane_id() * 4) : "memory");
}
template <int GAP = 1> DI void spin_until(char* ws, int off, int n, Tag t) {
  const int ln = lane_id();
  GAS unsigned long long* p = (GAS unsigned long long*)(ws + WS_FLAG + off) + (ln < n ? ln : 0);
  const unsigned long long want = ((unsigned long long)t.hi << 32) | t.lo;
  for (unsigned sp = 0; sp < (1u << 20); ++sp) {
    if (__all(__hip_atomic_load(p, __ATOMIC_RELAXED, __HIP_MEMORY_SCOPE_AGENT) == want)) break;
    __builtin_amdgcn_s_sleep(GAP);
  }
  asm volatile("" ::: "memory");
}
DI void peek_check(char* ws, unsigned lds_addr, int off, int n, Tag t) {
  const int ln = lane_id();
  const unsigned v = *(LAS volatile unsigned*)(size_t)(lds_addr + ln * 4);
  if (!__all(ln >= 2 * n || v == ((ln & 1) ? t.hi : t.lo))) spin_until(ws, off, n, t);
}
template <long MASK> DI void signal(char* ws, int off, Tag t) {
  const u32x2 v = {t.lo, t.hi};
  asm volatile("s_mov_b64 exec, %3\n\tglobal_store_dwordx2 %0, %1, %2 sc1\n\ts_mov_b64 exec, -1" ::"v"(off), "v"(v), "s"(ws + WS_FLAG), "s"(MASK) : "memory");
}
DI void dma4k(rsrc_t r, unsigned lds_addr, unsigned voff, unsigned soff) {
  asm volatile("s_mov_b32 m0, %0\n\ts_nop 0\n\tbuffer_load_dwordx4 %1, %2, %3 offen sc1 lds\n\tbuffer_load_dwordx4 %1, %2, %3 offen offset:1024 sc1 lds\n\t"
               "buffer_load_dwordx4 %1, %2, %3 offen offset:2048 sc1 lds\n\tbuffer_load_dwordx4 %1, %2, %3 offen offset:3072 sc1 lds"
               ::"s"(lds_addr), "v"(voff), "s"(r), "s"(soff) : "memory");
}
template <int W> DI void pf_line(int wid, rsrc_t r, unsigned voff, unsigned soff) {
  asm volatile("s_cmp_lg_u32 %0, %4\n\ts_cbranch_scc1 1f\n\tbuffer_load_dword v255, %1, %2, %3 offen\n1:"
               ::"s"(wid), "v"(voff), "s"(r), "s"(soff), "i"(W) : "scc", "memory", "v255");
}
template <int CTRL> DI float dppf(float x) { return __builtin_bit_cast(float, __builtin_amdgcn_mov_dpp(__builtin_bit_cast(int, x), CTRL, 0xf, 0xf, true)); }
DI float wave_sum(float v) {
  v += dppf<0xB1>(v); v += dppf<0x4E>(v); v += dppf<0x141>(v); v += dppf<0x140>(v);
  auto a = __builtin_amdgcn_permlane16_swap(__float_as_uint(v), __float_as_uint(v), false, false);
  v = __uint_as_float(a[0]) + __uint_as_float(a[1]);
  auto b = __builtin_amdgcn_permlane32_swap(__float_as_uint(v), __float_as_uint(v), false, false);
  return __uint_as_float(b[0]) + __uint_as_float(b[1]);
}

DI f32x2 ln16(unsigned (&w)[8]) {
  f32x2 v[8];
#pragma unroll
  for (int i = 0; i < 8; ++i) v[i] = (f32x2){bflo(w[i]), bfhi(w[i])};
  const f32x2 s = ((v[0] + v[1]) + (v[2] + v[3])) + ((v[4] + v[5]) + (v[6] + v[7]));
  const float mean = wave_sum(s[0] + s[1]) * (1.f / 1024.f);
  f32x2 q0 = {0.f, 0.f}, q1 = q0;
#pragma unroll
  for (int i = 0; i < 8; i += 2) {
    const f32x2 d0 = v[i] - mean, d1 = v[i + 1] - mean;
    q0 = __builtin_elementwise_fma(d0, d0, q0); q1 = __builtin_elementwise_fma(d1, d1, q1);
  }
  q0 += q1;
  const float var = wave_sum(q0[0] + q0[1]) * (1.f / 1024.f);
  const float mb = rbf(mean);
  const float rs = __builtin_amdgcn_rcpf(rbf(__builtin_amdgcn_sqrtf(rbf(rbf(var) + 1e-5f))));
#pragma unroll
  for (int i = 0; i < 8; ++i) {
    const f32x2 d = v[i] - mb;
    const unsigned c = cvtpk(d[0], d[1]);
    const f32x2 r = (f32x2){bflo(c), bfhi(c)} * rs;
    w[i] = cvtpk(r[0], r[1]);
  }
  return (f32x2){mb, rs};
}

struct Frags { bf16x8 x[8]; bf16x8 w[4]; };
struct Addr { unsigned x, w[4]; };
template <int XS = 1024> DI void load_frags(Frags& f, const Addr& a) {
#pragma unroll
  for (int c = 0; c < 4; ++c) {
    s16x4 a0 = __builtin_amdgcn_ds_read_tr16_b64_v4i16((LAS s16x4*)(size_t)(a.w[c]));
    s16x4 a1 = __builtin_amdgcn_ds_read_tr16_b64_v4i16((LAS s16x4*)(size_t)(a.w[c] + 4096));
    f.w[c] = __builtin_shufflevector(a0, a1, 0, 1, 2, 3, 4, 5, 6, 7);
  }
#pragma unroll
  for (int i = 0; i < 8; ++i) f.x[i] = *(LAS bf16x8*)(size_t)(a.x + i * XS);
}
template <bool WA, bool Z> DI void mma_all(f32x4 (&acc)[8][4], const Frags& f, const f32x4* c0) {
  const f32x4 zero = {0.f, 0.f, 0.f, 0.f};
#pragma unroll
  for (int i = 0; i < 8; ++i)
#pragma unroll
    for (int jj = 0; jj < 4; ++jj) {
      const int j = (i & 1) ? 3 - jj : jj;
      if (WA) acc[i][j] = __builtin_amdgcn_mfma_f32_16x16x32_bf16(f.w[j], f.x[i], Z ? (c0 ? c0[j] : zero) : acc[i][j], 0, 0, 0);
      else    acc[i][j] = __builtin_amdgcn_mfma_f32_16x16x32_bf16(f.x[i], f.w[j], Z ? (c0 ? c0[j] : zero) : acc[i][j], 0, 0, 0);
    }
}
DI void addr_add(Addr& a, int d) {
  a.x += d;
#pragma unroll
  for (int c = 0; c < 4; ++c) a.w[c] += d;
}
template <bool WA, int NSLOT, int SLOTB, bool Z = false>
DI void gstep(f32x4 (&acc)[8][4], Frags& Fn, const Frags& Fc, Addr& a, int& slot, const f32x4* c0 = nullptr) {
  SB0();
  load_frags(Fn, a);
  mma_all<WA, Z>(acc, Fc, c0);
  slot = (slot + 1 == NSLOT) ? 0 : slot + 1;
  addr_add(a, (slot == 0) ? -(NSLOT - 1) * SLOTB : SLOTB);
  asm volatile("" : "+v"(a.x), "+v"(a.w[0]), "+v"(a.w[1]), "+v"(a.w[2]), "+v"(a.w[3]));
#pragma unroll
  for (int i = 0; i < 8; ++i) { SGB(0x8, 1); SGB(0x100, 1); SGB(0x8, 1); SGB(0x100, 1); SGB(0x8, 1); }
#pragma unroll
  for (int i = 0; i < 6; ++i) { SGB(0x8, 1); SGB(0x4, 1); SGB(0x2, 1); }
  SGB(0x8, 2);
  SB0();
  wait_lgkm0();
  bar();
}
template <int MODE, bool CS, bool Z = false>
DI void qstep(f32x4 (&acc)[8][4], f32x4 (&cs)[2], u32x4& ones, unsigned onext, Frags& Fn, const Frags& Fc, Addr& a, int& xs, int& wsl) {
  const f32x4 zero = {0.f, 0.f, 0.f, 0.f};
  SB0();
  load_frags<MODE == 2 ? 1024 : 2048>(Fn, a);
  if (CS) {
#pragma unroll
    for (int j = 0; j < 2; ++j) cs[j] = __builtin_amdgcn_mfma_f32_16x16x32_bf16(Fc.w[j], __builtin_bit_cast(bf16x8, ones), Z ? zero : cs[j], 0, 0, 0);
    ones = (u32x4){onext, onext, onext, onext};
    asm volatile("" : "+v"(ones));
  }
  mma_all<true, Z>(acc, Fc, nullptr);
  if (MODE == 2) addr_add(a, 24576);
  else {
    wsl = (wsl + 1) & 3;
    const int dw = wsl ? 8192 : -24576;
#pragma unroll
    for (int c = 0; c < 4; ++c) a.w[c] += dw;
    a.x ^= 64;
    if (MODE == 1) { xs = (xs == 2) ? 0 : xs + 1; a.x += xs ? 32768 : -65536; }
  }
  asm volatile("" : "+v"(a.x), "+v"(a.w[0]), "+v"(a.w[1]), "+v"(a.w[2]), "+v"(a.w[3]));
#pragma unroll
  for (int i = 0; i < 8; ++i) { SGB(0x8, 1); SGB(0x100, 1); SGB(0x8, 1); SGB(0x100, 1); SGB(0x8, 1); }
#pragma unroll
  for (int i = 0; i < 7; ++i) { SGB(0x8, 1); SGB(0x4, 1); SGB(0x2, 1); }
  if (CS) { SGB(0x8, 1); SGB(0x2, 2); SGB(0x8, 1); SGB(0x2, 2); }
  SGB(0x8, 1);
  SB0();
  wait_lgkm0();
  bar();
}
DI void addr_init(Addr& a, unsigned xbase, unsigned wbase, int wj, int cx, int lane) {
  const int g = lane >> 4, q = (lane >> 2) & 3, p = lane & 3, row = 4 * g + q;
  a.x = xbase + lane * 16;
#pragma unroll
  for (int c = 0; c < 4; ++c) a.w[c] = wbase + row * 256 + (((2 * (4 * wj + (c ^ cx)) + (p >> 1)) ^ ((row & 7) << 1)) * 16) + (p & 1) * 8;
}
template <bool WA, int NSLOT, int SLOTB> DI void gemm_steps(f32x4 (&acc)[8][4], Frags& F0, Frags& F1, Addr& a, int& slot, int n) {
  for (int t = 0; t < n; t += 2) {
    gstep<WA, NSLOT, SLOTB>(acc, F0, F1, a, slot);
    gstep<WA, NSLOT, SLOTB>(acc, F1, F0, a, slot);
  }
}

DI u32x4 pack8(const f32x4& a, const f32x4& b) { return (u32x4){cvtpk(a[0], a[1]), cvtpk(a[2], a[3]), cvtpk(b[0], b[1]), cvtpk(b[2], b[3])}; }
DI f32x2 gate2(f32x2 u, f32x2 z) {
  f32x2 e = {__builtin_amdgcn_exp2f(z[0]), __builtin_amdgcn_exp2f(z[1])};
  e += 1.f;
  return u * (f32x2){__builtin_amdgcn_rcpf(e[0]), __builtin_amdgcn_rcpf(e[1])};
}
DI unsigned gelu2(f32x2 u) {
  const f32x2 r = gate2(u, u * __builtin_elementwise_fma(u * u, (f32x2){-0.1029432397f, -0.1029432397f}, (f32x2){-2.302208198f, -2.302208198f}));
  return cvtpk(r[0], r[1]);
}
DI unsigned silu2(f32x2 y) { const f32x2 r = gate2(y, y * -1.4426950409f); return cvtpk(r[0], r[1]); }
DI u32x4 ldsr(unsigned a) { return *(LAS u32x4*)(size_t)a; }
DI void add8(f32x4& a, f32x4& b, const u32x4& w) {
  a += (f32x4){bflo(w[0]), bfhi(w[0]), bflo(w[1]), bfhi(w[1])};
  b += (f32x4){bflo(w[2]), bfhi(w[2]), bflo(w[3]), bfhi(w[3])};
}

struct Sc { f32x4 v[2][2]; };
struct Att { bf16x8 q[2][4]; f32x4 o[8][2]; f32x4 negm[2]; Sc sE, sO; u32x4 pO[2]; bf16x8 pre[4], ones; f32x4 l[2]; };
DI float xg_max(float m) {
  auto a = __builtin_amdgcn_permlane16_swap(__float_as_uint(m), __float_as_uint(m), false, false);
  m = fmaxf(__uint_as_float(a[0]), __uint_as_float(a[1]));
  auto b = __builtin_amdgcn_permlane32_swap(__float_as_uint(m), __float_as_uint(m), false, false);
  return fmaxf(__uint_as_float(b[0]), __uint_as_float(b[1]));
}
DI float row_max(const Sc& s, int qb) {
  const f32x4 a = s.v[0][qb], b = s.v[1][qb];
  return xg_max(fmaxf(fmaxf(fmaxf(a[0], a[1]), fmaxf(a[2], a[3])), fmaxf(fmaxf(b[0], b[1]), fmaxf(b[2], b[3]))));
}
DI bf16x8 ldfrag(unsigned a) { return *(LAS bf16x8*)(size_t)a; }
DI void sm1(Sc& s, int e, u32x4 (&w)[2], int part = 3) {
  if (e < 16 && (part & 1)) {
    float x = __builtin_amdgcn_exp2f(s.v[e >> 3][(e >> 2) & 1][e & 3]);
    asm volatile("" : "+v"(x));
    s.v[e >> 3][(e >> 2) & 1][e & 3] = x;
  }
  if (e >= 2 && e <= 16 && !(e & 1) && (part & 2)) {
    const int kt = (e - 1) >> 3, qb = ((e - 1) >> 2) & 1, r = (e - 1) & 3;
    unsigned c = cvtpk(s.v[kt][qb][r - 1], s.v[kt][qb][r]);
    asm volatile("" : "+v"(c));
    w[qb][2 * kt + (r >> 1)] = c;
  }
}
#define SETPRIO(x) __builtin_amdgcn_s_setprio(x)
#define MFMA16(a, b, c) __builtin_amdgcn_mfma_f32_16x16x32_bf16(a, b, c, 0, 0, 0)
#define PFRAG(w) __builtin_bit_cast(bf16x8, w)
DI void mfma_acc(f32x4& c, const bf16x8& a, const u32x4& b) { asm volatile("v_mfma_f32_16x16x32_bf16 %0, %1, %2, %0" : "+v"(c) : "v"(a), "v"(b)); }
template <int POS, int n, class F> DI void att_slot(Att& A, bf16x8 (&f)[36], u32x4 (&pE)[2], unsigned kv, unsigned kvn, F& gap) {
  constexpr int u = n >> 3, k = n & 7;
  constexpr bool isqk = (u & 1) ? (u == 1 || POS < 2) : (u == 0 && POS == 0);
  const f32x4 zero = {0.f, 0.f, 0.f, 0.f};
  if constexpr (k == 0) SETPRIO(3 - u);
#pragma unroll
  for (int qb = 0; qb < 2; ++qb) {
    if constexpr (isqk && (k & 3) != 0 && (k & 3) != 3) mfma_acc(u == 1 ? A.sO.v[k >> 2][qb] : A.sE.v[k >> 2][qb], f[n], __builtin_bit_cast(u32x4, A.q[qb][k & 3]));
    else if constexpr (isqk && u == 1) A.sO.v[k >> 2][qb] = MFMA16(f[n], A.q[qb][k & 3], (k & 3) ? A.sO.v[k >> 2][qb] : A.negm[qb]);
    else if constexpr (isqk) A.sE.v[k >> 2][qb] = MFMA16(f[n], A.q[qb][k & 3], (k & 3) ? A.sE.v[k >> 2][qb] : (u ? A.negm[qb] : zero));
    else if constexpr (POS == 0) A.o[k][qb] = MFMA16(f[n], PFRAG(pE[qb]), zero);
    else if constexpr (u == 2) mfma_acc(A.o[k][qb], f[n], pE[qb]);
    else mfma_acc(A.o[k][qb], f[n], A.pO[qb]);
    SB0();
    if (qb == 1) {
      if constexpr (n < 28) f[n + 4] = ldfrag(kv + (n + 4) * 1024);
      else if constexpr (POS < 2) f[n + 4] = ldfrag(kvn + (n - 28) * 1024);
      SB0();
    }
    if constexpr (POS == 0 && u == 1) {
      if (k == 0 && qb == 0) sm1(A.sE, 0, pE);
      sm1(A.sE, 2 * k + 1 + qb, pE);
    }
    if constexpr (POS > 0 && n <= 14) sm1(A.sE, n + 2, pE, 1 + qb);
    if constexpr (n >= 14 && n <= (POS < 2 ? 30 : 15)) sm1(A.sO, n - 14, A.pO, 1 + qb);
    if constexpr (POS < 2 && n >= 30) sm1(A.sE, n - 30, pE, 1 + qb);
    if constexpr (POS == 2 && u == 2) sm1(A.sO, 2 + 2 * k + qb, A.pO);
    SB0();
  }
  if constexpr (!isqk && (k == 2 || k == 4)) {
    constexpr int qb = k == 4;
    if constexpr (POS == 0) A.l[qb] = MFMA16(A.ones, PFRAG(pE[qb]), zero);
    else if constexpr (u == 2) mfma_acc(A.l[qb], A.ones, pE[qb]);
    else mfma_acc(A.l[qb], A.ones, A.pO[qb]);
  }
  gap(n);
  SB0();
  if constexpr (POS == 2 && n == 23) { asm volatile("s_nop 1"); SB0(); }
  if constexpr (POS == 2 && n == 31) { asm volatile("s_nop 9"); SB0(); }
  if constexpr (POS == 0 && n == 7) {
#pragma unroll
    for (int qb = 0; qb < 2; ++qb) {
      const float mx = row_max(A.sE, qb);
      A.sE.v[0][qb] -= mx; A.sE.v[1][qb] -= mx;
      A.negm[qb] = (f32x4){-mx, -mx, -mx, -mx};
    }
    SB0();
  }
  if constexpr (n < 31) att_slot<POS, n + 1>(A, f, pE, kv, kvn, gap);
}
template <int POS, class F> DI void att_tile(Att& A, unsigned kv, unsigned kvn, F&& gap) {
  bf16x8 f[36];
  u32x4 pE[2];
  asm volatile("" : "+v"(kv), "+v"(kvn));
#pragma unroll
  for (int i = 0; i < 4; ++i) f[i] = A.pre[i];
  SB0();
  att_slot<POS, 0>(A, f, pE, kv, kvn, gap);
  if (POS < 2) {
#pragma unroll
    for (int i = 0; i < 4; ++i) A.pre[i] = f[32 + i];
  }
}
DI void att_redo(Att& A, rsrc_t rkv, unsigned vo) {
  const f32x4 zero = {0.f, 0.f, 0.f, 0.f};
  float m[2] = {-3.0e38f, -3.0e38f};
  A.l[0] = A.l[1] = zero;
#pragma unroll
  for (int d = 0; d < 8; ++d) A.o[d][0] = A.o[d][1] = zero;
  for (int blk = 0; blk < 16; ++blk) {
    const int kb = (blk ? 2 * blk - 1 : 0) * 8192, vb = (blk < 15 ? 2 * blk + 2 : 31) * 8192;
    Sc s;
#pragma unroll
    for (int n = 0; n < 8; ++n) {
      const bf16x8 kf = PFRAG(__builtin_amdgcn_raw_buffer_load_b128(rkv, vo + n * 1024, kb, 16));
#pragma unroll
      for (int qb = 0; qb < 2; ++qb) s.v[n >> 2][qb] = MFMA16(kf, A.q[qb][n & 3], (n & 3) ? s.v[n >> 2][qb] : zero);
    }
#pragma unroll
    for (int qb = 0; qb < 2; ++qb) {
      const float mn = fmaxf(m[qb], row_max(s, qb)), fct = __builtin_amdgcn_exp2f(m[qb] - mn);
      m[qb] = mn;
      A.l[qb] *= fct;
      s.v[0][qb] -= mn; s.v[1][qb] -= mn;
#pragma unroll
      for (int d = 0; d < 8; ++d) A.o[d][qb] *= fct;
    }
    u32x4 p[2];
#pragma unroll
    for (int i = 0; i < 17; ++i) sm1(s, i, p);
#pragma unroll
    for (int qb = 0; qb < 2; ++qb) A.l[qb] = MFMA16(A.ones, PFRAG(p[qb]), A.l[qb]);
#pragma unroll
    for (int n = 0; n < 8; ++n) {
      const bf16x8 vf = PFRAG(__builtin_amdgcn_raw_buffer_load_b128(rkv, vo + n * 1024, vb, 16));
#pragma unroll
      for (int qb = 0; qb < 2; ++qb) A.o[n][qb] = MFMA16(vf, PFRAG(p[qb]), A.o[n][qb]);
    }
  }
}

__global__ __launch_bounds__(512, 2) __attribute__((amdgpu_num_vgpr(254))) void k_main(const u16* __restrict__ x, const u16* __restrict__ kc, const u16* __restrict__ vc, const u16* __restrict__ wq, const u16* __restrict__ wo,
                                                 const u16* __restrict__ w1, const u16* __restrict__ b1, const u16* __restrict__ w2,
                                                 const u16* __restrict__ b2, char* __restrict__ ws, u16* __restrict__ out) {
  extern __shared__ __attribute__((aligned(16))) char smem[];
  LAS char* lds = (LAS char*)smem;
  const unsigned lb = (unsigned)(size_t)lds;
  const int tid = threadIdx.x, wid = __builtin_amdgcn_readfirstlane(tid >> 6);
  const int g8 = blockIdx.x & 7, idx = blockIdx.x >> 3;
  const rsrc_t rcode = mkrsrc((const void*)(__builtin_amdgcn_s_getpc() & ~255ull), CODE_BYTES);
  auto code_pf = [&](unsigned lds_addr) {
    asm volatile("s_mov_b32 m0, %0\n\ts_nop 0\n\tbuffer_load_dword %3, %1, %2 offen lds" ::"s"(lds_addr), "s"(rcode), "s"((idx % ((CODE_BYTES + 8191) / 8192)) * 8192), "v"(lane_id() * 128) : "memory");
  };
  if (wid == 7) code_pf(lb + 161792);
  STAMP(1, 0);
  STAMP(0, 0);
  const bool hasU = idx < 28;
  const int uh = g8 & 1, dq = idx & 3, ft = 7 * (g8 >> 1) + (idx >> 2), ut = ft * 4 + dq;
  const int kvh = g8 >> 1, head = 8 * kvh + (idx & 7), rb = 4 * (g8 & 1) + (idx >> 3);
  const int dc = idx & 7, tile = rb * 8 + dc;
  const int lw = wid - 4, wi = (wid >> 1) & 1, wj = wid & 1;
  constexpr int QS = 24576;
  constexpr int PARK = 98304;
  const rsrc_t rf = mkrsrc(ws + WS_FLAG, F_END + 256);
  const rsrc_t rkv = mkrsrc(ws + WS_KV + (size_t)kvh * 262144, 262144);
  const rsrc_t rxb = mkrsrc(ws + WS_XB + (size_t)tile * 393216, 393216);
  struct Strm { rsrc_t r; unsigned v0, v1, step, p0, p1, p2; int rot; };
  auto strm_x = [&](Strm& S, const char* base, unsigned bytes, unsigned step, int xr) {
    const int ln = lane_id();
    S.r = mkrsrc(base, bytes); S.v0 = ln * 16 + (xr & 1) * 1024; S.v1 = S.v0 ^ 1024; S.step = step;
    S.p0 = (xr >> 1) * 2048; S.p1 = S.p0 ^ 2048; S.p2 = 4096; S.rot = 0;
  };
  auto strm_w = [&](Strm& S, const char* base, unsigned rows, unsigned ld, bool nat) {
    const int ln = lane_id(), q4 = ln >> 4;
    const unsigned c0 = ((ln & 15) ^ (q4 << 1)) * 16;
    S.r = mkrsrc(base, (rows - 1) * ld + 256); S.v0 = q4 * ld + c0; S.v1 = ((nat ? 8 : 4) + q4) * ld + (c0 ^ 128); S.step = 32 * ld;
    S.p0 = 0; S.p1 = (nat ? 16 : 8) * ld; S.p2 = (nat ? 4 : 16) * ld; S.rot = 0;
  };
  auto issue = [&](const Strm& S, int k, int src = -1) {
    const unsigned s = lb + (k & 3) * QS + lw * 8192;
    if (src < 0) src = (k + S.rot) & 31;
#pragma unroll
    for (int i = 0; i < 4; ++i) {
      const unsigned so = src * S.step + ((i & 1) ? S.p1 : S.p0) + (i >> 1) * S.p2;
      dma16s(S.r, s + i * 2048, S.v0, so); dma16s(S.r, s + i * 2048 + 1024, S.v1, so);
    }
  };
  constexpr int QW = 98304, HST = 131072, HRS = 2112, RST = HST + 8 * HRS, PKQ = 148480, STT = 151552, CSX = 153600;
  const int lane0 = tid & 63;
  const rsrc_t rq = lw < 2 ? mkrsrc(x + (size_t)(rb * 256 + lw * 128) * DM, 128 * DM * 2) : mkrsrc((const char*)wq + head * 256, (DM - 1) * HD * 2 + 256);
  const unsigned qv0 = lw < 2 ? (lane0 >> 3) * 2048 + (((lane0 & 7) ^ (lane0 >> 4)) * 16) : (lane0 >> 4) * (HD * 2) + (((lane0 & 15) ^ ((lane0 >> 4) << 1)) * 16);
  const unsigned qv1 = lw < 2 ? (qv0 ^ 64) + 8 * 2048 : (qv0 ^ 128) + 8 * (HD * 2);
  const bool qx = lw < 2;
  const unsigned qp1 = qx ? 32768 : 16 * HD * 2, qp2 = qx ? 65536 : 4 * HD * 2;
  const unsigned qlb = lb + (qx ? lw * 16384 : QW), qlw = qx ? 98304 : 32768, qom = qx ? 0x20780 : 0x7c0000;
  unsigned ql = qx ? 65536 : 0, qdl = 8192, qo = rb * (qx ? 256 : 4 * 32 * HD * 2), qdo = qx ? 131072 : 32 * HD * 2;
  auto issq = [&]() {
    const unsigned s = qlb + ql;
    dma16(rq, s, qv0, qo); dma16(rq, s + 1024, qv1, qo);
    dma16(rq, s + 2048, qv0, qo + qp1); dma16(rq, s + 3072, qv1, qo + qp1);
    dma16(rq, s + 4096, qv0, qo + qp2); dma16(rq, s + 5120, qv1, qo + qp2);
    dma16(rq, s + 6144, qv0, qo + qp1 + qp2); dma16(rq, s + 7168, qv1, qo + qp1 + qp2);
    ql += qdl; ql = ql >= qlw ? ql - qlw : ql; qdl ^= qx ? (8192 ^ 24576) : 0;
    qo = (qo + qdo) & qom; qdo ^= qx ? 128 : 0;
  };
  auto h_out = [&](int it) {
    const rsrc_t rh = mkrsrc(ws + WS_HT + (size_t)(blockIdx.x >> 2) * 16384 + (blockIdx.x & 3) * 256, 3 * 1048576 + 16384);
    const unsigned a0 = HST + ((lane0 >> 2) & 3) * HRS + (it * 4 + (lane0 >> 4)) * 32 + (lane0 & 3) * 8;
    s16x4 lo = __builtin_amdgcn_ds_read_tr16_b64_v4i16((LAS s16x4*)(lds + a0));
    s16x4 hi = __builtin_amdgcn_ds_read_tr16_b64_v4i16((LAS s16x4*)(lds + a0 + 4 * HRS));
    st16s<0>(__builtin_bit_cast(u32x4, __builtin_shufflevector(lo, hi, 0, 1, 2, 3, 4, 5, 6, 7)), rh, (lane0 >> 4) * 1024 + (lane0 & 15) * 16,
             (it >> 2) * 1048576 + (it & 3) * 4096);
  };
  auto st_out = [&]() { st16s<0>(*(LAS u32x4*)(lds + RST + (lane0 & 3) * 16), mkrsrc(ws + WS_ST + blockIdx.x * 64, 64), lane0 * 16, 0); };
  Tag tag;
  {
    const int b = blockIdx.x, lane = lane0;
    u32x4 A, B;
    u32x2 ep;
    asm volatile("global_load_dwordx4 %0, %3, %4\n\tglobal_load_dwordx4 %1, %3, %4 offset:1024\n\tglobal_load_dwordx2 %2, %5, %6 sc1"
                 : "=&v"(A), "=&v"(B), "=&v"(ep) : "v"(lane * 16), "s"(x + (size_t)(8 * b + wid) * DM), "v"(F_EP), "s"(ws + WS_FLAG) : "memory");
    if (wid >= 4 && wid < 7) { issq(); issq(); if (lw == 2) { issq(); issq(); wait_vm<32>(); } else wait_vm<16>(); } else wait_vm<0>();
    asm volatile("" : "+v"(A), "+v"(B), "+v"(ep));
    unsigned v[8] = {A[0], A[1], A[2], A[3], B[0], B[1], B[2], B[3]};
    const unsigned long long e64 = ((unsigned long long)(unsigned)__builtin_amdgcn_readfirstlane(ep[1]) << 32) | (unsigned)__builtin_amdgcn_readfirstlane(ep[0]);
    const unsigned long long tg = e64 * 0x9E3779B97F4A7C15ull + 0x632BE59BD9B4E019ull;
    tag.lo = (unsigned)tg; tag.hi = (unsigned)(tg >> 32);
    *(LAS f32x2*)(lds + RST + wid * 8) = ln16(v);
    ln16(v);
    *(LAS u32x4*)(lds + HST + wid * HRS + lane * 16) = (u32x4){v[0], v[1], v[2], v[3]};
    *(LAS u32x4*)(lds + HST + wid * HRS + 1024 + lane * 16) = (u32x4){v[4], v[5], v[6], v[7]};
    wait_lgkm0();
  }
  STAMP(0, 1);
  bf16x8 qh[8];
  f32x4 uk[4][4];
  auto kv8 = [&](int t, int slot) {
    const unsigned v = lane_id() * 16;
    dma4k(rkv, lb + slot * 32768 + lw * 8192, v, t * 32768 + lw * 8192);
    dma4k(rkv, lb + slot * 32768 + lw * 8192 + 4096, v, t * 32768 + lw * 8192 + 4096);
  };
  if (wid < 4) {
    f32x4 acc[8][4], cs[2];
    Frags F0, F1;
    Addr a;
    int xs = 2, wsl = 1;
    {
      const int ln = lane_id(), r = ln & 15;
      addr_init(a, 0, lb + QW, wj, 2 * wi, ln);
      a.x = lb + 65536 + wi * 16384 + r * 128 + (((ln >> 4) ^ (r >> 1)) * 16);
    }
    bar();
    load_frags<2048>(F0, a);
    a.x ^= 64;
#pragma unroll
    for (int c = 0; c < 4; ++c) a.w[c] += 8192;
    wait_lgkm0();
    bar();
    STAMP(1, 1);
    {
      const unsigned o1 = 0x3f803f80;
      u32x4 ones = {o1, o1, o1, o1};
      qstep<1, true, true>(acc, cs, ones, o1, F1, F0, a, xs, wsl);
      for (int t = 0; t < 2; ++t) {
        const unsigned on = t == 0 ? o1 : 0;
        qstep<0, true>(acc, cs, ones, on, F0, F1, a, xs, wsl);
        qstep<1, true>(acc, cs, ones, on, F1, F0, a, xs, wsl);
      }
      const int ln = lane_id();
      const unsigned csa = lb + CSX + ((ln & 15) ? 7168 : (64 * wj + 32 * wi) * 4) + (ln & 48);
      *(LAS f32x4*)(size_t)csa = cs[0];
      *(LAS f32x4*)(size_t)(csa + 64) = cs[1];
      for (int t = 0; t < 13; ++t) { qstep<0, false>(acc, cs, ones, 0, F0, F1, a, xs, wsl); qstep<1, false>(acc, cs, ones, 0, F1, F0, a, xs, wsl); }
      addr_init(a, lb + wi * 8192, lb + 16384, wj, 0, lane_id());
      qstep<2, false>(acc, cs, ones, 0, F0, F1, a, xs, wsl);
    }
    STAMP(1, 2);
    f32x4 c0[4];
    {
      const int ln = lane_id(), g = ln >> 4;
      u32x2 bv[4];
#pragma unroll
      for (int j = 0; j < 4; ++j) bv[j] = *(LAS u32x2*)(lds + CSX + 5120 + (64 * wj + 16 * j + 4 * g) * 2);
      f32x4 cq[4];
#pragma unroll
      for (int j = 0; j < 4; ++j) cq[j] = *(LAS f32x4*)(lds + CSX + 512 + (64 * wj + 16 * (j ^ (2 * wi)) + 4 * g) * 4);
      f32x2 st[8];
#pragma unroll
      for (int i = 0; i < 8; ++i) st[i] = *(LAS f32x2*)(lds + STT + (128 * wi + 16 * i + (ln & 15)) * 8);
      wait_lgkm0();
      bar();
      const unsigned qa = lb + 98304 + wi * 32768 + (2 * wj + wi) * 1024 + ln * 16, qb = qa ^ 1024;
#pragma unroll
      for (int i = 0; i < 8; ++i) {
        const float sc = st[i][1] * 0.12751743082f, nm = -st[i][0];
#pragma unroll
        for (int mm = 0; mm < 2; ++mm)
          *(LAS u32x4*)(size_t)((mm ? qb : qa) + i * 4096) =
              pack8((acc[i][2 * mm] + cq[2 * mm] * nm) * sc, (acc[i][2 * mm + 1] + cq[2 * mm + 1] * nm) * sc);
      }
#pragma unroll
      for (int j = 0; j < 4; ++j) c0[j] = uh ? (f32x4){0.f, 0.f, 0.f, 0.f} : (f32x4){bflo(bv[j][0]), bfhi(bv[j][0]), bflo(bv[j][1]), bfhi(bv[j][1])};
    }
    STAMP(1, 3);
    if (hasU) {
      int slot = 1;
      gstep<true, 4, QS, true>(acc, F1, F0, a, slot, c0);
      gemm_steps<true, 4, QS>(acc, F0, F1, a, slot, 30);
      gstep<true, 4, QS>(acc, F0, F1, a, slot);
      STAMP(1, 4);
      const rsrc_t rux = mkrsrc(ws + WS_XB + (size_t)((tile ^ 32) * 4 + kvh) * 98304 + 49152 + wid * 8192, 8192);
      const int ln = lane_id();
#pragma unroll
      for (int ii = 0; ii < 4; ++ii) {
        st16s<0>(pack8(acc[2 * ii + 1][0], acc[2 * ii + 1][1]), rux, ln * 16, ii * 2048);
        st16s<1024>(pack8(acc[2 * ii + 1][2], acc[2 * ii + 1][3]), rux, ln * 16, ii * 2048);
#pragma unroll
        for (int j = 0; j < 4; ++j) uk[ii][j] = acc[2 * ii][j];
      }
    }
    STAMP(1, 5);
  } else {
    const int lane = lane0;
    Strm U;
    if (lw < 2) strm_x(U, ws + WS_HT + (size_t)dq * 1048576 + uh * 524288 + lw * 8192, 524288 - lw * 8192, 16384, uh);
    else strm_w(U, (const char*)w1 + (size_t)uh * 1024 * (FF * 2) + ft * 256, 1024, FF * 2, true);
    if (!hasU) U.r = mkrsrc(ws, 0);
    U.rot = 8 * dq;
    const unsigned pk = lb + PKQ + lw * 512;
    if (lw < 3) {
      if (lw < 2) wait_vm<0>(); else wait_vm<24>();
      bar();
      if (lw < 2) { issq(); issq(); } else wait_vm<16>();
      bar();
      if (lw < 2) issq();
      for (int t = 1; t <= 21; ++t) { issq(); wait_vm<16>(); bar(); }
      peek(rf, pk, F_L + lw * 512); peek(rf, pk + 256, F_L + lw * 512 + 256);
      for (int t = 22; t <= 26; ++t) { issq(); wait_vm<16>(); bar(); }
      issq(); wait_vm<16>();
    } else {
      peek(mkrsrc(b1, FF * 2), lb + CSX + 5120, ft * 256);
      bar();
      for (int t = 0; t <= 7; ++t) { h_out(2 * t); h_out(2 * t + 1); bar(); }
      st_out();
      st16s<0>(*(LAS u32x4*)(lds + CSX + (lane & 31) * 16), mkrsrc(ws + WS_CS + (head * 8 + rb) * 512, 512), lane * 16, 0);
      bar(); bar(); bar();
      wait_vm<0>();
      signal<1>(ws, F_L + blockIdx.x * 8, tag);
      for (int t = 11; t <= 21; ++t) bar();
      peek(rf, pk, F_L + lw * 512); peek(rf, pk + 256, F_L + lw * 512 + 256);
      for (int t = 22; t <= 26; ++t) bar();
      wait_vm<0>();
    }
    peek_check(ws, pk, F_L + lw * 512, 32, tag); peek_check(ws, pk + 256, F_L + lw * 512 + 256, 32, tag);
    bar();
    if (blockIdx.x == 0 && lw == 3) signal<1>(ws, F_EP, tag);
    if (lw == 2) issq();
    if (lw == 3) {
      const rsrc_t rs = mkrsrc(ws + WS_ST + rb * 2048, 2048);
      dma16s(rs, lb + STT, lane * 16, 0); dma16s(rs, lb + STT + 1024, lane * 16, 1024);
      dma4k(mkrsrc(ws + WS_CS + head * 4096, 4096), lb + CSX + 1024, lane * 16, 0);
    }
    const bool us = lw < 3;
    if (us) issue(U, 0);
    wait_vm<16>(); bar();
    if (us) wait_vm<8>(); else wait_vm<0>();
    bar();
    if (us) issue(U, 1);
    else {
      const LAS f32x2* cp = (const LAS f32x2*)(lds + CSX + 1024) + lane;
      *(LAS f32x2*)(lds + CSX + 512 + lane * 8) = ((cp[0] + cp[64]) + (cp[128] + cp[192])) + ((cp[256] + cp[320]) + (cp[384] + cp[448]));
      wait_lgkm0();
    }
    bar();
    wait_vm<8>(); bar();
    if (us) issue(U, 2);
    wait_vm<8>();
    bar();
    bar();
    if (hasU) {
      if (us) { issue(U, 3); for (int t = 1; t <= 28; ++t) { issue(U, t + 3); wait_vm<16>(); bar(); } }
      else {
        for (int t = 1; t <= 26; ++t) bar();
        spin_until(ws, F_X + kvh * 128, 8, tag);
        bar(); bar();
      }
      wait_vm<8>(); bar();
      kv8(0, 0); wait_vm<8>(); bar();
      kv8(1, 1); bar();
      kv8(2, 2); wait_vm<8>(); bar();
    }
  }
  if (!hasU) {
    const int sp = g8 * 4 + (idx - 28), lane = lane_id(), g = lane >> 4;
    unsigned e[4][8];
#pragma unroll
    for (int i = 0; i < 4; ++i) {
      const int kh = sp >> 3, unit = (sp & 7) * 4 + i, frag = wid;
      const bool isK = unit == 0 || ((unit & 1) && unit < 31);
      const u16* sk = kc + ((size_t)kh * SEQ + 1536 + 32 * ((unit + 1) >> 1) + 16 * (frag >> 2) + (lane & 15)) * 128 + 32 * (frag & 3) + 4 * g;
      const u16* sv = vc + ((size_t)kh * SEQ + 1536 + 32 * ((unit - 1) >> 1) + 4 * g) * 128 + 16 * frag + (lane & 15);
      const u16* src = isK ? sk : sv;
      const int s1 = isK ? 1 : 128, s4 = isK ? 16 : 2048;
#pragma unroll
      for (int j2 = 0; j2 < 8; ++j2) e[i][j2] = src[(j2 >> 2) * s4 + (j2 & 3) * s1];
    }
#pragma unroll
    for (int i = 0; i < 4; ++i)
      st16s<0>((u32x4){e[i][0] | (e[i][1] << 16), e[i][2] | (e[i][3] << 16), e[i][4] | (e[i][5] << 16), e[i][6] | (e[i][7] << 16)},
               mkrsrc(ws + WS_KV, 1048576), (wid * 64 + lane) * 16, (sp * 4 + i) * 8192);
    wait_vm<0>();
    wait_lgkm0();
    bar();
    if (wid == 0) signal<1>(ws, F_X + (sp >> 3) * 128 + (sp & 7) * 8, tag);
    unsigned pw[NSP];
#pragma unroll
    for (int i = 0; i < NSP; ++i) pw[i] = *(const unsigned*)((const char*)w2 + (size_t)i * PFB + (size_t)(sp * 512 + wid * 64 + lane) * 128);
#pragma unroll
    for (int i = 0; i < NSP; ++i) asm volatile("" ::"v"(pw[i]));
    if (wid >= 4) { spin_until(ws, F_X + kvh * 128, 8, tag); kv8(0, 0); kv8(1, 1); kv8(2, 2); wait_vm<8>(); }
    bar();
  }
  {
    const int ln = lane_id();
#pragma unroll
    for (int ks = 0; ks < 8; ++ks) qh[ks] = *(LAS bf16x8*)(lds + 98304 + wid * 8192 + ks * 1024 + ln * 16);
  }
  STAMP(1, 6);
  Strm Y;
  auto y_iss = [&](int k) { const int r = k + YROT * (dc & 3); issue(Y, k, r >= 28 ? r - 28 : r); };
  Frags FY;
  Addr aY;
  {
    const int lane = lane_id();
    Att A;
#pragma unroll
    for (int ks = 0; ks < 8; ++ks) A.q[ks >> 2][ks & 3] = qh[ks];
    A.ones = (bf16x8){0x3f80, 0x3f80, 0x3f80, 0x3f80, 0x3f80, 0x3f80, 0x3f80, 0x3f80};
    asm volatile("" : "+v"(A.ones));
    const unsigned kvl = lb + lane * 16;
#pragma unroll
    for (int i = 0; i < 4; ++i) A.pre[i] = ldfrag(kvl + i * 1024);
    const bool strm = wid >= 4 && wid < 7, hw = wid >= 4;
    const rsrc_t rp2 = mkrsrc(w2, SEQ * FF * 2), rpo = mkrsrc(wo, HD * DM * 2);
    const unsigned pv = (blockIdx.x * 64 + lane) * 128;
#define PF(W, R, C) do { if (&R != &rp2 || (C) >= NSP) pf_line<W>(wid, R, pv, (C) * 2097152u); } while (0)
    const int fp = F_P + (ut * 2 + uh) * 64 + (wid & 3) * 8;
    const bool cu = !hw && hasU;
    STAMPA(0);
    att_tile<0>(A, kvl + 0 * 32768, kvl + 1 * 32768, [&](int n) __attribute__((always_inline)) { if (n == 20 && cu) { wait_vm<0>(); signal<1>(ws, fp ^ 64, tag); }
      if (n == 23) PF(0, rp2, 0); });
    if (hw) wait_vm<0>();
    bar();
    STAMPA(1);
    att_tile<1>(A, kvl + 1 * 32768, kvl + 2 * 32768, [&](int n) __attribute__((always_inline)) {
      if (n == 0 && hw) kv8(3, 3); if (n == 12 && hw) kv8(4, 0); if (n == 28 && hw) peek(rf, lb + 131072 + lw * 256, fp); if (n == 3) PF(1, rp2, 1); if (n == 10) PF(2, rp2, 2); });
    if (hw) wait_vm<9>();
    bar();
    STAMPA(2);
    att_tile<1>(A, kvl + 2 * 32768, kvl + 3 * 32768, [&](int n) __attribute__((always_inline)) {
      if (n == 8 && hw) {
        wait_vm<0>();
        if (hasU) peek_check(ws, lb + 131072 + lw * 256, fp, 1, tag);
        const rsrc_t rux = mkrsrc(ws + WS_XB + (size_t)(tile * 4 + kvh) * 98304 + 49152, hasU ? 32768 : 0);
        dma4k(rux, lb + 32768 + lw * 8192, lane * 16, lw * 8192); dma4k(rux, lb + 32768 + lw * 8192 + 4096, lane * 16, lw * 8192 + 4096);
      }
      if (n == 27) PF(3, rp2, 3); });
    if (hw) wait_vm<0>();
    bar();
    if (hw) { kv8(5, 4); kv8(7, 2); }
    STAMPA(3);
    if (cu) {
      u32x4 p[8];
      p[0] = ldsr(kvl + 32768 + wid * 8192);
#pragma unroll
      for (int k = 0; k < 8; ++k) {
        if (k < 7) p[k + 1] = ldsr(kvl + 32768 + wid * 8192 + (k + 1) * 1024);
        f32x4 a = uk[k >> 1][2 * (k & 1)], b = uk[k >> 1][2 * (k & 1) + 1];
        add8(a, b, p[k]);
        p[k] = (u32x4){gelu2(a.xy), gelu2(a.zw), gelu2(b.xy), gelu2(b.zw)};
        asm volatile("" : "+v"(p[k]));
        if (k == 1) PF(0, rp2, 4);
        if (k == 4) PF(1, rp2, 5);
        SB0();
      }
      bar();
      const rsrc_t ru = mkrsrc(ws + WS_U + (size_t)((2 * dq + wi) * 112 + 4 * ft + 2 * wj) * 8192 + uh * 1024, 16384);
#pragma unroll
      for (int k = 0; k < 8; ++k) st16s<0>(p[k], ru, lane * 16, (k & 1) * 8192 + (k >> 1) * 2048);
    } else { PF(0, rp2, 4); PF(1, rp2, 5); bar(); }
    STAMPA(4);
    att_tile<1>(A, kvl + 3 * 32768, kvl + 0 * 32768, [&](int n) __attribute__((always_inline)) {
      if (n == 20 && cu) { wait_vm<0>(); signal<1>(ws, F_U + ((2 * dq + wi) * 4 + kvh) * 256 + (((idx >> 2) * 2 + uh) * 2 + wj) * 8, tag); }
      if (n == 23) PF(2, rp2, 6); });
    if (hw) wait_vm<0>();
    bar();
    STAMPA(5);
    att_tile<1>(A, kvl + 0 * 32768, kvl + 4 * 32768, [&](int n) __attribute__((always_inline)) { if (n == 0 && hw) kv8(6, 3); if (n == 5) PF(3, rpo, 0); if (n == 18) PF(0, rpo, 1); });
    if (hw) wait_vm<0>();
    bar();
    STAMPA(6);
    att_tile<1>(A, kvl + 4 * 32768, kvl + 3 * 32768, [&](int n) __attribute__((always_inline)) { if (n == 1) PF(1, rpo, 2); if (n == 13) PF(2, rpo, 3); });
    if (lw == 0) strm_x(Y, ws + WS_U + (size_t)(dc * 112 + 28 * kvh) * 8192, 28 * 8192, 8192, 0);
    else strm_w(Y, (const char*)w2 + (size_t)kvh * 896 * (SEQ * 2) + rb * 512 + ((lw - 1) & 1) * 256, 896, SEQ * 2, false);
    bar();
    STAMPA(7);
    att_tile<1>(A, kvl + 3 * 32768, kvl + 2 * 32768, [&](int n) __attribute__((always_inline)) {
      if (n == 4 && wid == 4) peek(rf, lb + 49152, F_U + (dc * 4 + kvh) * 256);
      if (n == 18 && wid == 4) { wait_vm<0>(); peek_check(ws, lb + 49152, F_U + (dc * 4 + kvh) * 256, 28, tag); }
      if (n == 19 && strm) y_iss(0); if (n == 27 && strm) y_iss(1); });
    bar();
    STAMPA(8);
    att_tile<2>(A, kvl + 2 * 32768, 0, [&](int n) __attribute__((always_inline)) {
      if (n == 9) { if (hw) wait_vm<8>(); bar(); }
      if (n == 10 && strm) y_iss(2); });
    STAMP(1, 7);
    STAMPA(9);
    if (__builtin_expect(__any(!(A.l[0][0] + A.l[1][0] <= 1.0e9f)), 0)) att_redo(A, rkv, lane * 16);
    addr_init(aY, lb, lb + 8192 + wi * 8192, wj, kvh, lane);
    if (!hw) load_frags(FY, aY);
#pragma unroll
    for (int qb = 0; qb < 2; ++qb) {
      const float inv = __builtin_amdgcn_rcpf(A.l[qb][0]);
#pragma unroll
      for (int d = 0; d < 4; ++d) *(LAS u32x4*)(size_t)(kvl + PARK + wid * 2048 + qb * 1024 + d * 16384) = pack8(A.o[2 * d][qb] * inv, A.o[2 * d + 1][qb] * inv);
    }
    wait_lgkm0();
  }
  STAMP(1, 8);
  if (wid < 4) {
    f32x4 acc[8][4];
    Frags F0 = FY, F1;
    Addr a = aY;
    int slot = 1;
    addr_add(a, QS);
    bar();
    STAMP(1, 9);
    gstep<false, 4, QS, true>(acc, F1, F0, a, slot);
    gemm_steps<false, 4, QS>(acc, F0, F1, a, slot, 26);
    addr_init(a, lb + wi * 8192, lb + 16384, wj, 0, lane_id());
    gstep<false, 4, QS>(acc, F0, F1, a, slot);
    STAMP(1, 10);
    {
      const int lm = lane_id();
#pragma unroll
      for (int m = 0; m < 4; ++m) *(LAS u32x4*)(size_t)(lb + PARK + lm * 16 + wid * 4096 + m * 1024) = pack8(acc[2 * m][0], acc[2 * m + 1][0]);
#pragma unroll
      for (int j = 1; j < 4; ++j)
#pragma unroll
        for (int m = 0; m < 4; ++m) st16s<0>(pack8(acc[2 * m][j], acc[2 * m + 1][j]), rxb, lm * 16, (kvh ^ j) * 98304 + (j - 1) * 16384 + wid * 4096 + m * 1024);
    }
    STAMP(1, 11);
    gstep<true, 4, QS, true>(acc, F1, F0, a, slot);
    gemm_steps<true, 4, QS>(acc, F0, F1, a, slot, 8);
    wait_vm<0>();
    gemm_steps<true, 4, QS>(acc, F0, F1, a, slot, 22);
    gstep<true, 4, QS>(acc, F0, F1, a, slot);
    STAMP(1, 12);
    const int lt = lane_id(), g = lt >> 4;
    const unsigned wo_ = wi * 8192 + wj * 2048;
#pragma unroll
    for (int dl = 1; dl < 4; ++dl)
#pragma unroll
      for (int h = 0; h < 2; ++h)
#pragma unroll
        for (int mm = 0; mm < 2; ++mm)
          st16s<0>(pack8(acc[4 * h + dl][2 * mm], acc[4 * h + dl][2 * mm + 1]), rxb, lt * 16, (kvh ^ dl) * 98304 + 49152 + (dl - 1) * 16384 + wo_ + h * 4096 + mm * 1024);
    const int fs = F_S + tile * 512 + wid * 32;
    wait_vm<8>(); signal<1>(ws, fs + (kvh ^ 1) * 128, tag);
    wait_vm<5>(); signal<1>(ws, fs + (kvh ^ 2) * 128 + 8, tag);
    wait_vm<2>(); signal<1>(ws, fs + (kvh ^ 3) * 128 + 16, tag);
    STAMP(1, 13);
    const int srow = rb * 256 + (8 * wi + kvh) * 16;
    const rsrc_t rx = mkrsrc(x + (size_t)srow * DM, 80 * DM * 2), rout = mkrsrc(out + (size_t)srow * DM, 80 * DM * 2);
    const unsigned vo = ((lt & 15) * DM + dc * 128 + wj * 64 + 16 * (g & 1) + 8 * (g >> 1)) * 2;
    u32x4 ps[3][4], xv[4], gv[4];
#pragma unroll
    for (int p = 0; p < 4; ++p) xv[p] = __builtin_amdgcn_raw_buffer_load_b128(rx, vo + (p >> 1) * (64 * DM * 2) + (p & 1) * 64, 0, 0);
    bar();
#pragma unroll
    for (int p = 0; p < 4; ++p) gv[p] = ldsr(lb + PARK + lt * 16 + wo_ + (p >> 1) * 4096 + (p & 1) * 1024);
    {
      GAS unsigned long long* fp = (GAS unsigned long long*)(ws + WS_FLAG + fs + kvh * 128) + (lt & 3);
      const unsigned long long want = ((unsigned long long)tag.hi << 32) | tag.lo;
      unsigned got = 0;
      for (unsigned sp = 0; got != 7; ++sp) {
        unsigned now = (unsigned)__ballot(__hip_atomic_load(fp, __ATOMIC_RELAXED, __HIP_MEMORY_SCOPE_AGENT) == want) & 7;
        if (sp >= (1u << 20)) now = 7;
        asm volatile("" ::: "memory");
#pragma unroll
        for (int k = 0; k < 3; ++k)
          if ((now & ~got) >> k & 1) {
#pragma unroll
            for (int p = 0; p < 4; ++p)
              ps[k][p] = __builtin_amdgcn_raw_buffer_load_b128(rxb, lt * 16 + (p & 1) * 1024, kvh * 98304 + 49152 + k * 16384 + wo_ + (p >> 1) * 4096, 16);
          }
        got |= now;
        if (got != 7) __builtin_amdgcn_s_sleep(1);
      }
    }
    STAMP(1, 14);
#pragma unroll
    for (int p = 0; p < 4; ++p) {
      f32x4 s0 = acc[4 * (p >> 1)][2 * (p & 1)], s1 = acc[4 * (p >> 1)][2 * (p & 1) + 1];
#pragma unroll
      for (int k = 0; k < 3; ++k) add8(s0, s1, ps[k][p]);
      s0 *= (f32x4){bflo(gv[p][0]), bfhi(gv[p][0]), bflo(gv[p][1]), bfhi(gv[p][1])};
      s1 *= (f32x4){bflo(gv[p][2]), bfhi(gv[p][2]), bflo(gv[p][3]), bfhi(gv[p][3])};
      const u32x4 tp = pack8(s0, s1);
      const auto w0 = __builtin_amdgcn_permlane16_swap(tp[0], tp[2], false, false), w1 = __builtin_amdgcn_permlane16_swap(tp[1], tp[3], false, false);
      const unsigned t[4] = {w0[0], w1[0], w0[1], w1[1]};
      u32x4 o;
#pragma unroll
      for (int e = 0; e < 4; ++e) o[e] = cvtpk(bflo(xv[p][e]) + bflo(t[e]), bfhi(xv[p][e]) + bfhi(t[e]));
      __builtin_amdgcn_raw_buffer_store_b128(o, rout, vo + (p >> 1) * (64 * DM * 2) + (p & 1) * 64, 0, 0);
    }
    STAMP(1, 15);
  } else {
    const int lane = lane_id();
    const unsigned pl = lb + PARK + lane * 16, pk = lb + 159744 + lw * 256;
    const int fo = F_O + (rb * 4 + kvh) * 128;
    if (lw < 3) {
      Strm S;
      if (lw < 2) strm_x(S, ws + WS_O + (size_t)(rb * 128 + 32 * kvh) * 16384 + lw * 8192, 524288 - lw * 8192, 16384, kvh);
      else strm_w(S, (const char*)wo + (size_t)kvh * 1024 * (DM * 2) + dc * 256, 1024, DM * 2, false);
      wait_vm<8>();
      bar();
      y_iss(3);
      for (int t = 1; t <= 22; ++t) { y_iss(t + 3); wait_vm<16>(); bar(); }
      peek(rf, lb + PARK + lw * 256, fo);
      y_iss(26); wait_vm<16>(); bar();
      y_iss(27); wait_vm<16>(); bar();
      peek_check(ws, lb + PARK + lw * 256, fo, 8, tag);
      auto iss = [&](int k) { const int r = (k & 16) | ((k + SROT * (rb & 3)) & 15); issue(S, k, 4 * (r & 7) + (r >> 3)); wait_vm<16>(); bar(); };
      for (int t = 25; t <= 36; ++t) iss(t - 25);
      peek(rf, lb + PARK + 16384 + lw * 256, fo + 64);
      for (int t = 37; t <= 40; ++t) iss(t - 25);
      peek_check(ws, lb + PARK + 16384 + lw * 256, fo + 64, 8, tag);
      for (int t = 41; t <= 56; ++t) iss(t - 25);
      wait_vm<8>(); bar();
      wait_vm<0>(); bar();
    } else {
      bar();
      const rsrc_t ro = mkrsrc(ws + WS_O + (size_t)(rb * 128 + 4 * head) * 16384, 65536);
      for (int t = 1; t <= 25; t += 2) {
        const unsigned o = (t >> 1) * 5120;
        const u32x4 v0 = ldsr(pl + o), v1 = ldsr(pl + o + 1024), v2 = ldsr(pl + o + 2048), v3 = ldsr(pl + o + 3072), v4 = ldsr(pl + (t < 25 ? o + 4096 : 0));
        st16s<0>(v0, ro, lane * 16, o); st16s<1024>(v1, ro, lane * 16, o); st16s<2048>(v2, ro, lane * 16, o);
        if (t == 17) { wait_vm<11>(); signal<1>(ws, fo + dc * 8, tag); }
        bar();
        st16s<3072>(v3, ro, lane * 16, o); st16s<0>(v4, ro, lane * 16, o + 4096);
        bar();
      }
      bar(); bar(); bar();
      wait_vm<0>();
      signal<1>(ws, fo + 64 + dc * 8, tag);
      for (int t = 30; t <= 38; ++t) bar();
      signal<14>(ws, F_Y + (tile * 4 + (kvh ^ (lane & 3))) * 128 + ((lane & 3) - 1) * 8, tag);
      for (int t = 39; t <= 56; ++t) bar();
      peek(rf, pk, F_Y + (tile * 4 + kvh) * 128);
      bar();
      wait_vm<0>();
      peek_check(ws, pk, F_Y + (tile * 4 + kvh) * 128, 3, tag);
      bar();
    }
    u32x4 py[3][4];
#pragma unroll
    for (int k = 0; k < 3; ++k)
#pragma unroll
      for (int m = 0; m < 4; ++m) py[k][m] = __builtin_amdgcn_raw_buffer_load_b128(rxb, lane * 16 + m * 1024, kvh * 98304 + lw * 4096 + k * 16384, 16);
    const float bb = bflo(b2[rb * 256 + (4 * lw + kvh) * 16 + (lane & 15)]);
    bar();
    bar();
#pragma unroll
    for (int m = 0; m < 4; ++m) {
      f32x4 y0 = {bb, bb, bb, bb}, y1 = y0;
      add8(y0, y1, ldsr(pl + lw * 4096 + m * 1024));
#pragma unroll
      for (int k = 0; k < 3; ++k) add8(y0, y1, py[k][m]);
      *(LAS u32x4*)(size_t)(pl + lw * 4096 + m * 1024) = (u32x4){silu2(y0.xy), silu2(y0.zw), silu2(y1.xy), silu2(y1.zw)};
    }
    wait_lgkm0();
    bar();
  }
}

extern "C" void kernel_launch(void* const* d_in, const int* in_sizes, int n_in, void* d_out, int out_size, void* d_ws, size_t ws_size,
                              hipStream_t stream) {
  const u16* x = (const u16*)d_in[0];
  const u16* kc = (const u16*)d_in[1];
  const u16* vc = (const u16*)d_in[2];
  const u16* wq = (const u16*)d_in[3];
  const u16* wo = (const u16*)d_in[4];
  const u16* w1 = (const u16*)d_in[6];
  const u16* b1 = (const u16*)d_in[7];
  const u16* w2 = (const u16*)d_in[8];
  const u16* b2 = (const u16*)d_in[9];
  char* ws = (char*)d_ws;
  (void)hipFuncSetAttribute((const void*)k_main, hipFuncAttributeMaxDynamicSharedMemorySize, 163840);
  hipLaunchKernelGGL(k_main, dim3(256), dim3(512), 163840, stream, x, kc, vc, wq, wo, w1, b1, w2, b2, ws, (u16*)d_out);
}
```

```cpp
#include <hip/hip_runtime.h>
#include <hip/hip_bf16.h>

typedef unsigned short u16;
typedef short bf16x8 __attribute__((ext_vector_type(8)));
typedef short s16x4 __attribute__((ext_vector_type(4)));
typedef float f32x4 __attribute__((ext_vector_type(4)));
typedef float f32x2 __attribute__((ext_vector_type(2)));
typedef unsigned u32x4 __attribute__((ext_vector_type(4)));
typedef unsigned u32x2 __attribute__((ext_vector_type(2)));
typedef __bf16 bf16x2 __attribute__((ext_vector_type(2)));
typedef __amdgpu_buffer_rsrc_t rsrc_t;
#define LAS __attribute__((address_space(3)))
#define GAS __attribute__((address_space(1)))
#define DI __device__ __forceinline__
#define SGB(m, n) __builtin_amdgcn_sched_group_barrier(m, n, 0)
#define SB0() __builtin_amdgcn_sched_barrier(0)

constexpr int SEQ = 2048, DM = 1024, FF = 3584, HD = 4096;
constexpr int CODE_BYTES = 74240;
constexpr int NSP = 3;
constexpr size_t WS_FLAG = 0;
constexpr int F_EP = 0, F_X = 512, F_L = 2048, F_U = 4096, F_O = 12288, F_Y = 16384, F_S = 49152, F_P = 81920, F_END = 96256;
constexpr size_t WS_ST = 98304;
constexpr size_t WS_CS = 131072;
constexpr size_t WS_HT = 262144;
constexpr size_t WS_KV = WS_HT + 4194304;
constexpr size_t WS_O = WS_KV + 1048576;
constexpr size_t WS_U = WS_O + 16777216;
constexpr size_t WS_XB = WS_U + 7340032;
constexpr size_t WS_END = WS_XB + 25165824;

DI unsigned cvtpk(float lo, float hi) { f32x2 v = {lo, hi}; bf16x2 b = __builtin_convertvector(v, bf16x2); return __builtin_bit_cast(unsigned, b); }
DI float bflo(unsigned u) { return __uint_as_float(u << 16); }
DI float bfhi(unsigned u) { return __uint_as_float(u & 0xffff0000u); }
DI float rbf(float x) { return bflo(cvtpk(x, x)); }
template <int N> DI void wait_vm() { asm volatile("s_waitcnt vmcnt(%0)" ::"i"(N) : "memory"); }
DI void wait_lgkm0() { __builtin_amdgcn_s_waitcnt(0xC07F); }
DI void bar() { asm volatile("" ::: "memory"); __builtin_amdgcn_s_barrier(); asm volatile("" ::: "memory"); }
DI rsrc_t mkrsrc(const void* p, unsigned bytes) { return __builtin_amdgcn_make_buffer_rsrc((void*)p, 0, bytes, 0x00020000); }
DI void dma16(rsrc_t r, unsigned lds_addr, unsigned voff, unsigned soff) {
  asm volatile("s_mov_b32 m0, %0\n\ts_nop 0\n\tbuffer_load_dwordx4 %1, %2, %3 offen lds" ::"s"(lds_addr), "v"(voff), "s"(r), "s"(soff) : "memory");
}
DI void dma16s(rsrc_t r, unsigned lds_addr, unsigned voff, unsigned soff) {
  asm volatile("s_mov_b32 m0, %0\n\ts_nop 0\n\tbuffer_load_dwordx4 %1, %2, %3 offen sc1 lds" ::"s"(lds_addr), "v"(voff), "s"(r), "s"(soff) : "memory");
}
template <int OFF> DI void st16s(u32x4 v, rsrc_t r, unsigned voff, unsigned soff) {
  asm volatile("buffer_store_dwordx4 %0, %1, %2, %3 offen offset:%4 sc1\n\ts_nop 1" ::"v"(v), "v"(voff), "s"(r), "s"(soff), "i"(OFF) : "memory");
}
DI int lane_id() { int l; asm volatile("v_mbcnt_lo_u32_b32 %0, -1, 0\n\tv_mbcnt_hi_u32_b32 %0, -1, %0" : "=v"(l)); return l; }
struct Tag { unsigned lo, hi; };
DI void peek(rsrc_t rf, unsigned lds_addr, int off) {
  asm volatile("s_mov_b32 m0, %0\n\ts_nop 0\n\tbuffer_load_dword %3, %1, %2 offen sc1 lds" ::"s"(lds_addr), "s"(rf), "s"(off), "v"(lane_id() * 4) : "memory");
}
template <int GAP = 1> DI void spin_until(char* ws, int off, int n, Tag t) {
  const int ln = lane_id();
  GAS unsigned long long* p = (GAS unsigned long long*)(ws + WS_FLAG + off) + (ln < n ? ln : 0);
  const unsigned long long want = ((unsigned long long)t.hi << 32) | t.lo;
  for (unsigned sp = 0; sp < (1u << 20); ++sp) {
    if (__all(__hip_atomic_load(p, __ATOMIC_RELAXED, __HIP_MEMORY_SCOPE_AGENT) == want)) break;
    __builtin_amdgcn_s_sleep(GAP);
  }
  asm volatile("" ::: "memory");
}
DI void peek_check(char* ws, unsigned lds_addr, int off, int n, Tag t) {
  const int ln = lane_id();
  const unsigned v = *(LAS volatile unsigned*)(size_t)(lds_addr + ln * 4);
  if (!__all(ln >= 2 * n || v == ((ln & 1) ? t.hi : t.lo))) spin_until(ws, off, n, t);
}
template <long MASK> DI void signal(char* ws, int off, Tag t) {
  const u32x2 v = {t.lo, t.hi};
  asm volatile("s_mov_b64 exec, %3\n\tglobal_store_dwordx2 %0, %1, %2 sc1\n\ts_mov_b64 exec, -1" ::"v"(off), "v"(v), "s"(ws + WS_FLAG), "s"(MASK) : "memory");
}
DI void dma4k(rsrc_t r, unsigned lds_addr, unsigned voff, unsigned soff) {
  asm volatile("s_mov_b32 m0, %0\n\ts_nop 0\n\tbuffer_load_dwordx4 %1, %2, %3 offen sc1 lds\n\tbuffer_load_dwordx4 %1, %2, %3 offen offset:1024 sc1 lds\n\t"
               "buffer_load_dwordx4 %1, %2, %3 offen offset:2048 sc1 lds\n\tbuffer_load_dwordx4 %1, %2, %3 offen offset:3072 sc1 lds"
               ::"s"(lds_addr), "v"(voff), "s"(r), "s"(soff) : "memory");
}
template <int W> DI void pf_line(int wid, rsrc_t r, unsigned voff, unsigned soff) {
  asm volatile("s_cmp_lg_u32 %0, %4\n\ts_cbranch_scc1 1f\n\tbuffer_load_dword v255, %1, %2, %3 offen\n1:"
               ::"s"(wid), "v"(voff), "s"(r), "s"(soff), "i"(W) : "scc", "memory", "v255");
}
template <int CTRL> DI float dppf(float x) { return __builtin_bit_cast(float, __builtin_amdgcn_mov_dpp(__builtin_bit_cast(int, x), CTRL, 0xf, 0xf, true)); }
DI float wave_sum(float v) {
  v += dppf<0xB1>(v); v += dppf<0x4E>(v); v += dppf<0x141>(v); v += dppf<0x140>(v);
  auto a = __builtin_amdgcn_permlane16_swap(__float_as_uint(v), __float_as_uint(v), false, false);
  v = __uint_as_float(a[0]) + __uint_as_float(a[1]);
  auto b = __builtin_amdgcn_permlane32_swap(__float_as_uint(v), __float_as_uint(v), false, false);
  return __uint_as_float(b[0]) + __uint_as_float(b[1]);
}

DI f32x2 ln16(unsigned (&w)[8]) {
  f32x2 v[8];
#pragma unroll
  for (int i = 0; i < 8; ++i) v[i] = (f32x2){bflo(w[i]), bfhi(w[i])};
  const f32x2 s = ((v[0] + v[1]) + (v[2] + v[3])) + ((v[4] + v[5]) + (v[6] + v[7]));
  const float mean = wave_sum(s[0] + s[1]) * (1.f / 1024.f);
  f32x2 q0 = {0.f, 0.f}, q1 = q0;
#pragma unroll
  for (int i = 0; i < 8; i += 2) {
    const f32x2 d0 = v[i] - mean, d1 = v[i + 1] - mean;
    q0 = __builtin_elementwise_fma(d0, d0, q0); q1 = __builtin_elementwise_fma(d1, d1, q1);
  }
  q0 += q1;
  const float var = wave_sum(q0[0] + q0[1]) * (1.f / 1024.f);
  const float mb = rbf(mean);
  const float rs = __builtin_amdgcn_rcpf(rbf(__builtin_amdgcn_sqrtf(rbf(rbf(var) + 1e-5f))));
#pragma unroll
  for (int i = 0; i < 8; ++i) {
    const f32x2 d = v[i] - mb;
    const unsigned c = cvtpk(d[0], d[1]);
    const f32x2 r = (f32x2){bflo(c), bfhi(c)} * rs;
    w[i] = cvtpk(r[0], r[1]);
  }
  return (f32x2){mb, rs};
}

struct Frags { bf16x8 x[8]; bf16x8 w[4]; };
struct Addr { unsigned x, w[4]; };
template <int XS = 1024> DI void load_frags(Frags& f, const Addr& a) {
#pragma unroll
  for (int c = 0; c < 4; ++c) {
    s16x4 a0 = __builtin_amdgcn_ds_read_tr16_b64_v4i16((LAS s16x4*)(size_t)(a.w[c]));
    s16x4 a1 = __builtin_amdgcn_ds_read_tr16_b64_v4i16((LAS s16x4*)(size_t)(a.w[c] + 4096));
    f.w[c] = __builtin_shufflevector(a0, a1, 0, 1, 2, 3, 4, 5, 6, 7);
  }
#pragma unroll
  for (int i = 0; i < 8; ++i) f.x[i] = *(LAS bf16x8*)(size_t)(a.x + i * XS);
}
template <bool WA, bool Z> DI void mma_all(f32x4 (&acc)[8][4], const Frags& f, const f32x4* c0) {
  const f32x4 zero = {0.f, 0.f, 0.f, 0.f};
#pragma unroll
  for (int i = 0; i < 8; ++i)
#pragma unroll
    for (int jj = 0; jj < 4; ++jj) {
      const int j = (i & 1) ? 3 - jj : jj;
      if (WA) acc[i][j] = __builtin_amdgcn_mfma_f32_16x16x32_bf16(f.w[j], f.x[i], Z ? (c0 ? c0[j] : zero) : acc[i][j], 0, 0, 0);
      else    acc[i][j] = __builtin_amdgcn_mfma_f32_16x16x32_bf16(f.x[i], f.w[j], Z ? (c0 ? c0[j] : zero) : acc[i][j], 0, 0, 0);
    }
}
DI void addr_add(Addr& a, int d) {
  a.x += d;
#pragma unroll
  for (int c = 0; c < 4; ++c) a.w[c] += d;
}
template <bool WA, int NSLOT, int SLOTB, bool Z = false>
DI void gstep(f32x4 (&acc)[8][4], Frags& Fn, const Frags& Fc, Addr& a, int& slot, const f32x4* c0 = nullptr) {
  SB0();
  load_frags(Fn, a);
  mma_all<WA, Z>(acc, Fc, c0);
  slot = (slot + 1 == NSLOT) ? 0 : slot + 1;
  addr_add(a, (slot == 0) ? -(NSLOT - 1) * SLOTB : SLOTB);
  asm volatile("" : "+v"(a.x), "+v"(a.w[0]), "+v"(a.w[1]), "+v"(a.w[2]), "+v"(a.w[3]));
#pragma unroll
  for (int i = 0; i < 8; ++i) { SGB(0x8, 1); SGB(0x100, 1); SGB(0x8, 1); SGB(0x100, 1); SGB(0x8, 1); }
#pragma unroll
  for (int i = 0; i < 6; ++i) { SGB(0x8, 1); SGB(0x4, 1); SGB(0x2, 1); }
  SGB(0x8, 2);
  SB0();
  wait_lgkm0();
  bar();
}
template <int MODE, bool CS, bool Z = false>
DI void qstep(f32x4 (&acc)[8][4], f32x4 (&cs)[2], u32x4& ones, unsigned onext, Frags& Fn, const Frags& Fc, Addr& a, int& xs, int& wsl) {
  const f32x4 zero = {0.f, 0.f, 0.f, 0.f};
  SB0();
  load_frags<MODE == 2 ? 1024 : 2048>(Fn, a);
  if (CS) {
#pragma unroll
    for (int j = 0; j < 2; ++j) cs[j] = __builtin_amdgcn_mfma_f32_16x16x32_bf16(Fc.w[j], __builtin_bit_cast(bf16x8, ones), Z ? zero : cs[j], 0, 0, 0);
    ones = (u32x4){onext, onext, onext, onext};
    asm volatile("" : "+v"(ones));
  }
  mma_all<true, Z>(acc, Fc, nullptr);
  if (MODE == 2) addr_add(a, 24576);
  else {
    wsl = (wsl + 1) & 3;
    const int dw = wsl ? 8192 : -24576;
#pragma unroll
    for (int c = 0; c < 4; ++c) a.w[c] += dw;
    a.x ^= 64;
    if (MODE == 1) { xs = (xs == 2) ? 0 : xs + 1; a.x += xs ? 32768 : -65536; }
  }
  asm volatile("" : "+v"(a.x), "+v"(a.w[0]), "+v"(a.w[1]), "+v"(a.w[2]), "+v"(a.w[3]));
#pragma unroll
  for (int i = 0; i < 8; ++i) { SGB(0x8, 1); SGB(0x100, 1); SGB(0x8, 1); SGB(0x100, 1); SGB(0x8, 1); }
#pragma unroll
  for (int i = 0; i < 7; ++i) { SGB(0x8, 1); SGB(0x4, 1); SGB(0x2, 1); }
  if (CS) { SGB(0x8, 1); SGB(0x2, 2); SGB(0x8, 1); SGB(0x2, 2); }
  SGB(0x8, 1);
  SB0();
  wait_lgkm0();
  bar();
}
DI void addr_init(Addr& a, unsigned xbase, unsigned wbase, int wj, int cx, int lane) {
  const int g = lane >> 4, q = (lane >> 2) & 3, p = lane & 3, row = 4 * g + q;
  a.x = xbase + lane * 16;
#pragma unroll
  for (int c = 0; c < 4; ++c) a.w[c] = wbase + row * 256 + (((2 * (4 * wj + (c ^ cx)) + (p >> 1)) ^ ((row & 7) << 1)) * 16) + (p & 1) * 8;
}
template <bool WA, int NSLOT, int SLOTB> DI void gemm_steps(f32x4 (&acc)[8][4], Frags& F0, Frags& F1, Addr& a, int& slot, int n) {
  for (int t = 0; t < n; t += 2) {
    gstep<WA, NSLOT, SLOTB>(acc, F0, F1, a, slot);
    gstep<WA, NSLOT, SLOTB>(acc, F1, F0, a, slot);
  }
}

DI u32x4 pack8(const f32x4& a, const f32x4& b) { return (u32x4){cvtpk(a[0], a[1]), cvtpk(a[2], a[3]), cvtpk(b[0], b[1]), cvtpk(b[2], b[3])}; }
DI f32x2 gate2(f32x2 u, f32x2 z) {
  f32x2 e = {__builtin_amdgcn_exp2f(z[0]), __builtin_amdgcn_exp2f(z[1])};
  e += 1.f;
  return u * (f32x2){__builtin_amdgcn_rcpf(e[0]), __builtin_amdgcn_rcpf(e[1])};
}
DI unsigned gelu2(f32x2 u) {
  const f32x2 r = gate2(u, u * __builtin_elementwise_fma(u * u, (f32x2){-0.1029432397f, -0.1029432397f}, (f32x2){-2.302208198f, -2.302208198f}));
  return cvtpk(r[0], r[1]);
}
DI unsigned silu2(f32x2 y) { const f32x2 r = gate2(y, y * -1.4426950409f); return cvtpk(r[0], r[1]); }
DI u32x4 ldsr(unsigned a) { return *(LAS u32x4*)(size_t)a; }
DI void add8(f32x4& a, f32x4& b, const u32x4& w) {
  a += (f32x4){bflo(w[0]), bfhi(w[0]), bflo(w[1]), bfhi(w[1])};
  b += (f32x4){bflo(w[2]), bfhi(w[2]), bflo(w[3]), bfhi(w[3])};
}

struct Sc { f32x4 v[2][2]; };
struct Att { bf16x8 q[2][4]; f32x4 o[8][2]; f32x4 negm[2]; Sc sE, sO; u32x4 pO[2]; bf16x8 pre[4], ones; f32x4 l[2]; };
DI float xg_max(float m) {
  auto a = __builtin_amdgcn_permlane16_swap(__float_as_uint(m), __float_as_uint(m), false, false);
  m = fmaxf(__uint_as_float(a[0]), __uint_as_float(a[1]));
  auto b = __builtin_amdgcn_permlane32_swap(__float_as_uint(m), __float_as_uint(m), false, false);
  return fmaxf(__uint_as_float(b[0]), __uint_as_float(b[1]));
}
DI float row_max(const Sc& s, int qb) {
  const f32x4 a = s.v[0][qb], b = s.v[1][qb];
  return xg_max(fmaxf(fmaxf(fmaxf(a[0], a[1]), fmaxf(a[2], a[3])), fmaxf(fmaxf(b[0], b[1]), fmaxf(b[2], b[3]))));
}
DI bf16x8 ldfrag(unsigned a) { return *(LAS bf16x8*)(size_t)a; }
DI void sm1(Sc& s, int e, u32x4 (&w)[2], int part = 3) {
  if (e < 16 && (part & 1)) {
    float x = __builtin_amdgcn_exp2f(s.v[e >> 3][(e >> 2) & 1][e & 3]);
    asm volatile("" : "+v"(x));
    s.v[e >> 3][(e >> 2) & 1][e & 3] = x;
  }
  if (e >= 2 && e <= 16 && !(e & 1) && (part & 2)) {
    const int kt = (e - 1) >> 3, qb = ((e - 1) >> 2) & 1, r = (e - 1) & 3;
    unsigned c = cvtpk(s.v[kt][qb][r - 1], s.v[kt][qb][r]);
    asm volatile("" : "+v"(c));
    w[qb][2 * kt + (r >> 1)] = c;
  }
}
#define SETPRIO(x) __builtin_amdgcn_s_setprio(x)
#define MFMA16(a, b, c) __builtin_amdgcn_mfma_f32_16x16x32_bf16(a, b, c, 0, 0, 0)
#define PFRAG(w) __builtin_bit_cast(bf16x8, w)
DI void mfma_acc(f32x4& c, const bf16x8& a, const u32x4& b) { asm volatile("v_mfma_f32_16x16x32_bf16 %0, %1, %2, %0" : "+v"(c) : "v"(a), "v"(b)); }
template <int POS, int n, class F> DI void att_slot(Att& A, bf16x8 (&f)[36], u32x4 (&pE)[2], unsigned kv, unsigned kvn, F& gap) {
  constexpr int u = n >> 3, k = n & 7;
  constexpr bool isqk = (u & 1) ? (u == 1 || POS < 2) : (u == 0 && POS == 0);
  const f32x4 zero = {0.f, 0.f, 0.f, 0.f};
  if constexpr (k == 0) SETPRIO(3 - u);
#pragma unroll
  for (int qb = 0; qb < 2; ++qb) {
    if constexpr (isqk && (k & 3) != 0) mfma_acc(u == 1 ? A.sO.v[k >> 2][qb] : A.sE.v[k >> 2][qb], f[n], __builtin_bit_cast(u32x4, A.q[qb][k & 3]));
    else if constexpr (isqk && u == 1) A.sO.v[k >> 2][qb] = MFMA16(f[n], A.q[qb][0], A.negm[qb]);
    else if constexpr (isqk) A.sE.v[k >> 2][qb] = MFMA16(f[n], A.q[qb][0], u ? A.negm[qb] : zero);
    else if constexpr (POS == 0) A.o[k][qb] = MFMA16(f[n], PFRAG(pE[qb]), zero);
    else if constexpr (u == 2) mfma_acc(A.o[k][qb], f[n], pE[qb]);
    else mfma_acc(A.o[k][qb], f[n], A.pO[qb]);
    SB0();
    if (qb == 1) {
      if constexpr (n < 28) f[n + 4] = ldfrag(kv + (n + 4) * 1024);
      else if constexpr (POS < 2) f[n + 4] = ldfrag(kvn + (n - 28) * 1024);
      SB0();
    }
    if constexpr (POS == 0 && u == 1) {
      if (k == 0 && qb == 0) sm1(A.sE, 0, pE);
      sm1(A.sE, 2 * k + 1 + qb, pE);
    }
    if constexpr (POS > 0 && n <= 14) sm1(A.sE, n + 2, pE, 1 + qb);
    if constexpr (n >= 14 && n <= (POS < 2 ? 30 : 15)) sm1(A.sO, n - 14, A.pO, 1 + qb);
    if constexpr (POS < 2 && n >= 30) sm1(A.sE, n - 30, pE, 1 + qb);
    if constexpr (POS == 2 && u == 2) sm1(A.sO, 2 + 2 * k + qb, A.pO);
    SB0();
  }
  if constexpr (!isqk && (k == 2 || k == 4)) {
    constexpr int qb = k == 4;
    if constexpr (POS == 0) A.l[qb] = MFMA16(A.ones, PFRAG(pE[qb]), zero);
    else if constexpr (u == 2) mfma_acc(A.l[qb], A.ones, pE[qb]);
    else mfma_acc(A.l[qb], A.ones, A.pO[qb]);
  }
  gap(n);
  SB0();
  if constexpr (POS == 2 && n == 23) { asm volatile("s_nop 1"); SB0(); }
  if constexpr ((POS == 2 && n == 31) || (POS == 0 && n == 7)) { asm volatile("s_nop 9"); SB0(); }
  if constexpr (POS == 0 && n == 7) {
#pragma unroll
    for (int qb = 0; qb < 2; ++qb) {
      const float mx = row_max(A.sE, qb);
      A.sE.v[0][qb] -= mx; A.sE.v[1][qb] -= mx;
      A.negm[qb] = (f32x4){-mx, -mx, -mx, -mx};
    }
    SB0();
  }
  if constexpr (n < 31) att_slot<POS, n + 1>(A, f, pE, kv, kvn, gap);
}
template <int POS, class F> DI void att_tile(Att& A, unsigned kv, unsigned kvn, F&& gap) {
  bf16x8 f[36];
  u32x4 pE[2];
  asm volatile("" : "+v"(kv), "+v"(kvn));
#pragma unroll
  for (int i = 0; i < 4; ++i) f[i] = A.pre[i];
  SB0();
  att_slot<POS, 0>(A, f, pE, kv, kvn, gap);
  if (POS < 2) {
#pragma unroll
    for (int i = 0; i < 4; ++i) A.pre[i] = f[32 + i];
  }
}
DI void att_redo(Att& A, rsrc_t rkv, unsigned vo) {
  const f32x4 zero = {0.f, 0.f, 0.f, 0.f};
  float m[2] = {-3.0e38f, -3.0e38f};
  A.l[0] = A.l[1] = zero;
#pragma unroll
  for (int d = 0; d < 8; ++d) A.o[d][0] = A.o[d][1] = zero;
  for (int blk = 0; blk < 16; ++blk) {
    const int kb = (blk ? 2 * blk - 1 : 0) * 8192, vb = (blk < 15 ? 2 * blk + 2 : 31) * 8192;
    Sc s;
#pragma unroll
    for (int n = 0; n < 8; ++n) {
      const bf16x8 kf = PFRAG(__builtin_amdgcn_raw_buffer_load_b128(rkv, vo + n * 1024, kb, 16));
#pragma unroll
      for (int qb = 0; qb < 2; ++qb) s.v[n >> 2][qb] = MFMA16(kf, A.q[qb][n & 3], (n & 3) ? s.v[n >> 2][qb] : zero);
    }
#pragma unroll
    for (int qb = 0; qb < 2; ++qb) {
      const float mn = fmaxf(m[qb], row_max(s, qb)), fct = __builtin_amdgcn_exp2f(m[qb] - mn);
      m[qb] = mn;
      A.l[qb] *= fct;
      s.v[0][qb] -= mn; s.v[1][qb] -= mn;
#pragma unroll
      for (int d = 0; d < 8; ++d) A.o[d][qb] *= fct;
    }
    u32x4 p[2];
#pragma unroll
    for (int i = 0; i < 17; ++i) sm1(s, i, p);
#pragma unroll
    for (int qb = 0; qb < 2; ++qb) A.l[qb] = MFMA16(A.ones, PFRAG(p[qb]), A.l[qb]);
#pragma unroll
    for (int n = 0; n < 8; ++n) {
      const bf16x8 vf = PFRAG(__builtin_amdgcn_raw_buffer_load_b128(rkv, vo + n * 1024, vb, 16));
#pragma unroll
      for (int qb = 0; qb < 2; ++qb) A.o[n][qb] = MFMA16(vf, PFRAG(p[qb]), A.o[n][qb]);
    }
  }
}

__global__ __launch_bounds__(512, 2) __attribute__((amdgpu_num_vgpr(254))) void k_main(const u16* __restrict__ x, const u16* __restrict__ kc, const u16* __restrict__ vc, const u16* __restrict__ wq, const u16* __restrict__ wo,
                                                 const u16* __restrict__ w1, const u16* __restrict__ b1, const u16* __restrict__ w2,
                                                 const u16* __restrict__ b2, char* __restrict__ ws, u16* __restrict__ out) {
  extern __shared__ __attribute__((aligned(16))) char smem[];
  LAS char* lds = (LAS char*)smem;
  const unsigned lb = (unsigned)(size_t)lds;
  const int tid = threadIdx.x, wid = __builtin_amdgcn_readfirstlane(tid >> 6);
  const int g8 = blockIdx.x & 7, idx = blockIdx.x >> 3;
  const rsrc_t rcode = mkrsrc((const void*)(__builtin_amdgcn_s_getpc() & ~255ull), CODE_BYTES);
  auto code_pf = [&](unsigned lds_addr) {
    asm volatile("s_mov_b32 m0, %0\n\ts_nop 0\n\tbuffer_load_dword %3, %1, %2 offen lds" ::"s"(lds_addr), "s"(rcode), "s"((idx % ((CODE_BYTES + 8191) / 8192)) * 8192), "v"(lane_id() * 128) : "memory");
  };
  if (wid == 7) code_pf(lb + 161792);
  const bool hasU = idx < 28;
  const int uh = g8 & 1, dq = idx & 3, ft = 7 * (g8 >> 1) + (idx >> 2), ut = ft * 4 + dq;
  const int kvh = g8 >> 1, head = 8 * kvh + (idx & 7), rb = 4 * (g8 & 1) + (idx >> 3);
  const int dc = idx & 7, tile = rb * 8 + dc;
  const int lw = wid - 4, wi = (wid >> 1) & 1, wj = wid & 1;
  constexpr int QS = 24576;
  constexpr int PARK = 98304;
  const rsrc_t rf = mkrsrc(ws + WS_FLAG, F_END + 256);
  const rsrc_t rkv = mkrsrc(ws + WS_KV + (size_t)kvh * 262144, 262144);
  const rsrc_t rxb = mkrsrc(ws + WS_XB + (size_t)tile * 393216, 393216);
  struct Strm { rsrc_t r; unsigned v0, v1, step, p0, p1, p2; int rot; };
  auto strm_x = [&](Strm& S, const char* base, unsigned bytes, unsigned step, int xr) {
    const int ln = lane_id();
    S.r = mkrsrc(base, bytes); S.v0 = ln * 16 + (xr & 1) * 1024; S.v1 = S.v0 ^ 1024; S.step = step;
    S.p0 = (xr >> 1) * 2048; S.p1 = S.p0 ^ 2048; S.p2 = 4096; S.rot = 0;
  };
  auto strm_w = [&](Strm& S, const char* base, unsigned rows, unsigned ld, bool nat) {
    const int ln = lane_id(), q4 = ln >> 4;
    const unsigned c0 = ((ln & 15) ^ (q4 << 1)) * 16;
    S.r = mkrsrc(base, (rows - 1) * ld + 256); S.v0 = q4 * ld + c0; S.v1 = ((nat ? 8 : 4) + q4) * ld + (c0 ^ 128); S.step = 32 * ld;
    S.p0 = 0; S.p1 = (nat ? 16 : 8) * ld; S.p2 = (nat ? 4 : 16) * ld; S.rot = 0;
  };
  auto issue = [&](const Strm& S, int k, int src = -1) {
    const unsigned s = lb + (k & 3) * QS + lw * 8192;
    if (src < 0) src = (k + S.rot) & 31;
#pragma unroll
    for (int i = 0; i < 4; ++i) {
      const unsigned so = src * S.step + ((i & 1) ? S.p1 : S.p0) + (i >> 1) * S.p2;
      dma16s(S.r, s + i * 2048, S.v0, so); dma16s(S.r, s + i * 2048 + 1024, S.v1, so);
    }
  };
  constexpr int QW = 98304, HST = 131072, HRS = 2112, RST = HST + 8 * HRS, PKQ = 148480, STT = 151552, CSX = 153600;
  const int lane0 = tid & 63;
  const rsrc_t rq = lw < 2 ? mkrsrc(x + (size_t)(rb * 256 + lw * 128) * DM, 128 * DM * 2) : mkrsrc((const char*)wq + head * 256, (DM - 1) * HD * 2 + 256);
  const unsigned qv0 = lw < 2 ? (lane0 >> 3) * 2048 + (((lane0 & 7) ^ (lane0 >> 4)) * 16) : (lane0 >> 4) * (HD * 2) + (((lane0 & 15) ^ ((lane0 >> 4) << 1)) * 16);
  const unsigned qv1 = lw < 2 ? (qv0 ^ 64) + 8 * 2048 : (qv0 ^ 128) + 8 * (HD * 2);
  const bool qx = lw < 2;
  const unsigned qp1 = qx ? 32768 : 16 * HD * 2, qp2 = qx ? 65536 : 4 * HD * 2;
  const unsigned qlb = lb + (qx ? lw * 16384 : QW), qlw = qx ? 98304 : 32768, qom = qx ? 0x20780 : 0x7c0000;
  unsigned ql = qx ? 65536 : 0, qdl = 8192, qo = rb * (qx ? 256 : 4 * 32 * HD * 2), qdo = qx ? 131072 : 32 * HD * 2;
  auto issq = [&]() {
    const unsigned s = qlb + ql;
    dma16(rq, s, qv0, qo); dma16(rq, s + 1024, qv1, qo);
    dma16(rq, s + 2048, qv0, qo + qp1); dma16(rq, s + 3072, qv1, qo + qp1);
    dma16(rq, s + 4096, qv0, qo + qp2); dma16(rq, s + 5120, qv1, qo + qp2);
    dma16(rq, s + 6144, qv0, qo + qp1 + qp2); dma16(rq, s + 7168, qv1, qo + qp1 + qp2);
    ql += qdl; ql = ql >= qlw ? ql - qlw : ql; qdl ^= qx ? (8192 ^ 24576) : 0;
    qo = (qo + qdo) & qom; qdo ^= qx ? 128 : 0;
  };
  auto h_out = [&](int it) {
    const rsrc_t rh = mkrsrc(ws + WS_HT + (size_t)(blockIdx.x >> 2) * 16384 + (blockIdx.x & 3) * 256, 3 * 1048576 + 16384);
    const unsigned a0 = HST + ((lane0 >> 2) & 3) * HRS + (it * 4 + (lane0 >> 4)) * 32 + (lane0 & 3) * 8;
    s16x4 lo = __builtin_amdgcn_ds_read_tr16_b64_v4i16((LAS s16x4*)(lds + a0));
    s16x4 hi = __builtin_amdgcn_ds_read_tr16_b64_v4i16((LAS s16x4*)(lds + a0 + 4 * HRS));
    st16s<0>(__builtin_bit_cast(u32x4, __builtin_shufflevector(lo, hi, 0, 1, 2, 3, 4, 5, 6, 7)), rh, (lane0 >> 4) * 1024 + (lane0 & 15) * 16,
             (it >> 2) * 1048576 + (it & 3) * 4096);
  };
  auto st_out = [&]() { st16s<0>(*(LAS u32x4*)(lds + RST + (lane0 & 3) * 16), mkrsrc(ws + WS_ST + blockIdx.x * 64, 64), lane0 * 16, 0); };
  Tag tag;
  {
    const int b = blockIdx.x, lane = lane0;
    u32x4 A, B;
    u32x2 ep;
    asm volatile("global_load_dwordx4 %0, %3, %4\n\tglobal_load_dwordx4 %1, %3, %4 offset:1024\n\tglobal_load_dwordx2 %2, %5, %6 sc1"
                 : "=&v"(A), "=&v"(B), "=&v"(ep) : "v"(lane * 16), "s"(x + (size_t)(8 * b + wid) * DM), "v"(F_EP), "s"(ws + WS_FLAG) : "memory");
    if (wid >= 4 && wid < 7) { issq(); issq(); if (lw == 2) { issq(); issq(); wait_vm<32>(); } else wait_vm<16>(); } else wait_vm<0>();
    asm volatile("" : "+v"(A), "+v"(B), "+v"(ep));
    unsigned v[8] = {A[0], A[1], A[2], A[3], B[0], B[1], B[2], B[3]};
    const unsigned long long e64 = ((unsigned long long)(unsigned)__builtin_amdgcn_readfirstlane(ep[1]) << 32) | (unsigned)__builtin_amdgcn_readfirstlane(ep[0]);
    const unsigned long long tg = e64 * 0x9E3779B97F4A7C15ull + 0x632BE59BD9B4E019ull;
    tag.lo = (unsigned)tg; tag.hi = (unsigned)(tg >> 32);
    *(LAS f32x2*)(lds + RST + wid * 8) = ln16(v);
    ln16(v);
    *(LAS u32x4*)(lds + HST + wid * HRS + lane * 16) = (u32x4){v[0], v[1], v[2], v[3]};
    *(LAS u32x4*)(lds + HST + wid * HRS + 1024 + lane * 16) = (u32x4){v[4], v[5], v[6], v[7]};
    wait_lgkm0();
  }
  bf16x8 qh[8];
  f32x4 uk[4][4];
  auto kv8 = [&](int t, int slot) {
    const unsigned v = lane_id() * 16;
    dma4k(rkv, lb + slot * 32768 + lw * 8192, v, t * 32768 + lw * 8192);
    dma4k(rkv, lb + slot * 32768 + lw * 8192 + 4096, v, t * 32768 + lw * 8192 + 4096);
  };
  if (wid < 4) {
    f32x4 acc[8][4], cs[2];
    Frags F0, F1;
    Addr a;
    int xs = 2, wsl = 1;
    {
      const int ln = lane_id(), r = ln & 15;
      addr_init(a, 0, lb + QW, wj, 2 * wi, ln);
      a.x = lb + 65536 + wi * 16384 + r * 128 + (((ln >> 4) ^ (r >> 1)) * 16);
    }
    bar();
    load_frags<2048>(F0, a);
    a.x ^= 64;
#pragma unroll
    for (int c = 0; c < 4; ++c) a.w[c] += 8192;
    wait_lgkm0();
    bar();
    {
      const unsigned o1 = 0x3f803f80;
      u32x4 ones = {o1, o1, o1, o1};
      qstep<1, true, true>(acc, cs, ones, o1, F1, F0, a, xs, wsl);
      for (int t = 0; t < 2; ++t) {
        const unsigned on = t == 0 ? o1 : 0;
        qstep<0, true>(acc, cs, ones, on, F0, F1, a, xs, wsl);
        qstep<1, true>(acc, cs, ones, on, F1, F0, a, xs, wsl);
      }
      const int ln = lane_id();
      const unsigned csa = lb + CSX + ((ln & 15) ? 7168 : (64 * wj + 32 * wi) * 4) + (ln & 48);
      *(LAS f32x4*)(size_t)csa = cs[0];
      *(LAS f32x4*)(size_t)(csa + 64) = cs[1];
      for (int t = 0; t < 13; ++t) { qstep<0, false>(acc, cs, ones, 0, F0, F1, a, xs, wsl); qstep<1, false>(acc, cs, ones, 0, F1, F0, a, xs, wsl); }
      addr_init(a, lb + wi * 8192, lb + 16384, wj, 0, lane_id());
      qstep<2, false>(acc, cs, ones, 0, F0, F1, a, xs, wsl);
    }
    f32x4 c0[4];
    {
      const int ln = lane_id(), g = ln >> 4;
      u32x2 bv[4];
#pragma unroll
      for (int j = 0; j < 4; ++j) bv[j] = *(LAS u32x2*)(lds + CSX + 5120 + (64 * wj + 16 * j + 4 * g) * 2);
      f32x4 cq[4];
#pragma unroll
      for (int j = 0; j < 4; ++j) cq[j] = *(LAS f32x4*)(lds + CSX + 512 + (64 * wj + 16 * (j ^ (2 * wi)) + 4 * g) * 4);
      f32x2 st[8];
#pragma unroll
      for (int i = 0; i < 8; ++i) st[i] = *(LAS f32x2*)(lds + STT + (128 * wi + 16 * i + (ln & 15)) * 8);
      wait_lgkm0();
      bar();
      const unsigned qa = lb + 98304 + wi * 32768 + (2 * wj + wi) * 1024 + ln * 16, qb = qa ^ 1024;
#pragma unroll
      for (int i = 0; i < 8; ++i) {
        const float sc = st[i][1] * 0.12751743082f, nm = -st[i][0];
#pragma unroll
        for (int mm = 0; mm < 2; ++mm)
          *(LAS u32x4*)(size_t)((mm ? qb : qa) + i * 4096) =
              pack8((acc[i][2 * mm] + cq[2 * mm] * nm) * sc, (acc[i][2 * mm + 1] + cq[2 * mm + 1] * nm) * sc);
      }
#pragma unroll
      for (int j = 0; j < 4; ++j) c0[j] = uh ? (f32x4){0.f, 0.f, 0.f, 0.f} : (f32x4){bflo(bv[j][0]), bfhi(bv[j][0]), bflo(bv[j][1]), bfhi(bv[j][1])};
    }
    if (hasU) {
      int slot = 1;
      gstep<true, 4, QS, true>(acc, F1, F0, a, slot, c0);
      gemm_steps<true, 4, QS>(acc, F0, F1, a, slot, 30);
      gstep<true, 4, QS>(acc, F0, F1, a, slot);
      const rsrc_t rux = mkrsrc(ws + WS_XB + (size_t)((tile ^ 32) * 4 + kvh) * 98304 + 49152 + wid * 8192, 8192);
      const int ln = lane_id();
#pragma unroll
      for (int ii = 0; ii < 4; ++ii) {
        st16s<0>(pack8(acc[2 * ii + 1][0], acc[2 * ii + 1][1]), rux, ln * 16, ii * 2048);
        st16s<1024>(pack8(acc[2 * ii + 1][2], acc[2 * ii + 1][3]), rux, ln * 16, ii * 2048);
#pragma unroll
        for (int j = 0; j < 4; ++j) uk[ii][j] = acc[2 * ii][j];
      }
    }
  } else {
    const int lane = lane0;
    Strm U;
    if (lw < 2) strm_x(U, ws + WS_HT + (size_t)dq * 1048576 + uh * 524288 + lw * 8192, 524288 - lw * 8192, 16384, uh);
    else strm_w(U, (const char*)w1 + (size_t)uh * 1024 * (FF * 2) + ft * 256, 1024, FF * 2, true);
    if (!hasU) U.r = mkrsrc(ws, 0);
    U.rot = 8 * dq;
    const unsigned pk = lb + PKQ + lw * 512;
    if (lw < 3) {
      if (lw < 2) wait_vm<0>(); else wait_vm<24>();
      bar();
      if (lw < 2) { issq(); issq(); } else wait_vm<16>();
      bar();
      if (lw < 2) issq();
      for (int t = 1; t <= 21; ++t) { issq(); wait_vm<16>(); bar(); }
      peek(rf, pk, F_L + lw * 512); peek(rf, pk + 256, F_L + lw * 512 + 256);
      for (int t = 22; t <= 26; ++t) { issq(); wait_vm<16>(); bar(); }
      issq(); wait_vm<16>();
    } else {
      peek(mkrsrc(b1, FF * 2), lb + CSX + 5120, ft * 256);
      bar();
      for (int t = 0; t <= 7; ++t) { h_out(2 * t); h_out(2 * t + 1); bar(); }
      st_out();
      st16s<0>(*(LAS u32x4*)(lds + CSX + (lane & 31) * 16), mkrsrc(ws + WS_CS + (head * 8 + rb) * 512, 512), lane * 16, 0);
      bar(); bar(); bar();
      wait_vm<0>();
      signal<1>(ws, F_L + blockIdx.x * 8, tag);
      for (int t = 11; t <= 21; ++t) bar();
      peek(rf, pk, F_L + lw * 512); peek(rf, pk + 256, F_L + lw * 512 + 256);
      for (int t = 22; t <= 26; ++t) bar();
      wait_vm<0>();
    }
    peek_check(ws, pk, F_L + lw * 512, 32, tag); peek_check(ws, pk + 256, F_L + lw * 512 + 256, 32, tag);
    bar();
    if (blockIdx.x == 0 && lw == 3) signal<1>(ws, F_EP, tag);
    if (lw == 2) issq();
    if (lw == 3) {
      const rsrc_t rs = mkrsrc(ws + WS_ST + rb * 2048, 2048);
      dma16s(rs, lb + STT, lane * 16, 0); dma16s(rs, lb + STT + 1024, lane * 16, 1024);
      dma4k(mkrsrc(ws + WS_CS + head * 4096, 4096), lb + CSX + 1024, lane * 16, 0);
    }
    const bool us = lw < 3;
    if (us) issue(U, 0);
    wait_vm<16>(); bar();
    if (us) wait_vm<8>(); else wait_vm<0>();
    bar();
    if (us) issue(U, 1);
    else {
      const LAS f32x2* cp = (const LAS f32x2*)(lds + CSX + 1024) + lane;
      *(LAS f32x2*)(lds + CSX + 512 + lane * 8) = ((cp[0] + cp[64]) + (cp[128] + cp[192])) + ((cp[256] + cp[320]) + (cp[384] + cp[448]));
      wait_lgkm0();
    }
    bar();
    wait_vm<8>(); bar();
    if (us) issue(U, 2);
    wait_vm<8>();
    bar();
    bar();
    if (hasU) {
      if (us) { issue(U, 3); for (int t = 1; t <= 28; ++t) { issue(U, t + 3); wait_vm<16>(); bar(); } }
      else {
        for (int t = 1; t <= 26; ++t) bar();
        spin_until(ws, F_X + kvh * 128, 8, tag);
        pf_line<7>(wid, rcode, lane * 128, (idx % ((CODE_BYTES + 8191) / 8192)) * 8192);
        bar(); bar();
      }
      wait_vm<8>(); bar();
      kv8(0, 0); wait_vm<8>(); bar();
      kv8(1, 1); bar();
      kv8(2, 2); wait_vm<8>(); bar();
    }
  }
  if (!hasU) {
    const int sp = g8 * 4 + (idx - 28), lane = lane_id(), g = lane >> 4;
    unsigned e[4][8];
#pragma unroll
    for (int i = 0; i < 4; ++i) {
      const int kh = sp >> 3, unit = (sp & 7) * 4 + i, frag = wid;
      const bool isK = unit == 0 || ((unit & 1) && unit < 31);
      const u16* sk = kc + ((size_t)kh * SEQ + 1536 + 32 * ((unit + 1) >> 1) + 16 * (frag >> 2) + (lane & 15)) * 128 + 32 * (frag & 3) + 4 * g;
      const u16* sv = vc + ((size_t)kh * SEQ + 1536 + 32 * ((unit - 1) >> 1) + 4 * g) * 128 + 16 * frag + (lane & 15);
      const u16* src = isK ? sk : sv;
      const int s1 = isK ? 1 : 128, s4 = isK ? 16 : 2048;
#pragma unroll
      for (int j2 = 0; j2 < 8; ++j2) e[i][j2] = src[(j2 >> 2) * s4 + (j2 & 3) * s1];
    }
#pragma unroll
    for (int i = 0; i < 4; ++i)
      st16s<0>((u32x4){e[i][0] | (e[i][1] << 16), e[i][2] | (e[i][3] << 16), e[i][4] | (e[i][5] << 16), e[i][6] | (e[i][7] << 16)},
               mkrsrc(ws + WS_KV, 1048576), (wid * 64 + lane) * 16, (sp * 4 + i) * 8192);
    wait_vm<0>();
    wait_lgkm0();
    bar();
    if (wid == 0) signal<1>(ws, F_X + (sp >> 3) * 128 + (sp & 7) * 8, tag);
    unsigned pw[NSP];
#pragma unroll
    for (int i = 0; i < NSP; ++i) pw[i] = *(const unsigned*)((const char*)w2 + (size_t)i * 2097152 + (size_t)(sp * 512 + wid * 64 + lane) * 128);
#pragma unroll
    for (int i = 0; i < NSP; ++i) asm volatile("" ::"v"(pw[i]));
    if (wid >= 4) { spin_until(ws, F_X + kvh * 128, 8, tag); kv8(0, 0); kv8(1, 1); kv8(2, 2); wait_vm<8>(); }
    bar();
  }
  {
    const int ln = lane_id();
#pragma unroll
    for (int ks = 0; ks < 8; ++ks) qh[ks] = *(LAS bf16x8*)(lds + 98304 + wid * 8192 + ks * 1024 + ln * 16);
  }
  Strm Y;
  auto y_iss = [&](int k) { const int r = k + 7 * (dc & 3); issue(Y, k, r >= 28 ? r - 28 : r); };
  Frags FY;
  Addr aY;
  {
    const int lane = lane_id();
    Att A;
#pragma unroll
    for (int ks = 0; ks < 8; ++ks) A.q[ks >> 2][ks & 3] = qh[ks];
    A.ones = (bf16x8){0x3f80, 0x3f80, 0x3f80, 0x3f80, 0x3f80, 0x3f80, 0x3f80, 0x3f80};
    asm volatile("" : "+v"(A.ones));
    const unsigned kvl = lb + lane * 16;
#pragma unroll
    for (int i = 0; i < 4; ++i) A.pre[i] = ldfrag(kvl + i * 1024);
    const bool strm = wid >= 4 && wid < 7, hw = wid >= 4;
    const rsrc_t rp2 = mkrsrc(w2, SEQ * FF * 2), rpo = mkrsrc(wo, HD * DM * 2);
    const unsigned pv = (blockIdx.x * 64 + lane) * 128;
#define PF(W, R, C) pf_line<W>(wid, R, pv, (C) * 2097152u)
    const int fp = F_P + (ut * 2 + uh) * 64 + (wid & 3) * 8;
    const bool cu = !hw && hasU;
    att_tile<0>(A, kvl + 0 * 32768, kvl + 1 * 32768, [&](int n) __attribute__((always_inline)) { if (n == 20 && cu) { wait_vm<0>(); signal<1>(ws, fp ^ 64, tag); } });
    if (hw) wait_vm<0>();
    bar();
    att_tile<1>(A, kvl + 1 * 32768, kvl + 2 * 32768, [&](int n) __attribute__((always_inline)) {
      if (n == 0 && hw) kv8(3, 3); if (n == 12 && hw) kv8(4, 0); if (n == 28 && hw) peek(rf, lb + 131072 + lw * 256, fp); if (n == 4) PF(0, rp2, 3); if (n == 9) PF(1, rp2, 4); });
    if (hw) wait_vm<9>();
    bar();
    att_tile<1>(A, kvl + 2 * 32768, kvl + 3 * 32768, [&](int n) __attribute__((always_inline)) {
      if (n == 8 && hw) {
        wait_vm<0>();
        if (hasU) peek_check(ws, lb + 131072 + lw * 256, fp, 1, tag);
        const rsrc_t rux = mkrsrc(ws + WS_XB + (size_t)(tile * 4 + kvh) * 98304 + 49152, hasU ? 32768 : 0);
        dma4k(rux, lb + 32768 + lw * 8192, lane * 16, lw * 8192); dma4k(rux, lb + 32768 + lw * 8192 + 4096, lane * 16, lw * 8192 + 4096);
      }
      if (n == 27) PF(3, rp2, 5); });
    if (hw) wait_vm<0>();
    bar();
    if (hw) { kv8(5, 4); kv8(7, 2); }
    if (cu) {
      u32x4 p[8];
      p[0] = ldsr(kvl + 32768 + wid * 8192);
#pragma unroll
      for (int k = 0; k < 8; ++k) {
        if (k < 7) p[k + 1] = ldsr(kvl + 32768 + wid * 8192 + (k + 1) * 1024);
        f32x4 a = uk[k >> 1][2 * (k & 1)], b = uk[k >> 1][2 * (k & 1) + 1];
        add8(a, b, p[k]);
        p[k] = (u32x4){gelu2(a.xy), gelu2(a.zw), gelu2(b.xy), gelu2(b.zw)};
        asm volatile("" : "+v"(p[k]));
        if (k == 0) PF(2, rp2, 6);
        SB0();
      }
      bar();
      const rsrc_t ru = mkrsrc(ws + WS_U + (size_t)((2 * dq + wi) * 112 + 4 * ft + 2 * wj) * 8192 + uh * 1024, 16384);
#pragma unroll
      for (int k = 0; k < 8; ++k) st16s<0>(p[k], ru, lane * 16, (k & 1) * 8192 + (k >> 1) * 2048);
    } else { PF(2, rp2, 6); bar(); }
    att_tile<1>(A, kvl + 3 * 32768, kvl + 0 * 32768, [&](int n) __attribute__((always_inline)) {
      if (n == 20 && cu) { wait_vm<0>(); signal<1>(ws, F_U + ((2 * dq + wi) * 4 + kvh) * 256 + (((idx >> 2) * 2 + uh) * 2 + wj) * 8, tag); } });
    if (hw) wait_vm<0>();
    bar();
    att_tile<1>(A, kvl + 0 * 32768, kvl + 4 * 32768, [&](int n) __attribute__((always_inline)) { if (n == 0 && hw) kv8(6, 3); if (n == 6) PF(3, rpo, 0); if (n == 15) PF(0, rpo, 1); if (n == 24) PF(1, rpo, 2); });
    if (hw) wait_vm<0>();
    bar();
    att_tile<1>(A, kvl + 4 * 32768, kvl + 3 * 32768, [&](int n) __attribute__((always_inline)) { if (n == 3) PF(2, rpo, 3); });
    if (lw == 0) strm_x(Y, ws + WS_U + (size_t)(dc * 112 + 28 * kvh) * 8192, 28 * 8192, 8192, 0);
    else strm_w(Y, (const char*)w2 + (size_t)kvh * 896 * (SEQ * 2) + rb * 512 + ((lw - 1) & 1) * 256, 896, SEQ * 2, false);
    bar();
    att_tile<1>(A, kvl + 3 * 32768, kvl + 2 * 32768, [&](int n) __attribute__((always_inline)) {
      if (n == 4 && wid == 4) peek(rf, lb + 49152, F_U + (dc * 4 + kvh) * 256);
      if (n == 18 && wid == 4) { wait_vm<0>(); peek_check(ws, lb + 49152, F_U + (dc * 4 + kvh) * 256, 28, tag); }
      if (n == 19 && strm) y_iss(0); if (n == 27 && strm) y_iss(1); });
    bar();
    att_tile<2>(A, kvl + 2 * 32768, 0, [&](int n) __attribute__((always_inline)) {
      if (n == 9) { if (hw) wait_vm<8>(); bar(); }
      if (n == 10 && strm) y_iss(2); });
    if (__builtin_expect(__any(!(A.l[0][0] + A.l[1][0] <= 1.0e9f)), 0)) att_redo(A, rkv, lane * 16);
    addr_init(aY, lb, lb + 8192 + wi * 8192, wj, kvh, lane);
    if (!hw) load_frags(FY, aY);
#pragma unroll
    for (int qb = 0; qb < 2; ++qb) {
      const float inv = __builtin_amdgcn_rcpf(A.l[qb][0]);
#pragma unroll
      for (int d = 0; d < 4; ++d) *(LAS u32x4*)(size_t)(kvl + PARK + wid * 2048 + qb * 1024 + d * 16384) = pack8(A.o[2 * d][qb] * inv, A.o[2 * d + 1][qb] * inv);
    }
    wait_lgkm0();
  }
  if (wid < 4) {
    f32x4 acc[8][4];
    Frags F0 = FY, F1;
    Addr a = aY;
    int slot = 1;
    addr_add(a, QS);
    bar();
    gstep<false, 4, QS, true>(acc, F1, F0, a, slot);
    gemm_steps<false, 4, QS>(acc, F0, F1, a, slot, 26);
    addr_init(a, lb + wi * 8192, lb + 16384, wj, 0, lane_id());
    gstep<false, 4, QS>(acc, F0, F1, a, slot);
    {
      const int lm = lane_id();
#pragma unroll
      for (int m = 0; m < 4; ++m) *(LAS u32x4*)(size_t)(lb + PARK + lm * 16 + wid * 4096 + m * 1024) = pack8(acc[2 * m][0], acc[2 * m + 1][0]);
#pragma unroll
      for (int j = 1; j < 4; ++j)
#pragma unroll
        for (int m = 0; m < 4; ++m) st16s<0>(pack8(acc[2 * m][j], acc[2 * m + 1][j]), rxb, lm * 16, (kvh ^ j) * 98304 + (j - 1) * 16384 + wid * 4096 + m * 1024);
    }
    gstep<true, 4, QS, true>(acc, F1, F0, a, slot);
    gemm_steps<true, 4, QS>(acc, F0, F1, a, slot, 8);
    wait_vm<0>();
    gemm_steps<true, 4, QS>(acc, F0, F1, a, slot, 22);
    gstep<true, 4, QS>(acc, F0, F1, a, slot);
    const int lt = lane_id(), g = lt >> 4;
    const unsigned wo_ = wi * 8192 + wj * 2048;
#pragma unroll
    for (int dl = 1; dl < 4; ++dl)
#pragma unroll
      for (int h = 0; h < 2; ++h)
#pragma unroll
        for (int mm = 0; mm < 2; ++mm)
          st16s<0>(pack8(acc[4 * h + dl][2 * mm], acc[4 * h + dl][2 * mm + 1]), rxb, lt * 16, (kvh ^ dl) * 98304 + 49152 + (dl - 1) * 16384 + wo_ + h * 4096 + mm * 1024);
    const int fs = F_S + tile * 512 + wid * 32;
    wait_vm<8>(); signal<1>(ws, fs + (kvh ^ 1) * 128, tag);
    wait_vm<5>(); signal<1>(ws, fs + (kvh ^ 2) * 128 + 8, tag);
    wait_vm<2>(); signal<1>(ws, fs + (kvh ^ 3) * 128 + 16, tag);
    const int srow = rb * 256 + (8 * wi + kvh) * 16;
    const rsrc_t rx = mkrsrc(x + (size_t)srow * DM, 80 * DM * 2), rout = mkrsrc(out + (size_t)srow * DM, 80 * DM * 2);
    const unsigned vo = ((lt & 15) * DM + dc * 128 + wj * 64 + 16 * (g & 1) + 8 * (g >> 1)) * 2;
    u32x4 ps[3][4], xv[4], gv[4];
#pragma unroll
    for (int p = 0; p < 4; ++p) xv[p] = __builtin_amdgcn_raw_buffer_load_b128(rx, vo + (p >> 1) * (64 * DM * 2) + (p & 1) * 64, 0, 0);
    bar();
#pragma unroll
    for (int p = 0; p < 4; ++p) gv[p] = ldsr(lb + PARK + lt * 16 + wo_ + (p >> 1) * 4096 + (p & 1) * 1024);
    {
      GAS unsigned long long* fp = (GAS unsigned long long*)(ws + WS_FLAG + fs + kvh * 128) + (lt & 3);
      const unsigned long long want = ((unsigned long long)tag.hi << 32) | tag.lo;
      unsigned got = 0;
      for (unsigned sp = 0; got != 7; ++sp) {
        unsigned now = (unsigned)__ballot(__hip_atomic_load(fp, __ATOMIC_RELAXED, __HIP_MEMORY_SCOPE_AGENT) == want) & 7;
        if (sp >= (1u << 20)) now = 7;
        asm volatile("" ::: "memory");
#pragma unroll
        for (int k = 0; k < 3; ++k)
          if ((now & ~got) >> k & 1) {
#pragma unroll
            for (int p = 0; p < 4; ++p)
              ps[k][p] = __builtin_amdgcn_raw_buffer_load_b128(rxb, lt * 16 + (p & 1) * 1024, kvh * 98304 + 49152 + k * 16384 + wo_ + (p >> 1) * 4096, 16);
          }
        got |= now;
        if (got != 7) __builtin_amdgcn_s_sleep(1);
      }
    }
#pragma unroll
    for (int p = 0; p < 4; ++p) {
      f32x4 s0 = acc[4 * (p >> 1)][2 * (p & 1)], s1 = acc[4 * (p >> 1)][2 * (p & 1) + 1];
#pragma unroll
      for (int k = 0; k < 3; ++k) add8(s0, s1, ps[k][p]);
      s0 *= (f32x4){bflo(gv[p][0]), bfhi(gv[p][0]), bflo(gv[p][1]), bfhi(gv[p][1])};
      s1 *= (f32x4){bflo(gv[p][2]), bfhi(gv[p][2]), bflo(gv[p][3]), bfhi(gv[p][3])};
      const u32x4 tp = pack8(s0, s1);
      const auto w0 = __builtin_amdgcn_permlane16_swap(tp[0], tp[2], false, false), w1 = __builtin_amdgcn_permlane16_swap(tp[1], tp[3], false, false);
      const unsigned t[4] = {w0[0], w1[0], w0[1], w1[1]};
      u32x4 o;
#pragma unroll
      for (int e = 0; e < 4; ++e) o[e] = cvtpk(bflo(xv[p][e]) + bflo(t[e]), bfhi(xv[p][e]) + bfhi(t[e]));
      __builtin_amdgcn_raw_buffer_store_b128(o, rout, vo + (p >> 1) * (64 * DM * 2) + (p & 1) * 64, 0, 0);
    }
  } else {
    const int lane = lane_id();
    const unsigned pl = lb + PARK + lane * 16, pk = lb + 159744 + lw * 256;
    const int fo = F_O + (rb * 4 + kvh) * 128;
    if (lw < 3) {
      Strm S;
      if (lw < 2) strm_x(S, ws + WS_O + (size_t)(rb * 128 + 32 * kvh) * 16384 + lw * 8192, 524288 - lw * 8192, 16384, kvh);
      else strm_w(S, (const char*)wo + (size_t)kvh * 1024 * (DM * 2) + dc * 256, 1024, DM * 2, false);
      wait_vm<8>();
      bar();
      y_iss(3);
      for (int t = 1; t <= 22; ++t) { y_iss(t + 3); wait_vm<16>(); bar(); }
      peek(rf, lb + PARK + lw * 256, fo);
      y_iss(26); wait_vm<16>(); bar();
      y_iss(27); wait_vm<16>(); bar();
      peek_check(ws, lb + PARK + lw * 256, fo, 8, tag);
      auto iss = [&](int k) { const int r = (k & 16) | ((k + 4 * (rb & 3)) & 15); issue(S, k, 4 * (r & 7) + (r >> 3)); wait_vm<16>(); bar(); };
      for (int t = 25; t <= 36; ++t) iss(t - 25);
      peek(rf, lb + PARK + 16384 + lw * 256, fo + 64);
      for (int t = 37; t <= 40; ++t) iss(t - 25);
      peek_check(ws, lb + PARK + 16384 + lw * 256, fo + 64, 8, tag);
      for (int t = 41; t <= 56; ++t) iss(t - 25);
      wait_vm<8>(); bar();
      wait_vm<0>(); bar();
    } else {
      bar();
      const rsrc_t ro = mkrsrc(ws + WS_O + (size_t)(rb * 128 + 4 * head) * 16384, 65536);
      for (int t = 1; t <= 25; t += 2) {
        const unsigned o = (t >> 1) * 5120;
        const u32x4 v0 = ldsr(pl + o), v1 = ldsr(pl + o + 1024), v2 = ldsr(pl + o + 2048), v3 = ldsr(pl + o + 3072), v4 = ldsr(pl + (t < 25 ? o + 4096 : 0));
        st16s<0>(v0, ro, lane * 16, o); st16s<1024>(v1, ro, lane * 16, o); st16s<2048>(v2, ro, lane * 16, o);
        if (t == 17) { wait_vm<11>(); signal<1>(ws, fo + dc * 8, tag); }
        bar();
        st16s<3072>(v3, ro, lane * 16, o); st16s<0>(v4, ro, lane * 16, o + 4096);
        bar();
      }
      bar(); bar(); bar();
      wait_vm<0>();
      signal<1>(ws, fo + 64 + dc * 8, tag);
      for (int t = 30; t <= 38; ++t) bar();
      signal<14>(ws, F_Y + (tile * 4 + (kvh ^ (lane & 3))) * 128 + ((lane & 3) - 1) * 8, tag);
      for (int t = 39; t <= 56; ++t) bar();
      peek(rf, pk, F_Y + (tile * 4 + kvh) * 128);
      bar();
      wait_vm<0>();
      peek_check(ws, pk, F_Y + (tile * 4 + kvh) * 128, 3, tag);
      bar();
    }
    u32x4 py[3][4];
#pragma unroll
    for (int k = 0; k < 3; ++k)
#pragma unroll
      for (int m = 0; m < 4; ++m) py[k][m] = __builtin_amdgcn_raw_buffer_load_b128(rxb, lane * 16 + m * 1024, kvh * 98304 + lw * 4096 + k * 16384, 16);
    const float bb = bflo(b2[rb * 256 + (4 * lw + kvh) * 16 + (lane & 15)]);
    bar();
    bar();
#pragma unroll
    for (int m = 0; m < 4; ++m) {
      f32x4 y0 = {bb, bb, bb, bb}, y1 = y0;
      add8(y0, y1, ldsr(pl + lw * 4096 + m * 1024));
#pragma unroll
      for (int k = 0; k < 3; ++k) add8(y0, y1, py[k][m]);
      *(LAS u32x4*)(size_t)(pl + lw * 4096 + m * 1024) = (u32x4){silu2(y0.xy), silu2(y0.zw), silu2(y1.xy), silu2(y1.zw)};
    }
    wait_lgkm0();
    bar();
  }
}

extern "C" void kernel_launch(void* const* d_in, const int* in_sizes, int n_in, void* d_out, int out_size, void* d_ws, size_t ws_size,
                              hipStream_t stream) {
  const u16* x = (const u16*)d_in[0];
  const u16* kc = (const u16*)d_in[1];
  const u16* vc = (const u16*)d_in[2];
  const u16* wq = (const u16*)d_in[3];
  const u16* wo = (const u16*)d_in[4];
  const u16* w1 = (const u16*)d_in[6];
  const u16* b1 = (const u16*)d_in[7];
  const u16* w2 = (const u16*)d_in[8];
  const u16* b2 = (const u16*)d_in[9];
  char* ws = (char*)d_ws;
  (void)hipFuncSetAttribute((const void*)k_main, hipFuncAttributeMaxDynamicSharedMemorySize, 163840);
  hipLaunchKernelGGL(k_main, dim3(256), dim3(512), 163840, stream, x, kc, vc, wq, wo, w1, b1, w2, b2, ws, (u16*)d_out);
}
```

```cpp
#include <hip/hip_runtime.h>
#include <hip/hip_bf16.h>

typedef unsigned short u16;
typedef short bf16x8 __attribute__((ext_vector_type(8)));
typedef short s16x4 __attribute__((ext_vector_type(4)));
typedef float f32x4 __attribute__((ext_vector_type(4)));
typedef float f32x2 __attribute__((ext_vector_type(2)));
typedef unsigned u32x4 __attribute__((ext_vector_type(4)));
typedef unsigned u32x2 __attribute__((ext_vector_type(2)));
typedef __bf16 bf16x2 __attribute__((ext_vector_type(2)));
typedef __amdgpu_buffer_rsrc_t rsrc_t;
#define LAS __attribute__((address_space(3)))
#define GAS __attribute__((address_space(1)))
#define DI __device__ __forceinline__
#define SGB(m, n) __builtin_amdgcn_sched_group_barrier(m, n, 0)
#define SB0() __builtin_amdgcn_sched_barrier(0)

constexpr int SEQ = 2048, DM = 1024, FF = 3584, HD = 4096;
constexpr int CODE_BYTES = 73728;
constexpr int NSP = 3;
constexpr size_t WS_FLAG = 0;
constexpr int F_EP = 0, F_X = 512, F_L = 2048, F_U = 4096, F_O = 12288, F_Y = 16384, F_S = 49152, F_P = 81920, F_END = 96256;
constexpr size_t WS_ST = 98304;
constexpr size_t WS_CS = 131072;
constexpr size_t WS_HT = 262144;
constexpr size_t WS_KV = WS_HT + 4194304;
constexpr size_t WS_O = WS_KV + 1048576;
constexpr size_t WS_U = WS_O + 16777216;
constexpr size_t WS_XB = WS_U + 7340032;
constexpr size_t WS_END = WS_XB + 25165824;

DI unsigned cvtpk(float lo, float hi) { f32x2 v = {lo, hi}; bf16x2 b = __builtin_convertvector(v, bf16x2); return __builtin_bit_cast(unsigned, b); }
DI float bflo(unsigned u) { return __uint_as_float(u << 16); }
DI float bfhi(unsigned u) { return __uint_as_float(u & 0xffff0000u); }
DI float rbf(float x) { return bflo(cvtpk(x, x)); }
template <int N> DI void wait_vm() { asm volatile("s_waitcnt vmcnt(%0)" ::"i"(N) : "memory"); }
DI void wait_lgkm0() { __builtin_amdgcn_s_waitcnt(0xC07F); }
DI void bar() { asm volatile("" ::: "memory"); __builtin_amdgcn_s_barrier(); asm volatile("" ::: "memory"); }
DI rsrc_t mkrsrc(const void* p, unsigned bytes) { return __builtin_amdgcn_make_buffer_rsrc((void*)p, 0, bytes, 0x00020000); }
DI void dma16(rsrc_t r, unsigned lds_addr, unsigned voff, unsigned soff) {
  asm volatile("s_mov_b32 m0, %0\n\ts_nop 0\n\tbuffer_load_dwordx4 %1, %2, %3 offen lds" ::"s"(lds_addr), "v"(voff), "s"(r), "s"(soff) : "memory");
}
DI void dma16s(rsrc_t r, unsigned lds_addr, unsigned voff, unsigned soff) {
  asm volatile("s_mov_b32 m0, %0\n\ts_nop 0\n\tbuffer_load_dwordx4 %1, %2, %3 offen sc1 lds" ::"s"(lds_addr), "v"(voff), "s"(r), "s"(soff) : "memory");
}
template <int OFF> DI void st16s(u32x4 v, rsrc_t r, unsigned voff, unsigned soff) {
  asm volatile("buffer_store_dwordx4 %0, %1, %2, %3 offen offset:%4 sc1\n\ts_nop 1" ::"v"(v), "v"(voff), "s"(r), "s"(soff), "i"(OFF) : "memory");
}
DI int lane_id() { int l; asm volatile("v_mbcnt_lo_u32_b32 %0, -1, 0\n\tv_mbcnt_hi_u32_b32 %0, -1, %0" : "=v"(l)); return l; }
struct Tag { unsigned lo, hi; };
DI void peek(rsrc_t rf, unsigned lds_addr, int off) {
  asm volatile("s_mov_b32 m0, %0\n\ts_nop 0\n\tbuffer_load_dword %3, %1, %2 offen sc1 lds" ::"s"(lds_addr), "s"(rf), "s"(off), "v"(lane_id() * 4) : "memory");
}
template <int GAP = 1> DI void spin_until(char* ws, int off, int n, Tag t) {
  const int ln = lane_id();
  GAS unsigned long long* p = (GAS unsigned long long*)(ws + WS_FLAG + off) + (ln < n ? ln : 0);
  const unsigned long long want = ((unsigned long long)t.hi << 32) | t.lo;
  for (unsigned sp = 0; sp < (1u << 20); ++sp) {
    if (__all(__hip_atomic_load(p, __ATOMIC_RELAXED, __HIP_MEMORY_SCOPE_AGENT) == want)) break;
    __builtin_amdgcn_s_sleep(GAP);
  }
  asm volatile("" ::: "memory");
}
DI void peek_check(char* ws, unsigned lds_addr, int off, int n, Tag t) {
  const int ln = lane_id();
  const unsigned v = *(LAS volatile unsigned*)(size_t)(lds_addr + ln * 4);
  if (!__all(ln >= 2 * n || v == ((ln & 1) ? t.hi : t.lo))) spin_until(ws, off, n, t);
}
template <long MASK> DI void signal(char* ws, int off, Tag t) {
  const u32x2 v = {t.lo, t.hi};
  asm volatile("s_mov_b64 exec, %3\n\tglobal_store_dwordx2 %0, %1, %2 sc1\n\ts_mov_b64 exec, -1" ::"v"(off), "v"(v), "s"(ws + WS_FLAG), "s"(MASK) : "memory");
}
DI void dma4k(rsrc_t r, unsigned lds_addr, unsigned voff, unsigned soff) {
  asm volatile("s_mov_b32 m0, %0\n\ts_nop 0\n\tbuffer_load_dwordx4 %1, %2, %3 offen sc1 lds\n\tbuffer_load_dwordx4 %1, %2, %3 offen offset:1024 sc1 lds\n\t"
               "buffer_load_dwordx4 %1, %2, %3 offen offset:2048 sc1 lds\n\tbuffer_load_dwordx4 %1, %2, %3 offen offset:3072 sc1 lds"
               ::"s"(lds_addr), "v"(voff), "s"(r), "s"(soff) : "memory");
}
template <int W> DI void pf_line(int wid, rsrc_t r, unsigned voff, unsigned soff) {
  asm volatile("s_cmp_lg_u32 %0, %4\n\ts_cbranch_scc1 1f\n\tbuffer_load_dword v255, %1, %2, %3 offen\n1:"
               ::"s"(wid), "v"(voff), "s"(r), "s"(soff), "i"(W) : "scc", "memory", "v255");
}
template <int CTRL> DI float dppf(float x) { return __builtin_bit_cast(float, __builtin_amdgcn_mov_dpp(__builtin_bit_cast(int, x), CTRL, 0xf, 0xf, true)); }
DI float wave_sum(float v) {
  v += dppf<0xB1>(v); v += dppf<0x4E>(v); v += dppf<0x141>(v); v += dppf<0x140>(v);
  auto a = __builtin_amdgcn_permlane16_swap(__float_as_uint(v), __float_as_uint(v), false, false);
  v = __uint_as_float(a[0]) + __uint_as_float(a[1]);
  auto b = __builtin_amdgcn_permlane32_swap(__float_as_uint(v), __float_as_uint(v), false, false);
  return __uint_as_float(b[0]) + __uint_as_float(b[1]);
}

DI f32x2 ln16(unsigned (&w)[8]) {
  f32x2 v[8];
#pragma unroll
  for (int i = 0; i < 8; ++i) v[i] = (f32x2){bflo(w[i]), bfhi(w[i])};
  const f32x2 s = ((v[0] + v[1]) + (v[2] + v[3])) + ((v[4] + v[5]) + (v[6] + v[7]));
  const float mean = wave_sum(s[0] + s[1]) * (1.f / 1024.f);
  f32x2 q0 = {0.f, 0.f}, q1 = q0;
#pragma unroll
  for (int i = 0; i < 8; i += 2) {
    const f32x2 d0 = v[i] - mean, d1 = v[i + 1] - mean;
    q0 = __builtin_elementwise_fma(d0, d0, q0); q1 = __builtin_elementwise_fma(d1, d1, q1);
  }
  q0 += q1;
  const float var = wave_sum(q0[0] + q0[1]) * (1.f / 1024.f);
  const float mb = rbf(mean);
  const float rs = __builtin_amdgcn_rcpf(rbf(__builtin_amdgcn_sqrtf(rbf(rbf(var) + 1e-5f))));
#pragma unroll
  for (int i = 0; i < 8; ++i) {
    const f32x2 d = v[i] - mb;
    const unsigned c = cvtpk(d[0], d[1]);
    const f32x2 r = (f32x2){bflo(c), bfhi(c)} * rs;
    w[i] = cvtpk(r[0], r[1]);
  }
  return (f32x2){mb, rs};
}

struct Frags { bf16x8 x[8]; bf16x8 w[4]; };
struct Addr { unsigned x, w[4]; };
template <int XS = 1024> DI void load_frags(Frags& f, const Addr& a) {
#pragma unroll
  for (int c = 0; c < 4; ++c) {
    s16x4 a0 = __builtin_amdgcn_ds_read_tr16_b64_v4i16((LAS s16x4*)(size_t)(a.w[c]));
    s16x4 a1 = __builtin_amdgcn_ds_read_tr16_b64_v4i16((LAS s16x4*)(size_t)(a.w[c] + 4096));
    f.w[c] = __builtin_shufflevector(a0, a1, 0, 1, 2, 3, 4, 5, 6, 7);
  }
#pragma unroll
  for (int i = 0; i < 8; ++i) f.x[i] = *(LAS bf16x8*)(size_t)(a.x + i * XS);
}
template <bool WA, bool Z> DI void mma_all(f32x4 (&acc)[8][4], const Frags& f, const f32x4* c0) {
  const f32x4 zero = {0.f, 0.f, 0.f, 0.f};
#pragma unroll
  for (int i = 0; i < 8; ++i)
#pragma unroll
    for (int jj = 0; jj < 4; ++jj) {
      const int j = (i & 1) ? 3 - jj : jj;
      if (WA) acc[i][j] = __builtin_amdgcn_mfma_f32_16x16x32_bf16(f.w[j], f.x[i], Z ? (c0 ? c0[j] : zero) : acc[i][j], 0, 0, 0);
      else    acc[i][j] = __builtin_amdgcn_mfma_f32_16x16x32_bf16(f.x[i], f.w[j], Z ? (c0 ? c0[j] : zero) : acc[i][j], 0, 0, 0);
    }
}
DI void addr_add(Addr& a, int d) {
  a.x += d;
#pragma unroll
  for (int c = 0; c < 4; ++c) a.w[c] += d;
}
template <bool WA, int NSLOT, int SLOTB, bool Z = false>
DI void gstep(f32x4 (&acc)[8][4], Frags& Fn, const Frags& Fc, Addr& a, int& slot, const f32x4* c0 = nullptr) {
  SB0();
  load_frags(Fn, a);
  mma_all<WA, Z>(acc, Fc, c0);
  slot = (slot + 1 == NSLOT) ? 0 : slot + 1;
  addr_add(a, (slot == 0) ? -(NSLOT - 1) * SLOTB : SLOTB);
  asm volatile("" : "+v"(a.x), "+v"(a.w[0]), "+v"(a.w[1]), "+v"(a.w[2]), "+v"(a.w[3]));
#pragma unroll
  for (int i = 0; i < 8; ++i) { SGB(0x8, 1); SGB(0x100, 1); SGB(0x8, 1); SGB(0x100, 1); SGB(0x8, 1); }
#pragma unroll
  for (int i = 0; i < 6; ++i) { SGB(0x8, 1); SGB(0x4, 1); SGB(0x2, 1); }
  SGB(0x8, 2);
  SB0();
  wait_lgkm0();
  bar();
}
template <int MODE, bool CS, bool Z = false>
DI void qstep(f32x4 (&acc)[8][4], f32x4 (&cs)[2], u32x4& ones, unsigned onext, Frags& Fn, const Frags& Fc, Addr& a, int& xs, int& wsl) {
  const f32x4 zero = {0.f, 0.f, 0.f, 0.f};
  SB0();
  load_frags<MODE == 2 ? 1024 : 2048>(Fn, a);
  if (CS) {
#pragma unroll
    for (int j = 0; j < 2; ++j) cs[j] = __builtin_amdgcn_mfma_f32_16x16x32_bf16(Fc.w[j], __builtin_bit_cast(bf16x8, ones), Z ? zero : cs[j], 0, 0, 0);
    ones = (u32x4){onext, onext, onext, onext};
    asm volatile("" : "+v"(ones));
  }
  mma_all<true, Z>(acc, Fc, nullptr);
  if (MODE == 2) addr_add(a, 24576);
  else {
    wsl = (wsl + 1) & 3;
    const int dw = wsl ? 8192 : -24576;
#pragma unroll
    for (int c = 0; c < 4; ++c) a.w[c] += dw;
    a.x ^= 64;
    if (MODE == 1) { xs = (xs == 2) ? 0 : xs + 1; a.x += xs ? 32768 : -65536; }
  }
  asm volatile("" : "+v"(a.x), "+v"(a.w[0]), "+v"(a.w[1]), "+v"(a.w[2]), "+v"(a.w[3]));
#pragma unroll
  for (int i = 0; i < 8; ++i) { SGB(0x8, 1); SGB(0x100, 1); SGB(0x8, 1); SGB(0x100, 1); SGB(0x8, 1); }
#pragma unroll
  for (int i = 0; i < 7; ++i) { SGB(0x8, 1); SGB(0x4, 1); SGB(0x2, 1); }
  if (CS) { SGB(0x8, 1); SGB(0x2, 2); SGB(0x8, 1); SGB(0x2, 2); }
  SGB(0x8, 1);
  SB0();
  wait_lgkm0();
  bar();
}
DI void addr_init(Addr& a, unsigned xbase, unsigned wbase, int wj, int cx, int lane) {
  const int g = lane >> 4, q = (lane >> 2) & 3, p = lane & 3, row = 4 * g + q;
  a.x = xbase + lane * 16;
#pragma unroll
  for (int c = 0; c < 4; ++c) a.w[c] = wbase + row * 256 + (((2 * (4 * wj + (c ^ cx)) + (p >> 1)) ^ ((row & 7) << 1)) * 16) + (p & 1) * 8;
}
template <bool WA, int NSLOT, int SLOTB> DI void gemm_steps(f32x4 (&acc)[8][4], Frags& F0, Frags& F1, Addr& a, int& slot, int n) {
  for (int t = 0; t < n; t += 2) {
    gstep<WA, NSLOT, SLOTB>(acc, F0, F1, a, slot);
    gstep<WA, NSLOT, SLOTB>(acc, F1, F0, a, slot);
  }
}

DI u32x4 pack8(const f32x4& a, const f32x4& b) { return (u32x4){cvtpk(a[0], a[1]), cvtpk(a[2], a[3]), cvtpk(b[0], b[1]), cvtpk(b[2], b[3])}; }
DI f32x2 gate2(f32x2 u, f32x2 z) {
  f32x2 e = {__builtin_amdgcn_exp2f(z[0]), __builtin_amdgcn_exp2f(z[1])};
  e += 1.f;
  return u * (f32x2){__builtin_amdgcn_rcpf(e[0]), __builtin_amdgcn_rcpf(e[1])};
}
DI unsigned gelu2(f32x2 u) {
  const f32x2 r = gate2(u, u * __builtin_elementwise_fma(u * u, (f32x2){-0.1029432397f, -0.1029432397f}, (f32x2){-2.302208198f, -2.302208198f}));
  return cvtpk(r[0], r[1]);
}
DI unsigned silu2(f32x2 y) { const f32x2 r = gate2(y, y * -1.4426950409f); return cvtpk(r[0], r[1]); }
DI u32x4 ldsr(unsigned a) { return *(LAS u32x4*)(size_t)a; }
DI void add8(f32x4& a, f32x4& b, const u32x4& w) {
  a += (f32x4){bflo(w[0]), bfhi(w[0]), bflo(w[1]), bfhi(w[1])};
  b += (f32x4){bflo(w[2]), bfhi(w[2]), bflo(w[3]), bfhi(w[3])};
}

struct Sc { f32x4 v[2][2]; };
struct Att { bf16x8 q[2][4]; f32x4 o[8][2]; f32x4 negm[2]; Sc sE, sO; u32x4 pO[2]; bf16x8 pre[4], ones; f32x4 l[2]; };
DI float xg_max(float m) {
  auto a = __builtin_amdgcn_permlane16_swap(__float_as_uint(m), __float_as_uint(m), false, false);
  m = fmaxf(__uint_as_float(a[0]), __uint_as_float(a[1]));
  auto b = __builtin_amdgcn_permlane32_swap(__float_as_uint(m), __float_as_uint(m), false, false);
  return fmaxf(__uint_as_float(b[0]), __uint_as_float(b[1]));
}
DI float row_max(const Sc& s, int qb) {
  const f32x4 a = s.v[0][qb], b = s.v[1][qb];
  return xg_max(fmaxf(fmaxf(fmaxf(a[0], a[1]), fmaxf(a[2], a[3])), fmaxf(fmaxf(b[0], b[1]), fmaxf(b[2], b[3]))));
}
DI bf16x8 ldfrag(unsigned a) { return *(LAS bf16x8*)(size_t)a; }
DI void sm1(Sc& s, int e, u32x4 (&w)[2], int part = 3) {
  if (e < 16 && (part & 1)) {
    float x = __builtin_amdgcn_exp2f(s.v[e >> 3][(e >> 2) & 1][e & 3]);
    asm volatile("" : "+v"(x));
    s.v[e >> 3][(e >> 2) & 1][e & 3] = x;
  }
  if (e >= 2 && e <= 16 && !(e & 1) && (part & 2)) {
    const int kt = (e - 1) >> 3, qb = ((e - 1) >> 2) & 1, r = (e - 1) & 3;
    unsigned c = cvtpk(s.v[kt][qb][r - 1], s.v[kt][qb][r]);
    asm volatile("" : "+v"(c));
    w[qb][2 * kt + (r >> 1)] = c;
  }
}
#define SETPRIO(x) __builtin_amdgcn_s_setprio(x)
#define MFMA16(a, b, c) __builtin_amdgcn_mfma_f32_16x16x32_bf16(a, b, c, 0, 0, 0)
#define PFRAG(w) __builtin_bit_cast(bf16x8, w)
DI void mfma_acc(f32x4& c, const bf16x8& a, const u32x4& b) { asm volatile("v_mfma_f32_16x16x32_bf16 %0, %1, %2, %0" : "+v"(c) : "v"(a), "v"(b)); }
template <int POS, int n, class F> DI void att_slot(Att& A, bf16x8 (&f)[36], u32x4 (&pE)[2], unsigned kv, unsigned kvn, F& gap) {
  constexpr int u = n >> 3, k = n & 7;
  constexpr bool isqk = (u & 1) ? (u == 1 || POS < 2) : (u == 0 && POS == 0);
  const f32x4 zero = {0.f, 0.f, 0.f, 0.f};
  if constexpr (k == 0) SETPRIO(3 - u);
#pragma unroll
  for (int qb = 0; qb < 2; ++qb) {
    if constexpr (isqk && (k & 3) != 0) mfma_acc(u == 1 ? A.sO.v[k >> 2][qb] : A.sE.v[k >> 2][qb], f[n], __builtin_bit_cast(u32x4, A.q[qb][k & 3]));
    else if constexpr (isqk && u == 1) A.sO.v[k >> 2][qb] = MFMA16(f[n], A.q[qb][0], A.negm[qb]);
    else if constexpr (isqk) A.sE.v[k >> 2][qb] = MFMA16(f[n], A.q[qb][0], u ? A.negm[qb] : zero);
    else if constexpr (POS == 0) A.o[k][qb] = MFMA16(f[n], PFRAG(pE[qb]), zero);
    else if constexpr (u == 2) mfma_acc(A.o[k][qb], f[n], pE[qb]);
    else mfma_acc(A.o[k][qb], f[n], A.pO[qb]);
    SB0();
    if (qb == 1) {
      if constexpr (n < 28) f[n + 4] = ldfrag(kv + (n + 4) * 1024);
      else if constexpr (POS < 2) f[n + 4] = ldfrag(kvn + (n - 28) * 1024);
      SB0();
    }
    if constexpr (POS == 0 && u == 1) {
      if (k == 0 && qb == 0) sm1(A.sE, 0, pE);
      sm1(A.sE, 2 * k + 1 + qb, pE);
    }
    if constexpr (POS > 0 && n <= 14) sm1(A.sE, n + 2, pE, 1 + qb);
    if constexpr (n >= 14 && n <= (POS < 2 ? 30 : 15)) sm1(A.sO, n - 14, A.pO, 1 + qb);
    if constexpr (POS < 2 && n >= 30) sm1(A.sE, n - 30, pE, 1 + qb);
    if constexpr (POS == 2 && u == 2) sm1(A.sO, 2 + 2 * k + qb, A.pO);
    SB0();
  }
  if constexpr (!isqk && (k == 2 || k == 4)) {
    constexpr int qb = k == 4;
    if constexpr (POS == 0) A.l[qb] = MFMA16(A.ones, PFRAG(pE[qb]), zero);
    else if constexpr (u == 2) mfma_acc(A.l[qb], A.ones, pE[qb]);
    else mfma_acc(A.l[qb], A.ones, A.pO[qb]);
  }
  gap(n);
  SB0();
  if constexpr (POS == 2 && n == 23) { asm volatile("s_nop 1"); SB0(); }
  if constexpr ((POS == 2 && n == 31) || (POS == 0 && n == 7)) { asm volatile("s_nop 9"); SB0(); }
  if constexpr (POS == 0 && n == 7) {
#pragma unroll
    for (int qb = 0; qb < 2; ++qb) {
      const float mx = row_max(A.sE, qb);
      A.sE.v[0][qb] -= mx; A.sE.v[1][qb] -= mx;
      A.negm[qb] = (f32x4){-mx, -mx, -mx, -mx};
    }
    SB0();
  }
  if constexpr (n < 31) att_slot<POS, n + 1>(A, f, pE, kv, kvn, gap);
}
template <int POS, class F> DI void att_tile(Att& A, unsigned kv, unsigned kvn, F&& gap) {
  bf16x8 f[36];
  u32x4 pE[2];
  asm volatile("" : "+v"(kv), "+v"(kvn));
#pragma unroll
  for (int i = 0; i < 4; ++i) f[i] = A.pre[i];
  SB0();
  att_slot<POS, 0>(A, f, pE, kv, kvn, gap);
  if (POS < 2) {
#pragma unroll
    for (int i = 0; i < 4; ++i) A.pre[i] = f[32 + i];
  }
}
DI void att_redo(Att& A, rsrc_t rkv, unsigned vo) {
  const f32x4 zero = {0.f, 0.f, 0.f, 0.f};
  float m[2] = {-3.0e38f, -3.0e38f};
  A.l[0] = A.l[1] = zero;
#pragma unroll
  for (int d = 0; d < 8; ++d) A.o[d][0] = A.o[d][1] = zero;
  for (int blk = 0; blk < 16; ++blk) {
    const int kb = (blk ? 2 * blk - 1 : 0) * 8192, vb = (blk < 15 ? 2 * blk + 2 : 31) * 8192;
    Sc s;
#pragma unroll
    for (int n = 0; n < 8; ++n) {
      const bf16x8 kf = PFRAG(__builtin_amdgcn_raw_buffer_load_b128(rkv, vo + n * 1024, kb, 16));
#pragma unroll
      for (int qb = 0; qb < 2; ++qb) s.v[n >> 2][qb] = MFMA16(kf, A.q[qb][n & 3], (n & 3) ? s.v[n >> 2][qb] : zero);
    }
#pragma unroll
    for (int qb = 0; qb < 2; ++qb) {
      const float mn = fmaxf(m[qb], row_max(s, qb)), fct = __builtin_amdgcn_exp2f(m[qb] - mn);
      m[qb] = mn;
      A.l[qb] *= fct;
      s.v[0][qb] -= mn; s.v[1][qb] -= mn;
#pragma unroll
      for (int d = 0; d < 8; ++d) A.o[d][qb] *= fct;
    }
    u32x4 p[2];
#pragma unroll
    for (int i = 0; i < 17; ++i) sm1(s, i, p);
#pragma unroll
    for (int qb = 0; qb < 2; ++qb) A.l[qb] = MFMA16(A.ones, PFRAG(p[qb]), A.l[qb]);
#pragma unroll
    for (int n = 0; n < 8; ++n) {
      const bf16x8 vf = PFRAG(__builtin_amdgcn_raw_buffer_load_b128(rkv, vo + n * 1024, vb, 16));
#pragma unroll
      for (int qb = 0; qb < 2; ++qb) A.o[n][qb] = MFMA16(vf, PFRAG(p[qb]), A.o[n][qb]);
    }
  }
}

__global__ __launch_bounds__(512, 2) __attribute__((amdgpu_num_vgpr(127))) void k_main(const u16* __restrict__ x, const u16* __restrict__ kc, const u16* __restrict__ vc, const u16* __restrict__ wq, const u16* __restrict__ wo,
                                                 const u16* __restrict__ w1, const u16* __restrict__ b1, const u16* __restrict__ w2,
                                                 const u16* __restrict__ b2, char* __restrict__ ws, u16* __restrict__ out) {
  extern __shared__ __attribute__((aligned(16))) char smem[];
  LAS char* lds = (LAS char*)smem;
  const unsigned lb = (unsigned)(size_t)lds;
  const int tid = threadIdx.x, wid = __builtin_amdgcn_readfirstlane(tid >> 6);
  const int g8 = blockIdx.x & 7, idx = blockIdx.x >> 3;
  const rsrc_t rcode = mkrsrc((const void*)(__builtin_amdgcn_s_getpc() & ~255ull), CODE_BYTES);
  const unsigned code_o = (idx % ((CODE_BYTES + 8191) / 8192)) * 8192;
  const bool hasU = idx < 28;
  const int uh = g8 & 1, dq = idx & 3, ft = 7 * (g8 >> 1) + (idx >> 2), ut = ft * 4 + dq;
  const int kvh = g8 >> 1, head = 8 * kvh + (idx & 7), rb = 4 * (g8 & 1) + (idx >> 3);
  const int dc = idx & 7, tile = rb * 8 + dc;
  const int lw = wid - 4, wi = (wid >> 1) & 1, wj = wid & 1;
  constexpr int QS = 24576;
  constexpr int PARK = 98304;
  const rsrc_t rf = mkrsrc(ws + WS_FLAG, F_END + 256);
  const rsrc_t rkv = mkrsrc(ws + WS_KV + (size_t)kvh * 262144, 262144);
  const rsrc_t rxb = mkrsrc(ws + WS_XB + (size_t)tile * 393216, 393216);
  struct Strm { rsrc_t r; unsigned v0, v1, step, p0, p1, p2; int rot; };
  auto strm_x = [&](Strm& S, const char* base, unsigned bytes, unsigned step, int xr) {
    const int ln = lane_id();
    S.r = mkrsrc(base, bytes); S.v0 = ln * 16 + (xr & 1) * 1024; S.v1 = S.v0 ^ 1024; S.step = step;
    S.p0 = (xr >> 1) * 2048; S.p1 = S.p0 ^ 2048; S.p2 = 4096; S.rot = 0;
  };
  auto strm_w = [&](Strm& S, const char* base, unsigned rows, unsigned ld, bool nat) {
    const int ln = lane_id(), q4 = ln >> 4;
    const unsigned c0 = ((ln & 15) ^ (q4 << 1)) * 16;
    S.r = mkrsrc(base, (rows - 1) * ld + 256); S.v0 = q4 * ld + c0; S.v1 = ((nat ? 8 : 4) + q4) * ld + (c0 ^ 128); S.step = 32 * ld;
    S.p0 = 0; S.p1 = (nat ? 16 : 8) * ld; S.p2 = (nat ? 4 : 16) * ld; S.rot = 0;
  };
  auto issue = [&](const Strm& S, int k, int src = -1) {
    const unsigned s = lb + (k & 3) * QS + lw * 8192;
    if (src < 0) src = (k + S.rot) & 31;
#pragma unroll
    for (int i = 0; i < 4; ++i) {
      const unsigned so = src * S.step + ((i & 1) ? S.p1 : S.p0) + (i >> 1) * S.p2;
      dma16s(S.r, s + i * 2048, S.v0, so); dma16s(S.r, s + i * 2048 + 1024, S.v1, so);
    }
  };
  constexpr int QW = 98304, HST = 131072, HRS = 2112, RST = HST + 8 * HRS, PKQ = 148480, STT = 151552, CSX = 153600;
  const int lane0 = tid & 63;
  const rsrc_t rq = lw < 2 ? mkrsrc(x + (size_t)(rb * 256 + lw * 128) * DM, 128 * DM * 2) : mkrsrc((const char*)wq + head * 256, (DM - 1) * HD * 2 + 256);
  const unsigned qv0 = lw < 2 ? (lane0 >> 3) * 2048 + (((lane0 & 7) ^ (lane0 >> 4)) * 16) : (lane0 >> 4) * (HD * 2) + (((lane0 & 15) ^ ((lane0 >> 4) << 1)) * 16);
  const unsigned qv1 = lw < 2 ? (qv0 ^ 64) + 8 * 2048 : (qv0 ^ 128) + 8 * (HD * 2);
  const bool qx = lw < 2;
  const unsigned qp1 = qx ? 32768 : 16 * HD * 2, qp2 = qx ? 65536 : 4 * HD * 2;
  const unsigned qlb = lb + (qx ? lw * 16384 : QW), qlw = qx ? 98304 : 32768, qom = qx ? 0x20780 : 0x7c0000;
  unsigned ql = qx ? 65536 : 0, qdl = 8192, qo = rb * (qx ? 256 : 4 * 32 * HD * 2), qdo = qx ? 131072 : 32 * HD * 2;
  auto issq = [&]() {
    const unsigned s = qlb + ql;
    dma16(rq, s, qv0, qo); dma16(rq, s + 1024, qv1, qo);
    dma16(rq, s + 2048, qv0, qo + qp1); dma16(rq, s + 3072, qv1, qo + qp1);
    dma16(rq, s + 4096, qv0, qo + qp2); dma16(rq, s + 5120, qv1, qo + qp2);
    dma16(rq, s + 6144, qv0, qo + qp1 + qp2); dma16(rq, s + 7168, qv1, qo + qp1 + qp2);
    ql += qdl; ql = ql >= qlw ? ql - qlw : ql; qdl ^= qx ? (8192 ^ 24576) : 0;
    qo = (qo + qdo) & qom; qdo ^= qx ? 128 : 0;
  };
  auto h_out = [&](int it) {
    const rsrc_t rh = mkrsrc(ws + WS_HT + (size_t)(blockIdx.x >> 2) * 16384 + (blockIdx.x & 3) * 256, 3 * 1048576 + 16384);
    const unsigned a0 = HST + ((lane0 >> 2) & 3) * HRS + (it * 4 + (lane0 >> 4)) * 32 + (lane0 & 3) * 8;
    s16x4 lo = __builtin_amdgcn_ds_read_tr16_b64_v4i16((LAS s16x4*)(lds + a0));
    s16x4 hi = __builtin_amdgcn_ds_read_tr16_b64_v4i16((LAS s16x4*)(lds + a0 + 4 * HRS));
    st16s<0>(__builtin_bit_cast(u32x4, __builtin_shufflevector(lo, hi, 0, 1, 2, 3, 4, 5, 6, 7)), rh, (lane0 >> 4) * 1024 + (lane0 & 15) * 16,
             (it >> 2) * 1048576 + (it & 3) * 4096);
  };
  auto st_out = [&]() { st16s<0>(*(LAS u32x4*)(lds + RST + (lane0 & 3) * 16), mkrsrc(ws + WS_ST + blockIdx.x * 64, 64), lane0 * 16, 0); };
  Tag tag;
  {
    const int b = blockIdx.x, lane = lane0;
    u32x4 A, B;
    u32x2 ep;
    asm volatile("global_load_dwordx4 %0, %3, %4\n\tglobal_load_dwordx4 %1, %3, %4 offset:1024\n\tglobal_load_dwordx2 %2, %5, %6 sc1"
                 : "=&v"(A), "=&v"(B), "=&v"(ep) : "v"(lane * 16), "s"(x + (size_t)(8 * b + wid) * DM), "v"(F_EP), "s"(ws + WS_FLAG) : "memory");
    pf_line<7>(wid, rcode, lane * 128, code_o);
    if (wid >= 4 && wid < 7) {
      issq(); issq();
      if (lw == 2) { issq(); issq(); wait_vm<32>(); } else wait_vm<16>();
    } else if (wid == 7) wait_vm<1>(); else wait_vm<0>();
    asm volatile("" : "+v"(A), "+v"(B), "+v"(ep));
    unsigned v[8] = {A[0], A[1], A[2], A[3], B[0], B[1], B[2], B[3]};
    const unsigned long long e64 = ((unsigned long long)(unsigned)__builtin_amdgcn_readfirstlane(ep[1]) << 32) | (unsigned)__builtin_amdgcn_readfirstlane(ep[0]);
    const unsigned long long tg = e64 * 0x9E3779B97F4A7C15ull + 0x632BE59BD9B4E019ull;
    tag.lo = (unsigned)tg; tag.hi = (unsigned)(tg >> 32);
    *(LAS f32x2*)(lds + RST + wid * 8) = ln16(v);
    ln16(v);
    *(LAS u32x4*)(lds + HST + wid * HRS + lane * 16) = (u32x4){v[0], v[1], v[2], v[3]};
    *(LAS u32x4*)(lds + HST + wid * HRS + 1024 + lane * 16) = (u32x4){v[4], v[5], v[6], v[7]};
    wait_lgkm0();
  }
  bf16x8 qh[8];
  f32x4 uk[4][4];
  auto kv8 = [&](int t, int slot) {
    const unsigned v = lane_id() * 16;
    dma4k(rkv, lb + slot * 32768 + lw * 8192, v, t * 32768 + lw * 8192);
    dma4k(rkv, lb + slot * 32768 + lw * 8192 + 4096, v, t * 32768 + lw * 8192 + 4096);
  };
  if (wid < 4) {
    f32x4 acc[8][4], cs[2];
    Frags F0, F1;
    Addr a;
    int xs = 2, wsl = 1;
    {
      const int ln = lane_id(), r = ln & 15;
      addr_init(a, 0, lb + QW, wj, 2 * wi, ln);
      a.x = lb + 65536 + wi * 16384 + r * 128 + (((ln >> 4) ^ (r >> 1)) * 16);
    }
    bar();
    load_frags<2048>(F0, a);
    a.x ^= 64;
#pragma unroll
    for (int c = 0; c < 4; ++c) a.w[c] += 8192;
    wait_lgkm0();
    bar();
    {
      const unsigned o1 = 0x3f803f80;
      u32x4 ones = {o1, o1, o1, o1};
      qstep<1, true, true>(acc, cs, ones, o1, F1, F0, a, xs, wsl);
      for (int t = 0; t < 2; ++t) {
        const unsigned on = t == 0 ? o1 : 0;
        qstep<0, true>(acc, cs, ones, on, F0, F1, a, xs, wsl);
        qstep<1, true>(acc, cs, ones, on, F1, F0, a, xs, wsl);
      }
      const int ln = lane_id();
      const unsigned csa = lb + CSX + ((ln & 15) ? 7168 : (64 * wj + 32 * wi) * 4) + (ln & 48);
      *(LAS f32x4*)(size_t)csa = cs[0];
      *(LAS f32x4*)(size_t)(csa + 64) = cs[1];
      for (int t = 0; t < 13; ++t) { qstep<0, false>(acc, cs, ones, 0, F0, F1, a, xs, wsl); qstep<1, false>(acc, cs, ones, 0, F1, F0, a, xs, wsl); }
      addr_init(a, lb + wi * 8192, lb + 16384, wj, 0, lane_id());
      qstep<2, false>(acc, cs, ones, 0, F0, F1, a, xs, wsl);
    }
    f32x4 c0[4];
    {
      const int ln = lane_id(), g = ln >> 4;
      u32x2 bv[4];
#pragma unroll
      for (int j = 0; j < 4; ++j) bv[j] = *(LAS u32x2*)(lds + CSX + 5120 + (64 * wj + 16 * j + 4 * g) * 2);
      f32x4 cq[4];
#pragma unroll
      for (int j = 0; j < 4; ++j) cq[j] = *(LAS f32x4*)(lds + CSX + 512 + (64 * wj + 16 * (j ^ (2 * wi)) + 4 * g) * 4);
      f32x2 st[8];
#pragma unroll
      for (int i = 0; i < 8; ++i) st[i] = *(LAS f32x2*)(lds + STT + (128 * wi + 16 * i + (ln & 15)) * 8);
      wait_lgkm0();
      bar();
      const unsigned qa = lb + 98304 + wi * 32768 + (2 * wj + wi) * 1024 + ln * 16, qb = qa ^ 1024;
#pragma unroll
      for (int i = 0; i < 8; ++i) {
        const float sc = st[i][1] * 0.12751743082f, nm = -st[i][0];
#pragma unroll
        for (int mm = 0; mm < 2; ++mm)
          *(LAS u32x4*)(size_t)((mm ? qb : qa) + i * 4096) =
              pack8((acc[i][2 * mm] + cq[2 * mm] * nm) * sc, (acc[i][2 * mm + 1] + cq[2 * mm + 1] * nm) * sc);
      }
#pragma unroll
      for (int j = 0; j < 4; ++j) c0[j] = uh ? (f32x4){0.f, 0.f, 0.f, 0.f} : (f32x4){bflo(bv[j][0]), bfhi(bv[j][0]), bflo(bv[j][1]), bfhi(bv[j][1])};
    }
    if (hasU) {
      int slot = 1;
      gstep<true, 4, QS, true>(acc, F1, F0, a, slot, c0);
      gemm_steps<true, 4, QS>(acc, F0, F1, a, slot, 30);
      gstep<true, 4, QS>(acc, F0, F1, a, slot);
      const rsrc_t rux = mkrsrc(ws + WS_XB + (size_t)((tile ^ 32) * 4 + kvh) * 98304 + 49152 + wid * 8192, 8192);
      const int ln = lane_id();
#pragma unroll
      for (int ii = 0; ii < 4; ++ii) {
        st16s<0>(pack8(acc[2 * ii + 1][0], acc[2 * ii + 1][1]), rux, ln * 16, ii * 2048);
        st16s<1024>(pack8(acc[2 * ii + 1][2], acc[2 * ii + 1][3]), rux, ln * 16, ii * 2048);
#pragma unroll
        for (int j = 0; j < 4; ++j) uk[ii][j] = acc[2 * ii][j];
      }
    }
  } else {
    const int lane = lane0;
    Strm U;
    if (lw < 2) strm_x(U, ws + WS_HT + (size_t)dq * 1048576 + uh * 524288 + lw * 8192, 524288 - lw * 8192, 16384, uh);
    else strm_w(U, (const char*)w1 + (size_t)uh * 1024 * (FF * 2) + ft * 256, 1024, FF * 2, true);
    if (!hasU) U.r = mkrsrc(ws, 0);
    U.rot = 8 * dq;
    const unsigned pk = lb + PKQ + lw * 512;
    if (lw < 3) {
      if (lw < 2) wait_vm<0>(); else wait_vm<24>();
      bar();
      if (lw < 2) { issq(); issq(); } else wait_vm<16>();
      bar();
      if (lw < 2) issq();
      for (int t = 1; t <= 21; ++t) { issq(); wait_vm<16>(); bar(); }
      peek(rf, pk, F_L + lw * 512); peek(rf, pk + 256, F_L + lw * 512 + 256);
      for (int t = 22; t <= 26; ++t) { issq(); wait_vm<16>(); bar(); }
      issq(); wait_vm<16>();
    } else {
      peek(mkrsrc(b1, FF * 2), lb + CSX + 5120, ft * 256);
      bar();
      for (int t = 0; t <= 7; ++t) { h_out(2 * t); h_out(2 * t + 1); bar(); }
      st_out();
      st16s<0>(*(LAS u32x4*)(lds + CSX + (lane & 31) * 16), mkrsrc(ws + WS_CS + (head * 8 + rb) * 512, 512), lane * 16, 0);
      bar(); bar(); bar();
      wait_vm<0>();
      signal<1>(ws, F_L + blockIdx.x * 8, tag);
      for (int t = 11; t <= 21; ++t) bar();
      peek(rf, pk, F_L + lw * 512); peek(rf, pk + 256, F_L + lw * 512 + 256);
      for (int t = 22; t <= 26; ++t) bar();
      wait_vm<0>();
    }
    peek_check(ws, pk, F_L + lw * 512, 32, tag); peek_check(ws, pk + 256, F_L + lw * 512 + 256, 32, tag);
    bar();
    if (blockIdx.x == 0 && lw == 3) signal<1>(ws, F_EP, tag);
    if (lw == 2) issq();
    if (lw == 3) {
      const rsrc_t rs = mkrsrc(ws + WS_ST + rb * 2048, 2048);
      dma16s(rs, lb + STT, lane * 16, 0); dma16s(rs, lb + STT + 1024, lane * 16, 1024);
      dma4k(mkrsrc(ws + WS_CS + head * 4096, 4096), lb + CSX + 1024, lane * 16, 0);
    }
    const bool us = lw < 3;
    if (us) issue(U, 0);
    wait_vm<16>(); bar();
    if (us) wait_vm<8>(); else wait_vm<0>();
    bar();
    if (us) issue(U, 1);
    else {
      const LAS f32x2* cp = (const LAS f32x2*)(lds + CSX + 1024) + lane;
      *(LAS f32x2*)(lds + CSX + 512 + lane * 8) = ((cp[0] + cp[64]) + (cp[128] + cp[192])) + ((cp[256] + cp[320]) + (cp[384] + cp[448]));
      wait_lgkm0();
    }
    bar();
    wait_vm<8>(); bar();
    if (us) issue(U, 2);
    wait_vm<8>();
    bar();
    bar();
    if (hasU) {
      if (us) { issue(U, 3); for (int t = 1; t <= 28; ++t) { issue(U, t + 3); wait_vm<16>(); bar(); } }
      else {
        for (int t = 1; t <= 26; ++t) bar();
        spin_until(ws, F_X + kvh * 128, 8, tag);
        pf_line<7>(wid, rcode, lane * 128, code_o);
        bar(); bar();
      }
      wait_vm<8>(); bar();
      kv8(0, 0); wait_vm<8>(); bar();
      kv8(1, 1); bar();
      kv8(2, 2); wait_vm<8>(); bar();
    }
  }
  if (!hasU) {
    const int sp = g8 * 4 + (idx - 28), lane = lane_id(), g = lane >> 4;
    unsigned e[4][8];
#pragma unroll
    for (int i = 0; i < 4; ++i) {
      const int kh = sp >> 3, unit = (sp & 7) * 4 + i, frag = wid;
      const bool isK = unit == 0 || ((unit & 1) && unit < 31);
      const u16* sk = kc + ((size_t)kh * SEQ + 1536 + 32 * ((unit + 1) >> 1) + 16 * (frag >> 2) + (lane & 15)) * 128 + 32 * (frag & 3) + 4 * g;
      const u16* sv = vc + ((size_t)kh * SEQ + 1536 + 32 * ((unit - 1) >> 1) + 4 * g) * 128 + 16 * frag + (lane & 15);
      const u16* src = isK ? sk : sv;
      const int s1 = isK ? 1 : 128, s4 = isK ? 16 : 2048;
#pragma unroll
      for (int j2 = 0; j2 < 8; ++j2) e[i][j2] = src[(j2 >> 2) * s4 + (j2 & 3) * s1];
    }
#pragma unroll
    for (int i = 0; i < 4; ++i)
      st16s<0>((u32x4){e[i][0] | (e[i][1] << 16), e[i][2] | (e[i][3] << 16), e[i][4] | (e[i][5] << 16), e[i][6] | (e[i][7] << 16)},
               mkrsrc(ws + WS_KV, 1048576), (wid * 64 + lane) * 16, (sp * 4 + i) * 8192);
    wait_vm<0>();
    wait_lgkm0();
    bar();
    if (wid == 0) signal<1>(ws, F_X + (sp >> 3) * 128 + (sp & 7) * 8, tag);
    unsigned pw[NSP];
#pragma unroll
    for (int i = 0; i < NSP; ++i) pw[i] = *(const unsigned*)((const char*)w2 + (size_t)i * 2097152 + (size_t)(sp * 512 + wid * 64 + lane) * 128);
#pragma unroll
    for (int i = 0; i < NSP; ++i) asm volatile("" ::"v"(pw[i]));
    if (wid >= 4) { spin_until(ws, F_X + kvh * 128, 8, tag); kv8(0, 0); kv8(1, 1); kv8(2, 2); wait_vm<8>(); }
    bar();
  }
  {
    const int ln = lane_id();
#pragma unroll
    for (int ks = 0; ks < 8; ++ks) qh[ks] = *(LAS bf16x8*)(lds + 98304 + wid * 8192 + ks * 1024 + ln * 16);
  }
  Strm Y;
  auto y_iss = [&](int k) { const int r = k + 7 * (dc & 3); issue(Y, k, r >= 28 ? r - 28 : r); };
  Frags FY;
  Addr aY;
  {
    const int lane = lane_id();
    Att A;
#pragma unroll
    for (int ks = 0; ks < 8; ++ks) A.q[ks >> 2][ks & 3] = qh[ks];
    A.ones = (bf16x8){0x3f80, 0x3f80, 0x3f80, 0x3f80, 0x3f80, 0x3f80, 0x3f80, 0x3f80};
    asm volatile("" : "+v"(A.ones));
    const unsigned kvl = lb + lane * 16;
#pragma unroll
    for (int i = 0; i < 4; ++i) A.pre[i] = ldfrag(kvl + i * 1024);
    const bool strm = wid >= 4 && wid < 7, hw = wid >= 4;
    const rsrc_t rp2 = mkrsrc(w2, SEQ * FF * 2), rpo = mkrsrc(wo, HD * DM * 2);
    const unsigned pv = (blockIdx.x * 64 + lane) * 128;
#define PF(W, R, C) pf_line<W>(wid, R, pv, (C) * 2097152u)
    const int fp = F_P + (ut * 2 + uh) * 64 + (wid & 3) * 8;
    const bool cu = !hw && hasU;
    att_tile<0>(A, kvl + 0 * 32768, kvl + 1 * 32768, [&](int n) __attribute__((always_inline)) { if (n == 20 && cu) { wait_vm<0>(); signal<1>(ws, fp ^ 64, tag); } });
    if (hw) wait_vm<0>();
    bar();
    att_tile<1>(A, kvl + 1 * 32768, kvl + 2 * 32768, [&](int n) __attribute__((always_inline)) {
      if (n == 0 && hw) kv8(3, 3); if (n == 12 && hw) kv8(4, 0); if (n == 28 && hw) peek(rf, lb + 131072 + lw * 256, fp); if (n == 4) PF(0, rp2, 3); if (n == 9) PF(1, rp2, 4); });
    if (hw) wait_vm<9>();
    bar();
    att_tile<1>(A, kvl + 2 * 32768, kvl + 3 * 32768, [&](int n) __attribute__((always_inline)) {
      if (n == 8 && hw) {
        wait_vm<0>();
        if (hasU) peek_check(ws, lb + 131072 + lw * 256, fp, 1, tag);
        const rsrc_t rux = mkrsrc(ws + WS_XB + (size_t)(tile * 4 + kvh) * 98304 + 49152, hasU ? 32768 : 0);
        dma4k(rux, lb + 32768 + lw * 8192, lane * 16, lw * 8192); dma4k(rux, lb + 32768 + lw * 8192 + 4096, lane * 16, lw * 8192 + 4096);
      }
      if (n == 27) PF(3, rp2, 5); });
    if (hw) wait_vm<0>();
    bar();
    if (hw) { kv8(5, 4); kv8(7, 2); }
    if (cu) {
      u32x4 p[8];
      p[0] = ldsr(kvl + 32768 + wid * 8192);
#pragma unroll
      for (int k = 0; k < 8; ++k) {
        if (k < 7) p[k + 1] = ldsr(kvl + 32768 + wid * 8192 + (k + 1) * 1024);
        f32x4 a = uk[k >> 1][2 * (k & 1)], b = uk[k >> 1][2 * (k & 1) + 1];
        add8(a, b, p[k]);
        p[k] = (u32x4){gelu2(a.xy), gelu2(a.zw), gelu2(b.xy), gelu2(b.zw)};
        asm volatile("" : "+v"(p[k]));
        if (k == 0) PF(2, rp2, 6);
        SB0();
      }
      bar();
      const rsrc_t ru = mkrsrc(ws + WS_U + (size_t)((2 * dq + wi) * 112 + 4 * ft + 2 * wj) * 8192 + uh * 1024, 16384);
#pragma unroll
      for (int k = 0; k < 8; ++k) st16s<0>(p[k], ru, lane * 16, (k & 1) * 8192 + (k >> 1) * 2048);
    } else { PF(2, rp2, 6); bar(); }
    att_tile<1>(A, kvl + 3 * 32768, kvl + 0 * 32768, [&](int n) __attribute__((always_inline)) {
      if (n == 20 && cu) { wait_vm<0>(); signal<1>(ws, F_U + ((2 * dq + wi) * 4 + kvh) * 256 + (((idx >> 2) * 2 + uh) * 2 + wj) * 8, tag); } });
    if (hw) wait_vm<0>();
    bar();
    att_tile<1>(A, kvl + 0 * 32768, kvl + 4 * 32768, [&](int n) __attribute__((always_inline)) { if (n == 0 && hw) kv8(6, 3); if (n == 6) PF(3, rpo, 0); if (n == 15) PF(0, rpo, 1); if (n == 24) PF(1, rpo, 2); });
    if (hw) wait_vm<0>();
    bar();
    att_tile<1>(A, kvl + 4 * 32768, kvl + 3 * 32768, [&](int n) __attribute__((always_inline)) { if (n == 3) PF(2, rpo, 3); });
    if (lw == 0) strm_x(Y, ws + WS_U + (size_t)(dc * 112 + 28 * kvh) * 8192, 28 * 8192, 8192, 0);
    else strm_w(Y, (const char*)w2 + (size_t)kvh * 896 * (SEQ * 2) + rb * 512 + ((lw - 1) & 1) * 256, 896, SEQ * 2, false);
    bar();
    att_tile<1>(A, kvl + 3 * 32768, kvl + 2 * 32768, [&](int n) __attribute__((always_inline)) {
      if (n == 4 && wid == 4) peek(rf, lb + 49152, F_U + (dc * 4 + kvh) * 256);
      if (n == 18 && wid == 4) { wait_vm<0>(); peek_check(ws, lb + 49152, F_U + (dc * 4 + kvh) * 256, 28, tag); }
      if (n == 19 && strm) y_iss(0); if (n == 27 && strm) y_iss(1); });
    bar();
    att_tile<2>(A, kvl + 2 * 32768, 0, [&](int n) __attribute__((always_inline)) {
      if (n == 9) { if (hw) wait_vm<8>(); bar(); }
      if (n == 10 && strm) y_iss(2); });
    if (__builtin_expect(__any(!(A.l[0][0] + A.l[1][0] <= 1.0e9f)), 0)) att_redo(A, rkv, lane * 16);
    addr_init(aY, lb, lb + 8192 + wi * 8192, wj, kvh, lane);
    if (!hw) load_frags(FY, aY);
#pragma unroll
    for (int qb = 0; qb < 2; ++qb) {
      const float inv = __builtin_amdgcn_rcpf(A.l[qb][0]);
#pragma unroll
      for (int d = 0; d < 4; ++d) *(LAS u32x4*)(size_t)(kvl + PARK + wid * 2048 + qb * 1024 + d * 16384) = pack8(A.o[2 * d][qb] * inv, A.o[2 * d + 1][qb] * inv);
    }
    wait_lgkm0();
  }
  if (wid < 4) {
    f32x4 acc[8][4];
    Frags F0 = FY, F1;
    Addr a = aY;
    int slot = 1;
    addr_add(a, QS);
    bar();
    gstep<false, 4, QS, true>(acc, F1, F0, a, slot);
    gemm_steps<false, 4, QS>(acc, F0, F1, a, slot, 26);
    addr_init(a, lb + wi * 8192, lb + 16384, wj, 0, lane_id());
    gstep<false, 4, QS>(acc, F0, F1, a, slot);
    {
      const int lm = lane_id();
#pragma unroll
      for (int m = 0; m < 4; ++m) *(LAS u32x4*)(size_t)(lb + PARK + lm * 16 + wid * 4096 + m * 1024) = pack8(acc[2 * m][0], acc[2 * m + 1][0]);
#pragma unroll
      for (int j = 1; j < 4; ++j)
#pragma unroll
        for (int m = 0; m < 4; ++m) st16s<0>(pack8(acc[2 * m][j], acc[2 * m + 1][j]), rxb, lm * 16, (kvh ^ j) * 98304 + (j - 1) * 16384 + wid * 4096 + m * 1024);
    }
    gstep<true, 4, QS, true>(acc, F1, F0, a, slot);
    gemm_steps<true, 4, QS>(acc, F0, F1, a, slot, 8);
    wait_vm<0>();
    gemm_steps<true, 4, QS>(acc, F0, F1, a, slot, 22);
    gstep<true, 4, QS>(acc, F0, F1, a, slot);
    const int lt = lane_id(), g = lt >> 4;
    const unsigned wo_ = wi * 8192 + wj * 2048;
#pragma unroll
    for (int dl = 1; dl < 4; ++dl)
#pragma unroll
      for (int h = 0; h < 2; ++h)
#pragma unroll
        for (int mm = 0; mm < 2; ++mm)
          st16s<0>(pack8(acc[4 * h + dl][2 * mm], acc[4 * h + dl][2 * mm + 1]), rxb, lt * 16, (kvh ^ dl) * 98304 + 49152 + (dl - 1) * 16384 + wo_ + h * 4096 + mm * 1024);
    const int fs = F_S + tile * 512 + wid * 32;
    wait_vm<8>(); signal<1>(ws, fs + (kvh ^ 1) * 128, tag);
    wait_vm<5>(); signal<1>(ws, fs + (kvh ^ 2) * 128 + 8, tag);
    wait_vm<2>(); signal<1>(ws, fs + (kvh ^ 3) * 128 + 16, tag);
    const int srow = rb * 256 + (8 * wi + kvh) * 16;
    const rsrc_t rx = mkrsrc(x + (size_t)srow * DM, 80 * DM * 2), rout = mkrsrc(out + (size_t)srow * DM, 80 * DM * 2);
    const unsigned vo = ((lt & 15) * DM + dc * 128 + wj * 64 + 16 * (g & 1) + 8 * (g >> 1)) * 2;
    u32x4 ps[3][4], xv[4], gv[4];
#pragma unroll
    for (int p = 0; p < 4; ++p) xv[p] = __builtin_amdgcn_raw_buffer_load_b128(rx, vo + (p >> 1) * (64 * DM * 2) + (p & 1) * 64, 0, 0);
    bar();
#pragma unroll
    for (int p = 0; p < 4; ++p) gv[p] = ldsr(lb + PARK + lt * 16 + wo_ + (p >> 1) * 4096 + (p & 1) * 1024);
    {
      GAS unsigned long long* fp = (GAS unsigned long long*)(ws + WS_FLAG + fs + kvh * 128) + (lt & 3);
      const unsigned long long want = ((unsigned long long)tag.hi << 32) | tag.lo;
      unsigned got = 0;
      for (unsigned sp = 0; got != 7; ++sp) {
        unsigned now = (unsigned)__ballot(__hip_atomic_load(fp, __ATOMIC_RELAXED, __HIP_MEMORY_SCOPE_AGENT) == want) & 7;
        if (sp >= (1u << 20)) now = 7;
        asm volatile("" ::: "memory");
#pragma unroll
        for (int k = 0; k < 3; ++k)
          if ((now & ~got) >> k & 1) {
#pragma unroll
            for (int p = 0; p < 4; ++p)
              ps[k][p] = __builtin_amdgcn_raw_buffer_load_b128(rxb, lt * 16 + (p & 1) * 1024, kvh * 98304 + 49152 + k * 16384 + wo_ + (p >> 1) * 4096, 16);
          }
        got |= now;
        if (got != 7) __builtin_amdgcn_s_sleep(1);
      }
    }
#pragma unroll
    for (int p = 0; p < 4; ++p) {
      f32x4 s0 = acc[4 * (p >> 1)][2 * (p & 1)], s1 = acc[4 * (p >> 1)][2 * (p & 1) + 1];
#pragma unroll
      for (int k = 0; k < 3; ++k) add8(s0, s1, ps[k][p]);
      s0 *= (f32x4){bflo(gv[p][0]), bfhi(gv[p][0]), bflo(gv[p][1]), bfhi(gv[p][1])};
      s1 *= (f32x4){bflo(gv[p][2]), bfhi(gv[p][2]), bflo(gv[p][3]), bfhi(gv[p][3])};
      const u32x4 tp = pack8(s0, s1);
      const auto w0 = __builtin_amdgcn_permlane16_swap(tp[0], tp[2], false, false), w1 = __builtin_amdgcn_permlane16_swap(tp[1], tp[3], false, false);
      const unsigned t[4] = {w0[0], w1[0], w0[1], w1[1]};
      u32x4 o;
#pragma unroll
      for (int e = 0; e < 4; ++e) o[e] = cvtpk(bflo(xv[p][e]) + bflo(t[e]), bfhi(xv[p][e]) + bfhi(t[e]));
      __builtin_amdgcn_raw_buffer_store_b128(o, rout, vo + (p >> 1) * (64 * DM * 2) + (p & 1) * 64, 0, 0);
    }
  } else {
    const int lane = lane_id();
    const unsigned pl = lb + PARK + lane * 16, pk = lb + 159744 + lw * 256;
    const int fo = F_O + (rb * 4 + kvh) * 128;
    if (lw < 3) {
      Strm S;
      if (lw < 2) strm_x(S, ws + WS_O + (size_t)(rb * 128 + 32 * kvh) * 16384 + lw * 8192, 524288 - lw * 8192, 16384, kvh);
      else strm_w(S, (const char*)wo + (size_t)kvh * 1024 * (DM * 2) + dc * 256, 1024, DM * 2, false);
      wait_vm<8>();
      bar();
      y_iss(3);
      for (int t = 1; t <= 22; ++t) { y_iss(t + 3); wait_vm<16>(); bar(); }
      peek(rf, lb + PARK + lw * 256, fo);
      y_iss(26); wait_vm<16>(); bar();
      y_iss(27); wait_vm<16>(); bar();
      peek_check(ws, lb + PARK + lw * 256, fo, 8, tag);
      auto iss = [&](int k) { const int r = (k & 16) | ((k + 4 * (rb & 3)) & 15); issue(S, k, 4 * (r & 7) + (r >> 3)); wait_vm<16>(); bar(); };
      for (int t = 25; t <= 36; ++t) iss(t - 25);
      peek(rf, lb + PARK + 16384 + lw * 256, fo + 64);
      for (int t = 37; t <= 40; ++t) iss(t - 25);
      peek_check(ws, lb + PARK + 16384 + lw * 256, fo + 64, 8, tag);
      for (int t = 41; t <= 56; ++t) iss(t - 25);
      wait_vm<8>(); bar();
      wait_vm<0>(); bar();
    } else {
      bar();
      const rsrc_t ro = mkrsrc(ws + WS_O + (size_t)(rb * 128 + 4 * head) * 16384, 65536);
      for (int t = 1; t <= 25; t += 2) {
        const unsigned o = (t >> 1) * 5120;
        const u32x4 v0 = ldsr(pl + o), v1 = ldsr(pl + o + 1024), v2 = ldsr(pl + o + 2048), v3 = ldsr(pl + o + 3072), v4 = ldsr(pl + (t < 25 ? o + 4096 : 0));
        st16s<0>(v0, ro, lane * 16, o); st16s<1024>(v1, ro, lane * 16, o); st16s<2048>(v2, ro, lane * 16, o);
        if (t == 17) { wait_vm<11>(); signal<1>(ws, fo + dc * 8, tag); }
        bar();
        st16s<3072>(v3, ro, lane * 16, o); st16s<0>(v4, ro, lane * 16, o + 4096);
        bar();
      }
      bar(); bar(); bar();
      wait_vm<0>();
      signal<1>(ws, fo + 64 + dc * 8, tag);
      for (int t = 30; t <= 38; ++t) bar();
      signal<14>(ws, F_Y + (tile * 4 + (kvh ^ (lane & 3))) * 128 + ((lane & 3) - 1) * 8, tag);
      for (int t = 39; t <= 56; ++t) bar();
      peek(rf, pk, F_Y + (tile * 4 + kvh) * 128);
      bar();
      wait_vm<0>();
      peek_check(ws, pk, F_Y + (tile * 4 + kvh) * 128, 3, tag);
      bar();
    }
    u32x4 py[3][4];
#pragma unroll
    for (int k = 0; k < 3; ++k)
#pragma unroll
      for (int m = 0; m < 4; ++m) py[k][m] = __builtin_amdgcn_raw_buffer_load_b128(rxb, lane * 16 + m * 1024, kvh * 98304 + lw * 4096 + k * 16384, 16);
    const float bb = bflo(b2[rb * 256 + (4 * lw + kvh) * 16 + (lane & 15)]);
    bar();
    bar();
#pragma unroll
    for (int m = 0; m < 4; ++m) {
      f32x4 y0 = {bb, bb, bb, bb}, y1 = y0;
      add8(y0, y1, ldsr(pl + lw * 4096 + m * 1024));
#pragma unroll
      for (int k = 0; k < 3; ++k) add8(y0, y1, py[k][m]);
      *(LAS u32x4*)(size_t)(pl + lw * 4096 + m * 1024) = (u32x4){silu2(y0.xy), silu2(y0.zw), silu2(y1.xy), silu2(y1.zw)};
    }
    wait_lgkm0();
    bar();
  }
}

extern "C" void kernel_launch(void* const* d_in, const int* in_sizes, int n_in, void* d_out, int out_size, void* d_ws, size_t ws_size,
                              hipStream_t stream) {
  const u16* x = (const u16*)d_in[0];
  const u16* kc = (const u16*)d_in[1];
  const u16* vc = (const u16*)d_in[2];
  const u16* wq = (const u16*)d_in[3];
  const u16* wo = (const u16*)d_in[4];
  const u16* w1 = (const u16*)d_in[6];
  const u16* b1 = (const u16*)d_in[7];
  const u16* w2 = (const u16*)d_in[8];
  const u16* b2 = (const u16*)d_in[9];
  char* ws = (char*)d_ws;
  (void)hipFuncSetAttribute((const void*)k_main, hipFuncAttributeMaxDynamicSharedMemorySize, 163840);
  hipLaunchKernelGGL(k_main, dim3(256), dim3(512), 163840, stream, x, kc, vc, wq, wo, w1, b1, w2, b2, ws, (u16*)d_out);
}
```

```cpp
#include <hip/hip_runtime.h>
#include <hip/hip_bf16.h>

#define PFB 2097152
#define CODE_BYTES 57344
#define YROT 7
#define SROT 4
#define NSP 3
#define CPF0 1
typedef unsigned short u16;
typedef short bf16x8 __attribute__((ext_vector_type(8)));
typedef short s16x4 __attribute__((ext_vector_type(4)));
typedef float f32x16 __attribute__((ext_vector_type(16)));
typedef float f32x4 __attribute__((ext_vector_type(4)));
typedef float f32x2 __attribute__((ext_vector_type(2)));
typedef unsigned u32x4 __attribute__((ext_vector_type(4)));
typedef unsigned u32x2 __attribute__((ext_vector_type(2)));
typedef __bf16 bf16x2 __attribute__((ext_vector_type(2)));
typedef __amdgpu_buffer_rsrc_t rsrc_t;
#define LAS __attribute__((address_space(3)))
#define GAS __attribute__((address_space(1)))
#define DI __device__ __forceinline__
#define SGB(m, n) __builtin_amdgcn_sched_group_barrier(m, n, 0)
#define SB0() __builtin_amdgcn_sched_barrier(0)

constexpr int SEQ = 2048, DM = 1024, FF = 3584, HD = 4096;
constexpr size_t WS_FLAG = 0;
constexpr int F_EP = 0, F_X = 512, F_L = 2048, F_U = 4096, F_O = 12288, F_Y = 16384, F_S = 49152, F_P = 81920, F_END = 96256;
constexpr size_t WS_ST = 98304;
constexpr size_t WS_CS = 131072;
constexpr size_t WS_HT = 262144;
constexpr size_t WS_KV = WS_HT + 4194304;
constexpr size_t WS_O = WS_KV + 1048576;
constexpr size_t WS_U = WS_O + 16777216;
constexpr size_t WS_XB = WS_U + 7340032;
constexpr size_t WS_END = WS_XB + 25165824;

#define STAMP(kid, i)
#define STAMPA(i)
#define STAMPF(i)
DI unsigned cvtpk(float lo, float hi) { f32x2 v = {lo, hi}; bf16x2 b = __builtin_convertvector(v, bf16x2); return __builtin_bit_cast(unsigned, b); }
DI float bflo(unsigned u) { return __uint_as_float(u << 16); }
DI float bfhi(unsigned u) { return __uint_as_float(u & 0xffff0000u); }
DI float rbf(float x) { return bflo(cvtpk(x, x)); }
template <int N> DI void wait_vm() { asm volatile("s_waitcnt vmcnt(%0)" ::"i"(N) : "memory"); }
DI void wait_lgkm0() { __builtin_amdgcn_s_waitcnt(0xC07F); }
DI void bar() { asm volatile("" ::: "memory"); __builtin_amdgcn_s_barrier(); asm volatile("" ::: "memory"); }
DI rsrc_t mkrsrc(const void* p, unsigned bytes) { return __builtin_amdgcn_make_buffer_rsrc((void*)p, 0, bytes, 0x00020000); }
DI void dma16(rsrc_t r, unsigned lds_addr, unsigned voff, unsigned soff) {
  asm volatile("s_mov_b32 m0, %0\n\ts_nop 0\n\tbuffer_load_dwordx4 %1, %2, %3 offen lds" ::"s"(lds_addr), "v"(voff), "s"(r), "s"(soff) : "memory");
}
DI void dma16s(rsrc_t r, unsigned lds_addr, unsigned voff, unsigned soff) {
  asm volatile("s_mov_b32 m0, %0\n\ts_nop 0\n\tbuffer_load_dwordx4 %1, %2, %3 offen sc1 lds" ::"s"(lds_addr), "v"(voff), "s"(r), "s"(soff) : "memory");
}
template <int OFF> DI void st16s(u32x4 v, rsrc_t r, unsigned voff, unsigned soff) {
  asm volatile("buffer_store_dwordx4 %0, %1, %2, %3 offen offset:%4 sc1\n\ts_nop 1" ::"v"(v), "v"(voff), "s"(r), "s"(soff), "i"(OFF) : "memory");
}
DI int lane_id() { int l; asm volatile("v_mbcnt_lo_u32_b32 %0, -1, 0\n\tv_mbcnt_hi_u32_b32 %0, -1, %0" : "=v"(l)); return l; }
struct Tag { unsigned lo, hi; };
DI void peek(rsrc_t rf, unsigned lds_addr, int off) {
  asm volatile("s_mov_b32 m0, %0\n\ts_nop 0\n\tbuffer_load_dword %3, %1, %2 offen sc1 lds" ::"s"(lds_addr), "s"(rf), "s"(off), "v"(lane_id() * 4) : "memory");
}
template <int GAP = 1> DI void spin_until(char* ws, int off, int n, Tag t) {
  const int ln = lane_id();
  GAS unsigned long long* p = (GAS unsigned long long*)(ws + WS_FLAG + off) + (ln < n ? ln : 0);
  const unsigned long long want = ((unsigned long long)t.hi << 32) | t.lo;
  for (unsigned sp = 0; sp < (1u << 20); ++sp) {
    if (__all(__hip_atomic_load(p, __ATOMIC_RELAXED, __HIP_MEMORY_SCOPE_AGENT) == want)) break;
    __builtin_amdgcn_s_sleep(GAP);
  }
  asm volatile("" ::: "memory");
}
DI void peek_check(char* ws, unsigned lds_addr, int off, int n, Tag t) {
  const int ln = lane_id();
  const unsigned v = *(LAS volatile unsigned*)(size_t)(lds_addr + ln * 4);
  if (!__all(ln >= 2 * n || v == ((ln & 1) ? t.hi : t.lo))) spin_until(ws, off, n, t);
}
template <long MASK> DI void signal(char* ws, int off, Tag t) {
  const u32x2 v = {t.lo, t.hi};
  asm volatile("s_mov_b64 exec, %3\n\tglobal_store_dwordx2 %0, %1, %2 sc1\n\ts_mov_b64 exec, -1" ::"v"(off), "v"(v), "s"(ws + WS_FLAG), "s"(MASK) : "memory");
}
DI void dma4k(rsrc_t r, unsigned lds_addr, unsigned voff, unsigned soff) {
  asm volatile("s_mov_b32 m0, %0\n\ts_nop 0\n\tbuffer_load_dwordx4 %1, %2, %3 offen sc1 lds\n\tbuffer_load_dwordx4 %1, %2, %3 offen offset:1024 sc1 lds\n\t"
               "buffer_load_dwordx4 %1, %2, %3 offen offset:2048 sc1 lds\n\tbuffer_load_dwordx4 %1, %2, %3 offen offset:3072 sc1 lds"
               ::"s"(lds_addr), "v"(voff), "s"(r), "s"(soff) : "memory");
}
template <int W> DI void pf_line(int wid, rsrc_t r, unsigned voff, unsigned soff) {
  asm volatile("s_cmp_lg_u32 %0, %4\n\ts_cbranch_scc1 1f\n\tbuffer_load_dword v255, %1, %2, %3 offen\n1:"
               ::"s"(wid), "v"(voff), "s"(r), "s"(soff), "i"(W) : "scc", "memory", "v255");
}
template <int CTRL> DI float dppf(float x) { return __builtin_bit_cast(float, __builtin_amdgcn_mov_dpp(__builtin_bit_cast(int, x), CTRL, 0xf, 0xf, true)); }
DI float wave_sum(float v) {
  v += dppf<0xB1>(v); v += dppf<0x4E>(v); v += dppf<0x141>(v); v += dppf<0x140>(v);
  auto a = __builtin_amdgcn_permlane16_swap(__float_as_uint(v), __float_as_uint(v), false, false);
  v = __uint_as_float(a[0]) + __uint_as_float(a[1]);
  auto b = __builtin_amdgcn_permlane32_swap(__float_as_uint(v), __float_as_uint(v), false, false);
  return __uint_as_float(b[0]) + __uint_as_float(b[1]);
}

DI f32x2 ln16(unsigned (&w)[8]) {
  f32x2 v[8];
#pragma unroll
  for (int i = 0; i < 8; ++i) v[i] = (f32x2){bflo(w[i]), bfhi(w[i])};
  const f32x2 s = ((v[0] + v[1]) + (v[2] + v[3])) + ((v[4] + v[5]) + (v[6] + v[7]));
  const float mean = wave_sum(s[0] + s[1]) * (1.f / 1024.f);
  f32x2 q0 = {0.f, 0.f}, q1 = q0;
#pragma unroll
  for (int i = 0; i < 8; i += 2) {
    const f32x2 d0 = v[i] - mean, d1 = v[i + 1] - mean;
    q0 = __builtin_elementwise_fma(d0, d0, q0); q1 = __builtin_elementwise_fma(d1, d1, q1);
  }
  q0 += q1;
  const float var = wave_sum(q0[0] + q0[1]) * (1.f / 1024.f);
  const float mb = rbf(mean);
  const float rs = __builtin_amdgcn_rcpf(rbf(__builtin_amdgcn_sqrtf(rbf(rbf(var) + 1e-5f))));
#pragma unroll
  for (int i = 0; i < 8; ++i) {
    const f32x2 d = v[i] - mb;
    const unsigned c = cvtpk(d[0], d[1]);
    const f32x2 r = (f32x2){bflo(c), bfhi(c)} * rs;
    w[i] = cvtpk(r[0], r[1]);
  }
  return (f32x2){mb, rs};
}

struct Frags { bf16x8 x[8]; bf16x8 w[4]; };
struct Addr { unsigned x, w[4]; };
template <int XS = 1024> DI void load_frags(Frags& f, const Addr& a) {
#pragma unroll
  for (int c = 0; c < 4; ++c) {
    s16x4 a0 = __builtin_amdgcn_ds_read_tr16_b64_v4i16((LAS s16x4*)(size_t)(a.w[c]));
    s16x4 a1 = __builtin_amdgcn_ds_read_tr16_b64_v4i16((LAS s16x4*)(size_t)(a.w[c] + 4096));
    f.w[c] = __builtin_shufflevector(a0, a1, 0, 1, 2, 3, 4, 5, 6, 7);
  }
#pragma unroll
  for (int i = 0; i < 8; ++i) f.x[i] = *(LAS bf16x8*)(size_t)(a.x + i * XS);
}
template <bool WA, bool Z> DI void mma_all(f32x4 (&acc)[8][4], const Frags& f, const f32x4* c0) {
  const f32x4 zero = {0.f, 0.f, 0.f, 0.f};
#pragma unroll
  for (int i = 0; i < 8; ++i)
#pragma unroll
    for (int jj = 0; jj < 4; ++jj) {
      const int j = (i & 1) ? 3 - jj : jj;
      if (WA) acc[i][j] = __builtin_amdgcn_mfma_f32_16x16x32_bf16(f.w[j], f.x[i], Z ? (c0 ? c0[j] : zero) : acc[i][j], 0, 0, 0);
      else    acc[i][j] = __builtin_amdgcn_mfma_f32_16x16x32_bf16(f.x[i], f.w[j], Z ? (c0 ? c0[j] : zero) : acc[i][j], 0, 0, 0);
    }
}
DI void addr_add(Addr& a, int d) {
  a.x += d;
#pragma unroll
  for (int c = 0; c < 4; ++c) a.w[c] += d;
}
template <bool WA, int NSLOT, int SLOTB, bool Z = false>
DI void gstep(f32x4 (&acc)[8][4], Frags& Fn, const Frags& Fc, Addr& a, int& slot, const f32x4* c0 = nullptr) {
  SB0();
  load_frags(Fn, a);
  mma_all<WA, Z>(acc, Fc, c0);
  slot = (slot + 1 == NSLOT) ? 0 : slot + 1;
  addr_add(a, (slot == 0) ? -(NSLOT - 1) * SLOTB : SLOTB);
  asm volatile("" : "+v"(a.x), "+v"(a.w[0]), "+v"(a.w[1]), "+v"(a.w[2]), "+v"(a.w[3]));
#pragma unroll
  for (int i = 0; i < 8; ++i) { SGB(0x8, 1); SGB(0x100, 1); SGB(0x8, 1); SGB(0x100, 1); SGB(0x8, 1); }
#pragma unroll
  for (int i = 0; i < 6; ++i) { SGB(0x8, 1); SGB(0x4, 1); SGB(0x2, 1); }
  SGB(0x8, 2);
  SB0();
  wait_lgkm0();
  bar();
}
template <int MODE, bool CS, bool Z = false>
DI void qstep(f32x4 (&acc)[8][4], f32x4 (&cs)[2], u32x4& ones, unsigned onext, Frags& Fn, const Frags& Fc, Addr& a, int& xs, int& wsl) {
  const f32x4 zero = {0.f, 0.f, 0.f, 0.f};
  SB0();
  load_frags<MODE == 2 ? 1024 : 2048>(Fn, a);
  if (CS) {
#pragma unroll
    for (int j = 0; j < 2; ++j) cs[j] = __builtin_amdgcn_mfma_f32_16x16x32_bf16(Fc.w[j], __builtin_bit_cast(bf16x8, ones), Z ? zero : cs[j], 0, 0, 0);
    ones = (u32x4){onext, onext, onext, onext};
    asm volatile("" : "+v"(ones));
  }
  mma_all<true, Z>(acc, Fc, nullptr);
  if (MODE == 2) addr_add(a, 24576);
  else {
    wsl = (wsl + 1) & 3;
    const int dw = wsl ? 8192 : -24576;
#pragma unroll
    for (int c = 0; c < 4; ++c) a.w[c] += dw;
    a.x ^= 64;
    if (MODE == 1) { xs = (xs == 2) ? 0 : xs + 1; a.x += xs ? 32768 : -65536; }
  }
  asm volatile("" : "+v"(a.x), "+v"(a.w[0]), "+v"(a.w[1]), "+v"(a.w[2]), "+v"(a.w[3]));
#pragma unroll
  for (int i = 0; i < 8; ++i) { SGB(0x8, 1); SGB(0x100, 1); SGB(0x8, 1); SGB(0x100, 1); SGB(0x8, 1); }
#pragma unroll
  for (int i = 0; i < 7; ++i) { SGB(0x8, 1); SGB(0x4, 1); SGB(0x2, 1); }
  if (CS) { SGB(0x8, 1); SGB(0x2, 2); SGB(0x8, 1); SGB(0x2, 2); }
  SGB(0x8, 1);
  SB0();
  wait_lgkm0();
  bar();
}
DI void addr_init(Addr& a, unsigned xbase, unsigned wbase, int wj, int cx, int lane) {
  const int g = lane >> 4, q = (lane >> 2) & 3, p = lane & 3, row = 4 * g + q;
  a.x = xbase + lane * 16;
#pragma unroll
  for (int c = 0; c < 4; ++c) a.w[c] = wbase + row * 256 + (((2 * (4 * wj + (c ^ cx)) + (p >> 1)) ^ ((row & 7) << 1)) * 16) + (p & 1) * 8;
}
template <bool WA, int NSLOT, int SLOTB> DI void gemm_steps(f32x4 (&acc)[8][4], Frags& F0, Frags& F1, Addr& a, int& slot, int n) {
  for (int t = 0; t < n; t += 2) {
    gstep<WA, NSLOT, SLOTB>(acc, F0, F1, a, slot);
    gstep<WA, NSLOT, SLOTB>(acc, F1, F0, a, slot);
  }
}

DI u32x4 pack8(const f32x4& a, const f32x4& b) { return (u32x4){cvtpk(a[0], a[1]), cvtpk(a[2], a[3]), cvtpk(b[0], b[1]), cvtpk(b[2], b[3])}; }
DI f32x2 gate2(f32x2 u, f32x2 z) {
  f32x2 e = {__builtin_amdgcn_exp2f(z[0]), __builtin_amdgcn_exp2f(z[1])};
  e += 1.f;
  return u * (f32x2){__builtin_amdgcn_rcpf(e[0]), __builtin_amdgcn_rcpf(e[1])};
}
DI unsigned gelu2(f32x2 u) {
  const f32x2 r = gate2(u, u * __builtin_elementwise_fma(u * u, (f32x2){-0.1029432397f, -0.1029432397f}, (f32x2){-2.302208198f, -2.302208198f}));
  return cvtpk(r[0], r[1]);
}
DI unsigned silu2(f32x2 y) { const f32x2 r = gate2(y, y * -1.4426950409f); return cvtpk(r[0], r[1]); }
DI u32x4 ldsr(unsigned a) { return *(LAS u32x4*)(size_t)a; }
DI void add8(f32x4& a, f32x4& b, const u32x4& w) {
  a += (f32x4){bflo(w[0]), bfhi(w[0]), bflo(w[1]), bfhi(w[1])};
  b += (f32x4){bflo(w[2]), bfhi(w[2]), bflo(w[3]), bfhi(w[3])};
}

struct Sc { f32x4 v[2][2]; };
struct Att { bf16x8 q[2][4]; f32x4 o[8][2]; f32x4 negm[2]; Sc sE, sO; u32x4 pO[2]; bf16x8 pre[4], ones; f32x4 l[2]; };
DI float xg_max(float m) {
  auto a = __builtin_amdgcn_permlane16_swap(__float_as_uint(m), __float_as_uint(m), false, false);
  m = fmaxf(__uint_as_float(a[0]), __uint_as_float(a[1]));
  auto b = __builtin_amdgcn_permlane32_swap(__float_as_uint(m), __float_as_uint(m), false, false);
  return fmaxf(__uint_as_float(b[0]), __uint_as_float(b[1]));
}
DI float row_max(const Sc& s, int qb) {
  const f32x4 a = s.v[0][qb], b = s.v[1][qb];
  return xg_max(fmaxf(fmaxf(fmaxf(a[0], a[1]), fmaxf(a[2], a[3])), fmaxf(fmaxf(b[0], b[1]), fmaxf(b[2], b[3]))));
}
DI bf16x8 ldfrag(unsigned a) { return *(LAS bf16x8*)(size_t)a; }
DI void sm1(Sc& s, int e, u32x4 (&w)[2], int part = 3) {
  if (e < 16 && (part & 1)) {
    float x = __builtin_amdgcn_exp2f(s.v[e >> 3][(e >> 2) & 1][e & 3]);
    asm volatile("" : "+v"(x));
    s.v[e >> 3][(e >> 2) & 1][e & 3] = x;
  }
  if (e >= 2 && e <= 16 && !(e & 1) && (part & 2)) {
    const int kt = (e - 1) >> 3, qb = ((e - 1) >> 2) & 1, r = (e - 1) & 3;
    unsigned c = cvtpk(s.v[kt][qb][r - 1], s.v[kt][qb][r]);
    asm volatile("" : "+v"(c));
    w[qb][2 * kt + (r >> 1)] = c;
  }
}
#define SETPRIO(x) __builtin_amdgcn_s_setprio(x)
#define MFMA16(a, b, c) __builtin_amdgcn_mfma_f32_16x16x32_bf16(a, b, c, 0, 0, 0)
#define PFRAG(w) __builtin_bit_cast(bf16x8, w)
DI void mfma_acc(f32x4& c, const bf16x8& a, const u32x4& b) { asm volatile("v_mfma_f32_16x16x32_bf16 %0, %1, %2, %0" : "+v"(c) : "v"(a), "v"(b)); }
template <int POS, int n, class F> DI void att_slot(Att& A, bf16x8 (&f)[36], u32x4 (&pE)[2], unsigned kv, unsigned kvn, F& gap) {
  constexpr int u = n >> 3, k = n & 7;
  constexpr bool isqk = (u & 1) ? (u == 1 || POS < 2) : (u == 0 && POS == 0);
  const f32x4 zero = {0.f, 0.f, 0.f, 0.f};
  if constexpr (k == 0) SETPRIO(3 - u);
#pragma unroll
  for (int qb = 0; qb < 2; ++qb) {
    if constexpr (isqk && (k & 3) != 0) mfma_acc(u == 1 ? A.sO.v[k >> 2][qb] : A.sE.v[k >> 2][qb], f[n], __builtin_bit_cast(u32x4, A.q[qb][k & 3]));
    else if constexpr (isqk && u == 1) A.sO.v[k >> 2][qb] = MFMA16(f[n], A.q[qb][0], A.negm[qb]);
    else if constexpr (isqk) A.sE.v[k >> 2][qb] = MFMA16(f[n], A.q[qb][0], u ? A.negm[qb] : zero);
    else if constexpr (POS == 0) A.o[k][qb] = MFMA16(f[n], PFRAG(pE[qb]), zero);
    else if constexpr (u == 2) mfma_acc(A.o[k][qb], f[n], pE[qb]);
    else mfma_acc(A.o[k][qb], f[n], A.pO[qb]);
    SB0();
    if (qb == 1) {
      if constexpr (n < 28) f[n + 4] = ldfrag(kv + (n + 4) * 1024);
      else if constexpr (POS < 2) f[n + 4] = ldfrag(kvn + (n - 28) * 1024);
      SB0();
    }
    if constexpr (POS == 0 && u == 1) {
      if (k == 0 && qb == 0) sm1(A.sE, 0, pE);
      sm1(A.sE, 2 * k + 1 + qb, pE);
    }
    if constexpr (POS > 0 && n <= 14) sm1(A.sE, n + 2, pE, 1 + qb);
    if constexpr (n >= 14 && n <= (POS < 2 ? 30 : 15)) sm1(A.sO, n - 14, A.pO, 1 + qb);
    if constexpr (POS < 2 && n >= 30) sm1(A.sE, n - 30, pE, 1 + qb);
    if constexpr (POS == 2 && u == 2) sm1(A.sO, 2 + 2 * k + qb, A.pO);
    SB0();
  }
  if constexpr (!isqk && (k == 2 || k == 4)) {
    constexpr int qb = k == 4;
    if constexpr (POS == 0) A.l[qb] = MFMA16(A.ones, PFRAG(pE[qb]), zero);
    else if constexpr (u == 2) mfma_acc(A.l[qb], A.ones, pE[qb]);
    else mfma_acc(A.l[qb], A.ones, A.pO[qb]);
  }
  gap(n);
  SB0();
  if constexpr (POS == 2 && n == 23) { asm volatile("s_nop 1"); SB0(); }
  if constexpr ((POS == 2 && n == 31) || (POS == 0 && n == 7)) { asm volatile("s_nop 9"); SB0(); }
  if constexpr (POS == 0 && n == 7) {
#pragma unroll
    for (int qb = 0; qb < 2; ++qb) {
      const float mx = row_max(A.sE, qb);
      A.sE.v[0][qb] -= mx; A.sE.v[1][qb] -= mx;
      A.negm[qb] = (f32x4){-mx, -mx, -mx, -mx};
    }
    SB0();
  }
  if constexpr (n < 31) att_slot<POS, n + 1>(A, f, pE, kv, kvn, gap);
}
template <int POS, class F> DI void att_tile(Att& A, unsigned kv, unsigned kvn, F&& gap) {
  bf16x8 f[36];
  u32x4 pE[2];
  asm volatile("" : "+v"(kv), "+v"(kvn));
#pragma unroll
  for (int i = 0; i < 4; ++i) f[i] = A.pre[i];
  SB0();
  att_slot<POS, 0>(A, f, pE, kv, kvn, gap);
  if (POS < 2) {
#pragma unroll
    for (int i = 0; i < 4; ++i) A.pre[i] = f[32 + i];
  }
}
DI void att_redo(Att& A, rsrc_t rkv, unsigned vo) {
  const f32x4 zero = {0.f, 0.f, 0.f, 0.f};
  float m[2] = {-3.0e38f, -3.0e38f};
  A.l[0] = A.l[1] = zero;
#pragma unroll
  for (int d = 0; d < 8; ++d) A.o[d][0] = A.o[d][1] = zero;
  for (int blk = 0; blk < 16; ++blk) {
    const int kb = (blk ? 2 * blk - 1 : 0) * 8192, vb = (blk < 15 ? 2 * blk + 2 : 31) * 8192;
    Sc s;
#pragma unroll
    for (int n = 0; n < 8; ++n) {
      const bf16x8 kf = PFRAG(__builtin_amdgcn_raw_buffer_load_b128(rkv, vo + n * 1024, kb, 16));
#pragma unroll
      for (int qb = 0; qb < 2; ++qb) s.v[n >> 2][qb] = MFMA16(kf, A.q[qb][n & 3], (n & 3) ? s.v[n >> 2][qb] : zero);
    }
#pragma unroll
    for (int qb = 0; qb < 2; ++qb) {
      const float mn = fmaxf(m[qb], row_max(s, qb)), fct = __builtin_amdgcn_exp2f(m[qb] - mn);
      m[qb] = mn;
      A.l[qb] *= fct;
      s.v[0][qb] -= mn; s.v[1][qb] -= mn;
#pragma unroll
      for (int d = 0; d < 8; ++d) A.o[d][qb] *= fct;
    }
    u32x4 p[2];
#pragma unroll
    for (int i = 0; i < 17; ++i) sm1(s, i, p);
#pragma unroll
    for (int qb = 0; qb < 2; ++qb) A.l[qb] = MFMA16(A.ones, PFRAG(p[qb]), A.l[qb]);
#pragma unroll
    for (int n = 0; n < 8; ++n) {
      const bf16x8 vf = PFRAG(__builtin_amdgcn_raw_buffer_load_b128(rkv, vo + n * 1024, vb, 16));
#pragma unroll
      for (int qb = 0; qb < 2; ++qb) A.o[n][qb] = MFMA16(vf, PFRAG(p[qb]), A.o[n][qb]);
    }
  }
}

__global__ __launch_bounds__(512, 2) __attribute__((amdgpu_num_vgpr(127))) void k_main(const u16* __restrict__ x, const u16* __restrict__ kc, const u16* __restrict__ vc, const u16* __restrict__ wq, const u16* __restrict__ wo,
                                                 const u16* __restrict__ w1, const u16* __restrict__ b1, const u16* __restrict__ w2,
                                                 const u16* __restrict__ b2, char* __restrict__ ws, u16* __restrict__ out) {
  extern __shared__ __attribute__((aligned(16))) char smem[];
  LAS char* lds = (LAS char*)smem;
  const unsigned lb = (unsigned)(size_t)lds;
  const int tid = threadIdx.x, wid = __builtin_amdgcn_readfirstlane(tid >> 6);
  const int g8 = blockIdx.x & 7, idx = blockIdx.x >> 3;
  const rsrc_t rcode = mkrsrc((const void*)(__builtin_amdgcn_s_getpc() & ~255ull), CODE_BYTES);
  const unsigned code_o = (idx % ((CODE_BYTES + 8191) / 8192)) * 8192;
  pf_line<7>(wid, rcode, (tid & 63) * 128, code_o);
  u32x4 xa, xb;
  u32x2 ep;
  asm volatile("global_load_dwordx4 %0, %3, %4\n\tglobal_load_dwordx4 %1, %3, %4 offset:1024\n\tglobal_load_dwordx2 %2, %5, %6 sc1"
               : "=&v"(xa), "=&v"(xb), "=&v"(ep) : "v"((tid & 63) * 16), "s"(x + (size_t)(8 * blockIdx.x + wid) * DM), "v"(F_EP), "s"(ws + WS_FLAG) : "memory");
  SB0();
  STAMP(1, 0);
  STAMP(0, 0);
  const bool hasU = idx < 28;
  const int uh = g8 & 1, dq = idx & 3, ft = 7 * (g8 >> 1) + (idx >> 2), ut = ft * 4 + dq;
  const int kvh = g8 >> 1, head = 8 * kvh + (idx & 7), rb = 4 * (g8 & 1) + (idx >> 3);
  const int dc = idx & 7, tile = rb * 8 + dc;
  const int lw = wid - 4, wi = (wid >> 1) & 1, wj = wid & 1;
  constexpr int QS = 24576;
  constexpr int PARK = 98304;
  const rsrc_t rf = mkrsrc(ws + WS_FLAG, F_END + 256);
  const rsrc_t rkv = mkrsrc(ws + WS_KV + (size_t)kvh * 262144, 262144);
  const rsrc_t rxb = mkrsrc(ws + WS_XB + (size_t)tile * 393216, 393216);
  struct Strm { rsrc_t r; unsigned v0, v1, step, p0, p1, p2; int rot; };
  auto strm_x = [&](Strm& S, const char* base, unsigned bytes, unsigned step, int xr) {
    const int ln = lane_id();
    S.r = mkrsrc(base, bytes); S.v0 = ln * 16 + (xr & 1) * 1024; S.v1 = S.v0 ^ 1024; S.step = step;
    S.p0 = (xr >> 1) * 2048; S.p1 = S.p0 ^ 2048; S.p2 = 4096; S.rot = 0;
  };
  auto strm_w = [&](Strm& S, const char* base, unsigned rows, unsigned ld, bool nat) {
    const int ln = lane_id(), q4 = ln >> 4;
    const unsigned c0 = ((ln & 15) ^ (q4 << 1)) * 16;
    S.r = mkrsrc(base, (rows - 1) * ld + 256); S.v0 = q4 * ld + c0; S.v1 = ((nat ? 8 : 4) + q4) * ld + (c0 ^ 128); S.step = 32 * ld;
    S.p0 = 0; S.p1 = (nat ? 16 : 8) * ld; S.p2 = (nat ? 4 : 16) * ld; S.rot = 0;
  };
  auto issue = [&](const Strm& S, int k, int src = -1) {
    const unsigned s = lb + (k & 3) * QS + lw * 8192;
    if (src < 0) src = (k + S.rot) & 31;
#pragma unroll
    for (int i = 0; i < 4; ++i) {
      const unsigned so = src * S.step + ((i & 1) ? S.p1 : S.p0) + (i >> 1) * S.p2;
      dma16s(S.r, s + i * 2048, S.v0, so); dma16s(S.r, s + i * 2048 + 1024, S.v1, so);
    }
  };
  constexpr int QW = 98304, HST = 131072, HRS = 2112, RST = HST + 8 * HRS, PKQ = 148480, STT = 151552, CSX = 153600;
  const int lane0 = tid & 63;
  const rsrc_t rq = lw < 2 ? mkrsrc(x + (size_t)(rb * 256 + lw * 128) * DM, 128 * DM * 2) : mkrsrc((const char*)wq + head * 256, (DM - 1) * HD * 2 + 256);
  const unsigned qv0 = lw < 2 ? (lane0 >> 3) * 2048 + (((lane0 & 7) ^ (lane0 >> 4)) * 16) : (lane0 >> 4) * (HD * 2) + (((lane0 & 15) ^ ((lane0 >> 4) << 1)) * 16);
  const unsigned qv1 = lw < 2 ? (qv0 ^ 64) + 8 * 2048 : (qv0 ^ 128) + 8 * (HD * 2);
  const bool qx = lw < 2;
  const unsigned qp1 = qx ? 32768 : 16 * HD * 2, qp2 = qx ? 65536 : 4 * HD * 2;
  const unsigned qlb = lb + (qx ? lw * 16384 : QW), qlw = qx ? 98304 : 32768, qom = qx ? 0x20780 : 0x7c0000;
  unsigned ql = qx ? 65536 : 0, qdl = 8192, qo = rb * (qx ? 256 : 4 * 32 * HD * 2), qdo = qx ? 131072 : 32 * HD * 2;
  auto issq = [&]() {
    const unsigned s = qlb + ql;
    dma16(rq, s, qv0, qo); dma16(rq, s + 1024, qv1, qo);
    dma16(rq, s + 2048, qv0, qo + qp1); dma16(rq, s + 3072, qv1, qo + qp1);
    dma16(rq, s + 4096, qv0, qo + qp2); dma16(rq, s + 5120, qv1, qo + qp2);
    dma16(rq, s + 6144, qv0, qo + qp1 + qp2); dma16(rq, s + 7168, qv1, qo + qp1 + qp2);
    ql += qdl; ql = ql >= qlw ? ql - qlw : ql; qdl ^= qx ? (8192 ^ 24576) : 0;
    qo = (qo + qdo) & qom; qdo ^= qx ? 128 : 0;
  };
  auto h_out = [&](int it) {
    const rsrc_t rh = mkrsrc(ws + WS_HT + (size_t)(blockIdx.x >> 2) * 16384 + (blockIdx.x & 3) * 256, 3 * 1048576 + 16384);
    const unsigned a0 = HST + ((lane0 >> 2) & 3) * HRS + (it * 4 + (lane0 >> 4)) * 32 + (lane0 & 3) * 8;
    s16x4 lo = __builtin_amdgcn_ds_read_tr16_b64_v4i16((LAS s16x4*)(lds + a0));
    s16x4 hi = __builtin_amdgcn_ds_read_tr16_b64_v4i16((LAS s16x4*)(lds + a0 + 4 * HRS));
    st16s<0>(__builtin_bit_cast(u32x4, __builtin_shufflevector(lo, hi, 0, 1, 2, 3, 4, 5, 6, 7)), rh, (lane0 >> 4) * 1024 + (lane0 & 15) * 16,
             (it >> 2) * 1048576 + (it & 3) * 4096);
  };
  auto st_out = [&]() { st16s<0>(*(LAS u32x4*)(lds + RST + (lane0 & 3) * 16), mkrsrc(ws + WS_ST + blockIdx.x * 64, 64), lane0 * 16, 0); };
  Tag tag;
  {
    const int lane = lane0;
    if (wid >= 4 && wid < 7) {
      issq(); issq();
      if (lw == 2) { issq(); issq(); wait_vm<32>(); } else wait_vm<16>();
    } else if (!CPF0 && wid == 7) wait_vm<1>(); else wait_vm<0>();
    asm volatile("" : "+v"(xa), "+v"(xb), "+v"(ep));
    unsigned v[8] = {xa[0], xa[1], xa[2], xa[3], xb[0], xb[1], xb[2], xb[3]};
    const unsigned long long e64 = ((unsigned long long)(unsigned)__builtin_amdgcn_readfirstlane(ep[1]) << 32) | (unsigned)__builtin_amdgcn_readfirstlane(ep[0]);
    const unsigned long long tg = e64 * 0x9E3779B97F4A7C15ull + 0x632BE59BD9B4E019ull;
    tag.lo = (unsigned)tg; tag.hi = (unsigned)(tg >> 32);
    *(LAS f32x2*)(lds + RST + wid * 8) = ln16(v);
    ln16(v);
    *(LAS u32x4*)(lds + HST + wid * HRS + lane * 16) = (u32x4){v[0], v[1], v[2], v[3]};
    *(LAS u32x4*)(lds + HST + wid * HRS + 1024 + lane * 16) = (u32x4){v[4], v[5], v[6], v[7]};
    wait_lgkm0();
  }
  STAMP(0, 1);
  bf16x8 qh[8];
  f32x4 uk[4][4];
  auto kv8 = [&](int t, int slot) {
    const unsigned v = lane_id() * 16;
    dma4k(rkv, lb + slot * 32768 + lw * 8192, v, t * 32768 + lw * 8192);
    dma4k(rkv, lb + slot * 32768 + lw * 8192 + 4096, v, t * 32768 + lw * 8192 + 4096);
  };
  if (wid < 4) {
    f32x4 acc[8][4], cs[2];
    Frags F0, F1;
    Addr a;
    int xs = 2, wsl = 1;
    {
      const int ln = lane_id(), r = ln & 15;
      addr_init(a, 0, lb + QW, wj, 2 * wi, ln);
      a.x = lb + 65536 + wi * 16384 + r * 128 + (((ln >> 4) ^ (r >> 1)) * 16);
    }
    bar();
    load_frags<2048>(F0, a);
    a.x ^= 64;
#pragma unroll
    for (int c = 0; c < 4; ++c) a.w[c] += 8192;
    wait_lgkm0();
    bar();
    STAMP(1, 1);
    {
      const unsigned o1 = 0x3f803f80;
      u32x4 ones = {o1, o1, o1, o1};
      qstep<1, true, true>(acc, cs, ones, o1, F1, F0, a, xs, wsl);
      for (int t = 0; t < 2; ++t) {
        const unsigned on = t == 0 ? o1 : 0;
        qstep<0, true>(acc, cs, ones, on, F0, F1, a, xs, wsl);
        qstep<1, true>(acc, cs, ones, on, F1, F0, a, xs, wsl);
      }
      const int ln = lane_id();
      const unsigned csa = lb + CSX + ((ln & 15) ? 7168 : (64 * wj + 32 * wi) * 4) + (ln & 48);
      *(LAS f32x4*)(size_t)csa = cs[0];
      *(LAS f32x4*)(size_t)(csa + 64) = cs[1];
      for (int t = 0; t < 13; ++t) { qstep<0, false>(acc, cs, ones, 0, F0, F1, a, xs, wsl); qstep<1, false>(acc, cs, ones, 0, F1, F0, a, xs, wsl); }
      addr_init(a, lb + wi * 8192, lb + 16384, wj, 0, lane_id());
      qstep<2, false>(acc, cs, ones, 0, F0, F1, a, xs, wsl);
    }
    STAMP(1, 2);
    f32x4 c0[4];
    {
      const int ln = lane_id(), g = ln >> 4;
      u32x2 bv[4];
#pragma unroll
      for (int j = 0; j < 4; ++j) bv[j] = *(LAS u32x2*)(lds + CSX + 5120 + (64 * wj + 16 * j + 4 * g) * 2);
      f32x4 cq[4];
#pragma unroll
      for (int j = 0; j < 4; ++j) cq[j] = *(LAS f32x4*)(lds + CSX + 512 + (64 * wj + 16 * (j ^ (2 * wi)) + 4 * g) * 4);
      f32x2 st[8];
#pragma unroll
      for (int i = 0; i < 8; ++i) st[i] = *(LAS f32x2*)(lds + STT + (128 * wi + 16 * i + (ln & 15)) * 8);
      wait_lgkm0();
      bar();
      const unsigned qa = lb + 98304 + wi * 32768 + (2 * wj + wi) * 1024 + ln * 16, qb = qa ^ 1024;
#pragma unroll
      for (int i = 0; i < 8; ++i) {
        const float sc = st[i][1] * 0.12751743082f, nm = -st[i][0];
#pragma unroll
        for (int mm = 0; mm < 2; ++mm)
          *(LAS u32x4*)(size_t)((mm ? qb : qa) + i * 4096) =
              pack8((acc[i][2 * mm] + cq[2 * mm] * nm) * sc, (acc[i][2 * mm + 1] + cq[2 * mm + 1] * nm) * sc);
      }
#pragma unroll
      for (int j = 0; j < 4; ++j) c0[j] = uh ? (f32x4){0.f, 0.f, 0.f, 0.f} : (f32x4){bflo(bv[j][0]), bfhi(bv[j][0]), bflo(bv[j][1]), bfhi(bv[j][1])};
    }
    STAMP(1, 3);
    if (hasU) {
      int slot = 1;
      gstep<true, 4, QS, true>(acc, F1, F0, a, slot, c0);
      gemm_steps<true, 4, QS>(acc, F0, F1, a, slot, 30);
      gstep<true, 4, QS>(acc, F0, F1, a, slot);
      STAMP(1, 4);
      const rsrc_t rux = mkrsrc(ws + WS_XB + (size_t)((tile ^ 32) * 4 + kvh) * 98304 + 49152 + wid * 8192, 8192);
      const int ln = lane_id();
#pragma unroll
      for (int ii = 0; ii < 4; ++ii) {
        st16s<0>(pack8(acc[2 * ii + 1][0], acc[2 * ii + 1][1]), rux, ln * 16, ii * 2048);
        st16s<1024>(pack8(acc[2 * ii + 1][2], acc[2 * ii + 1][3]), rux, ln * 16, ii * 2048);
#pragma unroll
        for (int j = 0; j < 4; ++j) uk[ii][j] = acc[2 * ii][j];
      }
    }
    STAMP(1, 5);
  } else {
    const int lane = lane0;
    Strm U;
    if (lw < 2) strm_x(U, ws + WS_HT + (size_t)dq * 1048576 + uh * 524288 + lw * 8192, 524288 - lw * 8192, 16384, uh);
    else strm_w(U, (const char*)w1 + (size_t)uh * 1024 * (FF * 2) + ft * 256, 1024, FF * 2, true);
    if (!hasU) U.r = mkrsrc(ws, 0);
    U.rot = 8 * dq;
    const unsigned pk = lb + PKQ + lw * 512;
    if (lw < 3) {
      if (lw < 2) wait_vm<0>(); else wait_vm<24>();
      bar();
      if (lw < 2) { issq(); issq(); } else wait_vm<16>();
      bar();
      if (lw < 2) issq();
      for (int t = 1; t <= 21; ++t) { issq(); wait_vm<16>(); bar(); }
      peek(rf, pk, F_L + lw * 512); peek(rf, pk + 256, F_L + lw * 512 + 256);
      for (int t = 22; t <= 26; ++t) { issq(); wait_vm<16>(); bar(); }
      issq(); wait_vm<16>();
    } else {
      peek(mkrsrc(b1, FF * 2), lb + CSX + 5120, ft * 256);
      bar();
      for (int t = 0; t <= 7; ++t) { h_out(2 * t); h_out(2 * t + 1); bar(); }
      st_out();
      st16s<0>(*(LAS u32x4*)(lds + CSX + (lane & 31) * 16), mkrsrc(ws + WS_CS + (head * 8 + rb) * 512, 512), lane * 16, 0);
      bar(); bar(); bar();
      wait_vm<0>();
      signal<1>(ws, F_L + blockIdx.x * 8, tag);
      for (int t = 11; t <= 21; ++t) bar();
      peek(rf, pk, F_L + lw * 512); peek(rf, pk + 256, F_L + lw * 512 + 256);
      for (int t = 22; t <= 26; ++t) bar();
      wait_vm<0>();
    }
    peek_check(ws, pk, F_L + lw * 512, 32, tag); peek_check(ws, pk + 256, F_L + lw * 512 + 256, 32, tag);
    bar();
    if (blockIdx.x == 0 && lw == 3) signal<1>(ws, F_EP, tag);
    if (lw == 2) issq();
    if (lw == 3) {
      const rsrc_t rs = mkrsrc(ws + WS_ST + rb * 2048, 2048);
      dma16s(rs, lb + STT, lane * 16, 0); dma16s(rs, lb + STT + 1024, lane * 16, 1024);
      dma4k(mkrsrc(ws + WS_CS + head * 4096, 4096), lb + CSX + 1024, lane * 16, 0);
    }
    const bool us = lw < 3;
    if (us) issue(U, 0);
    wait_vm<16>(); bar();
    if (us) wait_vm<8>(); else wait_vm<0>();
    bar();
    if (us) issue(U, 1);
    else {
      const LAS f32x2* cp = (const LAS f32x2*)(lds + CSX + 1024) + lane;
      *(LAS f32x2*)(lds + CSX + 512 + lane * 8) = ((cp[0] + cp[64]) + (cp[128] + cp[192])) + ((cp[256] + cp[320]) + (cp[384] + cp[448]));
      wait_lgkm0();
    }
    bar();
    wait_vm<8>(); bar();
    if (us) issue(U, 2);
    wait_vm<8>();
    bar();
    bar();
    if (hasU) {
      if (us) { issue(U, 3); for (int t = 1; t <= 28; ++t) { issue(U, t + 3); wait_vm<16>(); bar(); } }
      else {
        for (int t = 1; t <= 26; ++t) bar();
        spin_until(ws, F_X + kvh * 128, 8, tag);
        pf_line<7>(wid, rcode, lane * 128, code_o);
        bar(); bar();
      }
      wait_vm<8>(); bar();
      kv8(0, 0); wait_vm<8>(); bar();
      kv8(1, 1); bar();
      kv8(2, 2); wait_vm<8>(); bar();
    }
  }
  if (!hasU) {
    const int sp = g8 * 4 + (idx - 28), lane = lane_id(), g = lane >> 4;
    unsigned e[4][8];
#pragma unroll
    for (int i = 0; i < 4; ++i) {
      const int kh = sp >> 3, unit = (sp & 7) * 4 + i, frag = wid;
      const bool isK = unit == 0 || ((unit & 1) && unit < 31);
      const u16* sk = kc + ((size_t)kh * SEQ + 1536 + 32 * ((unit + 1) >> 1) + 16 * (frag >> 2) + (lane & 15)) * 128 + 32 * (frag & 3) + 4 * g;
      const u16* sv = vc + ((size_t)kh * SEQ + 1536 + 32 * ((unit - 1) >> 1) + 4 * g) * 128 + 16 * frag + (lane & 15);
      const u16* src = isK ? sk : sv;
      const int s1 = isK ? 1 : 128, s4 = isK ? 16 : 2048;
#pragma unroll
      for (int j2 = 0; j2 < 8; ++j2) e[i][j2] = src[(j2 >> 2) * s4 + (j2 & 3) * s1];
    }
#pragma unroll
    for (int i = 0; i < 4; ++i)
      st16s<0>((u32x4){e[i][0] | (e[i][1] << 16), e[i][2] | (e[i][3] << 16), e[i][4] | (e[i][5] << 16), e[i][6] | (e[i][7] << 16)},
               mkrsrc(ws + WS_KV, 1048576), (wid * 64 + lane) * 16, (sp * 4 + i) * 8192);
    wait_vm<0>();
    wait_lgkm0();
    bar();
    if (wid == 0) signal<1>(ws, F_X + (sp >> 3) * 128 + (sp & 7) * 8, tag);
    unsigned pw[NSP];
#pragma unroll
    for (int i = 0; i < NSP; ++i) pw[i] = *(const unsigned*)((const char*)w2 + (size_t)i * PFB + (size_t)(sp * 512 + wid * 64 + lane) * 128);
#pragma unroll
    for (int i = 0; i < NSP; ++i) asm volatile("" ::"v"(pw[i]));
    if (wid >= 4) { spin_until(ws, F_X + kvh * 128, 8, tag); kv8(0, 0); kv8(1, 1); kv8(2, 2); wait_vm<8>(); }
    bar();
  }
  {
    const int ln = lane_id();
#pragma unroll
    for (int ks = 0; ks < 8; ++ks) qh[ks] = *(LAS bf16x8*)(lds + 98304 + wid * 8192 + ks * 1024 + ln * 16);
  }
  STAMP(1, 6);
  Strm Y;
  auto y_iss = [&](int k) { const int r = k + YROT * (dc & 3); issue(Y, k, r >= 28 ? r - 28 : r); };
  Frags FY;
  Addr aY;
  {
    const int lane = lane_id();
    Att A;
#pragma unroll
    for (int ks = 0; ks < 8; ++ks) A.q[ks >> 2][ks & 3] = qh[ks];
    A.ones = (bf16x8){0x3f80, 0x3f80, 0x3f80, 0x3f80, 0x3f80, 0x3f80, 0x3f80, 0x3f80};
    asm volatile("" : "+v"(A.ones));
    const unsigned kvl = lb + lane * 16;
#pragma unroll
    for (int i = 0; i < 4; ++i) A.pre[i] = ldfrag(kvl + i * 1024);
    const bool strm = wid >= 4 && wid < 7, hw = wid >= 4;
    const rsrc_t rp2 = mkrsrc(w2, SEQ * FF * 2), rpo = mkrsrc(wo, HD * DM * 2);
    const unsigned pv = (blockIdx.x * 64 + lane) * 128;
#define PF(W, R, C) do { if (&R != &rp2 || (C) >= NSP) pf_line<W>(wid, R, pv, (C) * 2097152u); } while (0)
    const int fp = F_P + (ut * 2 + uh) * 64 + (wid & 3) * 8;
    const bool cu = !hw && hasU;
    STAMPA(0);
    att_tile<0>(A, kvl + 0 * 32768, kvl + 1 * 32768, [&](int n) __attribute__((always_inline)) { if (n == 20 && cu) { wait_vm<0>(); signal<1>(ws, fp ^ 64, tag); } });
    if (hw) wait_vm<0>();
    bar();
    STAMPA(1);
    att_tile<1>(A, kvl + 1 * 32768, kvl + 2 * 32768, [&](int n) __attribute__((always_inline)) {
      if (n == 0 && hw) kv8(3, 3); if (n == 12 && hw) kv8(4, 0); if (n == 28 && hw) peek(rf, lb + 131072 + lw * 256, fp); if (n == 4) PF(0, rp2, 3); if (n == 9) PF(1, rp2, 4); });
    if (hw) wait_vm<9>();
    bar();
    STAMPA(2);
    att_tile<1>(A, kvl + 2 * 32768, kvl + 3 * 32768, [&](int n) __attribute__((always_inline)) {
      if (n == 8 && hw) {
        wait_vm<0>();
        if (hasU) peek_check(ws, lb + 131072 + lw * 256, fp, 1, tag);
        const rsrc_t rux = mkrsrc(ws + WS_XB + (size_t)(tile * 4 + kvh) * 98304 + 49152, hasU ? 32768 : 0);
        dma4k(rux, lb + 32768 + lw * 8192, lane * 16, lw * 8192); dma4k(rux, lb + 32768 + lw * 8192 + 4096, lane * 16, lw * 8192 + 4096);
      }
      if (n == 27) PF(3, rp2, 5); });
    if (hw) wait_vm<0>();
    bar();
    if (hw) { kv8(5, 4); kv8(7, 2); }
    STAMPA(3);
    if (cu) {
      u32x4 p[8];
      p[0] = ldsr(kvl + 32768 + wid * 8192);
#pragma unroll
      for (int k = 0; k < 8; ++k) {
        if (k < 7) p[k + 1] = ldsr(kvl + 32768 + wid * 8192 + (k + 1) * 1024);
        f32x4 a = uk[k >> 1][2 * (k & 1)], b = uk[k >> 1][2 * (k & 1) + 1];
        add8(a, b, p[k]);
        p[k] = (u32x4){gelu2(a.xy), gelu2(a.zw), gelu2(b.xy), gelu2(b.zw)};
        asm volatile("" : "+v"(p[k]));
        if (k == 0) PF(2, rp2, 6);
        SB0();
      }
      bar();
      const rsrc_t ru = mkrsrc(ws + WS_U + (size_t)((2 * dq + wi) * 112 + 4 * ft + 2 * wj) * 8192 + uh * 1024, 16384);
#pragma unroll
      for (int k = 0; k < 8; ++k) st16s<0>(p[k], ru, lane * 16, (k & 1) * 8192 + (k >> 1) * 2048);
    } else { PF(2, rp2, 6); bar(); }
    STAMPA(4);
    att_tile<1>(A, kvl + 3 * 32768, kvl + 0 * 32768, [&](int n) __attribute__((always_inline)) {
      if (n == 20 && cu) { wait_vm<0>(); signal<1>(ws, F_U + ((2 * dq + wi) * 4 + kvh) * 256 + (((idx >> 2) * 2 + uh) * 2 + wj) * 8, tag); } });
    if (hw) wait_vm<0>();
    bar();
    STAMPA(5);
    att_tile<1>(A, kvl + 0 * 32768, kvl + 4 * 32768, [&](int n) __attribute__((always_inline)) { if (n == 0 && hw) kv8(6, 3); if (n == 6) PF(3, rpo, 0); if (n == 15) PF(0, rpo, 1); if (n == 24) PF(1, rpo, 2); });
    if (hw) wait_vm<0>();
    bar();
    STAMPA(6);
    att_tile<1>(A, kvl + 4 * 32768, kvl + 3 * 32768, [&](int n) __attribute__((always_inline)) { if (n == 3) PF(2, rpo, 3); });
    if (lw == 0) strm_x(Y, ws + WS_U + (size_t)(dc * 112 + 28 * kvh) * 8192, 28 * 8192, 8192, 0);
    else strm_w(Y, (const char*)w2 + (size_t)kvh * 896 * (SEQ * 2) + rb * 512 + ((lw - 1) & 1) * 256, 896, SEQ * 2, false);
    bar();
    STAMPA(7);
    att_tile<1>(A, kvl + 3 * 32768, kvl + 2 * 32768, [&](int n) __attribute__((always_inline)) {
      if (n == 4 && wid == 4) peek(rf, lb + 49152, F_U + (dc * 4 + kvh) * 256);
      if (n == 18 && wid == 4) { wait_vm<0>(); peek_check(ws, lb + 49152, F_U + (dc * 4 + kvh) * 256, 28, tag); }
      if (n == 19 && strm) y_iss(0); if (n == 27 && strm) y_iss(1); });
    bar();
    STAMPA(8);
    att_tile<2>(A, kvl + 2 * 32768, 0, [&](int n) __attribute__((always_inline)) {
      if (n == 9) { if (hw) wait_vm<8>(); bar(); }
      if (n == 10 && strm) y_iss(2); });
    STAMP(1, 7);
    STAMPA(9);
    if (__builtin_expect(__any(!(A.l[0][0] + A.l[1][0] <= 1.0e9f)), 0)) att_redo(A, rkv, lane * 16);
    addr_init(aY, lb, lb + 8192 + wi * 8192, wj, kvh, lane);
    if (!hw) load_frags(FY, aY);
#pragma unroll
    for (int qb = 0; qb < 2; ++qb) {
      const float inv = __builtin_amdgcn_rcpf(A.l[qb][0]);
#pragma unroll
      for (int d = 0; d < 4; ++d) *(LAS u32x4*)(size_t)(kvl + PARK + wid * 2048 + qb * 1024 + d * 16384) = pack8(A.o[2 * d][qb] * inv, A.o[2 * d + 1][qb] * inv);
    }
    wait_lgkm0();
  }
  STAMP(1, 8);
  if (wid < 4) {
    f32x4 acc[8][4];
    Frags F0 = FY, F1;
    Addr a = aY;
    int slot = 1;
    addr_add(a, QS);
    bar();
    STAMP(1, 9);
    gstep<false, 4, QS, true>(acc, F1, F0, a, slot);
    gemm_steps<false, 4, QS>(acc, F0, F1, a, slot, 26);
    addr_init(a, lb + wi * 8192, lb + 16384, wj, 0, lane_id());
    gstep<false, 4, QS>(acc, F0, F1, a, slot);
    STAMP(1, 10);
    {
      const int lm = lane_id();
#pragma unroll
      for (int m = 0; m < 4; ++m) *(LAS u32x4*)(size_t)(lb + PARK + lm * 16 + wid * 4096 + m * 1024) = pack8(acc[2 * m][0], acc[2 * m + 1][0]);
#pragma unroll
      for (int j = 1; j < 4; ++j)
#pragma unroll
        for (int m = 0; m < 4; ++m) st16s<0>(pack8(acc[2 * m][j], acc[2 * m + 1][j]), rxb, lm * 16, (kvh ^ j) * 98304 + (j - 1) * 16384 + wid * 4096 + m * 1024);
    }
    STAMP(1, 11);
    gstep<true, 4, QS, true>(acc, F1, F0, a, slot);
    gemm_steps<true, 4, QS>(acc, F0, F1, a, slot, 8);
    wait_vm<0>();
    gemm_steps<true, 4, QS>(acc, F0, F1, a, slot, 22);
    gstep<true, 4, QS>(acc, F0, F1, a, slot);
    STAMP(1, 12);
    const int lt = lane_id(), g = lt >> 4;
    const unsigned wo_ = wi * 8192 + wj * 2048;
#pragma unroll
    for (int dl = 1; dl < 4; ++dl)
#pragma unroll
      for (int h = 0; h < 2; ++h)
#pragma unroll
        for (int mm = 0; mm < 2; ++mm)
          st16s<0>(pack8(acc[4 * h + dl][2 * mm], acc[4 * h + dl][2 * mm + 1]), rxb, lt * 16, (kvh ^ dl) * 98304 + 49152 + (dl - 1) * 16384 + wo_ + h * 4096 + mm * 1024);
    const int fs = F_S + tile * 512 + wid * 32;
    wait_vm<8>(); signal<1>(ws, fs + (kvh ^ 1) * 128, tag);
    wait_vm<5>(); signal<1>(ws, fs + (kvh ^ 2) * 128 + 8, tag);
    wait_vm<2>(); signal<1>(ws, fs + (kvh ^ 3) * 128 + 16, tag);
    STAMP(1, 13);
    const int srow = rb * 256 + (8 * wi + kvh) * 16;
    const rsrc_t rx = mkrsrc(x + (size_t)srow * DM, 80 * DM * 2), rout = mkrsrc(out + (size_t)srow * DM, 80 * DM * 2);
    const unsigned vo = ((lt & 15) * DM + dc * 128 + wj * 64 + 16 * (g & 1) + 8 * (g >> 1)) * 2;
    u32x4 ps[3][4], xv[4], gv[4];
#pragma unroll
    for (int p = 0; p < 4; ++p) xv[p] = __builtin_amdgcn_raw_buffer_load_b128(rx, vo + (p >> 1) * (64 * DM * 2) + (p & 1) * 64, 0, 0);
    bar();
#pragma unroll
    for (int p = 0; p < 4; ++p) gv[p] = ldsr(lb + PARK + lt * 16 + wo_ + (p >> 1) * 4096 + (p & 1) * 1024);
    {
      GAS unsigned long long* fp = (GAS unsigned long long*)(ws + WS_FLAG + fs + kvh * 128) + (lt & 3);
      const unsigned long long want = ((unsigned long long)tag.hi << 32) | tag.lo;
      unsigned got = 0;
      for (unsigned sp = 0; got != 7; ++sp) {
        unsigned now = (unsigned)__ballot(__hip_atomic_load(fp, __ATOMIC_RELAXED, __HIP_MEMORY_SCOPE_AGENT) == want) & 7;
        if (sp >= (1u << 20)) now = 7;
        asm volatile("" ::: "memory");
#pragma unroll
        for (int k = 0; k < 3; ++k)
          if ((now & ~got) >> k & 1) {
#pragma unroll
            for (int p = 0; p < 4; ++p)
              ps[k][p] = __builtin_amdgcn_raw_buffer_load_b128(rxb, lt * 16 + (p & 1) * 1024, kvh * 98304 + 49152 + k * 16384 + wo_ + (p >> 1) * 4096, 16);
          }
        got |= now;
        if (got != 7) __builtin_amdgcn_s_sleep(1);
      }
    }
    STAMP(1, 14);
#pragma unroll
    for (int p = 0; p < 4; ++p) {
      f32x4 s0 = acc[4 * (p >> 1)][2 * (p & 1)], s1 = acc[4 * (p >> 1)][2 * (p & 1) + 1];
#pragma unroll
      for (int k = 0; k < 3; ++k) add8(s0, s1, ps[k][p]);
      s0 *= (f32x4){bflo(gv[p][0]), bfhi(gv[p][0]), bflo(gv[p][1]), bfhi(gv[p][1])};
      s1 *= (f32x4){bflo(gv[p][2]), bfhi(gv[p][2]), bflo(gv[p][3]), bfhi(gv[p][3])};
      const u32x4 tp = pack8(s0, s1);
      const auto w0 = __builtin_amdgcn_permlane16_swap(tp[0], tp[2], false, false), w1 = __builtin_amdgcn_permlane16_swap(tp[1], tp[3], false, false);
      const unsigned t[4] = {w0[0], w1[0], w0[1], w1[1]};
      u32x4 o;
#pragma unroll
      for (int e = 0; e < 4; ++e) o[e] = cvtpk(bflo(xv[p][e]) + bflo(t[e]), bfhi(xv[p][e]) + bfhi(t[e]));
      __builtin_amdgcn_raw_buffer_store_b128(o, rout, vo + (p >> 1) * (64 * DM * 2) + (p & 1) * 64, 0, 0);
    }
    STAMP(1, 15);
  } else {
    const int lane = lane_id();
    const unsigned pl = lb + PARK + lane * 16, pk = lb + 159744 + lw * 256;
    const int fo = F_O + (rb * 4 + kvh) * 128;
    if (lw < 3) {
      Strm S;
      if (lw < 2) strm_x(S, ws + WS_O + (size_t)(rb * 128 + 32 * kvh) * 16384 + lw * 8192, 524288 - lw * 8192, 16384, kvh);
      else strm_w(S, (const char*)wo + (size_t)kvh * 1024 * (DM * 2) + dc * 256, 1024, DM * 2, false);
      wait_vm<8>();
      bar();
      y_iss(3);
      for (int t = 1; t <= 22; ++t) { y_iss(t + 3); wait_vm<16>(); bar(); }
      peek(rf, lb + PARK + lw * 256, fo);
      y_iss(26); wait_vm<16>(); bar();
      y_iss(27); wait_vm<16>(); bar();
      peek_check(ws, lb + PARK + lw * 256, fo, 8, tag);
      auto iss = [&](int k) { const int r = (k & 16) | ((k + SROT * (rb & 3)) & 15); issue(S, k, 4 * (r & 7) + (r >> 3)); wait_vm<16>(); bar(); };
      for (int t = 25; t <= 36; ++t) iss(t - 25);
      peek(rf, lb + PARK + 16384 + lw * 256, fo + 64);
      for (int t = 37; t <= 40; ++t) iss(t - 25);
      peek_check(ws, lb + PARK + 16384 + lw * 256, fo + 64, 8, tag);
      for (int t = 41; t <= 56; ++t) iss(t - 25);
      wait_vm<8>(); bar();
      wait_vm<0>(); bar();
    } else {
      bar();
      const rsrc_t ro = mkrsrc(ws + WS_O + (size_t)(rb * 128 + 4 * head) * 16384, 65536);
      for (int t = 1; t <= 25; t += 2) {
        const unsigned o = (t >> 1) * 5120;
        const u32x4 v0 = ldsr(pl + o), v1 = ldsr(pl + o + 1024), v2 = ldsr(pl + o + 2048), v3 = ldsr(pl + o + 3072), v4 = ldsr(pl + (t < 25 ? o + 4096 : 0));
        st16s<0>(v0, ro, lane * 16, o); st16s<1024>(v1, ro, lane * 16, o); st16s<2048>(v2, ro, lane * 16, o);
        if (t == 17) { wait_vm<11>(); signal<1>(ws, fo + dc * 8, tag); }
        bar();
        st16s<3072>(v3, ro, lane * 16, o); st16s<0>(v4, ro, lane * 16, o + 4096);
        bar();
      }
      bar(); bar(); bar();
      wait_vm<0>();
      signal<1>(ws, fo + 64 + dc * 8, tag);
      for (int t = 30; t <= 38; ++t) bar();
      signal<14>(ws, F_Y + (tile * 4 + (kvh ^ (lane & 3))) * 128 + ((lane & 3) - 1) * 8, tag);
      for (int t = 39; t <= 56; ++t) bar();
      peek(rf, pk, F_Y + (tile * 4 + kvh) * 128);
      bar();
      wait_vm<0>();
      peek_check(ws, pk, F_Y + (tile * 4 + kvh) * 128, 3, tag);
      bar();
    }
    u32x4 py[3][4];
#pragma unroll
    for (int k = 0; k < 3; ++k)
#pragma unroll
      for (int m = 0; m < 4; ++m) py[k][m] = __builtin_amdgcn_raw_buffer_load_b128(rxb, lane * 16 + m * 1024, kvh * 98304 + lw * 4096 + k * 16384, 16);
    const float bb = bflo(b2[rb * 256 + (4 * lw + kvh) * 16 + (lane & 15)]);
    bar();
    bar();
#pragma unroll
    for (int m = 0; m < 4; ++m) {
      f32x4 y0 = {bb, bb, bb, bb}, y1 = y0;
      add8(y0, y1, ldsr(pl + lw * 4096 + m * 1024));
#pragma unroll
      for (int k = 0; k < 3; ++k) add8(y0, y1, py[k][m]);
      *(LAS u32x4*)(size_t)(pl + lw * 4096 + m * 1024) = (u32x4){silu2(y0.xy), silu2(y0.zw), silu2(y1.xy), silu2(y1.zw)};
    }
    wait_lgkm0();
    bar();
  }
}

extern "C" void kernel_launch(void* const* d_in, const int* in_sizes, int n_in, void* d_out, int out_size, void* d_ws, size_t ws_size,
                              hipStream_t stream) {
  const u16* x = (const u16*)d_in[0];
  const u16* kc = (const u16*)d_in[1];
  const u16* vc = (const u16*)d_in[2];
  const u16* wq = (const u16*)d_in[3];
  const u16* wo = (const u16*)d_in[4];
  const u16* w1 = (const u16*)d_in[6];
  const u16* b1 = (const u16*)d_in[7];
  const u16* w2 = (const u16*)d_in[8];
  const u16* b2 = (const u16*)d_in[9];
  char* ws = (char*)d_ws;
  (void)hipFuncSetAttribute((const void*)k_main, hipFuncAttributeMaxDynamicSharedMemorySize, 163840);
  hipLaunchKernelGGL(k_main, dim3(256), dim3(512), 163840, stream, x, kc, vc, wq, wo, w1, b1, w2, b2, ws, (u16*)d_out);
}
```

```cpp
#include <hip/hip_runtime.h>
#include <hip/hip_bf16.h>

typedef unsigned short u16;
typedef short bf16x8 __attribute__((ext_vector_type(8)));
typedef short s16x4 __attribute__((ext_vector_type(4)));
typedef float f32x4 __attribute__((ext_vector_type(4)));
typedef float f32x2 __attribute__((ext_vector_type(2)));
typedef unsigned u32x4 __attribute__((ext_vector_type(4)));
typedef unsigned u32x2 __attribute__((ext_vector_type(2)));
typedef __bf16 bf16x2 __attribute__((ext_vector_type(2)));
typedef __amdgpu_buffer_rsrc_t rsrc_t;
#define LAS __attribute__((address_space(3)))
#define GAS __attribute__((address_space(1)))
#define DI __device__ __forceinline__
#define SGB(m, n) __builtin_amdgcn_sched_group_barrier(m, n, 0)
#define SB0() __builtin_amdgcn_sched_barrier(0)

constexpr int SEQ = 2048, DM = 1024, FF = 3584, HD = 4096;
constexpr int CODE_BYTES = 57600;
constexpr int NSP = 3;
constexpr size_t WS_FLAG = 0;
constexpr int F_EP = 0, F_X = 512, F_L = 2048, F_U = 4096, F_O = 12288, F_Y = 16384, F_S = 49152, F_P = 81920, F_END = 96256;
constexpr size_t WS_ST = 98304;
constexpr size_t WS_CS = 131072;
constexpr size_t WS_HT = 262144;
constexpr size_t WS_KV = WS_HT + 4194304;
constexpr size_t WS_O = WS_KV + 1048576;
constexpr size_t WS_U = WS_O + 16777216;
constexpr size_t WS_XB = WS_U + 7340032;
constexpr size_t WS_END = WS_XB + 25165824;

DI unsigned cvtpk(float lo, float hi) { f32x2 v = {lo, hi}; bf16x2 b = __builtin_convertvector(v, bf16x2); return __builtin_bit_cast(unsigned, b); }
DI float bflo(unsigned u) { return __uint_as_float(u << 16); }
DI float bfhi(unsigned u) { return __uint_as_float(u & 0xffff0000u); }
DI float rbf(float x) { return bflo(cvtpk(x, x)); }
template <int N> DI void wait_vm() { asm volatile("s_waitcnt vmcnt(%0)" ::"i"(N) : "memory"); }
DI void wait_lgkm0() { __builtin_amdgcn_s_waitcnt(0xC07F); }
DI void bar() { asm volatile("" ::: "memory"); __builtin_amdgcn_s_barrier(); asm volatile("" ::: "memory"); }
DI rsrc_t mkrsrc(const void* p, unsigned bytes) { return __builtin_amdgcn_make_buffer_rsrc((void*)p, 0, bytes, 0x00020000); }
DI void dma16(rsrc_t r, unsigned lds_addr, unsigned voff, unsigned soff) {
  asm volatile("s_mov_b32 m0, %0\n\ts_nop 0\n\tbuffer_load_dwordx4 %1, %2, %3 offen lds" ::"s"(lds_addr), "v"(voff), "s"(r), "s"(soff) : "memory");
}
DI void dma16s(rsrc_t r, unsigned lds_addr, unsigned voff, unsigned soff) {
  asm volatile("s_mov_b32 m0, %0\n\ts_nop 0\n\tbuffer_load_dwordx4 %1, %2, %3 offen sc1 lds" ::"s"(lds_addr), "v"(voff), "s"(r), "s"(soff) : "memory");
}
template <int OFF> DI void st16s(u32x4 v, rsrc_t r, unsigned voff, unsigned soff) {
  asm volatile("buffer_store_dwordx4 %0, %1, %2, %3 offen offset:%4 sc1\n\ts_nop 1" ::"v"(v), "v"(voff), "s"(r), "s"(soff), "i"(OFF) : "memory");
}
DI int lane_id() { int l; asm volatile("v_mbcnt_lo_u32_b32 %0, -1, 0\n\tv_mbcnt_hi_u32_b32 %0, -1, %0" : "=v"(l)); return l; }
struct Tag { unsigned lo, hi; };
DI void peek(rsrc_t rf, unsigned lds_addr, int off) {
  asm volatile("s_mov_b32 m0, %0\n\ts_nop 0\n\tbuffer_load_dword %3, %1, %2 offen sc1 lds" ::"s"(lds_addr), "s"(rf), "s"(off), "v"(lane_id() * 4) : "memory");
}
template <int GAP = 1> DI void spin_until(char* ws, int off, int n, Tag t) {
  const int ln = lane_id();
  GAS unsigned long long* p = (GAS unsigned long long*)(ws + WS_FLAG + off) + (ln < n ? ln : 0);
  const unsigned long long want = ((unsigned long long)t.hi << 32) | t.lo;
  for (unsigned sp = 0; sp < (1u << 20); ++sp) {
    if (__all(__hip_atomic_load(p, __ATOMIC_RELAXED, __HIP_MEMORY_SCOPE_AGENT) == want)) break;
    __builtin_amdgcn_s_sleep(GAP);
  }
  asm volatile("" ::: "memory");
}
DI void peek_check(char* ws, unsigned lds_addr, int off, int n, Tag t) {
  const int ln = lane_id();
  const unsigned v = *(LAS volatile unsigned*)(size_t)(lds_addr + ln * 4);
  if (!__all(ln >= 2 * n || v == ((ln & 1) ? t.hi : t.lo))) spin_until(ws, off, n, t);
}
template <long MASK> DI void signal(char* ws, int off, Tag t) {
  const u32x2 v = {t.lo, t.hi};
  asm volatile("s_mov_b64 exec, %3\n\tglobal_store_dwordx2 %0, %1, %2 sc1\n\ts_mov_b64 exec, -1" ::"v"(off), "v"(v), "s"(ws + WS_FLAG), "s"(MASK) : "memory");
}
DI void dma4k(rsrc_t r, unsigned lds_addr, unsigned voff, unsigned soff) {
  asm volatile("s_mov_b32 m0, %0\n\ts_nop 0\n\tbuffer_load_dwordx4 %1, %2, %3 offen sc1 lds\n\tbuffer_load_dwordx4 %1, %2, %3 offen offset:1024 sc1 lds\n\t"
               "buffer_load_dwordx4 %1, %2, %3 offen offset:2048 sc1 lds\n\tbuffer_load_dwordx4 %1, %2, %3 offen offset:3072 sc1 lds"
               ::"s"(lds_addr), "v"(voff), "s"(r), "s"(soff) : "memory");
}
template <int W> DI void pf_line(int wid, rsrc_t r, unsigned voff, unsigned soff) {
  asm volatile("s_cmp_lg_u32 %0, %4\n\ts_cbranch_scc1 1f\n\tbuffer_load_dword v255, %1, %2, %3 offen\n1:"
               ::"s"(wid), "v"(voff), "s"(r), "s"(soff), "i"(W) : "scc", "memory", "v255");
}
template <int CTRL> DI float dppf(float x) { return __builtin_bit_cast(float, __builtin_amdgcn_mov_dpp(__builtin_bit_cast(int, x), CTRL, 0xf, 0xf, true)); }
DI float wave_sum(float v) {
  v += dppf<0xB1>(v); v += dppf<0x4E>(v); v += dppf<0x141>(v); v += dppf<0x140>(v);
  auto a = __builtin_amdgcn_permlane16_swap(__float_as_uint(v), __float_as_uint(v), false, false);
  v = __uint_as_float(a[0]) + __uint_as_float(a[1]);
  auto b = __builtin_amdgcn_permlane32_swap(__float_as_uint(v), __float_as_uint(v), false, false);
  return __uint_as_float(b[0]) + __uint_as_float(b[1]);
}

DI f32x2 ln16(unsigned (&w)[8]) {
  f32x2 v[8];
#pragma unroll
  for (int i = 0; i < 8; ++i) v[i] = (f32x2){bflo(w[i]), bfhi(w[i])};
  const f32x2 s = ((v[0] + v[1]) + (v[2] + v[3])) + ((v[4] + v[5]) + (v[6] + v[7]));
  const float mean = wave_sum(s[0] + s[1]) * (1.f / 1024.f);
  f32x2 q0 = {0.f, 0.f}, q1 = q0;
#pragma unroll
  for (int i = 0; i < 8; i += 2) {
    const f32x2 d0 = v[i] - mean, d1 = v[i + 1] - mean;
    q0 = __builtin_elementwise_fma(d0, d0, q0); q1 = __builtin_elementwise_fma(d1, d1, q1);
  }
  q0 += q1;
  const float var = wave_sum(q0[0] + q0[1]) * (1.f / 1024.f);
  const float mb = rbf(mean);
  const float rs = __builtin_amdgcn_rcpf(rbf(__builtin_amdgcn_sqrtf(rbf(rbf(var) + 1e-5f))));
#pragma unroll
  for (int i = 0; i < 8; ++i) {
    const f32x2 d = v[i] - mb;
    const unsigned c = cvtpk(d[0], d[1]);
    const f32x2 r = (f32x2){bflo(c), bfhi(c)} * rs;
    w[i] = cvtpk(r[0], r[1]);
  }
  return (f32x2){mb, rs};
}

struct Frags { bf16x8 x[8]; bf16x8 w[4]; };
struct Addr { unsigned x, w[4]; };
template <int XS = 1024> DI void load_frags(Frags& f, const Addr& a) {
#pragma unroll
  for (int c = 0; c < 4; ++c) {
    s16x4 a0 = __builtin_amdgcn_ds_read_tr16_b64_v4i16((LAS s16x4*)(size_t)(a.w[c]));
    s16x4 a1 = __builtin_amdgcn_ds_read_tr16_b64_v4i16((LAS s16x4*)(size_t)(a.w[c] + 4096));
    f.w[c] = __builtin_shufflevector(a0, a1, 0, 1, 2, 3, 4, 5, 6, 7);
  }
#pragma unroll
  for (int i = 0; i < 8; ++i) f.x[i] = *(LAS bf16x8*)(size_t)(a.x + i * XS);
}
template <bool WA, bool Z> DI void mma_all(f32x4 (&acc)[8][4], const Frags& f, const f32x4* c0) {
  const f32x4 zero = {0.f, 0.f, 0.f, 0.f};
#pragma unroll
  for (int i = 0; i < 8; ++i)
#pragma unroll
    for (int jj = 0; jj < 4; ++jj) {
      const int j = (i & 1) ? 3 - jj : jj;
      if (WA) acc[i][j] = __builtin_amdgcn_mfma_f32_16x16x32_bf16(f.w[j], f.x[i], Z ? (c0 ? c0[j] : zero) : acc[i][j], 0, 0, 0);
      else    acc[i][j] = __builtin_amdgcn_mfma_f32_16x16x32_bf16(f.x[i], f.w[j], Z ? (c0 ? c0[j] : zero) : acc[i][j], 0, 0, 0);
    }
}
DI void addr_add(Addr& a, int d) {
  a.x += d;
#pragma unroll
  for (int c = 0; c < 4; ++c) a.w[c] += d;
}
template <bool WA, int NSLOT, int SLOTB, bool Z = false>
DI void gstep(f32x4 (&acc)[8][4], Frags& Fn, const Frags& Fc, Addr& a, int& slot, const f32x4* c0 = nullptr) {
  SB0();
  load_frags(Fn, a);
  mma_all<WA, Z>(acc, Fc, c0);
  slot = (slot + 1 == NSLOT) ? 0 : slot + 1;
  addr_add(a, (slot == 0) ? -(NSLOT - 1) * SLOTB : SLOTB);
  asm volatile("" : "+v"(a.x), "+v"(a.w[0]), "+v"(a.w[1]), "+v"(a.w[2]), "+v"(a.w[3]));
#pragma unroll
  for (int i = 0; i < 8; ++i) { SGB(0x8, 1); SGB(0x100, 1); SGB(0x8, 1); SGB(0x100, 1); SGB(0x8, 1); }
#pragma unroll
  for (int i = 0; i < 6; ++i) { SGB(0x8, 1); SGB(0x4, 1); SGB(0x2, 1); }
  SGB(0x8, 2);
  SB0();
  wait_lgkm0();
  bar();
}
template <int MODE, bool CS, bool Z = false>
DI void qstep(f32x4 (&acc)[8][4], f32x4 (&cs)[2], u32x4& ones, unsigned onext, Frags& Fn, const Frags& Fc, Addr& a, int& xs, int& wsl) {
  const f32x4 zero = {0.f, 0.f, 0.f, 0.f};
  SB0();
  load_frags<MODE == 2 ? 1024 : 2048>(Fn, a);
  if (CS) {
#pragma unroll
    for (int j = 0; j < 2; ++j) cs[j] = __builtin_amdgcn_mfma_f32_16x16x32_bf16(Fc.w[j], __builtin_bit_cast(bf16x8, ones), Z ? zero : cs[j], 0, 0, 0);
    ones = (u32x4){onext, onext, onext, onext};
    asm volatile("" : "+v"(ones));
  }
  mma_all<true, Z>(acc, Fc, nullptr);
  if (MODE == 2) addr_add(a, 24576);
  else {
    wsl = (wsl + 1) & 3;
    const int dw = wsl ? 8192 : -24576;
#pragma unroll
    for (int c = 0; c < 4; ++c) a.w[c] += dw;
    a.x ^= 64;
    if (MODE == 1) { xs = (xs == 2) ? 0 : xs + 1; a.x += xs ? 32768 : -65536; }
  }
  asm volatile("" : "+v"(a.x), "+v"(a.w[0]), "+v"(a.w[1]), "+v"(a.w[2]), "+v"(a.w[3]));
#pragma unroll
  for (int i = 0; i < 8; ++i) { SGB(0x8, 1); SGB(0x100, 1); SGB(0x8, 1); SGB(0x100, 1); SGB(0x8, 1); }
#pragma unroll
  for (int i = 0; i < 7; ++i) { SGB(0x8, 1); SGB(0x4, 1); SGB(0x2, 1); }
  if (CS) { SGB(0x8, 1); SGB(0x2, 2); SGB(0x8, 1); SGB(0x2, 2); }
  SGB(0x8, 1);
  SB0();
  wait_lgkm0();
  bar();
}
DI void addr_init(Addr& a, unsigned xbase, unsigned wbase, int wj, int cx, int lane) {
  const int g = lane >> 4, q = (lane >> 2) & 3, p = lane & 3, row = 4 * g + q;
  a.x = xbase + lane * 16;
#pragma unroll
  for (int c = 0; c < 4; ++c) a.w[c] = wbase + row * 256 + (((2 * (4 * wj + (c ^ cx)) + (p >> 1)) ^ ((row & 7) << 1)) * 16) + (p & 1) * 8;
}
template <bool WA, int NSLOT, int SLOTB> DI void gemm_steps(f32x4 (&acc)[8][4], Frags& F0, Frags& F1, Addr& a, int& slot, int n) {
  for (int t = 0; t < n; t += 2) {
    gstep<WA, NSLOT, SLOTB>(acc, F0, F1, a, slot);
    gstep<WA, NSLOT, SLOTB>(acc, F1, F0, a, slot);
  }
}

DI u32x4 pack8(const f32x4& a, const f32x4& b) { return (u32x4){cvtpk(a[0], a[1]), cvtpk(a[2], a[3]), cvtpk(b[0], b[1]), cvtpk(b[2], b[3])}; }
DI f32x2 gate2(f32x2 u, f32x2 z) {
  f32x2 e = {__builtin_amdgcn_exp2f(z[0]), __builtin_amdgcn_exp2f(z[1])};
  e += 1.f;
  return u * (f32x2){__builtin_amdgcn_rcpf(e[0]), __builtin_amdgcn_rcpf(e[1])};
}
DI unsigned gelu2(f32x2 u) {
  const f32x2 r = gate2(u, u * __builtin_elementwise_fma(u * u, (f32x2){-0.1029432397f, -0.1029432397f}, (f32x2){-2.302208198f, -2.302208198f}));
  return cvtpk(r[0], r[1]);
}
DI unsigned silu2(f32x2 y) { const f32x2 r = gate2(y, y * -1.4426950409f); return cvtpk(r[0], r[1]); }
DI u32x4 ldsr(unsigned a) { return *(LAS u32x4*)(size_t)a; }
DI void add8(f32x4& a, f32x4& b, const u32x4& w) {
  a += (f32x4){bflo(w[0]), bfhi(w[0]), bflo(w[1]), bfhi(w[1])};
  b += (f32x4){bflo(w[2]), bfhi(w[2]), bflo(w[3]), bfhi(w[3])};
}

struct Sc { f32x4 v[2][2]; };
struct Att { bf16x8 q[2][4]; f32x4 o[8][2]; f32x4 negm[2]; Sc sE, sO; u32x4 pO[2]; bf16x8 pre[4], ones; f32x4 l[2]; };
DI float xg_max(float m) {
  auto a = __builtin_amdgcn_permlane16_swap(__float_as_uint(m), __float_as_uint(m), false, false);
  m = fmaxf(__uint_as_float(a[0]), __uint_as_float(a[1]));
  auto b = __builtin_amdgcn_permlane32_swap(__float_as_uint(m), __float_as_uint(m), false, false);
  return fmaxf(__uint_as_float(b[0]), __uint_as_float(b[1]));
}
DI float row_max(const Sc& s, int qb) {
  const f32x4 a = s.v[0][qb], b = s.v[1][qb];
  return xg_max(fmaxf(fmaxf(fmaxf(a[0], a[1]), fmaxf(a[2], a[3])), fmaxf(fmaxf(b[0], b[1]), fmaxf(b[2], b[3]))));
}
DI bf16x8 ldfrag(unsigned a) { return *(LAS bf16x8*)(size_t)a; }
DI void sm1(Sc& s, int e, u32x4 (&w)[2], int part = 3) {
  if (e < 16 && (part & 1)) {
    float x = __builtin_amdgcn_exp2f(s.v[e >> 3][(e >> 2) & 1][e & 3]);
    asm volatile("" : "+v"(x));
    s.v[e >> 3][(e >> 2) & 1][e & 3] = x;
  }
  if (e >= 2 && e <= 16 && !(e & 1) && (part & 2)) {
    const int kt = (e - 1) >> 3, qb = ((e - 1) >> 2) & 1, r = (e - 1) & 3;
    unsigned c = cvtpk(s.v[kt][qb][r - 1], s.v[kt][qb][r]);
    asm volatile("" : "+v"(c));
    w[qb][2 * kt + (r >> 1)] = c;
  }
}
#define SETPRIO(x) __builtin_amdgcn_s_setprio(x)
#define MFMA16(a, b, c) __builtin_amdgcn_mfma_f32_16x16x32_bf16(a, b, c, 0, 0, 0)
#define PFRAG(w) __builtin_bit_cast(bf16x8, w)
DI void mfma_acc(f32x4& c, const bf16x8& a, const u32x4& b) { asm volatile("v_mfma_f32_16x16x32_bf16 %0, %1, %2, %0" : "+v"(c) : "v"(a), "v"(b)); }
template <int POS, int n, class F> DI void att_slot(Att& A, bf16x8 (&f)[36], u32x4 (&pE)[2], unsigned kv, unsigned kvn, F& gap) {
  constexpr int u = n >> 3, k = n & 7;
  constexpr bool isqk = (u & 1) ? (u == 1 || POS < 2) : (u == 0 && POS == 0);
  const f32x4 zero = {0.f, 0.f, 0.f, 0.f};
  if constexpr (k == 0) SETPRIO(3 - u);
#pragma unroll
  for (int qb = 0; qb < 2; ++qb) {
    if constexpr (isqk && (k & 3) != 0) mfma_acc(u == 1 ? A.sO.v[k >> 2][qb] : A.sE.v[k >> 2][qb], f[n], __builtin_bit_cast(u32x4, A.q[qb][k & 3]));
    else if constexpr (isqk && u == 1) A.sO.v[k >> 2][qb] = MFMA16(f[n], A.q[qb][0], A.negm[qb]);
    else if constexpr (isqk) A.sE.v[k >> 2][qb] = MFMA16(f[n], A.q[qb][0], u ? A.negm[qb] : zero);
    else if constexpr (POS == 0) A.o[k][qb] = MFMA16(f[n], PFRAG(pE[qb]), zero);
    else if constexpr (u == 2) mfma_acc(A.o[k][qb], f[n], pE[qb]);
    else mfma_acc(A.o[k][qb], f[n], A.pO[qb]);
    SB0();
    if (qb == 1) {
      if constexpr (n < 28) f[n + 4] = ldfrag(kv + (n + 4) * 1024);
      else if constexpr (POS < 2) f[n + 4] = ldfrag(kvn + (n - 28) * 1024);
      SB0();
    }
    if constexpr (POS == 0 && u == 1) {
      if (k == 0 && qb == 0) sm1(A.sE, 0, pE);
      sm1(A.sE, 2 * k + 1 + qb, pE);
    }
    if constexpr (POS > 0 && n <= 14) sm1(A.sE, n + 2, pE, 1 + qb);
    if constexpr (n >= 14 && n <= (POS < 2 ? 30 : 15)) sm1(A.sO, n - 14, A.pO, 1 + qb);
    if constexpr (POS < 2 && n >= 30) sm1(A.sE, n - 30, pE, 1 + qb);
    if constexpr (POS == 2 && u == 2) sm1(A.sO, 2 + 2 * k + qb, A.pO);
    SB0();
  }
  if constexpr (!isqk && (k == 2 || k == 4)) {
    constexpr int qb = k == 4;
    if constexpr (POS == 0) A.l[qb] = MFMA16(A.ones, PFRAG(pE[qb]), zero);
    else if constexpr (u == 2) mfma_acc(A.l[qb], A.ones, pE[qb]);
    else mfma_acc(A.l[qb], A.ones, A.pO[qb]);
  }
  gap(n);
  SB0();
  if constexpr (POS == 2 && n == 23) { asm volatile("s_nop 1"); SB0(); }
  if constexpr ((POS == 2 && n == 31) || (POS == 0 && n == 7)) { asm volatile("s_nop 9"); SB0(); }
  if constexpr (POS == 0 && n == 7) {
#pragma unroll
    for (int qb = 0; qb < 2; ++qb) {
      const float mx = row_max(A.sE, qb);
      A.sE.v[0][qb] -= mx; A.sE.v[1][qb] -= mx;
      A.negm[qb] = (f32x4){-mx, -mx, -mx, -mx};
    }
    SB0();
  }
  if constexpr (n < 31) att_slot<POS, n + 1>(A, f, pE, kv, kvn, gap);
}
template <int POS, class F> DI void att_tile(Att& A, unsigned kv, unsigned kvn, F&& gap) {
  bf16x8 f[36];
  u32x4 pE[2];
  asm volatile("" : "+v"(kv), "+v"(kvn));
#pragma unroll
  for (int i = 0; i < 4; ++i) f[i] = A.pre[i];
  SB0();
  att_slot<POS, 0>(A, f, pE, kv, kvn, gap);
  if (POS < 2) {
#pragma unroll
    for (int i = 0; i < 4; ++i) A.pre[i] = f[32 + i];
  }
}
DI void att_redo(Att& A, rsrc_t rkv, unsigned vo) {
  const f32x4 zero = {0.f, 0.f, 0.f, 0.f};
  float m[2] = {-3.0e38f, -3.0e38f};
  A.l[0] = A.l[1] = zero;
#pragma unroll
  for (int d = 0; d < 8; ++d) A.o[d][0] = A.o[d][1] = zero;
  for (int blk = 0; blk < 16; ++blk) {
    const int kb = (blk ? 2 * blk - 1 : 0) * 8192, vb = (blk < 15 ? 2 * blk + 2 : 31) * 8192;
    Sc s;
#pragma unroll
    for (int n = 0; n < 8; ++n) {
      const bf16x8 kf = PFRAG(__builtin_amdgcn_raw_buffer_load_b128(rkv, vo + n * 1024, kb, 16));
#pragma unroll
      for (int qb = 0; qb < 2; ++qb) s.v[n >> 2][qb] = MFMA16(kf, A.q[qb][n & 3], (n & 3) ? s.v[n >> 2][qb] : zero);
    }
#pragma unroll
    for (int qb = 0; qb < 2; ++qb) {
      const float mn = fmaxf(m[qb], row_max(s, qb)), fct = __builtin_amdgcn_exp2f(m[qb] - mn);
      m[qb] = mn;
      A.l[qb] *= fct;
      s.v[0][qb] -= mn; s.v[1][qb] -= mn;
#pragma unroll
      for (int d = 0; d < 8; ++d) A.o[d][qb] *= fct;
    }
    u32x4 p[2];
#pragma unroll
    for (int i = 0; i < 17; ++i) sm1(s, i, p);
#pragma unroll
    for (int qb = 0; qb < 2; ++qb) A.l[qb] = MFMA16(A.ones, PFRAG(p[qb]), A.l[qb]);
#pragma unroll
    for (int n = 0; n < 8; ++n) {
      const bf16x8 vf = PFRAG(__builtin_amdgcn_raw_buffer_load_b128(rkv, vo + n * 1024, vb, 16));
#pragma unroll
      for (int qb = 0; qb < 2; ++qb) A.o[n][qb] = MFMA16(vf, PFRAG(p[qb]), A.o[n][qb]);
    }
  }
}

__global__ __launch_bounds__(512, 2) __attribute__((amdgpu_num_vgpr(127))) void k_main(const u16* __restrict__ x, const u16* __restrict__ kc, const u16* __restrict__ vc, const u16* __restrict__ wq, const u16* __restrict__ wo,
                                                 const u16* __restrict__ w1, const u16* __restrict__ b1, const u16* __restrict__ w2,
                                                 const u16* __restrict__ b2, char* __restrict__ ws, u16* __restrict__ out) {
  extern __shared__ __attribute__((aligned(16))) char smem[];
  LAS char* lds = (LAS char*)smem;
  const unsigned lb = (unsigned)(size_t)lds;
  const int tid = threadIdx.x, wid = __builtin_amdgcn_readfirstlane(tid >> 6);
  const int g8 = blockIdx.x & 7, idx = blockIdx.x >> 3;
  const rsrc_t rcode = mkrsrc((const void*)(__builtin_amdgcn_s_getpc() & ~255ull), CODE_BYTES);
  const unsigned code_o = (idx & 7) * 8192;
  pf_line<0>(wid, rcode, (tid & 63) * 128, code_o);
  SB0();
  u32x4 xa, xb;
  u32x2 ep;
  asm volatile("global_load_dwordx4 %0, %3, %4\n\tglobal_load_dwordx4 %1, %3, %4 offset:1024\n\tglobal_load_dwordx2 %2, %5, %6 sc1"
               : "=&v"(xa), "=&v"(xb), "=&v"(ep) : "v"((tid & 63) * 16), "s"(x + (size_t)(8 * blockIdx.x + wid) * DM), "v"(F_EP), "s"(ws + WS_FLAG) : "memory");
  SB0();
  const bool hasU = idx < 28;
  const int uh = g8 & 1, dq = idx & 3, ft = 7 * (g8 >> 1) + (idx >> 2), ut = ft * 4 + dq;
  const int kvh = g8 >> 1, head = 8 * kvh + (idx & 7), rb = 4 * (g8 & 1) + (idx >> 3);
  const int dc = idx & 7, tile = rb * 8 + dc;
  const int lw = wid - 4, wi = (wid >> 1) & 1, wj = wid & 1;
  constexpr int QS = 24576;
  constexpr int PARK = 98304;
  const rsrc_t rf = mkrsrc(ws + WS_FLAG, F_END + 256);
  const rsrc_t rkv = mkrsrc(ws + WS_KV + (size_t)kvh * 262144, 262144);
  const rsrc_t rxb = mkrsrc(ws + WS_XB + (size_t)tile * 393216, 393216);
  struct Strm { rsrc_t r; unsigned v0, v1, step, p0, p1, p2; int rot; };
  auto strm_x = [&](Strm& S, const char* base, unsigned bytes, unsigned step, int xr) {
    const int ln = lane_id();
    S.r = mkrsrc(base, bytes); S.v0 = ln * 16 + (xr & 1) * 1024; S.v1 = S.v0 ^ 1024; S.step = step;
    S.p0 = (xr >> 1) * 2048; S.p1 = S.p0 ^ 2048; S.p2 = 4096; S.rot = 0;
  };
  auto strm_w = [&](Strm& S, const char* base, unsigned rows, unsigned ld, bool nat) {
    const int ln = lane_id(), q4 = ln >> 4;
    const unsigned c0 = ((ln & 15) ^ (q4 << 1)) * 16;
    S.r = mkrsrc(base, (rows - 1) * ld + 256); S.v0 = q4 * ld + c0; S.v1 = ((nat ? 8 : 4) + q4) * ld + (c0 ^ 128); S.step = 32 * ld;
    S.p0 = 0; S.p1 = (nat ? 16 : 8) * ld; S.p2 = (nat ? 4 : 16) * ld; S.rot = 0;
  };
  auto issue = [&](const Strm& S, int k, int src = -1) {
    const unsigned s = lb + (k & 3) * QS + lw * 8192;
    if (src < 0) src = (k + S.rot) & 31;
#pragma unroll
    for (int i = 0; i < 4; ++i) {
      const unsigned so = src * S.step + ((i & 1) ? S.p1 : S.p0) + (i >> 1) * S.p2;
      dma16s(S.r, s + i * 2048, S.v0, so); dma16s(S.r, s + i * 2048 + 1024, S.v1, so);
    }
  };
  constexpr int QW = 98304, HST = 131072, HRS = 2112, RST = HST + 8 * HRS, PKQ = 148480, STT = 151552, CSX = 153600;
  const int lane0 = tid & 63;
  const rsrc_t rq = lw < 2 ? mkrsrc(x + (size_t)(rb * 256 + lw * 128) * DM, 128 * DM * 2) : mkrsrc((const char*)wq + head * 256, (DM - 1) * HD * 2 + 256);
  const unsigned qv0 = lw < 2 ? (lane0 >> 3) * 2048 + (((lane0 & 7) ^ (lane0 >> 4)) * 16) : (lane0 >> 4) * (HD * 2) + (((lane0 & 15) ^ ((lane0 >> 4) << 1)) * 16);
  const unsigned qv1 = lw < 2 ? (qv0 ^ 64) + 8 * 2048 : (qv0 ^ 128) + 8 * (HD * 2);
  const bool qx = lw < 2;
  const unsigned qp1 = qx ? 32768 : 16 * HD * 2, qp2 = qx ? 65536 : 4 * HD * 2;
  const unsigned qlb = lb + (qx ? lw * 16384 : QW), qlw = qx ? 98304 : 32768, qom = qx ? 0x20780 : 0x7c0000;
  unsigned ql = qx ? 65536 : 0, qdl = 8192, qo = rb * (qx ? 256 : 4 * 32 * HD * 2), qdo = qx ? 131072 : 32 * HD * 2;
  auto issq = [&]() {
    const unsigned s = qlb + ql;
    dma16(rq, s, qv0, qo); dma16(rq, s + 1024, qv1, qo);
    dma16(rq, s + 2048, qv0, qo + qp1); dma16(rq, s + 3072, qv1, qo + qp1);
    dma16(rq, s + 4096, qv0, qo + qp2); dma16(rq, s + 5120, qv1, qo + qp2);
    dma16(rq, s + 6144, qv0, qo + qp1 + qp2); dma16(rq, s + 7168, qv1, qo + qp1 + qp2);
    ql += qdl; ql = ql >= qlw ? ql - qlw : ql; qdl ^= qx ? (8192 ^ 24576) : 0;
    qo = (qo + qdo) & qom; qdo ^= qx ? 128 : 0;
  };
  auto h_out = [&](int it) {
    const rsrc_t rh = mkrsrc(ws + WS_HT + (size_t)(blockIdx.x >> 2) * 16384 + (blockIdx.x & 3) * 256, 3 * 1048576 + 16384);
    const unsigned a0 = HST + ((lane0 >> 2) & 3) * HRS + (it * 4 + (lane0 >> 4)) * 32 + (lane0 & 3) * 8;
    s16x4 lo = __builtin_amdgcn_ds_read_tr16_b64_v4i16((LAS s16x4*)(lds + a0));
    s16x4 hi = __builtin_amdgcn_ds_read_tr16_b64_v4i16((LAS s16x4*)(lds + a0 + 4 * HRS));
    st16s<0>(__builtin_bit_cast(u32x4, __builtin_shufflevector(lo, hi, 0, 1, 2, 3, 4, 5, 6, 7)), rh, (lane0 >> 4) * 1024 + (lane0 & 15) * 16,
             (it >> 2) * 1048576 + (it & 3) * 4096);
  };
  auto st_out = [&]() { st16s<0>(*(LAS u32x4*)(lds + RST + (lane0 & 3) * 16), mkrsrc(ws + WS_ST + blockIdx.x * 64, 64), lane0 * 16, 0); };
  Tag tag;
  {
    const int lane = lane0;
    if (wid >= 4 && wid < 7) {
      issq(); issq();
      if (lw == 2) { issq(); issq(); wait_vm<32>(); } else wait_vm<16>();
    } else wait_vm<0>();
    asm volatile("" : "+v"(xa), "+v"(xb), "+v"(ep));
    unsigned v[8] = {xa[0], xa[1], xa[2], xa[3], xb[0], xb[1], xb[2], xb[3]};
    const unsigned long long e64 = ((unsigned long long)(unsigned)__builtin_amdgcn_readfirstlane(ep[1]) << 32) | (unsigned)__builtin_amdgcn_readfirstlane(ep[0]);
    const unsigned long long tg = e64 * 0x9E3779B97F4A7C15ull + 0x632BE59BD9B4E019ull;
    tag.lo = (unsigned)tg; tag.hi = (unsigned)(tg >> 32);
    *(LAS f32x2*)(lds + RST + wid * 8) = ln16(v);
    ln16(v);
    *(LAS u32x4*)(lds + HST + wid * HRS + lane * 16) = (u32x4){v[0], v[1], v[2], v[3]};
    *(LAS u32x4*)(lds + HST + wid * HRS + 1024 + lane * 16) = (u32x4){v[4], v[5], v[6], v[7]};
    wait_lgkm0();
  }
  bf16x8 qh[8];
  f32x4 uk[4][4];
  auto kv8 = [&](int t, int slot) {
    const unsigned v = lane_id() * 16;
    dma4k(rkv, lb + slot * 32768 + lw * 8192, v, t * 32768 + lw * 8192);
    dma4k(rkv, lb + slot * 32768 + lw * 8192 + 4096, v, t * 32768 + lw * 8192 + 4096);
  };
  if (wid < 4) {
    f32x4 acc[8][4], cs[2];
    Frags F0, F1;
    Addr a;
    int xs = 2, wsl = 1;
    {
      const int ln = lane_id(), r = ln & 15;
      addr_init(a, 0, lb + QW, wj, 2 * wi, ln);
      a.x = lb + 65536 + wi * 16384 + r * 128 + (((ln >> 4) ^ (r >> 1)) * 16);
    }
    bar();
    load_frags<2048>(F0, a);
    a.x ^= 64;
#pragma unroll
    for (int c = 0; c < 4; ++c) a.w[c] += 8192;
    wait_lgkm0();
    bar();
    {
      const unsigned o1 = 0x3f803f80;
      u32x4 ones = {o1, o1, o1, o1};
      qstep<1, true, true>(acc, cs, ones, o1, F1, F0, a, xs, wsl);
      for (int t = 0; t < 2; ++t) {
        const unsigned on = t == 0 ? o1 : 0;
        qstep<0, true>(acc, cs, ones, on, F0, F1, a, xs, wsl);
        qstep<1, true>(acc, cs, ones, on, F1, F0, a, xs, wsl);
      }
      const int ln = lane_id();
      const unsigned csa = lb + CSX + ((ln & 15) ? 7168 : (64 * wj + 32 * wi) * 4) + (ln & 48);
      *(LAS f32x4*)(size_t)csa = cs[0];
      *(LAS f32x4*)(size_t)(csa + 64) = cs[1];
      for (int t = 0; t < 13; ++t) { qstep<0, false>(acc, cs, ones, 0, F0, F1, a, xs, wsl); qstep<1, false>(acc, cs, ones, 0, F1, F0, a, xs, wsl); }
      addr_init(a, lb + wi * 8192, lb + 16384, wj, 0, lane_id());
      qstep<2, false>(acc, cs, ones, 0, F0, F1, a, xs, wsl);
    }
    f32x4 c0[4];
    {
      const int ln = lane_id(), g = ln >> 4;
      u32x2 bv[4];
#pragma unroll
      for (int j = 0; j < 4; ++j) bv[j] = *(LAS u32x2*)(lds + CSX + 5120 + (64 * wj + 16 * j + 4 * g) * 2);
      f32x4 cq[4];
#pragma unroll
      for (int j = 0; j < 4; ++j) cq[j] = *(LAS f32x4*)(lds + CSX + 512 + (64 * wj + 16 * (j ^ (2 * wi)) + 4 * g) * 4);
      f32x2 st[8];
#pragma unroll
      for (int i = 0; i < 8; ++i) st[i] = *(LAS f32x2*)(lds + STT + (128 * wi + 16 * i + (ln & 15)) * 8);
      wait_lgkm0();
      bar();
      const unsigned qa = lb + 98304 + wi * 32768 + (2 * wj + wi) * 1024 + ln * 16, qb = qa ^ 1024;
#pragma unroll
      for (int i = 0; i < 8; ++i) {
        const float sc = st[i][1] * 0.12751743082f, nm = -st[i][0];
#pragma unroll
        for (int mm = 0; mm < 2; ++mm)
          *(LAS u32x4*)(size_t)((mm ? qb : qa) + i * 4096) =
              pack8((acc[i][2 * mm] + cq[2 * mm] * nm) * sc, (acc[i][2 * mm + 1] + cq[2 * mm + 1] * nm) * sc);
      }
#pragma unroll
      for (int j = 0; j < 4; ++j) c0[j] = uh ? (f32x4){0.f, 0.f, 0.f, 0.f} : (f32x4){bflo(bv[j][0]), bfhi(bv[j][0]), bflo(bv[j][1]), bfhi(bv[j][1])};
    }
    if (hasU) {
      int slot = 1;
      gstep<true, 4, QS, true>(acc, F1, F0, a, slot, c0);
      gemm_steps<true, 4, QS>(acc, F0, F1, a, slot, 30);
      gstep<true, 4, QS>(acc, F0, F1, a, slot);
      const rsrc_t rux = mkrsrc(ws + WS_XB + (size_t)((tile ^ 32) * 4 + kvh) * 98304 + 49152 + wid * 8192, 8192);
      const int ln = lane_id();
#pragma unroll
      for (int ii = 0; ii < 4; ++ii) {
        st16s<0>(pack8(acc[2 * ii + 1][0], acc[2 * ii + 1][1]), rux, ln * 16, ii * 2048);
        st16s<1024>(pack8(acc[2 * ii + 1][2], acc[2 * ii + 1][3]), rux, ln * 16, ii * 2048);
#pragma unroll
        for (int j = 0; j < 4; ++j) uk[ii][j] = acc[2 * ii][j];
      }
    }
  } else {
    const int lane = lane0;
    Strm U;
    if (lw < 2) strm_x(U, ws + WS_HT + (size_t)dq * 1048576 + uh * 524288 + lw * 8192, 524288 - lw * 8192, 16384, uh);
    else strm_w(U, (const char*)w1 + (size_t)uh * 1024 * (FF * 2) + ft * 256, 1024, FF * 2, true);
    if (!hasU) U.r = mkrsrc(ws, 0);
    U.rot = 8 * dq;
    const unsigned pk = lb + PKQ + lw * 512;
    if (lw < 3) {
      if (lw < 2) wait_vm<0>(); else wait_vm<24>();
      bar();
      if (lw < 2) { issq(); issq(); } else wait_vm<16>();
      bar();
      if (lw < 2) issq();
      for (int t = 1; t <= 21; ++t) { issq(); wait_vm<16>(); bar(); }
      peek(rf, pk, F_L + lw * 512); peek(rf, pk + 256, F_L + lw * 512 + 256);
      for (int t = 22; t <= 26; ++t) { issq(); wait_vm<16>(); bar(); }
      issq(); wait_vm<16>();
    } else {
      peek(mkrsrc(b1, FF * 2), lb + CSX + 5120, ft * 256);
      bar();
      for (int t = 0; t <= 7; ++t) { h_out(2 * t); h_out(2 * t + 1); bar(); }
      st_out();
      st16s<0>(*(LAS u32x4*)(lds + CSX + (lane & 31) * 16), mkrsrc(ws + WS_CS + (head * 8 + rb) * 512, 512), lane * 16, 0);
      bar(); bar(); bar();
      wait_vm<0>();
      signal<1>(ws, F_L + blockIdx.x * 8, tag);
      for (int t = 11; t <= 21; ++t) bar();
      peek(rf, pk, F_L + lw * 512); peek(rf, pk + 256, F_L + lw * 512 + 256);
      for (int t = 22; t <= 26; ++t) bar();
      wait_vm<0>();
    }
    peek_check(ws, pk, F_L + lw * 512, 32, tag); peek_check(ws, pk + 256, F_L + lw * 512 + 256, 32, tag);
    bar();
    if (blockIdx.x == 0 && lw == 3) signal<1>(ws, F_EP, tag);
    if (lw == 2) issq();
    if (lw == 3) {
      const rsrc_t rs = mkrsrc(ws + WS_ST + rb * 2048, 2048);
      dma16s(rs, lb + STT, lane * 16, 0); dma16s(rs, lb + STT + 1024, lane * 16, 1024);
      dma4k(mkrsrc(ws + WS_CS + head * 4096, 4096), lb + CSX + 1024, lane * 16, 0);
    }
    const bool us = lw < 3;
    if (us) issue(U, 0);
    wait_vm<16>(); bar();
    if (us) wait_vm<8>(); else wait_vm<0>();
    bar();
    if (us) issue(U, 1);
    else {
      const LAS f32x2* cp = (const LAS f32x2*)(lds + CSX + 1024) + lane;
      *(LAS f32x2*)(lds + CSX + 512 + lane * 8) = ((cp[0] + cp[64]) + (cp[128] + cp[192])) + ((cp[256] + cp[320]) + (cp[384] + cp[448]));
      wait_lgkm0();
    }
    bar();
    wait_vm<8>(); bar();
    if (us) issue(U, 2);
    wait_vm<8>();
    bar();
    bar();
    if (hasU) {
      if (us) { issue(U, 3); for (int t = 1; t <= 28; ++t) { issue(U, t + 3); wait_vm<16>(); bar(); } }
      else {
        for (int t = 1; t <= 26; ++t) bar();
        spin_until(ws, F_X + kvh * 128, 8, tag);
        pf_line<7>(wid, rcode, lane * 128, code_o);
        bar(); bar();
      }
      wait_vm<8>(); bar();
      kv8(0, 0); wait_vm<8>(); bar();
      kv8(1, 1); bar();
      kv8(2, 2); wait_vm<8>(); bar();
    }
  }
  if (!hasU) {
    const int sp = g8 * 4 + (idx - 28), lane = lane_id(), g = lane >> 4;
    unsigned e[4][8];
#pragma unroll
    for (int i = 0; i < 4; ++i) {
      const int kh = sp >> 3, unit = (sp & 7) * 4 + i, frag = wid;
      const bool isK = unit == 0 || ((unit & 1) && unit < 31);
      const u16* sk = kc + ((size_t)kh * SEQ + 1536 + 32 * ((unit + 1) >> 1) + 16 * (frag >> 2) + (lane & 15)) * 128 + 32 * (frag & 3) + 4 * g;
      const u16* sv = vc + ((size_t)kh * SEQ + 1536 + 32 * ((unit - 1) >> 1) + 4 * g) * 128 + 16 * frag + (lane & 15);
      const u16* src = isK ? sk : sv;
      const int s1 = isK ? 1 : 128, s4 = isK ? 16 : 2048;
#pragma unroll
      for (int j2 = 0; j2 < 8; ++j2) e[i][j2] = src[(j2 >> 2) * s4 + (j2 & 3) * s1];
    }
#pragma unroll
    for (int i = 0; i < 4; ++i)
      st16s<0>((u32x4){e[i][0] | (e[i][1] << 16), e[i][2] | (e[i][3] << 16), e[i][4] | (e[i][5] << 16), e[i][6] | (e[i][7] << 16)},
               mkrsrc(ws + WS_KV, 1048576), (wid * 64 + lane) * 16, (sp * 4 + i) * 8192);
    wait_vm<0>();
    wait_lgkm0();
    bar();
    if (wid == 0) signal<1>(ws, F_X + (sp >> 3) * 128 + (sp & 7) * 8, tag);
    unsigned pw[NSP];
#pragma unroll
    for (int i = 0; i < NSP; ++i) pw[i] = *(const unsigned*)((const char*)w2 + (size_t)i * 2097152 + (size_t)(sp * 512 + wid * 64 + lane) * 128);
#pragma unroll
    for (int i = 0; i < NSP; ++i) asm volatile("" ::"v"(pw[i]));
    if (wid >= 4) { spin_until(ws, F_X + kvh * 128, 8, tag); kv8(0, 0); kv8(1, 1); kv8(2, 2); wait_vm<8>(); }
    bar();
  }
  {
    const int ln = lane_id();
#pragma unroll
    for (int ks = 0; ks < 8; ++ks) qh[ks] = *(LAS bf16x8*)(lds + 98304 + wid * 8192 + ks * 1024 + ln * 16);
  }
  Strm Y;
  auto y_iss = [&](int k) { const int r = k + 7 * (dc & 3); issue(Y, k, r >= 28 ? r - 28 : r); };
  Frags FY;
  Addr aY;
  {
    const int lane = lane_id();
    Att A;
#pragma unroll
    for (int ks = 0; ks < 8; ++ks) A.q[ks >> 2][ks & 3] = qh[ks];
    A.ones = (bf16x8){0x3f80, 0x3f80, 0x3f80, 0x3f80, 0x3f80, 0x3f80, 0x3f80, 0x3f80};
    asm volatile("" : "+v"(A.ones));
    const unsigned kvl = lb + lane * 16;
#pragma unroll
    for (int i = 0; i < 4; ++i) A.pre[i] = ldfrag(kvl + i * 1024);
    const bool strm = wid >= 4 && wid < 7, hw = wid >= 4;
    const rsrc_t rp2 = mkrsrc(w2, SEQ * FF * 2), rpo = mkrsrc(wo, HD * DM * 2);
    const unsigned pv = (blockIdx.x * 64 + lane) * 128;
#define PF(W, R, C) pf_line<W>(wid, R, pv, (C) * 2097152u)
    const int fp = F_P + (ut * 2 + uh) * 64 + (wid & 3) * 8;
    const bool cu = !hw && hasU;
    att_tile<0>(A, kvl + 0 * 32768, kvl + 1 * 32768, [&](int n) __attribute__((always_inline)) { if (n == 20 && cu) { wait_vm<0>(); signal<1>(ws, fp ^ 64, tag); } });
    if (hw) wait_vm<0>();
    bar();
    att_tile<1>(A, kvl + 1 * 32768, kvl + 2 * 32768, [&](int n) __attribute__((always_inline)) {
      if (n == 0 && hw) kv8(3, 3); if (n == 12 && hw) kv8(4, 0); if (n == 28 && hw) peek(rf, lb + 131072 + lw * 256, fp); if (n == 4) PF(0, rp2, 3); if (n == 9) PF(1, rp2, 4); });
    if (hw) wait_vm<9>();
    bar();
    att_tile<1>(A, kvl + 2 * 32768, kvl + 3 * 32768, [&](int n) __attribute__((always_inline)) {
      if (n == 8 && hw) {
        wait_vm<0>();
        if (hasU) peek_check(ws, lb + 131072 + lw * 256, fp, 1, tag);
        const rsrc_t rux = mkrsrc(ws + WS_XB + (size_t)(tile * 4 + kvh) * 98304 + 49152, hasU ? 32768 : 0);
        dma4k(rux, lb + 32768 + lw * 8192, lane * 16, lw * 8192); dma4k(rux, lb + 32768 + lw * 8192 + 4096, lane * 16, lw * 8192 + 4096);
      }
      if (n == 27) PF(3, rp2, 5); });
    if (hw) wait_vm<0>();
    bar();
    if (hw) { kv8(5, 4); kv8(7, 2); }
    if (cu) {
      u32x4 p[8];
      p[0] = ldsr(kvl + 32768 + wid * 8192);
#pragma unroll
      for (int k = 0; k < 8; ++k) {
        if (k < 7) p[k + 1] = ldsr(kvl + 32768 + wid * 8192 + (k + 1) * 1024);
        f32x4 a = uk[k >> 1][2 * (k & 1)], b = uk[k >> 1][2 * (k & 1) + 1];
        add8(a, b, p[k]);
        p[k] = (u32x4){gelu2(a.xy), gelu2(a.zw), gelu2(b.xy), gelu2(b.zw)};
        asm volatile("" : "+v"(p[k]));
        if (k == 0) PF(2, rp2, 6);
        SB0();
      }
      bar();
      const rsrc_t ru = mkrsrc(ws + WS_U + (size_t)((2 * dq + wi) * 112 + 4 * ft + 2 * wj) * 8192 + uh * 1024, 16384);
#pragma unroll
      for (int k = 0; k < 8; ++k) st16s<0>(p[k], ru, lane * 16, (k & 1) * 8192 + (k >> 1) * 2048);
    } else { PF(2, rp2, 6); bar(); }
    att_tile<1>(A, kvl + 3 * 32768, kvl + 0 * 32768, [&](int n) __attribute__((always_inline)) {
      if (n == 20 && cu) { wait_vm<0>(); signal<1>(ws, F_U + ((2 * dq + wi) * 4 + kvh) * 256 + (((idx >> 2) * 2 + uh) * 2 + wj) * 8, tag); } });
    if (hw) wait_vm<0>();
    bar();
    att_tile<1>(A, kvl + 0 * 32768, kvl + 4 * 32768, [&](int n) __attribute__((always_inline)) { if (n == 0 && hw) kv8(6, 3); if (n == 6) PF(3, rpo, 0); if (n == 15) PF(0, rpo, 1); if (n == 24) PF(1, rpo, 2); });
    if (hw) wait_vm<0>();
    bar();
    att_tile<1>(A, kvl + 4 * 32768, kvl + 3 * 32768, [&](int n) __attribute__((always_inline)) { if (n == 3) PF(2, rpo, 3); });
    if (lw == 0) strm_x(Y, ws + WS_U + (size_t)(dc * 112 + 28 * kvh) * 8192, 28 * 8192, 8192, 0);
    else strm_w(Y, (const char*)w2 + (size_t)kvh * 896 * (SEQ * 2) + rb * 512 + ((lw - 1) & 1) * 256, 896, SEQ * 2, false);
    bar();
    att_tile<1>(A, kvl + 3 * 32768, kvl + 2 * 32768, [&](int n) __attribute__((always_inline)) {
      if (n == 4 && wid == 4) peek(rf, lb + 49152, F_U + (dc * 4 + kvh) * 256);
      if (n == 18 && wid == 4) { wait_vm<0>(); peek_check(ws, lb + 49152, F_U + (dc * 4 + kvh) * 256, 28, tag); }
      if (n == 19 && strm) y_iss(0); if (n == 27 && strm) y_iss(1); });
    bar();
    att_tile<2>(A, kvl + 2 * 32768, 0, [&](int n) __attribute__((always_inline)) {
      if (n == 9) { if (hw) wait_vm<8>(); bar(); }
      if (n == 10 && strm) y_iss(2); });
    if (__builtin_expect(__any(!(A.l[0][0] + A.l[1][0] <= 1.0e9f)), 0)) att_redo(A, rkv, lane * 16);
    addr_init(aY, lb, lb + 8192 + wi * 8192, wj, kvh, lane);
    if (!hw) load_frags(FY, aY);
#pragma unroll
    for (int qb = 0; qb < 2; ++qb) {
      const float inv = __builtin_amdgcn_rcpf(A.l[qb][0]);
#pragma unroll
      for (int d = 0; d < 4; ++d) *(LAS u32x4*)(size_t)(kvl + PARK + wid * 2048 + qb * 1024 + d * 16384) = pack8(A.o[2 * d][qb] * inv, A.o[2 * d + 1][qb] * inv);
    }
    wait_lgkm0();
  }
  if (wid < 4) {
    f32x4 acc[8][4];
    Frags F0 = FY, F1;
    Addr a = aY;
    int slot = 1;
    addr_add(a, QS);
    bar();
    gstep<false, 4, QS, true>(acc, F1, F0, a, slot);
    gemm_steps<false, 4, QS>(acc, F0, F1, a, slot, 26);
    addr_init(a, lb + wi * 8192, lb + 16384, wj, 0, lane_id());
    gstep<false, 4, QS>(acc, F0, F1, a, slot);
    {
      const int lm = lane_id();
#pragma unroll
      for (int m = 0; m < 4; ++m) *(LAS u32x4*)(size_t)(lb + PARK + lm * 16 + wid * 4096 + m * 1024) = pack8(acc[2 * m][0], acc[2 * m + 1][0]);
#pragma unroll
      for (int j = 1; j < 4; ++j)
#pragma unroll
        for (int m = 0; m < 4; ++m) st16s<0>(pack8(acc[2 * m][j], acc[2 * m + 1][j]), rxb, lm * 16, (kvh ^ j) * 98304 + (j - 1) * 16384 + wid * 4096 + m * 1024);
    }
    gstep<true, 4, QS, true>(acc, F1, F0, a, slot);
    gemm_steps<true, 4, QS>(acc, F0, F1, a, slot, 8);
    wait_vm<0>();
    gemm_steps<true, 4, QS>(acc, F0, F1, a, slot, 22);
    gstep<true, 4, QS>(acc, F0, F1, a, slot);
    const int lt = lane_id(), g = lt >> 4;
    const unsigned wo_ = wi * 8192 + wj * 2048;
#pragma unroll
    for (int dl = 1; dl < 4; ++dl)
#pragma unroll
      for (int h = 0; h < 2; ++h)
#pragma unroll
        for (int mm = 0; mm < 2; ++mm)
          st16s<0>(pack8(acc[4 * h + dl][2 * mm], acc[4 * h + dl][2 * mm + 1]), rxb, lt * 16, (kvh ^ dl) * 98304 + 49152 + (dl - 1) * 16384 + wo_ + h * 4096 + mm * 1024);
    const int fs = F_S + tile * 512 + wid * 32;
    wait_vm<8>(); signal<1>(ws, fs + (kvh ^ 1) * 128, tag);
    wait_vm<5>(); signal<1>(ws, fs + (kvh ^ 2) * 128 + 8, tag);
    wait_vm<2>(); signal<1>(ws, fs + (kvh ^ 3) * 128 + 16, tag);
    const int srow = rb * 256 + (8 * wi + kvh) * 16;
    const rsrc_t rx = mkrsrc(x + (size_t)srow * DM, 80 * DM * 2), rout = mkrsrc(out + (size_t)srow * DM, 80 * DM * 2);
    const unsigned vo = ((lt & 15) * DM + dc * 128 + wj * 64 + 16 * (g & 1) + 8 * (g >> 1)) * 2;
    u32x4 ps[3][4], xv[4], gv[4];
#pragma unroll
    for (int p = 0; p < 4; ++p) xv[p] = __builtin_amdgcn_raw_buffer_load_b128(rx, vo + (p >> 1) * (64 * DM * 2) + (p & 1) * 64, 0, 0);
    bar();
#pragma unroll
    for (int p = 0; p < 4; ++p) gv[p] = ldsr(lb + PARK + lt * 16 + wo_ + (p >> 1) * 4096 + (p & 1) * 1024);
    {
      GAS unsigned long long* fp = (GAS unsigned long long*)(ws + WS_FLAG + fs + kvh * 128) + (lt & 3);
      const unsigned long long want = ((unsigned long long)tag.hi << 32) | tag.lo;
      unsigned got = 0;
      for (unsigned sp = 0; got != 7; ++sp) {
        unsigned now = (unsigned)__ballot(__hip_atomic_load(fp, __ATOMIC_RELAXED, __HIP_MEMORY_SCOPE_AGENT) == want) & 7;
        if (sp >= (1u << 20)) now = 7;
        asm volatile("" ::: "memory");
#pragma unroll
        for (int k = 0; k < 3; ++k)
          if ((now & ~got) >> k & 1) {
#pragma unroll
            for (int p = 0; p < 4; ++p)
              ps[k][p] = __builtin_amdgcn_raw_buffer_load_b128(rxb, lt * 16 + (p & 1) * 1024, kvh * 98304 + 49152 + k * 16384 + wo_ + (p >> 1) * 4096, 16);
          }
        got |= now;
        if (got != 7) __builtin_amdgcn_s_sleep(1);
      }
    }
#pragma unroll
    for (int p = 0; p < 4; ++p) {
      f32x4 s0 = acc[4 * (p >> 1)][2 * (p & 1)], s1 = acc[4 * (p >> 1)][2 * (p & 1) + 1];
#pragma unroll
      for (int k = 0; k < 3; ++k) add8(s0, s1, ps[k][p]);
      s0 *= (f32x4){bflo(gv[p][0]), bfhi(gv[p][0]), bflo(gv[p][1]), bfhi(gv[p][1])};
      s1 *= (f32x4){bflo(gv[p][2]), bfhi(gv[p][2]), bflo(gv[p][3]), bfhi(gv[p][3])};
      const u32x4 tp = pack8(s0, s1);
      const auto w0 = __builtin_amdgcn_permlane16_swap(tp[0], tp[2], false, false), w1 = __builtin_amdgcn_permlane16_swap(tp[1], tp[3], false, false);
      const unsigned t[4] = {w0[0], w1[0], w0[1], w1[1]};
      u32x4 o;
#pragma unroll
      for (int e = 0; e < 4; ++e) o[e] = cvtpk(bflo(xv[p][e]) + bflo(t[e]), bfhi(xv[p][e]) + bfhi(t[e]));
      __builtin_amdgcn_raw_buffer_store_b128(o, rout, vo + (p >> 1) * (64 * DM * 2) + (p & 1) * 64, 0, 16);
    }
  } else {
    const int lane = lane_id();
    const unsigned pl = lb + PARK + lane * 16, pk = lb + 159744 + lw * 256;
    const int fo = F_O + (rb * 4 + kvh) * 128;
    if (lw < 3) {
      Strm S;
      if (lw < 2) strm_x(S, ws + WS_O + (size_t)(rb * 128 + 32 * kvh) * 16384 + lw * 8192, 524288 - lw * 8192, 16384, kvh);
      else strm_w(S, (const char*)wo + (size_t)kvh * 1024 * (DM * 2) + dc * 256, 1024, DM * 2, false);
      wait_vm<8>();
      bar();
      y_iss(3);
      for (int t = 1; t <= 22; ++t) { y_iss(t + 3); wait_vm<16>(); bar(); }
      peek(rf, lb + PARK + lw * 256, fo);
      y_iss(26); wait_vm<16>(); bar();
      y_iss(27); wait_vm<16>(); bar();
      peek_check(ws, lb + PARK + lw * 256, fo, 8, tag);
      auto iss = [&](int k) { const int r = (k & 16) | ((k + 4 * (rb & 3)) & 15); issue(S, k, 4 * (r & 7) + (r >> 3)); wait_vm<16>(); bar(); };
      for (int t = 25; t <= 36; ++t) iss(t - 25);
      peek(rf, lb + PARK + 16384 + lw * 256, fo + 64);
      for (int t = 37; t <= 40; ++t) iss(t - 25);
      peek_check(ws, lb + PARK + 16384 + lw * 256, fo + 64, 8, tag);
      for (int t = 41; t <= 56; ++t) iss(t - 25);
      wait_vm<8>(); bar();
      wait_vm<0>(); bar();
    } else {
      bar();
      const rsrc_t ro = mkrsrc(ws + WS_O + (size_t)(rb * 128 + 4 * head) * 16384, 65536);
      for (int t = 1; t <= 25; t += 2) {
        const unsigned o = (t >> 1) * 5120;
        const u32x4 v0 = ldsr(pl + o), v1 = ldsr(pl + o + 1024), v2 = ldsr(pl + o + 2048), v3 = ldsr(pl + o + 3072), v4 = ldsr(pl + (t < 25 ? o + 4096 : 0));
        st16s<0>(v0, ro, lane * 16, o); st16s<1024>(v1, ro, lane * 16, o); st16s<2048>(v2, ro, lane * 16, o);
        if (t == 17) { wait_vm<11>(); signal<1>(ws, fo + dc * 8, tag); }
        bar();
        st16s<3072>(v3, ro, lane * 16, o); st16s<0>(v4, ro, lane * 16, o + 4096);
        bar();
      }
      bar(); bar(); bar();
      wait_vm<0>();
      signal<1>(ws, fo + 64 + dc * 8, tag);
      for (int t = 30; t <= 38; ++t) bar();
      signal<14>(ws, F_Y + (tile * 4 + (kvh ^ (lane & 3))) * 128 + ((lane & 3) - 1) * 8, tag);
      for (int t = 39; t <= 56; ++t) bar();
      peek(rf, pk, F_Y + (tile * 4 + kvh) * 128);
      bar();
      wait_vm<0>();
      peek_check(ws, pk, F_Y + (tile * 4 + kvh) * 128, 3, tag);
      bar();
    }
    u32x4 py[3][4];
#pragma unroll
    for (int k = 0; k < 3; ++k)
#pragma unroll
      for (int m = 0; m < 4; ++m) py[k][m] = __builtin_amdgcn_raw_buffer_load_b128(rxb, lane * 16 + m * 1024, kvh * 98304 + lw * 4096 + k * 16384, 16);
    const float bb = bflo(b2[rb * 256 + (4 * lw + kvh) * 16 + (lane & 15)]);
    bar();
    bar();
#pragma unroll
    for (int m = 0; m < 4; ++m) {
      f32x4 y0 = {bb, bb, bb, bb}, y1 = y0;
      add8(y0, y1, ldsr(pl + lw * 4096 + m * 1024));
#pragma unroll
      for (int k = 0; k < 3; ++k) add8(y0, y1, py[k][m]);
      *(LAS u32x4*)(size_t)(pl + lw * 4096 + m * 1024) = (u32x4){silu2(y0.xy), silu2(y0.zw), silu2(y1.xy), silu2(y1.zw)};
    }
    wait_lgkm0();
    bar();
  }
}

extern "C" void kernel_launch(void* const* d_in, const int* in_sizes, int n_in, void* d_out, int out_size, void* d_ws, size_t ws_size,
                              hipStream_t stream) {
  const u16* x = (const u16*)d_in[0];
  const u16* kc = (const u16*)d_in[1];
  const u16* vc = (const u16*)d_in[2];
  const u16* wq = (const u16*)d_in[3];
  const u16* wo = (const u16*)d_in[4];
  const u16* w1 = (const u16*)d_in[6];
  const u16* b1 = (const u16*)d_in[7];
  const u16* w2 = (const u16*)d_in[8];
  const u16* b2 = (const u16*)d_in[9];
  char* ws = (char*)d_ws;
  (void)hipFuncSetAttribute((const void*)k_main, hipFuncAttributeMaxDynamicSharedMemorySize, 163840);
  hipLaunchKernelGGL(k_main, dim3(256), dim3(512), 163840, stream, x, kc, vc, wq, wo, w1, b1, w2, b2, ws, (u16*)d_out);
}
```

```cpp
#include <hip/hip_runtime.h>
#include <hip/hip_bf16.h>

typedef unsigned short u16;
typedef short bf16x8 __attribute__((ext_vector_type(8)));
typedef short s16x4 __attribute__((ext_vector_type(4)));
typedef float f32x4 __attribute__((ext_vector_type(4)));
typedef float f32x2 __attribute__((ext_vector_type(2)));
typedef unsigned u32x4 __attribute__((ext_vector_type(4)));
typedef unsigned u32x2 __attribute__((ext_vector_type(2)));
typedef __bf16 bf16x2 __attribute__((ext_vector_type(2)));
typedef __amdgpu_buffer_rsrc_t rsrc_t;
#define LAS __attribute__((address_space(3)))
#define GAS __attribute__((address_space(1)))
#define DI __device__ __forceinline__
#define SGB(m, n) __builtin_amdgcn_sched_group_barrier(m, n, 0)
#define SB0() __builtin_amdgcn_sched_barrier(0)

constexpr int SEQ = 2048, DM = 1024, FF = 3584, HD = 4096;
constexpr int CODE_BYTES = 57600;
constexpr int NSP = 3;
constexpr size_t WS_FLAG = 0;
constexpr int F_EP = 0, F_X = 512, F_L = 2048, F_U = 4096, F_O = 12288, F_Y = 16384, F_S = 49152, F_P = 81920, F_END = 96256;
constexpr size_t WS_ST = 98304;
constexpr size_t WS_CS = 131072;
constexpr size_t WS_HT = 262144;
constexpr size_t WS_KV = WS_HT + 4194304;
constexpr size_t WS_O = WS_KV + 1048576;
constexpr size_t WS_U = WS_O + 16777216;
constexpr size_t WS_XB = WS_U + 7340032;
constexpr size_t WS_END = WS_XB + 25165824;

DI unsigned cvtpk(float lo, float hi) { f32x2 v = {lo, hi}; bf16x2 b = __builtin_convertvector(v, bf16x2); return __builtin_bit_cast(unsigned, b); }
DI float bflo(unsigned u) { return __uint_as_float(u << 16); }
DI float bfhi(unsigned u) { return __uint_as_float(u & 0xffff0000u); }
DI float rbf(float x) { return bflo(cvtpk(x, x)); }
template <int N> DI void wait_vm() { asm volatile("s_waitcnt vmcnt(%0)" ::"i"(N) : "memory"); }
DI void wait_lgkm0() { __builtin_amdgcn_s_waitcnt(0xC07F); }
DI void bar() { asm volatile("" ::: "memory"); __builtin_amdgcn_s_barrier(); asm volatile("" ::: "memory"); }
DI rsrc_t mkrsrc(const void* p, unsigned bytes) { return __builtin_amdgcn_make_buffer_rsrc((void*)p, 0, bytes, 0x00020000); }
DI void dma16(rsrc_t r, unsigned lds_addr, unsigned voff, unsigned soff) {
  asm volatile("s_mov_b32 m0, %0\n\ts_nop 0\n\tbuffer_load_dwordx4 %1, %2, %3 offen lds" ::"s"(lds_addr), "v"(voff), "s"(r), "s"(soff) : "memory");
}
DI void dma16s(rsrc_t r, unsigned lds_addr, unsigned voff, unsigned soff) {
  asm volatile("s_mov_b32 m0, %0\n\ts_nop 0\n\tbuffer_load_dwordx4 %1, %2, %3 offen sc1 lds" ::"s"(lds_addr), "v"(voff), "s"(r), "s"(soff) : "memory");
}
template <int OFF> DI void st16s(u32x4 v, rsrc_t r, unsigned voff, unsigned soff) {
  asm volatile("buffer_store_dwordx4 %0, %1, %2, %3 offen offset:%4 sc1\n\ts_nop 1" ::"v"(v), "v"(voff), "s"(r), "s"(soff), "i"(OFF) : "memory");
}
DI int lane_id() { int l; asm volatile("v_mbcnt_lo_u32_b32 %0, -1, 0\n\tv_mbcnt_hi_u32_b32 %0, -1, %0" : "=v"(l)); return l; }
struct Tag { unsigned lo, hi; };
DI void peek(rsrc_t rf, unsigned lds_addr, int off) {
  asm volatile("s_mov_b32 m0, %0\n\ts_nop 0\n\tbuffer_load_dword %3, %1, %2 offen sc1 lds" ::"s"(lds_addr), "s"(rf), "s"(off), "v"(lane_id() * 4) : "memory");
}
template <int GAP = 1> DI void spin_until(char* ws, int off, int n, Tag t) {
  const int ln = lane_id();
  GAS unsigned long long* p = (GAS unsigned long long*)(ws + WS_FLAG + off) + (ln < n ? ln : 0);
  const unsigned long long want = ((unsigned long long)t.hi << 32) | t.lo;
  for (unsigned sp = 0; sp < (1u << 20); ++sp) {
    if (__all(__hip_atomic_load(p, __ATOMIC_RELAXED, __HIP_MEMORY_SCOPE_AGENT) == want)) break;
    __builtin_amdgcn_s_sleep(GAP);
  }
  asm volatile("" ::: "memory");
}
DI void peek_check(char* ws, unsigned lds_addr, int off, int n, Tag t) {
  const int ln = lane_id();
  const unsigned v = *(LAS volatile unsigned*)(size_t)(lds_addr + ln * 4);
  if (!__all(ln >= 2 * n || v == ((ln & 1) ? t.hi : t.lo))) spin_until(ws, off, n, t);
}
template <long MASK> DI void signal(char* ws, int off, Tag t) {
  const u32x2 v = {t.lo, t.hi};
  asm volatile("s_mov_b64 exec, %3\n\tglobal_store_dwordx2 %0, %1, %2 sc1\n\ts_mov_b64 exec, -1" ::"v"(off), "v"(v), "s"(ws + WS_FLAG), "s"(MASK) : "memory");
}
DI void dma4k(rsrc_t r, unsigned lds_addr, unsigned voff, unsigned soff) {
  asm volatile("s_mov_b32 m0, %0\n\ts_nop 0\n\tbuffer_load_dwordx4 %1, %2, %3 offen sc1 lds\n\tbuffer_load_dwordx4 %1, %2, %3 offen offset:1024 sc1 lds\n\t"
               "buffer_load_dwordx4 %1, %2, %3 offen offset:2048 sc1 lds\n\tbuffer_load_dwordx4 %1, %2, %3 offen offset:3072 sc1 lds"
               ::"s"(lds_addr), "v"(voff), "s"(r), "s"(soff) : "memory");
}
template <int W> DI void pf_line(int wid, rsrc_t r, unsigned voff, unsigned soff) {
  asm volatile("s_cmp_lg_u32 %0, %4\n\ts_cbranch_scc1 1f\n\tbuffer_load_dword v255, %1, %2, %3 offen\n1:"
               ::"s"(wid), "v"(voff), "s"(r), "s"(soff), "i"(W) : "scc", "memory", "v255");
}
template <int CTRL> DI float dppf(float x) { return __builtin_bit_cast(float, __builtin_amdgcn_mov_dpp(__builtin_bit_cast(int, x), CTRL, 0xf, 0xf, true)); }
DI float wave_sum(float v) {
  v += dppf<0xB1>(v); v += dppf<0x4E>(v); v += dppf<0x141>(v); v += dppf<0x140>(v);
  auto a = __builtin_amdgcn_permlane16_swap(__float_as_uint(v), __float_as_uint(v), false, false);
  v = __uint_as_float(a[0]) + __uint_as_float(a[1]);
  auto b = __builtin_amdgcn_permlane32_swap(__float_as_uint(v), __float_as_uint(v), false, false);
  return __uint_as_float(b[0]) + __uint_as_float(b[1]);
}

DI f32x2 ln16(unsigned (&w)[8]) {
  f32x2 v[8];
#pragma unroll
  for (int i = 0; i < 8; ++i) v[i] = (f32x2){bflo(w[i]), bfhi(w[i])};
  const f32x2 s = ((v[0] + v[1]) + (v[2] + v[3])) + ((v[4] + v[5]) + (v[6] + v[7]));
  const float mean = wave_sum(s[0] + s[1]) * (1.f / 1024.f);
  f32x2 q0 = {0.f, 0.f}, q1 = q0;
#pragma unroll
  for (int i = 0; i < 8; i += 2) {
    const f32x2 d0 = v[i] - mean, d1 = v[i + 1] - mean;
    q0 = __builtin_elementwise_fma(d0, d0, q0); q1 = __builtin_elementwise_fma(d1, d1, q1);
  }
  q0 += q1;
  const float var = wave_sum(q0[0] + q0[1]) * (1.f / 1024.f);
  const float mb = rbf(mean);
  const float rs = __builtin_amdgcn_rcpf(rbf(__builtin_amdgcn_sqrtf(rbf(rbf(var) + 1e-5f))));
#pragma unroll
  for (int i = 0; i < 8; ++i) {
    const f32x2 d = v[i] - mb;
    const unsigned c = cvtpk(d[0], d[1]);
    const f32x2 r = (f32x2){bflo(c), bfhi(c)} * rs;
    w[i] = cvtpk(r[0], r[1]);
  }
  return (f32x2){mb, rs};
}

struct Frags { bf16x8 x[8]; bf16x8 w[4]; };
struct Addr { unsigned x, w[4]; };
template <int XS = 1024> DI void load_frags(Frags& f, const Addr& a) {
#pragma unroll
  for (int c = 0; c < 4; ++c) {
    s16x4 a0 = __builtin_amdgcn_ds_read_tr16_b64_v4i16((LAS s16x4*)(size_t)(a.w[c]));
    s16x4 a1 = __builtin_amdgcn_ds_read_tr16_b64_v4i16((LAS s16x4*)(size_t)(a.w[c] + 4096));
    f.w[c] = __builtin_shufflevector(a0, a1, 0, 1, 2, 3, 4, 5, 6, 7);
  }
#pragma unroll
  for (int i = 0; i < 8; ++i) f.x[i] = *(LAS bf16x8*)(size_t)(a.x + i * XS);
}
template <bool WA, bool Z> DI void mma_all(f32x4 (&acc)[8][4], const Frags& f, const f32x4* c0) {
  const f32x4 zero = {0.f, 0.f, 0.f, 0.f};
#pragma unroll
  for (int i = 0; i < 8; ++i)
#pragma unroll
    for (int jj = 0; jj < 4; ++jj) {
      const int j = (i & 1) ? 3 - jj : jj;
      if (WA) acc[i][j] = __builtin_amdgcn_mfma_f32_16x16x32_bf16(f.w[j], f.x[i], Z ? (c0 ? c0[j] : zero) : acc[i][j], 0, 0, 0);
      else    acc[i][j] = __builtin_amdgcn_mfma_f32_16x16x32_bf16(f.x[i], f.w[j], Z ? (c0 ? c0[j] : zero) : acc[i][j], 0, 0, 0);
    }
}
DI void addr_add(Addr& a, int d) {
  a.x += d;
#pragma unroll
  for (int c = 0; c < 4; ++c) a.w[c] += d;
}
template <bool WA, int NSLOT, int SLOTB, bool Z = false>
DI void gstep(f32x4 (&acc)[8][4], Frags& Fn, const Frags& Fc, Addr& a, int& slot, const f32x4* c0 = nullptr) {
  SB0();
  load_frags(Fn, a);
  mma_all<WA, Z>(acc, Fc, c0);
  slot = (slot + 1 == NSLOT) ? 0 : slot + 1;
  addr_add(a, (slot == 0) ? -(NSLOT - 1) * SLOTB : SLOTB);
  asm volatile("" : "+v"(a.x), "+v"(a.w[0]), "+v"(a.w[1]), "+v"(a.w[2]), "+v"(a.w[3]));
#pragma unroll
  for (int i = 0; i < 8; ++i) { SGB(0x8, 1); SGB(0x100, 1); SGB(0x8, 1); SGB(0x100, 1); SGB(0x8, 1); }
#pragma unroll
  for (int i = 0; i < 6; ++i) { SGB(0x8, 1); SGB(0x4, 1); SGB(0x2, 1); }
  SGB(0x8, 2);
  SB0();
  wait_lgkm0();
  bar();
}
template <int MODE, bool CS, bool Z = false>
DI void qstep(f32x4 (&acc)[8][4], f32x4 (&cs)[2], u32x4& ones, unsigned onext, Frags& Fn, const Frags& Fc, Addr& a, int& xs, int& wsl) {
  const f32x4 zero = {0.f, 0.f, 0.f, 0.f};
  SB0();
  load_frags<MODE == 2 ? 1024 : 2048>(Fn, a);
  if (CS) {
#pragma unroll
    for (int j = 0; j < 2; ++j) cs[j] = __builtin_amdgcn_mfma_f32_16x16x32_bf16(Fc.w[j], __builtin_bit_cast(bf16x8, ones), Z ? zero : cs[j], 0, 0, 0);
    ones = (u32x4){onext, onext, onext, onext};
    asm volatile("" : "+v"(ones));
  }
  mma_all<true, Z>(acc, Fc, nullptr);
  if (MODE == 2) addr_add(a, 24576);
  else {
    wsl = (wsl + 1) & 3;
    const int dw = wsl ? 8192 : -24576;
#pragma unroll
    for (int c = 0; c < 4; ++c) a.w[c] += dw;
    a.x ^= 64;
    if (MODE == 1) { xs = (xs == 2) ? 0 : xs + 1; a.x += xs ? 32768 : -65536; }
  }
  asm volatile("" : "+v"(a.x), "+v"(a.w[0]), "+v"(a.w[1]), "+v"(a.w[2]), "+v"(a.w[3]));
#pragma unroll
  for (int i = 0; i < 8; ++i) { SGB(0x8, 1); SGB(0x100, 1); SGB(0x8, 1); SGB(0x100, 1); SGB(0x8, 1); }
#pragma unroll
  for (int i = 0; i < 7; ++i) { SGB(0x8, 1); SGB(0x4, 1); SGB(0x2, 1); }
  if (CS) { SGB(0x8, 1); SGB(0x2, 2); SGB(0x8, 1); SGB(0x2, 2); }
  SGB(0x8, 1);
  SB0();
  wait_lgkm0();
  bar();
}
DI void addr_init(Addr& a, unsigned xbase, unsigned wbase, int wj, int cx, int lane) {
  const int g = lane >> 4, q = (lane >> 2) & 3, p = lane & 3, row = 4 * g + q;
  a.x = xbase + lane * 16;
#pragma unroll
  for (int c = 0; c < 4; ++c) a.w[c] = wbase + row * 256 + (((2 * (4 * wj + (c ^ cx)) + (p >> 1)) ^ ((row & 7) << 1)) * 16) + (p & 1) * 8;
}
template <bool WA, int NSLOT, int SLOTB> DI void gemm_steps(f32x4 (&acc)[8][4], Frags& F0, Frags& F1, Addr& a, int& slot, int n) {
  for (int t = 0; t < n; t += 2) {
    gstep<WA, NSLOT, SLOTB>(acc, F0, F1, a, slot);
    gstep<WA, NSLOT, SLOTB>(acc, F1, F0, a, slot);
  }
}

DI u32x4 pack8(const f32x4& a, const f32x4& b) { return (u32x4){cvtpk(a[0], a[1]), cvtpk(a[2], a[3]), cvtpk(b[0], b[1]), cvtpk(b[2], b[3])}; }
DI f32x2 gate2(f32x2 u, f32x2 z) {
  f32x2 e = {__builtin_amdgcn_exp2f(z[0]), __builtin_amdgcn_exp2f(z[1])};
  e += 1.f;
  return u * (f32x2){__builtin_amdgcn_rcpf(e[0]), __builtin_amdgcn_rcpf(e[1])};
}
DI unsigned gelu2(f32x2 u) {
  const f32x2 r = gate2(u, u * __builtin_elementwise_fma(u * u, (f32x2){-0.1029432397f, -0.1029432397f}, (f32x2){-2.302208198f, -2.302208198f}));
  return cvtpk(r[0], r[1]);
}
DI unsigned silu2(f32x2 y) { const f32x2 r = gate2(y, y * -1.4426950409f); return cvtpk(r[0], r[1]); }
DI u32x4 ldsr(unsigned a) { return *(LAS u32x4*)(size_t)a; }
DI void add8(f32x4& a, f32x4& b, const u32x4& w) {
  a += (f32x4){bflo(w[0]), bfhi(w[0]), bflo(w[1]), bfhi(w[1])};
  b += (f32x4){bflo(w[2]), bfhi(w[2]), bflo(w[3]), bfhi(w[3])};
}

struct Sc { f32x4 v[2][2]; };
struct Att { bf16x8 q[2][4]; f32x4 o[8][2]; f32x4 negm[2]; Sc sE, sO; u32x4 pO[2]; bf16x8 pre[4], ones; f32x4 l[2]; };
DI float xg_max(float m) {
  auto a = __builtin_amdgcn_permlane16_swap(__float_as_uint(m), __float_as_uint(m), false, false);
  m = fmaxf(__uint_as_float(a[0]), __uint_as_float(a[1]));
  auto b = __builtin_amdgcn_permlane32_swap(__float_as_uint(m), __float_as_uint(m), false, false);
  return fmaxf(__uint_as_float(b[0]), __uint_as_float(b[1]));
}
DI float row_max(const Sc& s, int qb) {
  const f32x4 a = s.v[0][qb], b = s.v[1][qb];
  return xg_max(fmaxf(fmaxf(fmaxf(a[0], a[1]), fmaxf(a[2], a[3])), fmaxf(fmaxf(b[0], b[1]), fmaxf(b[2], b[3]))));
}
DI bf16x8 ldfrag(unsigned a) { return *(LAS bf16x8*)(size_t)a; }
DI void sm1(Sc& s, int e, u32x4 (&w)[2], int part = 3) {
  if (e < 16 && (part & 1)) {
    float x = __builtin_amdgcn_exp2f(s.v[e >> 3][(e >> 2) & 1][e & 3]);
    asm volatile("" : "+v"(x));
    s.v[e >> 3][(e >> 2) & 1][e & 3] = x;
  }
  if (e >= 2 && e <= 16 && !(e & 1) && (part & 2)) {
    const int kt = (e - 1) >> 3, qb = ((e - 1) >> 2) & 1, r = (e - 1) & 3;
    unsigned c = cvtpk(s.v[kt][qb][r - 1], s.v[kt][qb][r]);
    asm volatile("" : "+v"(c));
    w[qb][2 * kt + (r >> 1)] = c;
  }
}
#define SETPRIO(x) __builtin_amdgcn_s_setprio(x)
#define MFMA16(a, b, c) __builtin_amdgcn_mfma_f32_16x16x32_bf16(a, b, c, 0, 0, 0)
#define PFRAG(w) __builtin_bit_cast(bf16x8, w)
DI void mfma_acc(f32x4& c, const bf16x8& a, const u32x4& b) { asm volatile("v_mfma_f32_16x16x32_bf16 %0, %1, %2, %0" : "+v"(c) : "v"(a), "v"(b)); }
template <int POS, int n, class F> DI void att_slot(Att& A, bf16x8 (&f)[36], u32x4 (&pE)[2], unsigned kv, unsigned kvn, F& gap) {
  constexpr int u = n >> 3, k = n & 7;
  constexpr bool isqk = (u & 1) ? (u == 1 || POS < 2) : (u == 0 && POS == 0);
  const f32x4 zero = {0.f, 0.f, 0.f, 0.f};
  if constexpr (k == 0) SETPRIO(3 - u);
#pragma unroll
  for (int qb = 0; qb < 2; ++qb) {
    if constexpr (isqk && (k & 3) != 0) mfma_acc(u == 1 ? A.sO.v[k >> 2][qb] : A.sE.v[k >> 2][qb], f[n], __builtin_bit_cast(u32x4, A.q[qb][k & 3]));
    else if constexpr (isqk && u == 1) A.sO.v[k >> 2][qb] = MFMA16(f[n], A.q[qb][0], A.negm[qb]);
    else if constexpr (isqk) A.sE.v[k >> 2][qb] = MFMA16(f[n], A.q[qb][0], u ? A.negm[qb] : zero);
    else if constexpr (POS == 0) A.o[k][qb] = MFMA16(f[n], PFRAG(pE[qb]), zero);
    else if constexpr (u == 2) mfma_acc(A.o[k][qb], f[n], pE[qb]);
    else mfma_acc(A.o[k][qb], f[n], A.pO[qb]);
    SB0();
    if (qb == 1) {
      if constexpr (n < 28) f[n + 4] = ldfrag(kv + (n + 4) * 1024);
      else if constexpr (POS < 2) f[n + 4] = ldfrag(kvn + (n - 28) * 1024);
      SB0();
    }
    if constexpr (POS == 0 && u == 1) {
      if (k == 0 && qb == 0) sm1(A.sE, 0, pE);
      sm1(A.sE, 2 * k + 1 + qb, pE);
    }
    if constexpr (POS > 0 && n <= 14) sm1(A.sE, n + 2, pE, 1 + qb);
    if constexpr (n >= 14 && n <= (POS < 2 ? 30 : 15)) sm1(A.sO, n - 14, A.pO, 1 + qb);
    if constexpr (POS < 2 && n >= 30) sm1(A.sE, n - 30, pE, 1 + qb);
    if constexpr (POS == 2 && u == 2) sm1(A.sO, 2 + 2 * k + qb, A.pO);
    SB0();
  }
  if constexpr (!isqk && (k == 2 || k == 4)) {
    constexpr int qb = k == 4;
    if constexpr (POS == 0) A.l[qb] = MFMA16(A.ones, PFRAG(pE[qb]), zero);
    else if constexpr (u == 2) mfma_acc(A.l[qb], A.ones, pE[qb]);
    else mfma_acc(A.l[qb], A.ones, A.pO[qb]);
  }
  gap(n);
  SB0();
  if constexpr (POS == 2 && n == 23) { asm volatile("s_nop 1"); SB0(); }
  if constexpr ((POS == 2 && n == 31) || (POS == 0 && n == 7)) { asm volatile("s_nop 9"); SB0(); }
  if constexpr (POS == 0 && n == 7) {
#pragma unroll
    for (int qb = 0; qb < 2; ++qb) {
      const float mx = row_max(A.sE, qb);
      A.sE.v[0][qb] -= mx; A.sE.v[1][qb] -= mx;
      A.negm[qb] = (f32x4){-mx, -mx, -mx, -mx};
    }
    SB0();
  }
  if constexpr (n < 31) att_slot<POS, n + 1>(A, f, pE, kv, kvn, gap);
}
template <int POS, class F> DI void att_tile(Att& A, unsigned kv, unsigned kvn, F&& gap) {
  bf16x8 f[36];
  u32x4 pE[2];
  asm volatile("" : "+v"(kv), "+v"(kvn));
#pragma unroll
  for (int i = 0; i < 4; ++i) f[i] = A.pre[i];
  SB0();
  att_slot<POS, 0>(A, f, pE, kv, kvn, gap);
  if (POS < 2) {
#pragma unroll
    for (int i = 0; i < 4; ++i) A.pre[i] = f[32 + i];
  }
}
DI void att_redo(Att& A, rsrc_t rkv, unsigned vo) {
  const f32x4 zero = {0.f, 0.f, 0.f, 0.f};
  float m[2] = {-3.0e38f, -3.0e38f};
  A.l[0] = A.l[1] = zero;
#pragma unroll
  for (int d = 0; d < 8; ++d) A.o[d][0] = A.o[d][1] = zero;
  for (int blk = 0; blk < 16; ++blk) {
    const int kb = (blk ? 2 * blk - 1 : 0) * 8192, vb = (blk < 15 ? 2 * blk + 2 : 31) * 8192;
    Sc s;
#pragma unroll
    for (int n = 0; n < 8; ++n) {
      const bf16x8 kf = PFRAG(__builtin_amdgcn_raw_buffer_load_b128(rkv, vo + n * 1024, kb, 16));
#pragma unroll
      for (int qb = 0; qb < 2; ++qb) s.v[n >> 2][qb] = MFMA16(kf, A.q[qb][n & 3], (n & 3) ? s.v[n >> 2][qb] : zero);
    }
#pragma unroll
    for (int qb = 0; qb < 2; ++qb) {
      const float mn = fmaxf(m[qb], row_max(s, qb)), fct = __builtin_amdgcn_exp2f(m[qb] - mn);
      m[qb] = mn;
      A.l[qb] *= fct;
      s.v[0][qb] -= mn; s.v[1][qb] -= mn;
#pragma unroll
      for (int d = 0; d < 8; ++d) A.o[d][qb] *= fct;
    }
    u32x4 p[2];
#pragma unroll
    for (int i = 0; i < 17; ++i) sm1(s, i, p);
#pragma unroll
    for (int qb = 0; qb < 2; ++qb) A.l[qb] = MFMA16(A.ones, PFRAG(p[qb]), A.l[qb]);
#pragma unroll
    for (int n = 0; n < 8; ++n) {
      const bf16x8 vf = PFRAG(__builtin_amdgcn_raw_buffer_load_b128(rkv, vo + n * 1024, vb, 16));
#pragma unroll
      for (int qb = 0; qb < 2; ++qb) A.o[n][qb] = MFMA16(vf, PFRAG(p[qb]), A.o[n][qb]);
    }
  }
}

__global__ __launch_bounds__(512, 2) __attribute__((amdgpu_num_vgpr(127))) void k_main(const u16* __restrict__ x, const u16* __restrict__ kc, const u16* __restrict__ vc, const u16* __restrict__ wq, const u16* __restrict__ wo,
                                                 const u16* __restrict__ w1, const u16* __restrict__ b1, const u16* __restrict__ w2,
                                                 const u16* __restrict__ b2, char* __restrict__ ws, u16* __restrict__ out) {
  extern __shared__ __attribute__((aligned(16))) char smem[];
  LAS char* lds = (LAS char*)smem;
  const unsigned lb = (unsigned)(size_t)lds;
  const int tid = threadIdx.x, wid = __builtin_amdgcn_readfirstlane(tid >> 6);
  const int g8 = blockIdx.x & 7, idx = blockIdx.x >> 3;
  const rsrc_t rcode = mkrsrc((const void*)(__builtin_amdgcn_s_getpc() & ~255ull), CODE_BYTES);
  const unsigned code_o = (idx & 7) * 8192;
  pf_line<0>(wid, rcode, (tid & 63) * 128, code_o);
  SB0();
  u32x4 xa, xb;
  u32x2 ep;
  asm volatile("global_load_dwordx4 %0, %3, %4\n\tglobal_load_dwordx4 %1, %3, %4 offset:1024\n\tglobal_load_dwordx2 %2, %5, %6 sc1"
               : "=&v"(xa), "=&v"(xb), "=&v"(ep) : "v"((tid & 63) * 16), "s"(x + (size_t)(8 * blockIdx.x + wid) * DM), "v"(F_EP), "s"(ws + WS_FLAG) : "memory");
  SB0();
  const bool hasU = idx < 28;
  const int uh = g8 & 1, dq = idx & 3, ft = 7 * (g8 >> 1) + (idx >> 2), ut = ft * 4 + dq;
  const int kvh = g8 >> 1, head = 8 * kvh + (idx & 7), rb = 4 * (g8 & 1) + (idx >> 3);
  const int dc = idx & 7, tile = rb * 8 + dc;
  const int lw = wid - 4, wi = (wid >> 1) & 1, wj = wid & 1;
  constexpr int QS = 24576;
  constexpr int PARK = 98304;
  const rsrc_t rf = mkrsrc(ws + WS_FLAG, F_END + 256);
  const rsrc_t rkv = mkrsrc(ws + WS_KV + (size_t)kvh * 262144, 262144);
  const rsrc_t rxb = mkrsrc(ws + WS_XB + (size_t)tile * 393216, 393216);
  struct Strm { rsrc_t r; unsigned v0, v1, step, p0, p1, p2; int rot; };
  auto strm_x = [&](Strm& S, const char* base, unsigned bytes, unsigned step, int xr) {
    const int ln = lane_id();
    S.r = mkrsrc(base, bytes); S.v0 = ln * 16 + (xr & 1) * 1024; S.v1 = S.v0 ^ 1024; S.step = step;
    S.p0 = (xr >> 1) * 2048; S.p1 = S.p0 ^ 2048; S.p2 = 4096; S.rot = 0;
  };
  auto strm_w = [&](Strm& S, const char* base, unsigned rows, unsigned ld, bool nat) {
    const int ln = lane_id(), q4 = ln >> 4;
    const unsigned c0 = ((ln & 15) ^ (q4 << 1)) * 16;
    S.r = mkrsrc(base, (rows - 1) * ld + 256); S.v0 = q4 * ld + c0; S.v1 = ((nat ? 8 : 4) + q4) * ld + (c0 ^ 128); S.step = 32 * ld;
    S.p0 = 0; S.p1 = (nat ? 16 : 8) * ld; S.p2 = (nat ? 4 : 16) * ld; S.rot = 0;
  };
  auto issue = [&](const Strm& S, int k, int src = -1) {
    const unsigned s = lb + (k & 3) * QS + lw * 8192;
    if (src < 0) src = (k + S.rot) & 31;
#pragma unroll
    for (int i = 0; i < 4; ++i) {
      const unsigned so = src * S.step + ((i & 1) ? S.p1 : S.p0) + (i >> 1) * S.p2;
      dma16s(S.r, s + i * 2048, S.v0, so); dma16s(S.r, s + i * 2048 + 1024, S.v1, so);
    }
  };
  constexpr int QW = 98304, HST = 131072, HRS = 2112, RST = HST + 8 * HRS, PKQ = 148480, STT = 151552, CSX = 153600;
  const int lane0 = tid & 63;
  const rsrc_t rq = lw < 2 ? mkrsrc(x + (size_t)(rb * 256 + lw * 128) * DM, 128 * DM * 2) : mkrsrc((const char*)wq + head * 256, (DM - 1) * HD * 2 + 256);
  const unsigned qv0 = lw < 2 ? (lane0 >> 3) * 2048 + (((lane0 & 7) ^ (lane0 >> 4)) * 16) : (lane0 >> 4) * (HD * 2) + (((lane0 & 15) ^ ((lane0 >> 4) << 1)) * 16);
  const unsigned qv1 = lw < 2 ? (qv0 ^ 64) + 8 * 2048 : (qv0 ^ 128) + 8 * (HD * 2);
  const bool qx = lw < 2;
  const unsigned qp1 = qx ? 32768 : 16 * HD * 2, qp2 = qx ? 65536 : 4 * HD * 2;
  const unsigned qlb = lb + (qx ? lw * 16384 : QW), qlw = qx ? 98304 : 32768, qom = qx ? 0x20780 : 0x7c0000;
  unsigned ql = qx ? 65536 : 0, qdl = 8192, qo = rb * (qx ? 256 : 4 * 32 * HD * 2), qdo = qx ? 131072 : 32 * HD * 2;
  auto issq = [&]() {
    const unsigned s = qlb + ql;
    dma16(rq, s, qv0, qo); dma16(rq, s + 1024, qv1, qo);
    dma16(rq, s + 2048, qv0, qo + qp1); dma16(rq, s + 3072, qv1, qo + qp1);
    dma16(rq, s + 4096, qv0, qo + qp2); dma16(rq, s + 5120, qv1, qo + qp2);
    dma16(rq, s + 6144, qv0, qo + qp1 + qp2); dma16(rq, s + 7168, qv1, qo + qp1 + qp2);
    ql += qdl; ql = ql >= qlw ? ql - qlw : ql; qdl ^= qx ? (8192 ^ 24576) : 0;
    qo = (qo + qdo) & qom; qdo ^= qx ? 128 : 0;
  };
  auto h_out = [&](int it) {
    const rsrc_t rh = mkrsrc(ws + WS_HT + (size_t)(blockIdx.x >> 2) * 16384 + (blockIdx.x & 3) * 256, 3 * 1048576 + 16384);
    const unsigned a0 = HST + ((lane0 >> 2) & 3) * HRS + (it * 4 + (lane0 >> 4)) * 32 + (lane0 & 3) * 8;
    s16x4 lo = __builtin_amdgcn_ds_read_tr16_b64_v4i16((LAS s16x4*)(lds + a0));
    s16x4 hi = __builtin_amdgcn_ds_read_tr16_b64_v4i16((LAS s16x4*)(lds + a0 + 4 * HRS));
    st16s<0>(__builtin_bit_cast(u32x4, __builtin_shufflevector(lo, hi, 0, 1, 2, 3, 4, 5, 6, 7)), rh, (lane0 >> 4) * 1024 + (lane0 & 15) * 16,
             (it >> 2) * 1048576 + (it & 3) * 4096);
  };
  auto st_out = [&]() { st16s<0>(*(LAS u32x4*)(lds + RST + (lane0 & 3) * 16), mkrsrc(ws + WS_ST + blockIdx.x * 64, 64), lane0 * 16, 0); };
  Tag tag;
  {
    const int lane = lane0;
    if (wid >= 4 && wid < 7) {
      issq(); issq();
      if (lw == 2) { issq(); issq(); wait_vm<32>(); } else wait_vm<16>();
    } else wait_vm<0>();
    asm volatile("" : "+v"(xa), "+v"(xb), "+v"(ep));
    unsigned v[8] = {xa[0], xa[1], xa[2], xa[3], xb[0], xb[1], xb[2], xb[3]};
    const unsigned long long e64 = ((unsigned long long)(unsigned)__builtin_amdgcn_readfirstlane(ep[1]) << 32) | (unsigned)__builtin_amdgcn_readfirstlane(ep[0]);
    const unsigned long long tg = e64 * 0x9E3779B97F4A7C15ull + 0x632BE59BD9B4E019ull;
    tag.lo = (unsigned)tg; tag.hi = (unsigned)(tg >> 32);
    *(LAS f32x2*)(lds + RST + wid * 8) = ln16(v);
    ln16(v);
    *(LAS u32x4*)(lds + HST + wid * HRS + lane * 16) = (u32x4){v[0], v[1], v[2], v[3]};
    *(LAS u32x4*)(lds + HST + wid * HRS + 1024 + lane * 16) = (u32x4){v[4], v[5], v[6], v[7]};
    wait_lgkm0();
  }
  bf16x8 qh[8];
  f32x4 uk[4][4];
  auto kv8 = [&](int t, int slot) {
    const unsigned v = lane_id() * 16;
    dma4k(rkv, lb + slot * 32768 + lw * 8192, v, t * 32768 + lw * 8192);
    dma4k(rkv, lb + slot * 32768 + lw * 8192 + 4096, v, t * 32768 + lw * 8192 + 4096);
  };
  if (wid < 4) {
    f32x4 acc[8][4], cs[2];
    Frags F0, F1;
    Addr a;
    int xs = 2, wsl = 1;
    {
      const int ln = lane_id(), r = ln & 15;
      addr_init(a, 0, lb + QW, wj, 2 * wi, ln);
      a.x = lb + 65536 + wi * 16384 + r * 128 + (((ln >> 4) ^ (r >> 1)) * 16);
    }
    bar();
    load_frags<2048>(F0, a);
    a.x ^= 64;
#pragma unroll
    for (int c = 0; c < 4; ++c) a.w[c] += 8192;
    wait_lgkm0();
    bar();
    {
      const unsigned o1 = 0x3f803f80;
      u32x4 ones = {o1, o1, o1, o1};
      qstep<1, true, true>(acc, cs, ones, o1, F1, F0, a, xs, wsl);
      for (int t = 0; t < 2; ++t) {
        const unsigned on = t == 0 ? o1 : 0;
        qstep<0, true>(acc, cs, ones, on, F0, F1, a, xs, wsl);
        qstep<1, true>(acc, cs, ones, on, F1, F0, a, xs, wsl);
      }
      const int ln = lane_id();
      const unsigned csa = lb + CSX + ((ln & 15) ? 7168 : (64 * wj + 32 * wi) * 4) + (ln & 48);
      *(LAS f32x4*)(size_t)csa = cs[0];
      *(LAS f32x4*)(size_t)(csa + 64) = cs[1];
      for (int t = 0; t < 13; ++t) { qstep<0, false>(acc, cs, ones, 0, F0, F1, a, xs, wsl); qstep<1, false>(acc, cs, ones, 0, F1, F0, a, xs, wsl); }
      addr_init(a, lb + wi * 8192, lb + 16384, wj, 0, lane_id());
      qstep<2, false>(acc, cs, ones, 0, F0, F1, a, xs, wsl);
    }
    f32x4 c0[4];
    {
      const int ln = lane_id(), g = ln >> 4;
      u32x2 bv[4];
#pragma unroll
      for (int j = 0; j < 4; ++j) bv[j] = *(LAS u32x2*)(lds + CSX + 5120 + (64 * wj + 16 * j + 4 * g) * 2);
      f32x4 cq[4];
#pragma unroll
      for (int j = 0; j < 4; ++j) cq[j] = *(LAS f32x4*)(lds + CSX + 512 + (64 * wj + 16 * (j ^ (2 * wi)) + 4 * g) * 4);
      f32x2 st[8];
#pragma unroll
      for (int i = 0; i < 8; ++i) st[i] = *(LAS f32x2*)(lds + STT + (128 * wi + 16 * i + (ln & 15)) * 8);
      wait_lgkm0();
      bar();
      const unsigned qa = lb + 98304 + wi * 32768 + (2 * wj + wi) * 1024 + ln * 16, qb = qa ^ 1024;
#pragma unroll
      for (int i = 0; i < 8; ++i) {
        const float sc = st[i][1] * 0.12751743082f, nm = -st[i][0];
#pragma unroll
        for (int mm = 0; mm < 2; ++mm)
          *(LAS u32x4*)(size_t)((mm ? qb : qa) + i * 4096) =
              pack8((acc[i][2 * mm] + cq[2 * mm] * nm) * sc, (acc[i][2 * mm + 1] + cq[2 * mm + 1] * nm) * sc);
      }
#pragma unroll
      for (int j = 0; j < 4; ++j) c0[j] = uh ? (f32x4){0.f, 0.f, 0.f, 0.f} : (f32x4){bflo(bv[j][0]), bfhi(bv[j][0]), bflo(bv[j][1]), bfhi(bv[j][1])};
    }
    if (hasU) {
      int slot = 1;
      gstep<true, 4, QS, true>(acc, F1, F0, a, slot, c0);
      gemm_steps<true, 4, QS>(acc, F0, F1, a, slot, 30);
      gstep<true, 4, QS>(acc, F0, F1, a, slot);
      const rsrc_t rux = mkrsrc(ws + WS_XB + (size_t)((tile ^ 32) * 4 + kvh) * 98304 + 49152 + wid * 8192, 8192);
      const int ln = lane_id();
#pragma unroll
      for (int ii = 0; ii < 4; ++ii) {
        st16s<0>(pack8(acc[2 * ii + 1][0], acc[2 * ii + 1][1]), rux, ln * 16, ii * 2048);
        st16s<1024>(pack8(acc[2 * ii + 1][2], acc[2 * ii + 1][3]), rux, ln * 16, ii * 2048);
#pragma unroll
        for (int j = 0; j < 4; ++j) uk[ii][j] = acc[2 * ii][j];
      }
    }
  } else {
    const int lane = lane0;
    Strm U;
    if (lw < 2) strm_x(U, ws + WS_HT + (size_t)dq * 1048576 + uh * 524288 + lw * 8192, 524288 - lw * 8192, 16384, uh);
    else strm_w(U, (const char*)w1 + (size_t)uh * 1024 * (FF * 2) + ft * 256, 1024, FF * 2, true);
    if (!hasU) U.r = mkrsrc(ws, 0);
    U.rot = 8 * dq;
    const unsigned pk = lb + PKQ + lw * 512;
    if (lw < 3) {
      if (lw < 2) wait_vm<0>(); else wait_vm<24>();
      bar();
      if (lw < 2) { issq(); issq(); } else wait_vm<16>();
      bar();
      if (lw < 2) issq();
      for (int t = 1; t <= 21; ++t) { issq(); wait_vm<16>(); bar(); }
      peek(rf, pk, F_L + lw * 512); peek(rf, pk + 256, F_L + lw * 512 + 256);
      for (int t = 22; t <= 26; ++t) { issq(); wait_vm<16>(); bar(); }
      issq(); wait_vm<16>();
    } else {
      peek(mkrsrc(b1, FF * 2), lb + CSX + 5120, ft * 256);
      bar();
      for (int t = 0; t <= 7; ++t) { h_out(2 * t); h_out(2 * t + 1); bar(); }
      st_out();
      st16s<0>(*(LAS u32x4*)(lds + CSX + (lane & 31) * 16), mkrsrc(ws + WS_CS + (head * 8 + rb) * 512, 512), lane * 16, 0);
      bar(); bar(); bar();
      wait_vm<0>();
      signal<1>(ws, F_L + blockIdx.x * 8, tag);
      for (int t = 11; t <= 21; ++t) bar();
      peek(rf, pk, F_L + lw * 512); peek(rf, pk + 256, F_L + lw * 512 + 256);
      for (int t = 22; t <= 26; ++t) bar();
      wait_vm<0>();
    }
    peek_check(ws, pk, F_L + lw * 512, 32, tag); peek_check(ws, pk + 256, F_L + lw * 512 + 256, 32, tag);
    bar();
    if (blockIdx.x == 0 && lw == 3) signal<1>(ws, F_EP, tag);
    if (lw == 2) issq();
    if (lw == 3) {
      const rsrc_t rs = mkrsrc(ws + WS_ST + rb * 2048, 2048);
      dma16s(rs, lb + STT, lane * 16, 0); dma16s(rs, lb + STT + 1024, lane * 16, 1024);
      dma4k(mkrsrc(ws + WS_CS + head * 4096, 4096), lb + CSX + 1024, lane * 16, 0);
    }
    const bool us = lw < 3;
    if (us) issue(U, 0);
    wait_vm<16>(); bar();
    if (us) wait_vm<8>(); else wait_vm<0>();
    bar();
    if (us) issue(U, 1);
    else {
      const LAS f32x2* cp = (const LAS f32x2*)(lds + CSX + 1024) + lane;
      *(LAS f32x2*)(lds + CSX + 512 + lane * 8) = ((cp[0] + cp[64]) + (cp[128] + cp[192])) + ((cp[256] + cp[320]) + (cp[384] + cp[448]));
      wait_lgkm0();
    }
    bar();
    wait_vm<8>(); bar();
    if (us) issue(U, 2);
    wait_vm<8>();
    bar();
    bar();
    if (hasU) {
      if (us) { issue(U, 3); for (int t = 1; t <= 28; ++t) { issue(U, t + 3); wait_vm<16>(); bar(); } }
      else {
        for (int t = 1; t <= 26; ++t) bar();
        spin_until(ws, F_X + kvh * 128, 8, tag);
        pf_line<7>(wid, rcode, lane * 128, code_o);
        bar(); bar();
      }
      wait_vm<8>(); bar();
      kv8(0, 0); wait_vm<8>(); bar();
      kv8(1, 1); bar();
      kv8(2, 2); wait_vm<8>(); bar();
    }
  }
  if (!hasU) {
    const int sp = g8 * 4 + (idx - 28), lane = lane_id(), g = lane >> 4;
    unsigned e[4][8];
#pragma unroll
    for (int i = 0; i < 4; ++i) {
      const int kh = sp >> 3, unit = (sp & 7) * 4 + i, frag = wid;
      const bool isK = unit == 0 || ((unit & 1) && unit < 31);
      const u16* sk = kc + ((size_t)kh * SEQ + 1536 + 32 * ((unit + 1) >> 1) + 16 * (frag >> 2) + (lane & 15)) * 128 + 32 * (frag & 3) + 4 * g;
      const u16* sv = vc + ((size_t)kh * SEQ + 1536 + 32 * ((unit - 1) >> 1) + 4 * g) * 128 + 16 * frag + (lane & 15);
      const u16* src = isK ? sk : sv;
      const int s1 = isK ? 1 : 128, s4 = isK ? 16 : 2048;
#pragma unroll
      for (int j2 = 0; j2 < 8; ++j2) e[i][j2] = src[(j2 >> 2) * s4 + (j2 & 3) * s1];
    }
#pragma unroll
    for (int i = 0; i < 4; ++i)
      st16s<0>((u32x4){e[i][0] | (e[i][1] << 16), e[i][2] | (e[i][3] << 16), e[i][4] | (e[i][5] << 16), e[i][6] | (e[i][7] << 16)},
               mkrsrc(ws + WS_KV, 1048576), (wid * 64 + lane) * 16, (sp * 4 + i) * 8192);
    wait_vm<0>();
    wait_lgkm0();
    bar();
    if (wid == 0) signal<1>(ws, F_X + (sp >> 3) * 128 + (sp & 7) * 8, tag);
    unsigned pw[NSP];
#pragma unroll
    for (int i = 0; i < NSP; ++i) pw[i] = *(const unsigned*)((const char*)w2 + (size_t)i * 2097152 + (size_t)(sp * 512 + wid * 64 + lane) * 128);
#pragma unroll
    for (int i = 0; i < NSP; ++i) asm volatile("" ::"v"(pw[i]));
    if (wid >= 4) { spin_until(ws, F_X + kvh * 128, 8, tag); kv8(0, 0); kv8(1, 1); kv8(2, 2); wait_vm<8>(); }
    bar();
  }
  {
    const int ln = lane_id();
#pragma unroll
    for (int ks = 0; ks < 8; ++ks) qh[ks] = *(LAS bf16x8*)(lds + 98304 + wid * 8192 + ks * 1024 + ln * 16);
  }
  Strm Y;
  auto y_iss = [&](int k) { const int r = k + 7 * (dc & 3); issue(Y, k, r >= 28 ? r - 28 : r); };
  Frags FY;
  Addr aY;
  {
    const int lane = lane_id();
    Att A;
#pragma unroll
    for (int ks = 0; ks < 8; ++ks) A.q[ks >> 2][ks & 3] = qh[ks];
    A.ones = (bf16x8){0x3f80, 0x3f80, 0x3f80, 0x3f80, 0x3f80, 0x3f80, 0x3f80, 0x3f80};
    asm volatile("" : "+v"(A.ones));
    const unsigned kvl = lb + lane * 16;
#pragma unroll
    for (int i = 0; i < 4; ++i) A.pre[i] = ldfrag(kvl + i * 1024);
    const bool strm = wid >= 4 && wid < 7, hw = wid >= 4;
    const rsrc_t rp2 = mkrsrc(w2, SEQ * FF * 2), rpo = mkrsrc(wo, HD * DM * 2);
    const unsigned pv = (blockIdx.x * 64 + lane) * 128;
#define PF(W, R, C) pf_line<W>(wid, R, pv, (C) * 2097152u)
    const int fp = F_P + (ut * 2 + uh) * 64 + (wid & 3) * 8;
    const bool cu = !hw && hasU;
    att_tile<0>(A, kvl + 0 * 32768, kvl + 1 * 32768, [&](int n) __attribute__((always_inline)) { if (n == 20 && cu) { wait_vm<0>(); signal<1>(ws, fp ^ 64, tag); } });
    if (hw) wait_vm<0>();
    bar();
    att_tile<1>(A, kvl + 1 * 32768, kvl + 2 * 32768, [&](int n) __attribute__((always_inline)) {
      if (n == 0 && hw) kv8(3, 3); if (n == 12 && hw) kv8(4, 0); if (n == 28 && hw) peek(rf, lb + 131072 + lw * 256, fp); if (n == 4) PF(0, rp2, 3); if (n == 9) PF(1, rp2, 4); });
    if (hw) wait_vm<9>();
    bar();
    att_tile<1>(A, kvl + 2 * 32768, kvl + 3 * 32768, [&](int n) __attribute__((always_inline)) {
      if (n == 8 && hw) {
        wait_vm<0>();
        if (hasU) peek_check(ws, lb + 131072 + lw * 256, fp, 1, tag);
        const rsrc_t rux = mkrsrc(ws + WS_XB + (size_t)(tile * 4 + kvh) * 98304 + 49152, hasU ? 32768 : 0);
        dma4k(rux, lb + 32768 + lw * 8192, lane * 16, lw * 8192); dma4k(rux, lb + 32768 + lw * 8192 + 4096, lane * 16, lw * 8192 + 4096);
      }
      if (n == 27) PF(3, rp2, 5); });
    if (hw) wait_vm<0>();
    bar();
    if (hw) { kv8(5, 4); kv8(7, 2); }
    if (cu) {
      u32x4 p[8];
      p[0] = ldsr(kvl + 32768 + wid * 8192);
#pragma unroll
      for (int k = 0; k < 8; ++k) {
        if (k < 7) p[k + 1] = ldsr(kvl + 32768 + wid * 8192 + (k + 1) * 1024);
        f32x4 a = uk[k >> 1][2 * (k & 1)], b = uk[k >> 1][2 * (k & 1) + 1];
        add8(a, b, p[k]);
        p[k] = (u32x4){gelu2(a.xy), gelu2(a.zw), gelu2(b.xy), gelu2(b.zw)};
        asm volatile("" : "+v"(p[k]));
        if (k == 0) PF(2, rp2, 6);
        SB0();
      }
      bar();
      const rsrc_t ru = mkrsrc(ws + WS_U + (size_t)((2 * dq + wi) * 112 + 4 * ft + 2 * wj) * 8192 + uh * 1024, 16384);
#pragma unroll
      for (int k = 0; k < 8; ++k) st16s<0>(p[k], ru, lane * 16, (k & 1) * 8192 + (k >> 1) * 2048);
    } else { PF(2, rp2, 6); bar(); }
    att_tile<1>(A, kvl + 3 * 32768, kvl + 0 * 32768, [&](int n) __attribute__((always_inline)) {
      if (n == 20 && cu) { wait_vm<0>(); signal<1>(ws, F_U + ((2 * dq + wi) * 4 + kvh) * 256 + (((idx >> 2) * 2 + uh) * 2 + wj) * 8, tag); } });
    if (hw) wait_vm<0>();
    bar();
    att_tile<1>(A, kvl + 0 * 32768, kvl + 4 * 32768, [&](int n) __attribute__((always_inline)) { if (n == 0 && hw) kv8(6, 3); if (n == 6) PF(3, rpo, 0); if (n == 15) PF(0, rpo, 1); if (n == 24) PF(1, rpo, 2); });
    if (hw) wait_vm<0>();
    bar();
    att_tile<1>(A, kvl + 4 * 32768, kvl + 3 * 32768, [&](int n) __attribute__((always_inline)) { if (n == 3) PF(2, rpo, 3); });
    if (lw == 0) strm_x(Y, ws + WS_U + (size_t)(dc * 112 + 28 * kvh) * 8192, 28 * 8192, 8192, 0);
    else strm_w(Y, (const char*)w2 + (size_t)kvh * 896 * (SEQ * 2) + rb * 512 + ((lw - 1) & 1) * 256, 896, SEQ * 2, false);
    bar();
    att_tile<1>(A, kvl + 3 * 32768, kvl + 2 * 32768, [&](int n) __attribute__((always_inline)) {
      if (n == 4 && wid == 4) peek(rf, lb + 49152, F_U + (dc * 4 + kvh) * 256);
      if (n == 18 && wid == 4) { wait_vm<0>(); peek_check(ws, lb + 49152, F_U + (dc * 4 + kvh) * 256, 28, tag); }
      if (n == 19 && strm) y_iss(0); if (n == 27 && strm) y_iss(1); });
    bar();
    att_tile<2>(A, kvl + 2 * 32768, 0, [&](int n) __attribute__((always_inline)) {
      if (n == 9) { if (hw) wait_vm<8>(); bar(); }
      if (n == 10 && strm) y_iss(2); });
    if (__builtin_expect(__any(!(A.l[0][0] + A.l[1][0] <= 1.0e9f)), 0)) att_redo(A, rkv, lane * 16);
    addr_init(aY, lb, lb + 8192 + wi * 8192, wj, kvh, lane);
    if (!hw) load_frags(FY, aY);
#pragma unroll
    for (int qb = 0; qb < 2; ++qb) {
      const float inv = __builtin_amdgcn_rcpf(A.l[qb][0]);
#pragma unroll
      for (int d = 0; d < 4; ++d) *(LAS u32x4*)(size_t)(kvl + PARK + wid * 2048 + qb * 1024 + d * 16384) = pack8(A.o[2 * d][qb] * inv, A.o[2 * d + 1][qb] * inv);
    }
    wait_lgkm0();
  }
  if (wid < 4) {
    f32x4 acc[8][4];
    Frags F0 = FY, F1;
    Addr a = aY;
    int slot = 1;
    addr_add(a, QS);
    bar();
    gstep<false, 4, QS, true>(acc, F1, F0, a, slot);
    gemm_steps<false, 4, QS>(acc, F0, F1, a, slot, 26);
    addr_init(a, lb + wi * 8192, lb + 16384, wj, 0, lane_id());
    gstep<false, 4, QS>(acc, F0, F1, a, slot);
    {
      const int lm = lane_id();
#pragma unroll
      for (int m = 0; m < 4; ++m) *(LAS u32x4*)(size_t)(lb + PARK + lm * 16 + wid * 4096 + m * 1024) = pack8(acc[2 * m][0], acc[2 * m + 1][0]);
#pragma unroll
      for (int j = 1; j < 4; ++j)
#pragma unroll
        for (int m = 0; m < 4; ++m) st16s<0>(pack8(acc[2 * m][j], acc[2 * m + 1][j]), rxb, lm * 16, (kvh ^ j) * 98304 + (j - 1) * 16384 + wid * 4096 + m * 1024);
    }
    gstep<true, 4, QS, true>(acc, F1, F0, a, slot);
    gemm_steps<true, 4, QS>(acc, F0, F1, a, slot, 8);
    wait_vm<0>();
    gemm_steps<true, 4, QS>(acc, F0, F1, a, slot, 22);
    gstep<true, 4, QS>(acc, F0, F1, a, slot);
    const int lt = lane_id(), g = lt >> 4;
    const unsigned wo_ = wi * 8192 + wj * 2048;
#pragma unroll
    for (int dl = 1; dl < 4; ++dl)
#pragma unroll
      for (int h = 0; h < 2; ++h)
#pragma unroll
        for (int mm = 0; mm < 2; ++mm)
          st16s<0>(pack8(acc[4 * h + dl][2 * mm], acc[4 * h + dl][2 * mm + 1]), rxb, lt * 16, (kvh ^ dl) * 98304 + 49152 + (dl - 1) * 16384 + wo_ + h * 4096 + mm * 1024);
    const int fs = F_S + tile * 512 + wid * 32;
    wait_vm<8>(); signal<1>(ws, fs + (kvh ^ 1) * 128, tag);
    wait_vm<5>(); signal<1>(ws, fs + (kvh ^ 2) * 128 + 8, tag);
    wait_vm<2>(); signal<1>(ws, fs + (kvh ^ 3) * 128 + 16, tag);
    const int srow = rb * 256 + (8 * wi + kvh) * 16;
    const rsrc_t rx = mkrsrc(x + (size_t)srow * DM, 80 * DM * 2), rout = mkrsrc(out + (size_t)srow * DM, 80 * DM * 2);
    const unsigned vo = ((lt & 15) * DM + dc * 128 + wj * 64 + 16 * (g & 1) + 8 * (g >> 1)) * 2;
    u32x4 ps[3][4], xv[4], gv[4];
#pragma unroll
    for (int p = 0; p < 4; ++p) xv[p] = __builtin_amdgcn_raw_buffer_load_b128(rx, vo + (p >> 1) * (64 * DM * 2) + (p & 1) * 64, 0, 0);
    bar();
#pragma unroll
    for (int p = 0; p < 4; ++p) gv[p] = ldsr(lb + PARK + lt * 16 + wo_ + (p >> 1) * 4096 + (p & 1) * 1024);
    {
      GAS unsigned long long* fp = (GAS unsigned long long*)(ws + WS_FLAG + fs + kvh * 128) + (lt & 3);
      const unsigned long long want = ((unsigned long long)tag.hi << 32) | tag.lo;
      unsigned got = 0;
      for (unsigned sp = 0; got != 7; ++sp) {
        unsigned now = (unsigned)__ballot(__hip_atomic_load(fp, __ATOMIC_RELAXED, __HIP_MEMORY_SCOPE_AGENT) == want) & 7;
        if (sp >= (1u << 20)) now = 7;
        asm volatile("" ::: "memory");
#pragma unroll
        for (int k = 0; k < 3; ++k)
          if ((now & ~got) >> k & 1) {
#pragma unroll
            for (int p = 0; p < 4; ++p)
              ps[k][p] = __builtin_amdgcn_raw_buffer_load_b128(rxb, lt * 16 + (p & 1) * 1024, kvh * 98304 + 49152 + k * 16384 + wo_ + (p >> 1) * 4096, 16);
          }
        got |= now;
        if (got != 7) __builtin_amdgcn_s_sleep(1);
      }
    }
#pragma unroll
    for (int p = 0; p < 4; ++p) {
      f32x4 s0 = acc[4 * (p >> 1)][2 * (p & 1)], s1 = acc[4 * (p >> 1)][2 * (p & 1) + 1];
#pragma unroll
      for (int k = 0; k < 3; ++k) add8(s0, s1, ps[k][p]);
      s0 *= (f32x4){bflo(gv[p][0]), bfhi(gv[p][0]), bflo(gv[p][1]), bfhi(gv[p][1])};
      s1 *= (f32x4){bflo(gv[p][2]), bfhi(gv[p][2]), bflo(gv[p][3]), bfhi(gv[p][3])};
      const u32x4 tp = pack8(s0, s1);
      const auto w0 = __builtin_amdgcn_permlane16_swap(tp[0], tp[2], false, false), w1 = __builtin_amdgcn_permlane16_swap(tp[1], tp[3], false, false);
      const unsigned t[4] = {w0[0], w1[0], w0[1], w1[1]};
      u32x4 o;
#pragma unroll
      for (int e = 0; e < 4; ++e) o[e] = cvtpk(bflo(xv[p][e]) + bflo(t[e]), bfhi(xv[p][e]) + bfhi(t[e]));
      __builtin_amdgcn_raw_buffer_store_b128(o, rout, vo + (p >> 1) * (64 * DM * 2) + (p & 1) * 64, 0, 2);
    }
  } else {
    const int lane = lane_id();
    const unsigned pl = lb + PARK + lane * 16, pk = lb + 159744 + lw * 256;
    const int fo = F_O + (rb * 4 + kvh) * 128;
    auto y_fetch = [&]() {
#pragma unroll
      for (int k = 0; k < 3; ++k) dma4k(rxb, lb + PARK + 16384 + k * 16384 + lw * 4096, lane * 16, kvh * 98304 + lw * 4096 + k * 16384);
    };
    if (lw < 3) {
      Strm S;
      if (lw < 2) strm_x(S, ws + WS_O + (size_t)(rb * 128 + 32 * kvh) * 16384 + lw * 8192, 524288 - lw * 8192, 16384, kvh);
      else strm_w(S, (const char*)wo + (size_t)kvh * 1024 * (DM * 2) + dc * 256, 1024, DM * 2, false);
      wait_vm<8>();
      bar();
      y_iss(3);
      for (int t = 1; t <= 22; ++t) { y_iss(t + 3); wait_vm<16>(); bar(); }
      peek(rf, lb + PARK + lw * 256, fo);
      y_iss(26); wait_vm<16>(); bar();
      y_iss(27); wait_vm<16>(); bar();
      peek_check(ws, lb + PARK + lw * 256, fo, 8, tag);
      auto iss = [&](int k) { const int r = (k & 16) | ((k + 4 * (rb & 3)) & 15); issue(S, k, 4 * (r & 7) + (r >> 3)); wait_vm<16>(); bar(); };
      for (int t = 25; t <= 36; ++t) iss(t - 25);
      peek(rf, lb + PARK + 16384 + lw * 256, fo + 64);
      for (int t = 37; t <= 40; ++t) iss(t - 25);
      peek_check(ws, lb + PARK + 16384 + lw * 256, fo + 64, 8, tag);
      for (int t = 41; t <= 52; ++t) iss(t - 25);
      y_fetch();
      for (int t = 53; t <= 56; ++t) iss(t - 25);
      wait_vm<8>(); bar();
      wait_vm<0>(); bar();
    } else {
      bar();
      const rsrc_t ro = mkrsrc(ws + WS_O + (size_t)(rb * 128 + 4 * head) * 16384, 65536);
      for (int t = 1; t <= 25; t += 2) {
        const unsigned o = (t >> 1) * 5120;
        const u32x4 v0 = ldsr(pl + o), v1 = ldsr(pl + o + 1024), v2 = ldsr(pl + o + 2048), v3 = ldsr(pl + o + 3072), v4 = ldsr(pl + (t < 25 ? o + 4096 : 0));
        st16s<0>(v0, ro, lane * 16, o); st16s<1024>(v1, ro, lane * 16, o); st16s<2048>(v2, ro, lane * 16, o);
        if (t == 17) { wait_vm<11>(); signal<1>(ws, fo + dc * 8, tag); }
        bar();
        st16s<3072>(v3, ro, lane * 16, o); st16s<0>(v4, ro, lane * 16, o + 4096);
        bar();
      }
      bar(); bar(); bar();
      wait_vm<0>();
      signal<1>(ws, fo + 64 + dc * 8, tag);
      for (int t = 30; t <= 38; ++t) bar();
      signal<14>(ws, F_Y + (tile * 4 + (kvh ^ (lane & 3))) * 128 + ((lane & 3) - 1) * 8, tag);
      for (int t = 39; t <= 49; ++t) bar();
      peek(rf, pk, F_Y + (tile * 4 + kvh) * 128);
      bar(); bar();
      wait_vm<0>();
      peek_check(ws, pk, F_Y + (tile * 4 + kvh) * 128, 3, tag);
      bar();
      y_fetch();
      for (int t = 53; t <= 58; ++t) bar();
    }
    wait_vm<0>();
    const float bb = bflo(b2[rb * 256 + (4 * lw + kvh) * 16 + (lane & 15)]);
    bar();
    bar();
#pragma unroll
    for (int m = 0; m < 4; ++m) {
      f32x4 y0 = {bb, bb, bb, bb}, y1 = y0;
      add8(y0, y1, ldsr(pl + lw * 4096 + m * 1024));
#pragma unroll
      for (int k = 1; k < 4; ++k) add8(y0, y1, ldsr(pl + k * 16384 + lw * 4096 + m * 1024));
      *(LAS u32x4*)(size_t)(pl + lw * 4096 + m * 1024) = (u32x4){silu2(y0.xy), silu2(y0.zw), silu2(y1.xy), silu2(y1.zw)};
    }
    wait_lgkm0();
    bar();
  }
}

extern "C" void kernel_launch(void* const* d_in, const int* in_sizes, int n_in, void* d_out, int out_size, void* d_ws, size_t ws_size,
                              hipStream_t stream) {
  const u16* x = (const u16*)d_in[0];
  const u16* kc = (const u16*)d_in[1];
  const u16* vc = (const u16*)d_in[2];
  const u16* wq = (const u16*)d_in[3];
  const u16* wo = (const u16*)d_in[4];
  const u16* w1 = (const u16*)d_in[6];
  const u16* b1 = (const u16*)d_in[7];
  const u16* w2 = (const u16*)d_in[8];
  const u16* b2 = (const u16*)d_in[9];
  char* ws = (char*)d_ws;
  (void)hipFuncSetAttribute((const void*)k_main, hipFuncAttributeMaxDynamicSharedMemorySize, 163840);
  hipLaunchKernelGGL(k_main, dim3(256), dim3(512), 163840, stream, x, kc, vc, wq, wo, w1, b1, w2, b2, ws, (u16*)d_out);
}
```

```cpp
#include <hip/hip_runtime.h>
#include <hip/hip_bf16.h>

typedef unsigned short u16;
typedef short bf16x8 __attribute__((ext_vector_type(8)));
typedef short s16x4 __attribute__((ext_vector_type(4)));
typedef float f32x4 __attribute__((ext_vector_type(4)));
typedef float f32x2 __attribute__((ext_vector_type(2)));
typedef unsigned u32x4 __attribute__((ext_vector_type(4)));
typedef unsigned u32x2 __attribute__((ext_vector_type(2)));
typedef __bf16 bf16x2 __attribute__((ext_vector_type(2)));
typedef __amdgpu_buffer_rsrc_t rsrc_t;
#define LAS __attribute__((address_space(3)))
#define GAS __attribute__((address_space(1)))
#define DI __device__ __forceinline__
#define SGB(m, n) __builtin_amdgcn_sched_group_barrier(m, n, 0)
#define SB0() __builtin_amdgcn_sched_barrier(0)

constexpr int SEQ = 2048, DM = 1024, FF = 3584, HD = 4096;
constexpr int CODE_BYTES = 57600;
constexpr int NSP = 3;
constexpr size_t WS_FLAG = 0;
constexpr int F_EP = 0, F_X = 512, F_L = 2048, F_U = 4096, F_O = 12288, F_Y = 16384, F_S = 49152, F_P = 81920, F_END = 96256;
constexpr size_t WS_ST = 98304;
constexpr size_t WS_CS = 131072;
constexpr size_t WS_HT = 262144;
constexpr size_t WS_KV = WS_HT + 4194304;
constexpr size_t WS_O = WS_KV + 1048576;
constexpr size_t WS_U = WS_O + 16777216;
constexpr size_t WS_XB = WS_U + 7340032;
constexpr size_t WS_END = WS_XB + 25165824;

DI unsigned cvtpk(float lo, float hi) { f32x2 v = {lo, hi}; bf16x2 b = __builtin_convertvector(v, bf16x2); return __builtin_bit_cast(unsigned, b); }
DI float bflo(unsigned u) { return __uint_as_float(u << 16); }
DI float bfhi(unsigned u) { return __uint_as_float(u & 0xffff0000u); }
DI float rbf(float x) { return bflo(cvtpk(x, x)); }
template <int N> DI void wait_vm() { asm volatile("s_waitcnt vmcnt(%0)" ::"i"(N) : "memory"); }
DI void wait_lgkm0() { __builtin_amdgcn_s_waitcnt(0xC07F); }
DI void bar() { asm volatile("" ::: "memory"); __builtin_amdgcn_s_barrier(); asm volatile("" ::: "memory"); }
DI rsrc_t mkrsrc(const void* p, unsigned bytes) { return __builtin_amdgcn_make_buffer_rsrc((void*)p, 0, bytes, 0x00020000); }
DI void dma16(rsrc_t r, unsigned lds_addr, unsigned voff, unsigned soff) {
  asm volatile("s_mov_b32 m0, %0\n\ts_nop 0\n\tbuffer_load_dwordx4 %1, %2, %3 offen lds" ::"s"(lds_addr), "v"(voff), "s"(r), "s"(soff) : "memory");
}
DI void dma16s(rsrc_t r, unsigned lds_addr, unsigned voff, unsigned soff) {
  asm volatile("s_mov_b32 m0, %0\n\ts_nop 0\n\tbuffer_load_dwordx4 %1, %2, %3 offen sc1 lds" ::"s"(lds_addr), "v"(voff), "s"(r), "s"(soff) : "memory");
}
template <int OFF> DI void st16s(u32x4 v, rsrc_t r, unsigned voff, unsigned soff) {
  asm volatile("buffer_store_dwordx4 %0, %1, %2, %3 offen offset:%4 sc1\n\ts_nop 1" ::"v"(v), "v"(voff), "s"(r), "s"(soff), "i"(OFF) : "memory");
}
DI int lane_id() { int l; asm volatile("v_mbcnt_lo_u32_b32 %0, -1, 0\n\tv_mbcnt_hi_u32_b32 %0, -1, %0" : "=v"(l)); return l; }
struct Tag { unsigned lo, hi; };
DI void peek(rsrc_t rf, unsigned lds_addr, int off) {
  asm volatile("s_mov_b32 m0, %0\n\ts_nop 0\n\tbuffer_load_dword %3, %1, %2 offen sc1 lds" ::"s"(lds_addr), "s"(rf), "s"(off), "v"(lane_id() * 4) : "memory");
}
template <int GAP = 1> DI void spin_until(char* ws, int off, int n, Tag t) {
  const int ln = lane_id();
  GAS unsigned long long* p = (GAS unsigned long long*)(ws + WS_FLAG + off) + (ln < n ? ln : 0);
  const unsigned long long want = ((unsigned long long)t.hi << 32) | t.lo;
  for (unsigned sp = 0; sp < (1u << 20); ++sp) {
    if (__all(__hip_atomic_load(p, __ATOMIC_RELAXED, __HIP_MEMORY_SCOPE_AGENT) == want)) break;
    __builtin_amdgcn_s_sleep(GAP);
  }
  asm volatile("" ::: "memory");
}
DI void peek_check(char* ws, unsigned lds_addr, int off, int n, Tag t) {
  const int ln = lane_id();
  const unsigned v = *(LAS volatile unsigned*)(size_t)(lds_addr + ln * 4);
  if (!__all(ln >= 2 * n || v == ((ln & 1) ? t.hi : t.lo))) spin_until(ws, off, n, t);
}
template <long MASK> DI void signal(char* ws, int off, Tag t) {
  const u32x2 v = {t.lo, t.hi};
  asm volatile("s_mov_b64 exec, %3\n\tglobal_store_dwordx2 %0, %1, %2 sc1\n\ts_mov_b64 exec, -1" ::"v"(off), "v"(v), "s"(ws + WS_FLAG), "s"(MASK) : "memory");
}
DI void dma4k(rsrc_t r, unsigned lds_addr, unsigned voff, unsigned soff) {
  asm volatile("s_mov_b32 m0, %0\n\ts_nop 0\n\tbuffer_load_dwordx4 %1, %2, %3 offen sc1 lds\n\tbuffer_load_dwordx4 %1, %2, %3 offen offset:1024 sc1 lds\n\t"
               "buffer_load_dwordx4 %1, %2, %3 offen offset:2048 sc1 lds\n\tbuffer_load_dwordx4 %1, %2, %3 offen offset:3072 sc1 lds"
               ::"s"(lds_addr), "v"(voff), "s"(r), "s"(soff) : "memory");
}
template <int W> DI void pf_line(int wid, rsrc_t r, unsigned voff, unsigned soff) {
  asm volatile("s_cmp_lg_u32 %0, %4\n\ts_cbranch_scc1 1f\n\tbuffer_load_dword v255, %1, %2, %3 offen\n1:"
               ::"s"(wid), "v"(voff), "s"(r), "s"(soff), "i"(W) : "scc", "memory", "v255");
}
template <int CTRL> DI float dppf(float x) { return __builtin_bit_cast(float, __builtin_amdgcn_mov_dpp(__builtin_bit_cast(int, x), CTRL, 0xf, 0xf, true)); }
DI float wave_sum(float v) {
  v += dppf<0xB1>(v); v += dppf<0x4E>(v); v += dppf<0x141>(v); v += dppf<0x140>(v);
  auto a = __builtin_amdgcn_permlane16_swap(__float_as_uint(v), __float_as_uint(v), false, false);
  v = __uint_as_float(a[0]) + __uint_as_float(a[1]);
  auto b = __builtin_amdgcn_permlane32_swap(__float_as_uint(v), __float_as_uint(v), false, false);
  return __uint_as_float(b[0]) + __uint_as_float(b[1]);
}

DI f32x2 ln16(unsigned (&w)[8]) {
  f32x2 v[8];
#pragma unroll
  for (int i = 0; i < 8; ++i) v[i] = (f32x2){bflo(w[i]), bfhi(w[i])};
  const f32x2 s = ((v[0] + v[1]) + (v[2] + v[3])) + ((v[4] + v[5]) + (v[6] + v[7]));
  const float mean = wave_sum(s[0] + s[1]) * (1.f / 1024.f);
  f32x2 q0 = {0.f, 0.f}, q1 = q0;
#pragma unroll
  for (int i = 0; i < 8; i += 2) {
    const f32x2 d0 = v[i] - mean, d1 = v[i + 1] - mean;
    q0 = __builtin_elementwise_fma(d0, d0, q0); q1 = __builtin_elementwise_fma(d1, d1, q1);
  }
  q0 += q1;
  const float var = wave_sum(q0[0] + q0[1]) * (1.f / 1024.f);
  const float mb = rbf(mean);
  const float rs = __builtin_amdgcn_rcpf(rbf(__builtin_amdgcn_sqrtf(rbf(rbf(var) + 1e-5f))));
#pragma unroll
  for (int i = 0; i < 8; ++i) {
    const f32x2 d = v[i] - mb;
    const unsigned c = cvtpk(d[0], d[1]);
    const f32x2 r = (f32x2){bflo(c), bfhi(c)} * rs;
    w[i] = cvtpk(r[0], r[1]);
  }
  return (f32x2){mb, rs};
}

struct Frags { bf16x8 x[8]; bf16x8 w[4]; };
struct Addr { unsigned x, w[4]; };
template <int XS = 1024> DI void load_frags(Frags& f, const Addr& a) {
#pragma unroll
  for (int c = 0; c < 4; ++c) {
    s16x4 a0 = __builtin_amdgcn_ds_read_tr16_b64_v4i16((LAS s16x4*)(size_t)(a.w[c]));
    s16x4 a1 = __builtin_amdgcn_ds_read_tr16_b64_v4i16((LAS s16x4*)(size_t)(a.w[c] + 4096));
    f.w[c] = __builtin_shufflevector(a0, a1, 0, 1, 2, 3, 4, 5, 6, 7);
  }
#pragma unroll
  for (int i = 0; i < 8; ++i) f.x[i] = *(LAS bf16x8*)(size_t)(a.x + i * XS);
}
template <bool WA, bool Z> DI void mma_all(f32x4 (&acc)[8][4], const Frags& f, const f32x4* c0) {
  const f32x4 zero = {0.f, 0.f, 0.f, 0.f};
#pragma unroll
  for (int i = 0; i < 8; ++i)
#pragma unroll
    for (int jj = 0; jj < 4; ++jj) {
      const int j = (i & 1) ? 3 - jj : jj;
      if (WA) acc[i][j] = __builtin_amdgcn_mfma_f32_16x16x32_bf16(f.w[j], f.x[i], Z ? (c0 ? c0[j] : zero) : acc[i][j], 0, 0, 0);
      else    acc[i][j] = __builtin_amdgcn_mfma_f32_16x16x32_bf16(f.x[i], f.w[j], Z ? (c0 ? c0[j] : zero) : acc[i][j], 0, 0, 0);
    }
}
DI void addr_add(Addr& a, int d) {
  a.x += d;
#pragma unroll
  for (int c = 0; c < 4; ++c) a.w[c] += d;
}
template <bool WA, int NSLOT, int SLOTB, bool Z = false>
DI void gstep(f32x4 (&acc)[8][4], Frags& Fn, const Frags& Fc, Addr& a, int& slot, const f32x4* c0 = nullptr) {
  SB0();
  load_frags(Fn, a);
  mma_all<WA, Z>(acc, Fc, c0);
  slot = (slot + 1 == NSLOT) ? 0 : slot + 1;
  addr_add(a, (slot == 0) ? -(NSLOT - 1) * SLOTB : SLOTB);
  asm volatile("" : "+v"(a.x), "+v"(a.w[0]), "+v"(a.w[1]), "+v"(a.w[2]), "+v"(a.w[3]));
#pragma unroll
  for (int i = 0; i < 8; ++i) { SGB(0x8, 1); SGB(0x100, 1); SGB(0x8, 1); SGB(0x100, 1); SGB(0x8, 1); }
#pragma unroll
  for (int i = 0; i < 6; ++i) { SGB(0x8, 1); SGB(0x4, 1); SGB(0x2, 1); }
  SGB(0x8, 2);
  SB0();
  wait_lgkm0();
  bar();
}
template <int MODE, bool CS, bool Z = false>
DI void qstep(f32x4 (&acc)[8][4], f32x4 (&cs)[2], u32x4& ones, unsigned onext, Frags& Fn, const Frags& Fc, Addr& a, int& xs, int& wsl) {
  const f32x4 zero = {0.f, 0.f, 0.f, 0.f};
  SB0();
  load_frags<MODE == 2 ? 1024 : 2048>(Fn, a);
  if (CS) {
#pragma unroll
    for (int j = 0; j < 2; ++j) cs[j] = __builtin_amdgcn_mfma_f32_16x16x32_bf16(Fc.w[j], __builtin_bit_cast(bf16x8, ones), Z ? zero : cs[j], 0, 0, 0);
    ones = (u32x4){onext, onext, onext, onext};
    asm volatile("" : "+v"(ones));
  }
  mma_all<true, Z>(acc, Fc, nullptr);
  if (MODE == 2) addr_add(a, 24576);
  else {
    wsl = (wsl + 1) & 3;
    const int dw = wsl ? 8192 : -24576;
#pragma unroll
    for (int c = 0; c < 4; ++c) a.w[c] += dw;
    a.x ^= 64;
    if (MODE == 1) { xs = (xs == 2) ? 0 : xs + 1; a.x += xs ? 32768 : -65536; }
  }
  asm volatile("" : "+v"(a.x), "+v"(a.w[0]), "+v"(a.w[1]), "+v"(a.w[2]), "+v"(a.w[3]));
#pragma unroll
  for (int i = 0; i < 8; ++i) { SGB(0x8, 1); SGB(0x100, 1); SGB(0x8, 1); SGB(0x100, 1); SGB(0x8, 1); }
#pragma unroll
  for (int i = 0; i < 7; ++i) { SGB(0x8, 1); SGB(0x4, 1); SGB(0x2, 1); }
  if (CS) { SGB(0x8, 1); SGB(0x2, 2); SGB(0x8, 1); SGB(0x2, 2); }
  SGB(0x8, 1);
  SB0();
  wait_lgkm0();
  bar();
}
DI void addr_init(Addr& a, unsigned xbase, unsigned wbase, int wj, int cx, int lane) {
  const int g = lane >> 4, q = (lane >> 2) & 3, p = lane & 3, row = 4 * g + q;
  a.x = xbase + lane * 16;
#pragma unroll
  for (int c = 0; c < 4; ++c) a.w[c] = wbase + row * 256 + (((2 * (4 * wj + (c ^ cx)) + (p >> 1)) ^ ((row & 7) << 1)) * 16) + (p & 1) * 8;
}
template <bool WA, int NSLOT, int SLOTB> DI void gemm_steps(f32x4 (&acc)[8][4], Frags& F0, Frags& F1, Addr& a, int& slot, int n) {
  for (int t = 0; t < n; t += 2) {
    gstep<WA, NSLOT, SLOTB>(acc, F0, F1, a, slot);
    gstep<WA, NSLOT, SLOTB>(acc, F1, F0, a, slot);
  }
}

DI u32x4 pack8(const f32x4& a, const f32x4& b) { return (u32x4){cvtpk(a[0], a[1]), cvtpk(a[2], a[3]), cvtpk(b[0], b[1]), cvtpk(b[2], b[3])}; }
DI f32x2 gate2(f32x2 u, f32x2 z) {
  f32x2 e = {__builtin_amdgcn_exp2f(z[0]), __builtin_amdgcn_exp2f(z[1])};
  e += 1.f;
  return u * (f32x2){__builtin_amdgcn_rcpf(e[0]), __builtin_amdgcn_rcpf(e[1])};
}
DI unsigned gelu2(f32x2 u) {
  const f32x2 r = gate2(u, u * __builtin_elementwise_fma(u * u, (f32x2){-0.1029432397f, -0.1029432397f}, (f32x2){-2.302208198f, -2.302208198f}));
  return cvtpk(r[0], r[1]);
}
DI unsigned silu2(f32x2 y) { const f32x2 r = gate2(y, y * -1.4426950409f); return cvtpk(r[0], r[1]); }
DI u32x4 ldsr(unsigned a) { return *(LAS u32x4*)(size_t)a; }
DI void add8(f32x4& a, f32x4& b, const u32x4& w) {
  a += (f32x4){bflo(w[0]), bfhi(w[0]), bflo(w[1]), bfhi(w[1])};
  b += (f32x4){bflo(w[2]), bfhi(w[2]), bflo(w[3]), bfhi(w[3])};
}

struct Sc { f32x4 v[2][2]; };
struct Att { bf16x8 q[2][4]; f32x4 o[8][2]; f32x4 negm[2]; Sc sE, sO; u32x4 pO[2]; bf16x8 pre[4], ones; f32x4 l[2]; };
DI float xg_max(float m) {
  auto a = __builtin_amdgcn_permlane16_swap(__float_as_uint(m), __float_as_uint(m), false, false);
  m = fmaxf(__uint_as_float(a[0]), __uint_as_float(a[1]));
  auto b = __builtin_amdgcn_permlane32_swap(__float_as_uint(m), __float_as_uint(m), false, false);
  return fmaxf(__uint_as_float(b[0]), __uint_as_float(b[1]));
}
DI float row_max(const Sc& s, int qb) {
  const f32x4 a = s.v[0][qb], b = s.v[1][qb];
  return xg_max(fmaxf(fmaxf(fmaxf(a[0], a[1]), fmaxf(a[2], a[3])), fmaxf(fmaxf(b[0], b[1]), fmaxf(b[2], b[3]))));
}
DI bf16x8 ldfrag(unsigned a) { return *(LAS bf16x8*)(size_t)a; }
DI void sm1(Sc& s, int e, u32x4 (&w)[2], int part = 3) {
  if (e < 16 && (part & 1)) {
    float x = __builtin_amdgcn_exp2f(s.v[e >> 3][(e >> 2) & 1][e & 3]);
    asm volatile("" : "+v"(x));
    s.v[e >> 3][(e >> 2) & 1][e & 3] = x;
  }
  if (e >= 2 && e <= 16 && !(e & 1) && (part & 2)) {
    const int kt = (e - 1) >> 3, qb = ((e - 1) >> 2) & 1, r = (e - 1) & 3;
    unsigned c = cvtpk(s.v[kt][qb][r - 1], s.v[kt][qb][r]);
    asm volatile("" : "+v"(c));
    w[qb][2 * kt + (r >> 1)] = c;
  }
}
#define SETPRIO(x) __builtin_amdgcn_s_setprio(x)
#define MFMA16(a, b, c) __builtin_amdgcn_mfma_f32_16x16x32_bf16(a, b, c, 0, 0, 0)
#define PFRAG(w) __builtin_bit_cast(bf16x8, w)
DI void mfma_acc(f32x4& c, const bf16x8& a, const u32x4& b) { asm volatile("v_mfma_f32_16x16x32_bf16 %0, %1, %2, %0" : "+v"(c) : "v"(a), "v"(b)); }
template <int POS, int n, class F> DI void att_slot(Att& A, bf16x8 (&f)[36], u32x4 (&pE)[2], unsigned kv, unsigned kvn, F& gap) {
  constexpr int u = n >> 3, k = n & 7;
  constexpr bool isqk = (u & 1) ? (u == 1 || POS < 2) : (u == 0 && POS == 0);
  const f32x4 zero = {0.f, 0.f, 0.f, 0.f};
  if constexpr (k == 0) SETPRIO(3 - u);
#pragma unroll
  for (int qb = 0; qb < 2; ++qb) {
    if constexpr (isqk && (k & 3) != 0) mfma_acc(u == 1 ? A.sO.v[k >> 2][qb] : A.sE.v[k >> 2][qb], f[n], __builtin_bit_cast(u32x4, A.q[qb][k & 3]));
    else if constexpr (isqk && u == 1) A.sO.v[k >> 2][qb] = MFMA16(f[n], A.q[qb][0], A.negm[qb]);
    else if constexpr (isqk) A.sE.v[k >> 2][qb] = MFMA16(f[n], A.q[qb][0], u ? A.negm[qb] : zero);
    else if constexpr (POS == 0) A.o[k][qb] = MFMA16(f[n], PFRAG(pE[qb]), zero);
    else if constexpr (u == 2) mfma_acc(A.o[k][qb], f[n], pE[qb]);
    else mfma_acc(A.o[k][qb], f[n], A.pO[qb]);
    SB0();
    if (qb == 1) {
      if constexpr (n < 28) f[n + 4] = ldfrag(kv + (n + 4) * 1024);
      else if constexpr (POS < 2) f[n + 4] = ldfrag(kvn + (n - 28) * 1024);
      SB0();
    }
    if constexpr (POS == 0 && u == 1) {
      if (k == 0 && qb == 0) sm1(A.sE, 0, pE);
      sm1(A.sE, 2 * k + 1 + qb, pE);
    }
    if constexpr (POS > 0 && n <= 14) sm1(A.sE, n + 2, pE, 1 + qb);
    if constexpr (n >= 14 && n <= (POS < 2 ? 30 : 15)) sm1(A.sO, n - 14, A.pO, 1 + qb);
    if constexpr (POS < 2 && n >= 30) sm1(A.sE, n - 30, pE, 1 + qb);
    if constexpr (POS == 2 && u == 2) sm1(A.sO, 2 + 2 * k + qb, A.pO);
    SB0();
  }
  if constexpr (!isqk && (k == 2 || k == 4)) {
    constexpr int qb = k == 4;
    if constexpr (POS == 0) A.l[qb] = MFMA16(A.ones, PFRAG(pE[qb]), zero);
    else if constexpr (u == 2) mfma_acc(A.l[qb], A.ones, pE[qb]);
    else mfma_acc(A.l[qb], A.ones, A.pO[qb]);
  }
  gap(n);
  SB0();
  if constexpr (POS == 2 && n == 23) { asm volatile("s_nop 1"); SB0(); }
  if constexpr ((POS == 2 && n == 31) || (POS == 0 && n == 7)) { asm volatile("s_nop 9"); SB0(); }
  if constexpr (POS == 0 && n == 7) {
#pragma unroll
    for (int qb = 0; qb < 2; ++qb) {
      const float mx = row_max(A.sE, qb);
      A.sE.v[0][qb] -= mx; A.sE.v[1][qb] -= mx;
      A.negm[qb] = (f32x4){-mx, -mx, -mx, -mx};
    }
    SB0();
  }
  if constexpr (n < 31) att_slot<POS, n + 1>(A, f, pE, kv, kvn, gap);
}
template <int POS, class F> DI void att_tile(Att& A, unsigned kv, unsigned kvn, F&& gap) {
  bf16x8 f[36];
  u32x4 pE[2];
  asm volatile("" : "+v"(kv), "+v"(kvn));
#pragma unroll
  for (int i = 0; i < 4; ++i) f[i] = A.pre[i];
  SB0();
  att_slot<POS, 0>(A, f, pE, kv, kvn, gap);
  if (POS < 2) {
#pragma unroll
    for (int i = 0; i < 4; ++i) A.pre[i] = f[32 + i];
  }
}
DI void att_redo(Att& A, rsrc_t rkv, unsigned vo) {
  const f32x4 zero = {0.f, 0.f, 0.f, 0.f};
  float m[2] = {-3.0e38f, -3.0e38f};
  A.l[0] = A.l[1] = zero;
#pragma unroll
  for (int d = 0; d < 8; ++d) A.o[d][0] = A.o[d][1] = zero;
  for (int blk = 0; blk < 16; ++blk) {
    const int kb = (blk ? 2 * blk - 1 : 0) * 8192, vb = (blk < 15 ? 2 * blk + 2 : 31) * 8192;
    Sc s;
#pragma unroll
    for (int n = 0; n < 8; ++n) {
      const bf16x8 kf = PFRAG(__builtin_amdgcn_raw_buffer_load_b128(rkv, vo + n * 1024, kb, 16));
#pragma unroll
      for (int qb = 0; qb < 2; ++qb) s.v[n >> 2][qb] = MFMA16(kf, A.q[qb][n & 3], (n & 3) ? s.v[n >> 2][qb] : zero);
    }
#pragma unroll
    for (int qb = 0; qb < 2; ++qb) {
      const float mn = fmaxf(m[qb], row_max(s, qb)), fct = __builtin_amdgcn_exp2f(m[qb] - mn);
      m[qb] = mn;
      A.l[qb] *= fct;
      s.v[0][qb] -= mn; s.v[1][qb] -= mn;
#pragma unroll
      for (int d = 0; d < 8; ++d) A.o[d][qb] *= fct;
    }
    u32x4 p[2];
#pragma unroll
    for (int i = 0; i < 17; ++i) sm1(s, i, p);
#pragma unroll
    for (int qb = 0; qb < 2; ++qb) A.l[qb] = MFMA16(A.ones, PFRAG(p[qb]), A.l[qb]);
#pragma unroll
    for (int n = 0; n < 8; ++n) {
      const bf16x8 vf = PFRAG(__builtin_amdgcn_raw_buffer_load_b128(rkv, vo + n * 1024, vb, 16));
#pragma unroll
      for (int qb = 0; qb < 2; ++qb) A.o[n][qb] = MFMA16(vf, PFRAG(p[qb]), A.o[n][qb]);
    }
  }
}

__global__ __launch_bounds__(512, 2) __attribute__((amdgpu_num_vgpr(127))) void k_main(const u16* __restrict__ x, const u16* __restrict__ kc, const u16* __restrict__ vc, const u16* __restrict__ wq, const u16* __restrict__ wo,
                                                 const u16* __restrict__ w1, const u16* __restrict__ b1, const u16* __restrict__ w2,
                                                 const u16* __restrict__ b2, char* __restrict__ ws, u16* __restrict__ out) {
  extern __shared__ __attribute__((aligned(16))) char smem[];
  LAS char* lds = (LAS char*)smem;
  const unsigned lb = (unsigned)(size_t)lds;
  const int tid = threadIdx.x, wid = __builtin_amdgcn_readfirstlane(tid >> 6);
  const int g8 = blockIdx.x & 7, idx = blockIdx.x >> 3;
  const rsrc_t rcode = mkrsrc((const void*)(__builtin_amdgcn_s_getpc() & ~255ull), CODE_BYTES);
  const unsigned code_o = (idx & 7) * 8192;
  pf_line<0>(wid, rcode, (tid & 63) * 128, code_o);
  SB0();
  u32x4 xa, xb;
  u32x2 ep;
  asm volatile("global_load_dwordx4 %0, %3, %4\n\tglobal_load_dwordx4 %1, %3, %4 offset:1024\n\tglobal_load_dwordx2 %2, %5, %6 sc1"
               : "=&v"(xa), "=&v"(xb), "=&v"(ep) : "v"((tid & 63) * 16), "s"(x + (size_t)(8 * blockIdx.x + wid) * DM), "v"(F_EP), "s"(ws + WS_FLAG) : "memory");
  SB0();
  const bool hasU = idx < 28;
  const int uh = g8 & 1, dq = idx & 3, ft = 7 * (g8 >> 1) + (idx >> 2), ut = ft * 4 + dq;
  const int kvh = g8 >> 1, head = 8 * kvh + (idx & 7), rb = 4 * (g8 & 1) + (idx >> 3);
  const int dc = idx & 7, tile = rb * 8 + dc;
  const int lw = wid - 4, wi = (wid >> 1) & 1, wj = wid & 1;
  constexpr int QS = 24576;
  constexpr int PARK = 98304;
  const rsrc_t rf = mkrsrc(ws + WS_FLAG, F_END + 256);
  const rsrc_t rkv = mkrsrc(ws + WS_KV + (size_t)kvh * 262144, 262144);
  const rsrc_t rxb = mkrsrc(ws + WS_XB + (size_t)tile * 393216, 393216);
  struct Strm { rsrc_t r; unsigned v0, v1, step, p0, p1, p2; int rot; };
  auto strm_x = [&](Strm& S, const char* base, unsigned bytes, unsigned step, int xr) {
    const int ln = lane_id();
    S.r = mkrsrc(base, bytes); S.v0 = ln * 16 + (xr & 1) * 1024; S.v1 = S.v0 ^ 1024; S.step = step;
    S.p0 = (xr >> 1) * 2048; S.p1 = S.p0 ^ 2048; S.p2 = 4096; S.rot = 0;
  };
  auto strm_w = [&](Strm& S, const char* base, unsigned rows, unsigned ld, bool nat) {
    const int ln = lane_id(), q4 = ln >> 4;
    const unsigned c0 = ((ln & 15) ^ (q4 << 1)) * 16;
    S.r = mkrsrc(base, (rows - 1) * ld + 256); S.v0 = q4 * ld + c0; S.v1 = ((nat ? 8 : 4) + q4) * ld + (c0 ^ 128); S.step = 32 * ld;
    S.p0 = 0; S.p1 = (nat ? 16 : 8) * ld; S.p2 = (nat ? 4 : 16) * ld; S.rot = 0;
  };
  auto issue = [&](const Strm& S, int k, int src = -1) {
    const unsigned s = lb + (k & 3) * QS + lw * 8192;
    if (src < 0) src = (k + S.rot) & 31;
#pragma unroll
    for (int i = 0; i < 4; ++i) {
      const unsigned so = src * S.step + ((i & 1) ? S.p1 : S.p0) + (i >> 1) * S.p2;
      dma16s(S.r, s + i * 2048, S.v0, so); dma16s(S.r, s + i * 2048 + 1024, S.v1, so);
    }
  };
  constexpr int QW = 98304, HST = 131072, HRS = 2112, RST = HST + 8 * HRS, PKQ = 148480, STT = 151552, CSX = 153600;
  const int lane0 = tid & 63;
  const rsrc_t rq = lw < 2 ? mkrsrc(x + (size_t)(rb * 256 + lw * 128) * DM, 128 * DM * 2) : mkrsrc((const char*)wq + head * 256, (DM - 1) * HD * 2 + 256);
  const unsigned qv0 = lw < 2 ? (lane0 >> 3) * 2048 + (((lane0 & 7) ^ (lane0 >> 4)) * 16) : (lane0 >> 4) * (HD * 2) + (((lane0 & 15) ^ ((lane0 >> 4) << 1)) * 16);
  const unsigned qv1 = lw < 2 ? (qv0 ^ 64) + 8 * 2048 : (qv0 ^ 128) + 8 * (HD * 2);
  const bool qx = lw < 2;
  const unsigned qp1 = qx ? 32768 : 16 * HD * 2, qp2 = qx ? 65536 : 4 * HD * 2;
  const unsigned qlb = lb + (qx ? lw * 16384 : QW), qlw = qx ? 98304 : 32768, qom = qx ? 0x20780 : 0x7c0000;
  unsigned ql = qx ? 65536 : 0, qdl = 8192, qo = rb * (qx ? 256 : 4 * 32 * HD * 2), qdo = qx ? 131072 : 32 * HD * 2;
  auto issq = [&]() {
    const unsigned s = qlb + ql;
    dma16(rq, s, qv0, qo); dma16(rq, s + 1024, qv1, qo);
    dma16(rq, s + 2048, qv0, qo + qp1); dma16(rq, s + 3072, qv1, qo + qp1);
    dma16(rq, s + 4096, qv0, qo + qp2); dma16(rq, s + 5120, qv1, qo + qp2);
    dma16(rq, s + 6144, qv0, qo + qp1 + qp2); dma16(rq, s + 7168, qv1, qo + qp1 + qp2);
    ql += qdl; ql = ql >= qlw ? ql - qlw : ql; qdl ^= qx ? (8192 ^ 24576) : 0;
    qo = (qo + qdo) & qom; qdo ^= qx ? 128 : 0;
  };
  auto h_out = [&](int it) {
    const rsrc_t rh = mkrsrc(ws + WS_HT + (size_t)(blockIdx.x >> 2) * 16384 + (blockIdx.x & 3) * 256, 3 * 1048576 + 16384);
    const unsigned a0 = HST + ((lane0 >> 2) & 3) * HRS + (it * 4 + (lane0 >> 4)) * 32 + (lane0 & 3) * 8;
    s16x4 lo = __builtin_amdgcn_ds_read_tr16_b64_v4i16((LAS s16x4*)(lds + a0));
    s16x4 hi = __builtin_amdgcn_ds_read_tr16_b64_v4i16((LAS s16x4*)(lds + a0 + 4 * HRS));
    st16s<0>(__builtin_bit_cast(u32x4, __builtin_shufflevector(lo, hi, 0, 1, 2, 3, 4, 5, 6, 7)), rh, (lane0 >> 4) * 1024 + (lane0 & 15) * 16,
             (it >> 2) * 1048576 + (it & 3) * 4096);
  };
  auto st_out = [&]() { st16s<0>(*(LAS u32x4*)(lds + RST + (lane0 & 3) * 16), mkrsrc(ws + WS_ST + blockIdx.x * 64, 64), lane0 * 16, 0); };
  Tag tag;
  {
    const int lane = lane0;
    if (wid >= 4 && wid < 7) {
      issq(); issq();
      if (lw == 2) { issq(); issq(); wait_vm<32>(); } else wait_vm<16>();
    } else wait_vm<0>();
    asm volatile("" : "+v"(xa), "+v"(xb), "+v"(ep));
    unsigned v[8] = {xa[0], xa[1], xa[2], xa[3], xb[0], xb[1], xb[2], xb[3]};
    const unsigned long long e64 = ((unsigned long long)(unsigned)__builtin_amdgcn_readfirstlane(ep[1]) << 32) | (unsigned)__builtin_amdgcn_readfirstlane(ep[0]);
    const unsigned long long tg = e64 * 0x9E3779B97F4A7C15ull + 0x632BE59BD9B4E019ull;
    tag.lo = (unsigned)tg; tag.hi = (unsigned)(tg >> 32);
    *(LAS f32x2*)(lds + RST + wid * 8) = ln16(v);
    ln16(v);
    *(LAS u32x4*)(lds + HST + wid * HRS + lane * 16) = (u32x4){v[0], v[1], v[2], v[3]};
    *(LAS u32x4*)(lds + HST + wid * HRS + 1024 + lane * 16) = (u32x4){v[4], v[5], v[6], v[7]};
    wait_lgkm0();
  }
  bf16x8 qh[8];
  f32x4 uk[4][4];
  auto kv8 = [&](int t, int slot) {
    const unsigned v = lane_id() * 16;
    dma4k(rkv, lb + slot * 32768 + lw * 8192, v, t * 32768 + lw * 8192);
    dma4k(rkv, lb + slot * 32768 + lw * 8192 + 4096, v, t * 32768 + lw * 8192 + 4096);
  };
  if (wid < 4) {
    f32x4 acc[8][4], cs[2];
    Frags F0, F1;
    Addr a;
    int xs = 2, wsl = 1;
    {
      const int ln = lane_id(), r = ln & 15;
      addr_init(a, 0, lb + QW, wj, 2 * wi, ln);
      a.x = lb + 65536 + wi * 16384 + r * 128 + (((ln >> 4) ^ (r >> 1)) * 16);
    }
    bar();
    load_frags<2048>(F0, a);
    a.x ^= 64;
#pragma unroll
    for (int c = 0; c < 4; ++c) a.w[c] += 8192;
    wait_lgkm0();
    bar();
    {
      const unsigned o1 = 0x3f803f80;
      u32x4 ones = {o1, o1, o1, o1};
      qstep<1, true, true>(acc, cs, ones, o1, F1, F0, a, xs, wsl);
      for (int t = 0; t < 2; ++t) {
        const unsigned on = t == 0 ? o1 : 0;
        qstep<0, true>(acc, cs, ones, on, F0, F1, a, xs, wsl);
        qstep<1, true>(acc, cs, ones, on, F1, F0, a, xs, wsl);
      }
      const int ln = lane_id();
      const unsigned csa = lb + CSX + ((ln & 15) ? 7168 : (64 * wj + 32 * wi) * 4) + (ln & 48);
      *(LAS f32x4*)(size_t)csa = cs[0];
      *(LAS f32x4*)(size_t)(csa + 64) = cs[1];
      for (int t = 0; t < 13; ++t) { qstep<0, false>(acc, cs, ones, 0, F0, F1, a, xs, wsl); qstep<1, false>(acc, cs, ones, 0, F1, F0, a, xs, wsl); }
      addr_init(a, lb + wi * 8192, lb + 16384, wj, 0, lane_id());
      qstep<2, false>(acc, cs, ones, 0, F0, F1, a, xs, wsl);
    }
    f32x4 c0[4];
    {
      const int ln = lane_id(), g = ln >> 4;
      u32x2 bv[4];
#pragma unroll
      for (int j = 0; j < 4; ++j) bv[j] = *(LAS u32x2*)(lds + CSX + 5120 + (64 * wj + 16 * j + 4 * g) * 2);
      f32x4 cq[4];
#pragma unroll
      for (int j = 0; j < 4; ++j) cq[j] = *(LAS f32x4*)(lds + CSX + 512 + (64 * wj + 16 * (j ^ (2 * wi)) + 4 * g) * 4);
      f32x2 st[8];
#pragma unroll
      for (int i = 0; i < 8; ++i) st[i] = *(LAS f32x2*)(lds + STT + (128 * wi + 16 * i + (ln & 15)) * 8);
      wait_lgkm0();
      bar();
      const unsigned qa = lb + 98304 + wi * 32768 + (2 * wj + wi) * 1024 + ln * 16, qb = qa ^ 1024;
#pragma unroll
      for (int i = 0; i < 8; ++i) {
        const float sc = st[i][1] * 0.12751743082f, nm = -st[i][0];
#pragma unroll
        for (int mm = 0; mm < 2; ++mm)
          *(LAS u32x4*)(size_t)((mm ? qb : qa) + i * 4096) =
              pack8((acc[i][2 * mm] + cq[2 * mm] * nm) * sc, (acc[i][2 * mm + 1] + cq[2 * mm + 1] * nm) * sc);
      }
#pragma unroll
      for (int j = 0; j < 4; ++j) c0[j] = uh ? (f32x4){0.f, 0.f, 0.f, 0.f} : (f32x4){bflo(bv[j][0]), bfhi(bv[j][0]), bflo(bv[j][1]), bfhi(bv[j][1])};
    }
    if (hasU) {
      int slot = 1;
      gstep<true, 4, QS, true>(acc, F1, F0, a, slot, c0);
      gemm_steps<true, 4, QS>(acc, F0, F1, a, slot, 30);
      gstep<true, 4, QS>(acc, F0, F1, a, slot);
      const rsrc_t rux = mkrsrc(ws + WS_XB + (size_t)((tile ^ 32) * 4 + kvh) * 98304 + 49152 + wid * 8192, 8192);
      const int ln = lane_id();
#pragma unroll
      for (int ii = 0; ii < 4; ++ii) {
        st16s<0>(pack8(acc[2 * ii + 1][0], acc[2 * ii + 1][1]), rux, ln * 16, ii * 2048);
        st16s<1024>(pack8(acc[2 * ii + 1][2], acc[2 * ii + 1][3]), rux, ln * 16, ii * 2048);
#pragma unroll
        for (int j = 0; j < 4; ++j) uk[ii][j] = acc[2 * ii][j];
      }
    }
  } else {
    const int lane = lane0;
    Strm U;
    if (lw < 2) strm_x(U, ws + WS_HT + (size_t)dq * 1048576 + uh * 524288 + lw * 8192, 524288 - lw * 8192, 16384, uh);
    else strm_w(U, (const char*)w1 + (size_t)uh * 1024 * (FF * 2) + ft * 256, 1024, FF * 2, true);
    if (!hasU) U.r = mkrsrc(ws, 0);
    U.rot = 8 * dq;
    const unsigned pk = lb + PKQ + lw * 512;
    if (lw < 3) {
      if (lw < 2) wait_vm<0>(); else wait_vm<24>();
      bar();
      if (lw < 2) { issq(); issq(); } else wait_vm<16>();
      bar();
      if (lw < 2) issq();
      for (int t = 1; t <= 21; ++t) { issq(); wait_vm<16>(); bar(); }
      peek(rf, pk, F_L + lw * 512); peek(rf, pk + 256, F_L + lw * 512 + 256);
      for (int t = 22; t <= 26; ++t) { issq(); wait_vm<16>(); bar(); }
      issq(); wait_vm<16>();
    } else {
      peek(mkrsrc(b1, FF * 2), lb + CSX + 5120, ft * 256);
      bar();
      for (int t = 0; t <= 7; ++t) { h_out(2 * t); h_out(2 * t + 1); bar(); }
      st_out();
      st16s<0>(*(LAS u32x4*)(lds + CSX + (lane & 31) * 16), mkrsrc(ws + WS_CS + (head * 8 + rb) * 512, 512), lane * 16, 0);
      bar(); bar(); bar();
      wait_vm<0>();
      signal<1>(ws, F_L + blockIdx.x * 8, tag);
      for (int t = 11; t <= 21; ++t) bar();
      peek(rf, pk, F_L + lw * 512); peek(rf, pk + 256, F_L + lw * 512 + 256);
      for (int t = 22; t <= 26; ++t) bar();
      wait_vm<0>();
    }
    peek_check(ws, pk, F_L + lw * 512, 32, tag); peek_check(ws, pk + 256, F_L + lw * 512 + 256, 32, tag);
    bar();
    if (blockIdx.x == 0 && lw == 3) signal<1>(ws, F_EP, tag);
    if (lw == 2) issq();
    if (lw == 3) {
      const rsrc_t rs = mkrsrc(ws + WS_ST + rb * 2048, 2048);
      dma16s(rs, lb + STT, lane * 16, 0); dma16s(rs, lb + STT + 1024, lane * 16, 1024);
      dma4k(mkrsrc(ws + WS_CS + head * 4096, 4096), lb + CSX + 1024, lane * 16, 0);
    }
    const bool us = lw < 3;
    if (us) issue(U, 0);
    wait_vm<16>(); bar();
    if (us) wait_vm<8>(); else wait_vm<0>();
    bar();
    if (us) issue(U, 1);
    else {
      const LAS f32x2* cp = (const LAS f32x2*)(lds + CSX + 1024) + lane;
      *(LAS f32x2*)(lds + CSX + 512 + lane * 8) = ((cp[0] + cp[64]) + (cp[128] + cp[192])) + ((cp[256] + cp[320]) + (cp[384] + cp[448]));
      wait_lgkm0();
    }
    bar();
    wait_vm<8>(); bar();
    if (us) issue(U, 2);
    wait_vm<8>();
    bar();
    bar();
    if (hasU) {
      if (us) { issue(U, 3); for (int t = 1; t <= 28; ++t) { issue(U, t + 3); wait_vm<16>(); bar(); } }
      else {
        for (int t = 1; t <= 26; ++t) bar();
        spin_until(ws, F_X + kvh * 128, 8, tag);
        pf_line<7>(wid, rcode, lane * 128, code_o);
        bar(); bar();
      }
      wait_vm<8>(); bar();
      kv8(0, 0); wait_vm<8>(); bar();
      kv8(1, 1); bar();
      kv8(2, 2); wait_vm<8>(); bar();
    }
  }
  if (!hasU) {
    const int sp = g8 * 4 + (idx - 28), lane = lane_id(), g = lane >> 4;
    unsigned e[4][8];
#pragma unroll
    for (int i = 0; i < 4; ++i) {
      const int kh = sp >> 3, unit = (sp & 7) * 4 + i, frag = wid;
      const bool isK = unit == 0 || ((unit & 1) && unit < 31);
      const u16* sk = kc + ((size_t)kh * SEQ + 1536 + 32 * ((unit + 1) >> 1) + 16 * (frag >> 2) + (lane & 15)) * 128 + 32 * (frag & 3) + 4 * g;
      const u16* sv = vc + ((size_t)kh * SEQ + 1536 + 32 * ((unit - 1) >> 1) + 4 * g) * 128 + 16 * frag + (lane & 15);
      const u16* src = isK ? sk : sv;
      const int s1 = isK ? 1 : 128, s4 = isK ? 16 : 2048;
#pragma unroll
      for (int j2 = 0; j2 < 8; ++j2) e[i][j2] = src[(j2 >> 2) * s4 + (j2 & 3) * s1];
    }
#pragma unroll
    for (int i = 0; i < 4; ++i)
      st16s<0>((u32x4){e[i][0] | (e[i][1] << 16), e[i][2] | (e[i][3] << 16), e[i][4] | (e[i][5] << 16), e[i][6] | (e[i][7] << 16)},
               mkrsrc(ws + WS_KV, 1048576), (wid * 64 + lane) * 16, (sp * 4 + i) * 8192);
    wait_vm<0>();
    wait_lgkm0();
    bar();
    if (wid == 0) signal<1>(ws, F_X + (sp >> 3) * 128 + (sp & 7) * 8, tag);
    unsigned pw[NSP];
#pragma unroll
    for (int i = 0; i < NSP; ++i) pw[i] = *(const unsigned*)((const char*)w2 + (size_t)i * 2097152 + (size_t)(sp * 512 + wid * 64 + lane) * 128);
#pragma unroll
    for (int i = 0; i < NSP; ++i) asm volatile("" ::"v"(pw[i]));
    if (wid >= 4) { spin_until(ws, F_X + kvh * 128, 8, tag); kv8(0, 0); kv8(1, 1); kv8(2, 2); wait_vm<8>(); }
    bar();
  }
  {
    const int ln = lane_id();
#pragma unroll
    for (int ks = 0; ks < 8; ++ks) qh[ks] = *(LAS bf16x8*)(lds + 98304 + wid * 8192 + ks * 1024 + ln * 16);
  }
  Strm Y;
  auto y_iss = [&](int k) { const int r = k + 7 * (dc & 3); issue(Y, k, r >= 28 ? r - 28 : r); };
  Frags FY;
  Addr aY;
  {
    const int lane = lane_id();
    Att A;
#pragma unroll
    for (int ks = 0; ks < 8; ++ks) A.q[ks >> 2][ks & 3] = qh[ks];
    A.ones = (bf16x8){0x3f80, 0x3f80, 0x3f80, 0x3f80, 0x3f80, 0x3f80, 0x3f80, 0x3f80};
    asm volatile("" : "+v"(A.ones));
    const unsigned kvl = lb + lane * 16;
#pragma unroll
    for (int i = 0; i < 4; ++i) A.pre[i] = ldfrag(kvl + i * 1024);
    const bool strm = wid >= 4 && wid < 7, hw = wid >= 4;
    const rsrc_t rp2 = mkrsrc(w2, SEQ * FF * 2), rpo = mkrsrc(wo, HD * DM * 2);
    const unsigned pv = (blockIdx.x * 64 + lane) * 128;
#define PF(W, R, C) pf_line<W>(wid, R, pv, (C) * 2097152u)
    const int fp = F_P + (ut * 2 + uh) * 64 + (wid & 3) * 8;
    const bool cu = !hw && hasU;
    att_tile<0>(A, kvl + 0 * 32768, kvl + 1 * 32768, [&](int n) __attribute__((always_inline)) { if (n == 20 && cu) { wait_vm<0>(); signal<1>(ws, fp ^ 64, tag); } });
    if (hw) wait_vm<0>();
    bar();
    att_tile<1>(A, kvl + 1 * 32768, kvl + 2 * 32768, [&](int n) __attribute__((always_inline)) {
      if (n == 0 && hw) kv8(3, 3); if (n == 12 && hw) kv8(4, 0); if (n == 28 && hw) peek(rf, lb + 131072 + lw * 256, fp); if (n == 4) PF(0, rp2, 3); if (n == 9) PF(1, rp2, 4); });
    if (hw) wait_vm<9>();
    bar();
    att_tile<1>(A, kvl + 2 * 32768, kvl + 3 * 32768, [&](int n) __attribute__((always_inline)) {
      if (n == 8 && hw) {
        wait_vm<0>();
        if (hasU) peek_check(ws, lb + 131072 + lw * 256, fp, 1, tag);
        const rsrc_t rux = mkrsrc(ws + WS_XB + (size_t)(tile * 4 + kvh) * 98304 + 49152, hasU ? 32768 : 0);
        dma4k(rux, lb + 32768 + lw * 8192, lane * 16, lw * 8192); dma4k(rux, lb + 32768 + lw * 8192 + 4096, lane * 16, lw * 8192 + 4096);
      }
      if (n == 27) PF(3, rp2, 5); });
    if (hw) wait_vm<0>();
    bar();
    if (hw) { kv8(5, 4); kv8(7, 2); }
    if (cu) {
      u32x4 p[8];
      p[0] = ldsr(kvl + 32768 + wid * 8192);
#pragma unroll
      for (int k = 0; k < 8; ++k) {
        if (k < 7) p[k + 1] = ldsr(kvl + 32768 + wid * 8192 + (k + 1) * 1024);
        f32x4 a = uk[k >> 1][2 * (k & 1)], b = uk[k >> 1][2 * (k & 1) + 1];
        add8(a, b, p[k]);
        p[k] = (u32x4){gelu2(a.xy), gelu2(a.zw), gelu2(b.xy), gelu2(b.zw)};
        asm volatile("" : "+v"(p[k]));
        if (k == 0) PF(2, rp2, 6);
        SB0();
      }
      bar();
      const rsrc_t ru = mkrsrc(ws + WS_U + (size_t)((2 * dq + wi) * 112 + 4 * ft + 2 * wj) * 8192 + uh * 1024, 16384);
#pragma unroll
      for (int k = 0; k < 8; ++k) st16s<0>(p[k], ru, lane * 16, (k & 1) * 8192 + (k >> 1) * 2048);
    } else { PF(2, rp2, 6); bar(); }
    att_tile<1>(A, kvl + 3 * 32768, kvl + 0 * 32768, [&](int n) __attribute__((always_inline)) {
      if (n == 20 && cu) { wait_vm<0>(); signal<1>(ws, F_U + ((2 * dq + wi) * 4 + kvh) * 256 + (((idx >> 2) * 2 + uh) * 2 + wj) * 8, tag); } });
    if (hw) wait_vm<0>();
    bar();
    att_tile<1>(A, kvl + 0 * 32768, kvl + 4 * 32768, [&](int n) __attribute__((always_inline)) { if (n == 0 && hw) kv8(6, 3); if (n == 6) PF(3, rpo, 0); if (n == 15) PF(0, rpo, 1); if (n == 24) PF(1, rpo, 2); });
    if (hw) wait_vm<0>();
    bar();
    att_tile<1>(A, kvl + 4 * 32768, kvl + 3 * 32768, [&](int n) __attribute__((always_inline)) { if (n == 3) PF(2, rpo, 3); });
    if (lw == 0) strm_x(Y, ws + WS_U + (size_t)(dc * 112 + 28 * kvh) * 8192, 28 * 8192, 8192, 0);
    else strm_w(Y, (const char*)w2 + (size_t)kvh * 896 * (SEQ * 2) + rb * 512 + ((lw - 1) & 1) * 256, 896, SEQ * 2, false);
    bar();
    att_tile<1>(A, kvl + 3 * 32768, kvl + 2 * 32768, [&](int n) __attribute__((always_inline)) {
      if (n == 4 && wid == 4) peek(rf, lb + 49152, F_U + (dc * 4 + kvh) * 256);
      if (n == 18 && wid == 4) { wait_vm<0>(); peek_check(ws, lb + 49152, F_U + (dc * 4 + kvh) * 256, 28, tag); }
      if (n == 19 && strm) y_iss(0); if (n == 27 && strm) y_iss(1); });
    bar();
    att_tile<2>(A, kvl + 2 * 32768, 0, [&](int n) __attribute__((always_inline)) {
      if (n == 9) { if (hw) wait_vm<8>(); bar(); }
      if (n == 10 && strm) y_iss(2); });
    if (__builtin_expect(__any(!(A.l[0][0] + A.l[1][0] <= 1.0e9f)), 0)) att_redo(A, rkv, lane * 16);
    addr_init(aY, lb, lb + 8192 + wi * 8192, wj, kvh, lane);
    if (!hw) load_frags(FY, aY);
#pragma unroll
    for (int qb = 0; qb < 2; ++qb) {
      const float inv = __builtin_amdgcn_rcpf(A.l[qb][0]);
#pragma unroll
      for (int d = 0; d < 4; ++d) *(LAS u32x4*)(size_t)(kvl + PARK + wid * 2048 + qb * 1024 + d * 16384) = pack8(A.o[2 * d][qb] * inv, A.o[2 * d + 1][qb] * inv);
    }
    wait_lgkm0();
  }
  if (wid < 4) {
    f32x4 acc[8][4];
    Frags F0 = FY, F1;
    Addr a = aY;
    int slot = 1;
    addr_add(a, QS);
    bar();
    gstep<false, 4, QS, true>(acc, F1, F0, a, slot);
    gemm_steps<false, 4, QS>(acc, F0, F1, a, slot, 26);
    addr_init(a, lb + wi * 8192, lb + 16384, wj, 0, lane_id());
    gstep<false, 4, QS>(acc, F0, F1, a, slot);
    {
      const int lm = lane_id();
#pragma unroll
      for (int m = 0; m < 4; ++m) *(LAS u32x4*)(size_t)(lb + PARK + lm * 16 + wid * 4096 + m * 1024) = pack8(acc[2 * m][0], acc[2 * m + 1][0]);
#pragma unroll
      for (int j = 1; j < 4; ++j)
#pragma unroll
        for (int m = 0; m < 4; ++m) st16s<0>(pack8(acc[2 * m][j], acc[2 * m + 1][j]), rxb, lm * 16, (kvh ^ j) * 98304 + (j - 1) * 16384 + wid * 4096 + m * 1024);
    }
    gstep<true, 4, QS, true>(acc, F1, F0, a, slot);
    gemm_steps<true, 4, QS>(acc, F0, F1, a, slot, 8);
    wait_vm<0>();
    gemm_steps<true, 4, QS>(acc, F0, F1, a, slot, 22);
    gstep<true, 4, QS>(acc, F0, F1, a, slot);
    const int lt = lane_id(), g = lt >> 4;
    const unsigned wo_ = wi * 8192 + wj * 2048;
#pragma unroll
    for (int dl = 1; dl < 4; ++dl)
#pragma unroll
      for (int h = 0; h < 2; ++h)
#pragma unroll
        for (int mm = 0; mm < 2; ++mm)
          st16s<0>(pack8(acc[4 * h + dl][2 * mm], acc[4 * h + dl][2 * mm + 1]), rxb, lt * 16, (kvh ^ dl) * 98304 + 49152 + (dl - 1) * 16384 + wo_ + h * 4096 + mm * 1024);
    const int fs = F_S + tile * 512 + wid * 32;
    wait_vm<8>(); signal<1>(ws, fs + (kvh ^ 1) * 128, tag);
    wait_vm<5>(); signal<1>(ws, fs + (kvh ^ 2) * 128 + 8, tag);
    wait_vm<2>(); signal<1>(ws, fs + (kvh ^ 3) * 128 + 16, tag);
    const int srow = rb * 256 + (8 * wi + kvh) * 16;
    const rsrc_t rx = mkrsrc(x + (size_t)srow * DM, 80 * DM * 2), rout = mkrsrc(out + (size_t)srow * DM, 80 * DM * 2);
    const unsigned vo = ((lt & 15) * DM + dc * 128 + wj * 64 + 16 * (g & 1) + 8 * (g >> 1)) * 2;
    u32x4 ps[3][4], xv[4], gv[4];
#pragma unroll
    for (int p = 0; p < 4; ++p) xv[p] = __builtin_amdgcn_raw_buffer_load_b128(rx, vo + (p >> 1) * (64 * DM * 2) + (p & 1) * 64, 0, 0);
    bar();
#pragma unroll
    for (int p = 0; p < 4; ++p) gv[p] = ldsr(lb + PARK + lt * 16 + wo_ + (p >> 1) * 4096 + (p & 1) * 1024);
    {
      GAS unsigned long long* fp = (GAS unsigned long long*)(ws + WS_FLAG + fs + kvh * 128) + (lt & 3);
      const unsigned long long want = ((unsigned long long)tag.hi << 32) | tag.lo;
      unsigned got = 0;
      for (unsigned sp = 0; got != 7; ++sp) {
        unsigned now = (unsigned)__ballot(__hip_atomic_load(fp, __ATOMIC_RELAXED, __HIP_MEMORY_SCOPE_AGENT) == want) & 7;
        if (sp >= (1u << 20)) now = 7;
        asm volatile("" ::: "memory");
#pragma unroll
        for (int k = 0; k < 3; ++k)
          if ((now & ~got) >> k & 1) {
#pragma unroll
            for (int p = 0; p < 4; ++p)
              ps[k][p] = __builtin_amdgcn_raw_buffer_load_b128(rxb, lt * 16 + (p & 1) * 1024, kvh * 98304 + 49152 + k * 16384 + wo_ + (p >> 1) * 4096, 16);
          }
        got |= now;
        if (got != 7) __builtin_amdgcn_s_sleep(1);
      }
    }
#pragma unroll
    for (int p = 0; p < 4; ++p) {
      f32x4 s0 = acc[4 * (p >> 1)][2 * (p & 1)], s1 = acc[4 * (p >> 1)][2 * (p & 1) + 1];
#pragma unroll
      for (int k = 0; k < 3; ++k) add8(s0, s1, ps[k][p]);
      s0 *= (f32x4){bflo(gv[p][0]), bfhi(gv[p][0]), bflo(gv[p][1]), bfhi(gv[p][1])};
      s1 *= (f32x4){bflo(gv[p][2]), bfhi(gv[p][2]), bflo(gv[p][3]), bfhi(gv[p][3])};
      const u32x4 tp = pack8(s0, s1);
      const auto w0 = __builtin_amdgcn_permlane16_swap(tp[0], tp[2], false, false), w1 = __builtin_amdgcn_permlane16_swap(tp[1], tp[3], false, false);
      const unsigned t[4] = {w0[0], w1[0], w0[1], w1[1]};
      u32x4 o;
#pragma unroll
      for (int e = 0; e < 4; ++e) o[e] = cvtpk(bflo(xv[p][e]) + bflo(t[e]), bfhi(xv[p][e]) + bfhi(t[e]));
      __builtin_amdgcn_raw_buffer_store_b128(o, rout, vo + (p >> 1) * (64 * DM * 2) + (p & 1) * 64, 0, 2);
    }
  } else {
    const int lane = lane_id();
    const unsigned pl = lb + PARK + lane * 16, pk = lb + 159744 + lw * 256;
    const int fo = F_O + (rb * 4 + kvh) * 128;
    if (lw < 3) {
      Strm S;
      if (lw < 2) strm_x(S, ws + WS_O + (size_t)(rb * 128 + 32 * kvh) * 16384 + lw * 8192, 524288 - lw * 8192, 16384, kvh);
      else strm_w(S, (const char*)wo + (size_t)kvh * 1024 * (DM * 2) + dc * 256, 1024, DM * 2, false);
      wait_vm<8>();
      bar();
      y_iss(3);
      for (int t = 1; t <= 22; ++t) { y_iss(t + 3); wait_vm<16>(); bar(); }
      peek(rf, lb + PARK + lw * 256, fo);
      y_iss(26); wait_vm<16>(); bar();
      y_iss(27); wait_vm<16>(); bar();
      peek_check(ws, lb + PARK + lw * 256, fo, 8, tag);
      auto iss = [&](int k) { const int r = (k & 16) | ((k + 4 * (rb & 3)) & 15); issue(S, k, 4 * (r & 7) + (r >> 3)); wait_vm<16>(); bar(); };
      for (int t = 25; t <= 36; ++t) iss(t - 25);
      peek(rf, lb + PARK + 16384 + lw * 256, fo + 64);
      for (int t = 37; t <= 40; ++t) iss(t - 25);
      peek_check(ws, lb + PARK + 16384 + lw * 256, fo + 64, 8, tag);
      for (int t = 41; t <= 56; ++t) iss(t - 25);
      wait_vm<8>(); bar();
      wait_vm<0>(); bar();
    } else {
      bar();
      const rsrc_t ro = mkrsrc(ws + WS_O + (size_t)(rb * 128 + 4 * head) * 16384, 65536);
      for (int t = 1; t <= 25; t += 2) {
        const unsigned o = (t >> 1) * 5120;
        const u32x4 v0 = ldsr(pl + o), v1 = ldsr(pl + o + 1024), v2 = ldsr(pl + o + 2048), v3 = ldsr(pl + o + 3072), v4 = ldsr(pl + (t < 25 ? o + 4096 : 0));
        st16s<0>(v0, ro, lane * 16, o); st16s<1024>(v1, ro, lane * 16, o); st16s<2048>(v2, ro, lane * 16, o);
        if (t == 17) { wait_vm<11>(); signal<1>(ws, fo + dc * 8, tag); }
        bar();
        st16s<3072>(v3, ro, lane * 16, o); st16s<0>(v4, ro, lane * 16, o + 4096);
        bar();
      }
      bar(); bar(); bar();
      wait_vm<0>();
      signal<1>(ws, fo + 64 + dc * 8, tag);
      for (int t = 30; t <= 38; ++t) bar();
      signal<14>(ws, F_Y + (tile * 4 + (kvh ^ (lane & 3))) * 128 + ((lane & 3) - 1) * 8, tag);
      for (int t = 39; t <= 56; ++t) bar();
      peek(rf, pk, F_Y + (tile * 4 + kvh) * 128);
      bar();
      wait_vm<0>();
      peek_check(ws, pk, F_Y + (tile * 4 + kvh) * 128, 3, tag);
      bar();
    }
    u32x4 py[3][4];
#pragma unroll
    for (int k = 0; k < 3; ++k)
#pragma unroll
      for (int m = 0; m < 4; ++m) py[k][m] = __builtin_amdgcn_raw_buffer_load_b128(rxb, lane * 16 + m * 1024, kvh * 98304 + lw * 4096 + k * 16384, 16);
    const float bb = bflo(b2[rb * 256 + (4 * lw + kvh) * 16 + (lane & 15)]);
    bar();
    bar();
#pragma unroll
    for (int m = 0; m < 4; ++m) {
      f32x4 y0 = {bb, bb, bb, bb}, y1 = y0;
      add8(y0, y1, ldsr(pl + lw * 4096 + m * 1024));
#pragma unroll
      for (int k = 0; k < 3; ++k) add8(y0, y1, py[k][m]);
      *(LAS u32x4*)(size_t)(pl + lw * 4096 + m * 1024) = (u32x4){silu2(y0.xy), silu2(y0.zw), silu2(y1.xy), silu2(y1.zw)};
    }
    wait_lgkm0();
    bar();
  }
}

extern "C" void kernel_launch(void* const* d_in, const int* in_sizes, int n_in, void* d_out, int out_size, void* d_ws, size_t ws_size,
                              hipStream_t stream) {
  const u16* x = (const u16*)d_in[0];
  const u16* kc = (const u16*)d_in[1];
  const u16* vc = (const u16*)d_in[2];
  const u16* wq = (const u16*)d_in[3];
  const u16* wo = (const u16*)d_in[4];
  const u16* w1 = (const u16*)d_in[6];
  const u16* b1 = (const u16*)d_in[7];
  const u16* w2 = (const u16*)d_in[8];
  const u16* b2 = (const u16*)d_in[9];
  char* ws = (char*)d_ws;
  (void)hipFuncSetAttribute((const void*)k_main, hipFuncAttributeMaxDynamicSharedMemorySize, 163840);
  hipLaunchKernelGGL(k_main, dim3(256), dim3(512), 163840, stream, x, kc, vc, wq, wo, w1, b1, w2, b2, ws, (u16*)d_out);
}
```
